```python
import torch

def _softmax(x, axis=-1):
    return torch.softmax(x, dim=axis)
def _gelu(x):
    return torch.nn.functional.gelu(x, approximate="tanh")
def _silu(x):
    return torch.nn.functional.silu(x)
def _sigmoid(x):
    return torch.sigmoid(x)
def _layernorm(x, axis=-1, eps=1e-5):
    m = x.mean(dim=axis, keepdim=True)
    v = x.var(dim=axis, keepdim=True, unbiased=False)
    return (x - m) / torch.sqrt(v + eps)
def _scatter_add(tgt, idx, src, axis=0):
    return tgt.clone().index_add_(axis, idx, src)
def _segment_sum(src, seg, n):
    out = torch.zeros((n,) + src.shape[1:], dtype=src.dtype)
    return out.index_add_(0, seg, src)
def _pad(x, pw):
    flat = tuple(v for pair in reversed(pw) for v in pair)
    return torch.nn.functional.pad(x, flat)

kernel_name = 'recurrent_scan__spectral_fnet__swiglu_ffn__prompt'

def setup_inputs():

    _w = (lambda a: (lambda t: t.to(torch.bfloat16) if t.is_floating_point() else t)(torch.as_tensor(a)))
    def _A_setup():
        B, S, D = 1, 2048, 1024
        ns = torch.arange(S)[:, None] * torch.arange(S)[None, :]
        nd = torch.arange(D)[:, None] * torch.arange(D)[None, :]
        Ws_re = torch.cos(-2.0 * torch.pi * ns / S).to(float)
        Ws_im = torch.sin(-2.0 * torch.pi * ns / S).to(float)
        Wd_re = torch.cos(-2.0 * torch.pi * nd / D).to(float)
        Wd_im = torch.sin(-2.0 * torch.pi * nd / D).to(float)
        return {
            "x": _w((lambda *s: torch.randn(*s, dtype=torch.bfloat16) * 0.05)(B, S, D)),
            "dft_seq_re": _w(Ws_re),
            "dft_seq_im": _w(Ws_im),
            "dft_hid_re": _w(Wd_re),
            "dft_hid_im": _w(Wd_im),
        }
    _A_in = _A_setup()

    def _B_setup():
        B, S, D, F = 1, 2048, 1024, 3584
        return {
            "x": _w((lambda *s: torch.randn(*s, dtype=torch.bfloat16) * 0.05)(B, S, D)),
            "w_gate": _w((lambda *s: torch.randn(*s, dtype=torch.bfloat16) * 0.05)(D, F)),
            "w_up": _w((lambda *s: torch.randn(*s, dtype=torch.bfloat16) * 0.05)(D, F)),
            "w_down": _w((lambda *s: torch.randn(*s, dtype=torch.bfloat16) * 0.05)(F, D)),
        }
    _B_in = _B_setup()

    return {
        "A_x": _A_in["x"],
        "A_dft_seq_re": _A_in["dft_seq_re"],
        "A_dft_seq_im": _A_in["dft_seq_im"],
        "A_dft_hid_re": _A_in["dft_hid_re"],
        "A_dft_hid_im": _A_in["dft_hid_im"],
        "B_x": _B_in["x"],
        "B_w_gate": _B_in["w_gate"],
        "B_w_up": _B_in["w_up"],
        "B_w_down": _B_in["w_down"],
    }

def reference(A_x, A_dft_seq_re, A_dft_seq_im, A_dft_hid_re, A_dft_hid_im, B_x, B_w_gate, B_w_up, B_w_down):
    def _A_ref(x, dft_seq_re, dft_seq_im, dft_hid_re, dft_hid_im):
        a = torch.einsum("bsd,ed->bse", x, dft_hid_re)
        b = torch.einsum("bsd,ed->bse", x, dft_hid_im)
        y = torch.einsum("ts,bse->bte", dft_seq_re, a) - torch.einsum("ts,bse->bte", dft_seq_im, b)
        return y

    def _B_ref(x, w_gate, w_up, w_down):
        g = _silu(x @ w_gate)
        u = x @ w_up
        return (g * u) @ w_down

    x = A_x
    xn = _layernorm(x, axis=-1)
    s = _A_ref(xn, A_dft_seq_re, A_dft_seq_im, A_dft_hid_re, A_dft_hid_im)
    g = _silu(_B_ref(xn, B_w_gate, B_w_up, B_w_down))
    return x + s * g
```

```cpp
#include <hip/hip_runtime.h>
#include <stdint.h>
typedef unsigned short bf16_t;
typedef short bf16x8 __attribute__((ext_vector_type(8)));
typedef float f32x4 __attribute__((ext_vector_type(4)));
constexpr int S=2048, D=1024, F=3584;

__device__ __forceinline__ float bf2f(bf16_t v){ return __uint_as_float(((unsigned)v)<<16); }
__device__ __forceinline__ bf16_t f2bf(float f){ unsigned u=__float_as_uint(f); u += 0x7FFF + ((u>>16)&1); return (bf16_t)(u>>16); }
__device__ __forceinline__ float rbf(float f){ return bf2f(f2bf(f)); }

__global__ void ln_kernel(const bf16_t* x, bf16_t* xn){
  int row = blockIdx.x*4 + (threadIdx.x>>6); int lane = threadIdx.x&63;
  const bf16_t* p = x + (size_t)row*D;
  float v[16]; float s=0;
  for(int i=0;i<16;i++){ v[i]=bf2f(p[lane*16+i]); s+=v[i]; }
  for(int o=32;o;o>>=1) s+=__shfl_xor(s,o);
  float m=s*(1.0f/D); float q=0;
  for(int i=0;i<16;i++){ float d=v[i]-m; q+=d*d; }
  for(int o=32;o;o>>=1) q+=__shfl_xor(q,o);
  float var=rbf(q*(1.0f/D)); float mb=rbf(m);
  float sd=rbf(sqrtf(rbf(var+1e-5f)));
  for(int i=0;i<16;i++){ xn[(size_t)row*D+lane*16+i]=f2bf(rbf(v[i]-mb)/sd); }
}

template<bool BT>
__device__ void gemm_tile(const bf16_t* A, int lda, const bf16_t* B, int ldb, int K, int m0, int n0, f32x4 (&acc)[4][4], bf16_t* As, bf16_t* Bs, float sign){
  int tid=threadIdx.x, lane=tid&63, w=tid>>6, wr=w>>1, wc=w&1, fr=lane&15, fq=lane>>4;
  for(int k0=0;k0<K;k0+=32){
    __syncthreads();
    for(int i=tid;i<128*4;i+=256){ int r=i>>2,c=(i&3)*8; *(bf16x8*)(As+r*40+c)=*(const bf16x8*)(A+(size_t)(m0+r)*lda+k0+c); }
    if(BT){ for(int i=tid;i<128*4;i+=256){ int r=i>>2,c=(i&3)*8; *(bf16x8*)(Bs+r*40+c)=*(const bf16x8*)(B+(size_t)(n0+r)*ldb+k0+c); } }
    else { for(int i=tid;i<32*16;i+=256){ int k=i>>4,c=(i&15)*8; bf16x8 v=*(const bf16x8*)(B+(size_t)(k0+k)*ldb+n0+c); for(int j=0;j<8;j++) Bs[(c+j)*40+k]=(bf16_t)v[j]; } }
    __syncthreads();
    bf16x8 a[4],b[4];
    for(int m=0;m<4;m++) a[m]=*(bf16x8*)(As+(wr*64+m*16+fr)*40+fq*8);
    for(int n=0;n<4;n++) b[n]=*(bf16x8*)(Bs+(wc*64+n*16+fr)*40+fq*8);
    for(int m=0;m<4;m++) for(int n=0;n<4;n++) acc[m][n]=__builtin_amdgcn_mfma_f32_16x16x32_bf16(a[m],b[n],acc[m][n],0,0,0);
  }
}
template<bool BT>
__global__ void gemm_k(const bf16_t* A,int lda,const bf16_t* B,int ldb,int K,bf16_t* C,int ldc){
  __shared__ __attribute__((aligned(16))) bf16_t As[128*40], Bs[128*40];
  f32x4 acc[4][4]={}; int m0=blockIdx.y*128,n0=blockIdx.x*128;
  gemm_tile<BT>(A,lda,B,ldb,K,m0,n0,acc,As,Bs,1.f);
  int lane=threadIdx.x&63,w=threadIdx.x>>6,wr=w>>1,wc=w&1,fr=lane&15,fq=lane>>4;
  for(int m=0;m<4;m++)for(int n=0;n<4;n++)for(int j=0;j<4;j++) C[(size_t)(m0+wr*64+m*16+fq*4+j)*ldc+n0+wc*64+n*16+fr]=f2bf(acc[m][n][j]);
}
__global__ void swiglu_k(const bf16_t* g,const bf16_t* u,bf16_t* h,int n){ int i=blockIdx.x*256+threadIdx.x; if(i<n){ float a=bf2f(g[i]); float s=rbf(a/(1.f+__expf(-a))); h[i]=f2bf(s*bf2f(u[i])); } }
__global__ void final_k(const bf16_t* x,const bf16_t* yr,const bf16_t* yi,const bf16_t* f,bf16_t* o,int n){ int i=blockIdx.x*256+threadIdx.x; if(i<n){ float y=rbf(bf2f(yr[i])-bf2f(yi[i])); float a=bf2f(f[i]); float s=rbf(a/(1.f+__expf(-a))); o[i]=f2bf(bf2f(x[i])+rbf(y*s)); } }

extern "C" void kernel_launch(void* const* d_in,const int* in_sizes,int n_in,void* d_out,int out_size,void* d_ws,size_t ws_size,hipStream_t stream){
  const bf16_t* x=(const bf16_t*)d_in[0]; const bf16_t* sre=(const bf16_t*)d_in[1]; const bf16_t* sim=(const bf16_t*)d_in[2];
  const bf16_t* hre=(const bf16_t*)d_in[3]; const bf16_t* him=(const bf16_t*)d_in[4];
  const bf16_t* wg=(const bf16_t*)d_in[6]; const bf16_t* wu=(const bf16_t*)d_in[7]; const bf16_t* wd=(const bf16_t*)d_in[8];
  bf16_t* ws=(bf16_t*)d_ws; bf16_t* xn=ws; bf16_t* a=xn+S*D; bf16_t* b=a+S*D; bf16_t* g=b+S*D; bf16_t* u=g+S*F; bf16_t* h=u+S*F; bf16_t* yr=h+S*F; bf16_t* yi=yr+S*D; bf16_t* ff=yi+S*D;
  ln_kernel<<<S/4,256,0,stream>>>(x,xn);
  gemm_k<true><<<dim3(D/128,S/128),256,0,stream>>>(xn,D,hre,D,D,a,D);
  gemm_k<true><<<dim3(D/128,S/128),256,0,stream>>>(xn,D,him,D,D,b,D);
  gemm_k<false><<<dim3(F/128,S/128),256,0,stream>>>(xn,D,wg,F,D,g,F);
  gemm_k<false><<<dim3(F/128,S/128),256,0,stream>>>(xn,D,wu,F,D,u,F);
  swiglu_k<<<S*F/256,256,0,stream>>>(g,u,h,S*F);
  gemm_k<false><<<dim3(D/128,S/128),256,0,stream>>>(sre,S,a,D,S,yr,D);
  gemm_k<false><<<dim3(D/128,S/128),256,0,stream>>>(sim,S,b,D,S,yi,D);
  gemm_k<false><<<dim3(D/128,S/128),256,0,stream>>>(h,F,wd,D,F,ff,D);
  final_k<<<S*D/256,256,0,stream>>>(x,yr,yi,ff,(bf16_t*)d_out,S*D);
}
```

```cpp
#include <hip/hip_runtime.h>
#include <stdint.h>

typedef unsigned short bf16_t;
typedef unsigned long long u64;
typedef float    f32x2  __attribute__((ext_vector_type(2)));
typedef float    f32x32 __attribute__((ext_vector_type(32)));
typedef unsigned u32x2  __attribute__((ext_vector_type(2)));
typedef unsigned u32x3  __attribute__((ext_vector_type(3)));
typedef unsigned u32x4  __attribute__((ext_vector_type(4)));
typedef unsigned u32x6  __attribute__((ext_vector_type(6)));
typedef unsigned u32x7  __attribute__((ext_vector_type(7)));
typedef unsigned u32x8  __attribute__((ext_vector_type(8)));
typedef unsigned u32x12 __attribute__((ext_vector_type(12)));
typedef unsigned u32x16 __attribute__((ext_vector_type(16)));
typedef unsigned u32x32 __attribute__((ext_vector_type(32)));
typedef __bf16   bf16x2 __attribute__((ext_vector_type(2)));
#define DI __device__ __forceinline__
#define GAS __attribute__((address_space(1)))
#define LAS __attribute__((address_space(3)))

constexpr int S_ = 2048, D_ = 1024, F_ = 3584;
constexpr size_t WS_HF = 4u * 1024 * 1024;
constexpr size_t WS_AB = WS_HF + 112u * 128 * 1024;
constexpr size_t WS_XB = WS_AB + 8u * 1024 * 1024;
constexpr size_t WS_FL = WS_XB + 16u * 1024 * 1024;
constexpr unsigned FO_REC = 0;
constexpr unsigned FO_A = 16384, FO_H = 24576, FO_X = 32768;
constexpr unsigned FO_E = 40960, FO_RS = 49152, FO_M = 57344;
constexpr unsigned FO_CS = 65536, FO_END = FO_CS + 64 * 4096;
constexpr int LDS_BYTES = 147456;
constexpr unsigned L_RS = 98304, L_M = 99328, L_CSP = 100352, L_T = 139264, L_CSF = 140416;

DI unsigned rfl(unsigned x) { return __builtin_amdgcn_readfirstlane(x); }
DI unsigned lane_id() { unsigned l; asm volatile("v_mbcnt_lo_u32_b32 %0, -1, 0\n\tv_mbcnt_hi_u32_b32 %0, -1, %0" : "=v"(l)); return l; }
DI unsigned pk2(float lo, float hi) { f32x2 v = {lo, hi}; return __builtin_bit_cast(unsigned, __builtin_convertvector(v, bf16x2)); }
DI float rbf(float f) { return __uint_as_float(pk2(f, 0.f) << 16); }
DI f32x2 unpk(unsigned d) { f32x2 v = {__uint_as_float(d << 16), __uint_as_float(d & 0xffff0000u)}; return v; }
DI f32x2 sigm2(f32x2 g) {
  f32x2 t = g * (-1.4426950408889634f);
  t.x = __builtin_amdgcn_exp2f(t.x); t.y = __builtin_amdgcn_exp2f(t.y);
  t = t + 1.0f;
  t.x = __builtin_amdgcn_rcpf(t.x); t.y = __builtin_amdgcn_rcpf(t.y);
  return t;
}
DI __amdgpu_buffer_rsrc_t mk_rsrc(const void* p, unsigned bytes) {
  return __builtin_amdgcn_make_buffer_rsrc((void*)p, 0, bytes, 0x00020000);
}
template <class V> DI void put_srd(V& s, int at, const void* p, unsigned bytes) {
  const u64 a = (u64)p;
  s[at] = rfl((unsigned)a); s[at + 1] = rfl((unsigned)(a >> 32)); s[at + 2] = rfl(bytes); s[at + 3] = 0x00020000u;
}
DI void wait_flags(unsigned char* fl, unsigned byte_off, u64 tag) {
  for (unsigned spins = 0; spins < (1u << 16); ++spins) {
    const u64 v = __hip_atomic_load((GAS u64*)(fl + byte_off), __ATOMIC_RELAXED, __HIP_MEMORY_SCOPE_AGENT);
    if (__all(v == tag)) break;
    __builtin_amdgcn_s_sleep(1);
  }
  asm volatile("" ::: "memory");
}
DI void put_flag(unsigned char* fl, unsigned byte_off, u64 tag) {
  if (lane_id() == 0) __hip_atomic_store((GAS u64*)(fl + byte_off), tag, __ATOMIC_RELAXED, __HIP_MEMORY_SCOPE_AGENT);
}
template <int CTRL> DI float dpp_f(float v) { return __int_as_float(__builtin_amdgcn_update_dpp(0, __float_as_int(v), CTRL, 0xf, 0xf, true)); }
DI float sum8(float v) { v += dpp_f<0xb1>(v); v += dpp_f<0x4e>(v); v += dpp_f<0x141>(v); return v; }

struct Args {
  const bf16_t *wg, *wu, *wdre, *wdim, *wsre, *wsim, *wdn, *x; bf16_t* out; unsigned char* ws;
};
typedef const __attribute__((address_space(4))) Args* ArgsP;
#define SECTION_VARS \
  asm volatile("" : "+s"(kp), "+s"(bid), "+s"(wv), "+s"(tag)); \
  const unsigned wave = wv, wm = wave & 1, wn = wave >> 1; \
  const unsigned b = bid, xcd = b & 7, idx = b >> 3; \
  const unsigned t4 = idx & 3, j = 4 * xcd + t4, p = idx >> 2, big = t4 != 3;           \
  const unsigned q = xcd >> 1, tpn = 4 * (xcd & 1) + (idx >> 3), ec = idx & 7;          \
  unsigned char* const ws = kp->ws; \
  unsigned char* const fl = ws + WS_FL; \
  (void)wm; (void)wn; (void)j; (void)p; (void)big; (void)t4; (void)q; (void)tpn; (void)ec; (void)fl;
#define A_BLOCK(pp, jj) (((jj) >> 2) + 8 * (4 * (pp) + ((jj) & 3)))

#define A_OPERANDS \
    const unsigned fr = lane & 15, fq = lane >> 4, tq = (lane >> 2) & 3, tp = lane & 3, l3 = lane >> 3; \
    const unsigned far = 0x80000000u;         \
    const unsigned bigu = big ? 1u : 0u;      \
    const unsigned f0 = 448 * xcd + 128 * t4;                   \
    const unsigned d0_ = 16 * xcd + 2 * t4;                      \
    unsigned vx[4], vg[4], vw[5]; \
    _Pragma("unroll") for (unsigned i = 0; i < 4; ++i) { \
      const unsigned row = 256 * p + 64 * wave + 16 * (i & 1) + 8 * (l3 >> 2) + 4 * (i >> 1) + (l3 & 3); \
      vx[i] = row * 2048 + (((lane & 7) ^ (4 * (i & 1) + (l3 >> 1))) * 16) - 1024 * i; \
        \
      const unsigned rho = 16 * wn + ((8 >> bigu) * i) + (lane >> (3 + bigu)); \
      const unsigned c = (lane & (7 + 8 * bigu)) ^ (2 * ((rho >> (1 - bigu)) & (3 + 4 * bigu))); \
      const unsigned k = 8 * ((rho >> 2) & 3) + 4 * ((rho >> 4) & 1) + (rho & 3); \
      vg[i] = (k * F_ + f0 + 8 * c) * 2 - 1024 * i; \
    } \
    _Pragma("unroll") for (unsigned i = 0; i < 5; ++i) { \
      const unsigned rho = 8 * ((5 - 4 * bigu) * wave + (1 - bigu) * i) + l3, c = (lane & 7) ^ ((rho >> 1) & 7); \
      vw[i] = (16 * (d0_ & 63) + rho) * 2048 + c * 16 - 1024 * (i & 3); \
    } \
    u32x16 sa; \
    put_srd(sa, 0, kp->x, (unsigned)(S_ * D_ * 2)); \
    put_srd(sa, 4, wm == 0 ? kp->wg : kp->wu, (unsigned)(D_ * F_ * 2)); \
    put_srd(sa, 8, xcd < 4 ? kp->wdre : kp->wdim, (unsigned)(D_ * D_ * 2)); \
    sa[12] = rfl(wave * 8192); \
    sa[13] = rfl(65536 + (big ? wm * 8192 + wn * 4096 : wm * 4096 + wn * 2048)); \
    sa[14] = rfl(98304 + bigu * 32768 + (5 - 4 * bigu) * wave * 1024); \
    sa[15] = p < 6 ? 7 - p : 0u;                           \
    _Pragma("unroll") for (int i = 0; i < 16; ++i) sa[i] = rfl(sa[i]); \
      \
    u32x4 st2; unsigned vt2, st2s; \
    { \
      const unsigned gran = 128, len = wave < 2 ? 32768u : 14336u, start = wave < 2 ? b * 32768u : (2 * b + (wave - 2)) * 14336u; \
      const unsigned nact = (len / gran + 30) / 31; \
      put_srd(st2, 0, wave == 0 ? kp->wsre : wave == 1 ? kp->wsim : kp->wdn, start + len); \
      st2s = rfl(nact * gran); vt2 = (lane < nact && ((15 >> wave) & 1)) ? start + lane * gran : far; \
    } \
    const unsigned vflag = FO_REC + (4 * b + wave) * 16; \
    (void)fr; (void)fq; (void)tq; (void)tp;

__global__ __launch_bounds__(256, 1) void k_fnet_ffn(Args args_) {
  extern __shared__ __attribute__((aligned(16))) unsigned char smem[];
  ArgsP kp = (ArgsP)__builtin_amdgcn_kernarg_segment_ptr();
  unsigned bid = blockIdx.x, wv = rfl(threadIdx.x >> 6);
  u64 tag = 0;
  unsigned vj;
  vj = 0;
  {
    const u64 pc = __builtin_amdgcn_s_getpc() & ~127ull;
    u32x4 sic; sic[0] = (unsigned)pc; sic[1] = (unsigned)(pc >> 32); sic[2] = 80384; sic[3] = 0x00020000u;
    const unsigned l = lane_id(), vo = l < 6 ? (((bid >> 3) * 4 + wv) * 6 + l) * 128 : 0x80000000u;
    asm volatile("buffer_load_dword %0, %1, %2, 0 offen" : "+v"(vj) : "v"(vo), "s"(sic) : "memory");
  }
  {
    f32x32 c0, c1, c2, c3, c4, c5, c6, d0, d1;
    {
      SECTION_VARS
      u32x16 raw; u32x2 ev; u32x4 sfl;
      const unsigned lane = lane_id(), r8 = lane >> 3, c8 = lane & 7;
      {
        u32x4 sx; put_srd(sx, 0, kp->x, (unsigned)(S_ * D_ * 2));
        put_srd(sfl, 0, fl, FO_END);
        const unsigned vst = (8 * b + r8) * 2048 + 512 * wave + 16 * c8, vE = FO_E;
        asm volatile("" :: "s"(sx), "s"(sfl));
        asm volatile(
      "s_nop 4\n\t"
      "buffer_load_dwordx4 v[32:35], v22, s[36:39], 0 offen\n\t"
      "buffer_load_dwordx4 v[36:39], v22, s[36:39], 0 offen offset:128\n\t"
      "buffer_load_dwordx4 v[40:43], v22, s[36:39], 0 offen offset:256\n\t"
      "buffer_load_dwordx4 v[44:47], v22, s[36:39], 0 offen offset:384\n\t"
      "buffer_load_dwordx2 v[48:49], v23, s[64:67], 0 offen sc1\n\t"
        : "=&{v[32:47]}"(raw), "=&{v[48:49]}"(ev) : "{v22}"(vst), "{v23}"(vE), "{s[36:39]}"(sx), "{s[64:67]}"(sfl) : "memory");
      }
      A_OPERANDS
      const unsigned xr = wm * 16384 + fr * 128 + ((fq ^ ((fr >> 1) & 7)) * 16);
      unsigned gr[4];
#pragma unroll
      for (unsigned l = 0; l < 4; ++l) {
        const unsigned rho = 4 * fq + tq;
        const unsigned rsh = 7 + bigu, nn = (2 << bigu) * wn + (l & (2 * bigu + 1)), sw = 2 * (rho >> (1 - bigu) & (3 + 4 * bigu));
        gr[l] = 65536 + (rho << rsh) + (((2 * nn + (tp >> 1)) ^ sw) * 16) + (tp & 1) * 8;
      }
      const unsigned wd = 98304 + bigu * 32768 + (5 - 4 * bigu) * wn * 2048 + fr * 128 + ((fq ^ ((fr >> 1) & 7)) * 16);
      const u32x4 va0 = {xr, xr ^ 64, gr[0], gr[1]};
      const u32x4 va1 = {big ? gr[2] : vw[3], big ? gr[3] : vw[4], wd, wd ^ 64};
      u32x4 va2 = {vx[0], vx[1], vx[2], vx[3]}, va3 = {vg[0], vg[1], big ? vg[2] : vw[0], big ? vg[3] : vw[1]};
      const unsigned vw0 = big ? vw[0] : vw[2];
      const u32x2 va4 = {vt2, vflag};
      const unsigned swn = rfl(big ? 0u : 1u);
      asm volatile(
      "s_cmp_eq_u32 s80, 0\n\t"
      "s_cbranch_scc0 LP0_role1_%=\n\t"
      "s_nop 4\n\t"
      "s_mov_b32 s52, 0\n\t"
      "s_mov_b32 s53, 0\n\t"
      "s_mov_b32 s54, 0\n\t"
      "s_mov_b32 s60, 0\n\t"
      "s_add_u32 m0, s49, 0\n\t"
      "s_nop 0\n\t"
      "buffer_load_dwordx4 v12, s[40:43], s53 offen lds\n\t"
      "v_mov_b32 v64, 0\n\t"
      "v_mov_b32 v65, 0\n\t"
      "v_mov_b32 v66, 0\n\t"
      "v_mov_b32 v67, 0\n\t"
      "v_mov_b32 v68, 0\n\t"
      "v_mov_b32 v69, 0\n\t"
      "v_mov_b32 v70, 0\n\t"
      "v_mov_b32 v71, 0\n\t"
      "v_mov_b32 v72, 0\n\t"
      "v_mov_b32 v73, 0\n\t"
      "v_mov_b32 v74, 0\n\t"
      "v_mov_b32 v75, 0\n\t"
      "v_mov_b32 v76, 0\n\t"
      "v_mov_b32 v77, 0\n\t"
      "v_mov_b32 v78, 0\n\t"
      "v_mov_b32 v79, 0\n\t"
      "v_mov_b32 v80, 0\n\t"
      "v_mov_b32 v81, 0\n\t"
      "v_mov_b32 v82, 0\n\t"
      "buffer_load_dwordx4 v13, s[40:43], s53 offen offset:1024 lds\n\t"
      "v_mov_b32 v83, 0\n\t"
      "v_mov_b32 v84, 0\n\t"
      "v_mov_b32 v85, 0\n\t"
      "v_mov_b32 v86, 0\n\t"
      "v_mov_b32 v87, 0\n\t"
      "v_mov_b32 v88, 0\n\t"
      "v_mov_b32 v89, 0\n\t"
      "v_mov_b32 v90, 0\n\t"
      "v_mov_b32 v91, 0\n\t"
      "v_mov_b32 v92, 0\n\t"
      "v_mov_b32 v93, 0\n\t"
      "v_mov_b32 v94, 0\n\t"
      "v_mov_b32 v95, 0\n\t"
      "v_mov_b32 v96, 0\n\t"
      "v_mov_b32 v97, 0\n\t"
      "v_mov_b32 v98, 0\n\t"
      "v_mov_b32 v99, 0\n\t"
      "v_mov_b32 v100, 0\n\t"
      "v_mov_b32 v101, 0\n\t"
      "buffer_load_dwordx4 v14, s[40:43], s53 offen offset:2048 lds\n\t"
      "v_mov_b32 v102, 0\n\t"
      "v_mov_b32 v103, 0\n\t"
      "v_mov_b32 v104, 0\n\t"
      "v_mov_b32 v105, 0\n\t"
      "v_mov_b32 v106, 0\n\t"
      "v_mov_b32 v107, 0\n\t"
      "v_mov_b32 v108, 0\n\t"
      "v_mov_b32 v109, 0\n\t"
      "v_mov_b32 v110, 0\n\t"
      "v_mov_b32 v111, 0\n\t"
      "v_mov_b32 v112, 0\n\t"
      "v_mov_b32 v113, 0\n\t"
      "v_mov_b32 v114, 0\n\t"
      "v_mov_b32 v115, 0\n\t"
      "v_mov_b32 v116, 0\n\t"
      "v_mov_b32 v117, 0\n\t"
      "v_mov_b32 v118, 0\n\t"
      "v_mov_b32 v119, 0\n\t"
      "v_mov_b32 v120, 0\n\t"
      "buffer_load_dwordx4 v15, s[40:43], s53 offen offset:3072 lds\n\t"
      "v_mov_b32 v121, 0\n\t"
      "v_mov_b32 v122, 0\n\t"
      "v_mov_b32 v123, 0\n\t"
      "v_mov_b32 v124, 0\n\t"
      "v_mov_b32 v125, 0\n\t"
      "v_mov_b32 v126, 0\n\t"
      "v_mov_b32 v127, 0\n\t"
      "v_mov_b32 v128, 0\n\t"
      "v_mov_b32 v129, 0\n\t"
      "v_mov_b32 v130, 0\n\t"
      "v_mov_b32 v131, 0\n\t"
      "v_mov_b32 v132, 0\n\t"
      "v_mov_b32 v133, 0\n\t"
      "v_mov_b32 v134, 0\n\t"
      "v_mov_b32 v135, 0\n\t"
      "v_mov_b32 v136, 0\n\t"
      "v_mov_b32 v137, 0\n\t"
      "v_mov_b32 v138, 0\n\t"
      "v_mov_b32 v139, 0\n\t"
      "s_add_u32 s53, s53, 229376\n\t"
      "buffer_load_dword v18, v16, s[56:59], s60 offen\n\t"
      "v_mov_b32 v140, 0\n\t"
      "v_mov_b32 v141, 0\n\t"
      "v_mov_b32 v142, 0\n\t"
      "v_mov_b32 v143, 0\n\t"
      "v_mov_b32 v144, 0\n\t"
      "v_mov_b32 v145, 0\n\t"
      "v_mov_b32 v146, 0\n\t"
      "v_mov_b32 v147, 0\n\t"
      "v_mov_b32 v148, 0\n\t"
      "v_mov_b32 v149, 0\n\t"
      "v_mov_b32 v150, 0\n\t"
      "v_mov_b32 v151, 0\n\t"
      "v_mov_b32 v152, 0\n\t"
      "v_mov_b32 v153, 0\n\t"
      "v_mov_b32 v154, 0\n\t"
      "v_mov_b32 v155, 0\n\t"
      "v_mov_b32 v156, 0\n\t"
      "v_mov_b32 v157, 0\n\t"
      "v_mov_b32 v158, 0\n\t"
      "s_add_u32 s60, s60, s61\n\t"
      "s_add_u32 s62, s52, 65536\n\t"
      "s_add_u32 m0, s48, 0\n\t"
      "s_nop 0\n\t"
      "buffer_load_dwordx4 v8, s[36:39], s52 offen lds\n\t"
      "v_mov_b32 v159, 0\n\t"
      "v_mov_b32 v160, 0\n\t"
      "v_mov_b32 v161, 0\n\t"
      "v_mov_b32 v162, 0\n\t"
      "v_mov_b32 v163, 0\n\t"
      "v_mov_b32 v164, 0\n\t"
      "v_mov_b32 v165, 0\n\t"
      "v_mov_b32 v166, 0\n\t"
      "v_mov_b32 v167, 0\n\t"
      "v_mov_b32 v168, 0\n\t"
      "v_mov_b32 v169, 0\n\t"
      "v_mov_b32 v170, 0\n\t"
      "v_mov_b32 v171, 0\n\t"
      "v_mov_b32 v172, 0\n\t"
      "v_mov_b32 v173, 0\n\t"
      "v_mov_b32 v174, 0\n\t"
      "v_mov_b32 v175, 0\n\t"
      "v_mov_b32 v176, 0\n\t"
      "v_mov_b32 v177, 0\n\t"
      "buffer_load_dwordx4 v9, s[36:39], s52 offen offset:1024 lds\n\t"
      "v_mov_b32 v178, 0\n\t"
      "v_mov_b32 v179, 0\n\t"
      "v_mov_b32 v180, 0\n\t"
      "v_mov_b32 v181, 0\n\t"
      "v_mov_b32 v182, 0\n\t"
      "v_mov_b32 v183, 0\n\t"
      "v_mov_b32 v184, 0\n\t"
      "v_mov_b32 v185, 0\n\t"
      "v_mov_b32 v186, 0\n\t"
      "v_mov_b32 v187, 0\n\t"
      "v_mov_b32 v188, 0\n\t"
      "v_mov_b32 v189, 0\n\t"
      "v_mov_b32 v190, 0\n\t"
      "v_mov_b32 v191, 0\n\t"
      "v_mov_b32 v192, 0\n\t"
      "v_mov_b32 v193, 0\n\t"
      "v_mov_b32 v194, 0\n\t"
      "v_mov_b32 v195, 0\n\t"
      "v_mov_b32 v196, 0\n\t"
      "buffer_load_dwordx4 v10, s[36:39], s52 offen offset:2048 lds\n\t"
      "v_mov_b32 v197, 0\n\t"
      "v_mov_b32 v198, 0\n\t"
      "v_mov_b32 v199, 0\n\t"
      "v_mov_b32 v200, 0\n\t"
      "v_mov_b32 v201, 0\n\t"
      "v_mov_b32 v202, 0\n\t"
      "v_mov_b32 v203, 0\n\t"
      "v_mov_b32 v204, 0\n\t"
      "v_mov_b32 v205, 0\n\t"
      "v_mov_b32 v206, 0\n\t"
      "v_mov_b32 v207, 0\n\t"
      "v_mov_b32 v208, 0\n\t"
      "v_mov_b32 v209, 0\n\t"
      "v_mov_b32 v210, 0\n\t"
      "v_mov_b32 v211, 0\n\t"
      "v_mov_b32 v212, 0\n\t"
      "v_mov_b32 v213, 0\n\t"
      "v_mov_b32 v214, 0\n\t"
      "v_mov_b32 v215, 0\n\t"
      "buffer_load_dwordx4 v11, s[36:39], s52 offen offset:3072 lds\n\t"
      "v_mov_b32 v216, 0\n\t"
      "v_mov_b32 v217, 0\n\t"
      "v_mov_b32 v218, 0\n\t"
      "v_mov_b32 v219, 0\n\t"
      "v_mov_b32 v220, 0\n\t"
      "v_mov_b32 v221, 0\n\t"
      "v_mov_b32 v222, 0\n\t"
      "v_mov_b32 v223, 0\n\t"
      "v_mov_b32 v224, 0\n\t"
      "v_mov_b32 v225, 0\n\t"
      "v_mov_b32 v226, 0\n\t"
      "v_mov_b32 v227, 0\n\t"
      "v_mov_b32 v228, 0\n\t"
      "v_mov_b32 v229, 0\n\t"
      "v_mov_b32 v230, 0\n\t"
      "v_mov_b32 v231, 0\n\t"
      "v_mov_b32 v232, 0\n\t"
      "v_mov_b32 v233, 0\n\t"
      "v_mov_b32 v234, 0\n\t"
      "s_add_u32 m0, s48, 4096\n\t"
      "s_nop 0\n\t"
      "buffer_load_dwordx4 v8, s[36:39], s62 offen lds\n\t"
      "v_mov_b32 v235, 0\n\t"
      "v_mov_b32 v236, 0\n\t"
      "v_mov_b32 v237, 0\n\t"
      "v_mov_b32 v238, 0\n\t"
      "v_mov_b32 v239, 0\n\t"
      "v_mov_b32 v240, 0\n\t"
      "v_mov_b32 v241, 0\n\t"
      "v_mov_b32 v242, 0\n\t"
      "v_mov_b32 v243, 0\n\t"
      "v_mov_b32 v244, 0\n\t"
      "v_mov_b32 v245, 0\n\t"
      "v_mov_b32 v246, 0\n\t"
      "v_mov_b32 v247, 0\n\t"
      "v_mov_b32 v248, 0\n\t"
      "v_mov_b32 v249, 0\n\t"
      "v_mov_b32 v250, 0\n\t"
      "v_mov_b32 v251, 0\n\t"
      "v_mov_b32 v252, 0\n\t"
      "v_mov_b32 v253, 0\n\t"
      "buffer_load_dwordx4 v9, s[36:39], s62 offen offset:1024 lds\n\t"
      "v_mov_b32 v254, 0\n\t"
      "v_mov_b32 v255, 0\n\t"
      "v_accvgpr_write_b32 a192, 0\n\t"
      "v_accvgpr_write_b32 a193, 0\n\t"
      "v_accvgpr_write_b32 a194, 0\n\t"
      "v_accvgpr_write_b32 a195, 0\n\t"
      "v_accvgpr_write_b32 a196, 0\n\t"
      "v_accvgpr_write_b32 a197, 0\n\t"
      "v_accvgpr_write_b32 a198, 0\n\t"
      "v_accvgpr_write_b32 a199, 0\n\t"
      "v_accvgpr_write_b32 a200, 0\n\t"
      "v_accvgpr_write_b32 a201, 0\n\t"
      "v_accvgpr_write_b32 a202, 0\n\t"
      "v_accvgpr_write_b32 a203, 0\n\t"
      "v_accvgpr_write_b32 a204, 0\n\t"
      "v_accvgpr_write_b32 a205, 0\n\t"
      "v_accvgpr_write_b32 a206, 0\n\t"
      "v_accvgpr_write_b32 a207, 0\n\t"
      "v_accvgpr_write_b32 a208, 0\n\t"
      "buffer_load_dwordx4 v10, s[36:39], s62 offen offset:2048 lds\n\t"
      "v_accvgpr_write_b32 a209, 0\n\t"
      "v_accvgpr_write_b32 a210, 0\n\t"
      "v_accvgpr_write_b32 a211, 0\n\t"
      "v_accvgpr_write_b32 a212, 0\n\t"
      "v_accvgpr_write_b32 a213, 0\n\t"
      "v_accvgpr_write_b32 a214, 0\n\t"
      "v_accvgpr_write_b32 a215, 0\n\t"
      "v_accvgpr_write_b32 a216, 0\n\t"
      "v_accvgpr_write_b32 a217, 0\n\t"
      "v_accvgpr_write_b32 a218, 0\n\t"
      "v_accvgpr_write_b32 a219, 0\n\t"
      "v_accvgpr_write_b32 a220, 0\n\t"
      "v_accvgpr_write_b32 a221, 0\n\t"
      "v_accvgpr_write_b32 a222, 0\n\t"
      "v_accvgpr_write_b32 a223, 0\n\t"
      "v_accvgpr_write_b32 a224, 0\n\t"
      "v_accvgpr_write_b32 a225, 0\n\t"
      "v_accvgpr_write_b32 a226, 0\n\t"
      "v_accvgpr_write_b32 a227, 0\n\t"
      "buffer_load_dwordx4 v11, s[36:39], s62 offen offset:3072 lds\n\t"
      "v_accvgpr_write_b32 a228, 0\n\t"
      "v_accvgpr_write_b32 a229, 0\n\t"
      "v_accvgpr_write_b32 a230, 0\n\t"
      "v_accvgpr_write_b32 a231, 0\n\t"
      "v_accvgpr_write_b32 a232, 0\n\t"
      "v_accvgpr_write_b32 a233, 0\n\t"
      "v_accvgpr_write_b32 a234, 0\n\t"
      "v_accvgpr_write_b32 a235, 0\n\t"
      "v_accvgpr_write_b32 a236, 0\n\t"
      "v_accvgpr_write_b32 a237, 0\n\t"
      "v_accvgpr_write_b32 a238, 0\n\t"
      "v_accvgpr_write_b32 a239, 0\n\t"
      "v_accvgpr_write_b32 a240, 0\n\t"
      "v_accvgpr_write_b32 a241, 0\n\t"
      "v_accvgpr_write_b32 a242, 0\n\t"
      "v_accvgpr_write_b32 a243, 0\n\t"
      "v_accvgpr_write_b32 a244, 0\n\t"
      "v_accvgpr_write_b32 a245, 0\n\t"
      "v_accvgpr_write_b32 a246, 0\n\t"
      "s_add_u32 s52, s52, 128\n\t"
      "s_add_u32 m0, s50, 0\n\t"
      "s_nop 0\n\t"
      "buffer_load_dwordx4 v19, s[44:47], s54 offen lds\n\t"
      "v_accvgpr_write_b32 a247, 0\n\t"
      "v_accvgpr_write_b32 a248, 0\n\t"
      "v_accvgpr_write_b32 a249, 0\n\t"
      "v_accvgpr_write_b32 a250, 0\n\t"
      "v_accvgpr_write_b32 a251, 0\n\t"
      "v_accvgpr_write_b32 a252, 0\n\t"
      "v_accvgpr_write_b32 a253, 0\n\t"
      "v_accvgpr_write_b32 a254, 0\n\t"
      "v_accvgpr_write_b32 a255, 0\n\t"
      "s_add_u32 s54, s54, 128\n\t"
      "s_waitcnt vmcnt(14)\n\t"
      "s_branch LP0_done_%=\n\t"
      "LP0_role1_%=:\n\t"
      "s_nop 4\n\t"
      "s_mov_b32 s52, 0\n\t"
      "s_mov_b32 s53, 0\n\t"
      "s_mov_b32 s54, 0\n\t"
      "s_mov_b32 s60, 0\n\t"
      "s_add_u32 m0, s49, 0\n\t"
      "s_nop 0\n\t"
      "buffer_load_dwordx4 v12, s[40:43], s53 offen lds\n\t"
      "v_mov_b32 v64, 0\n\t"
      "v_mov_b32 v65, 0\n\t"
      "v_mov_b32 v66, 0\n\t"
      "v_mov_b32 v67, 0\n\t"
      "v_mov_b32 v68, 0\n\t"
      "v_mov_b32 v69, 0\n\t"
      "v_mov_b32 v70, 0\n\t"
      "v_mov_b32 v71, 0\n\t"
      "v_mov_b32 v72, 0\n\t"
      "v_mov_b32 v73, 0\n\t"
      "v_mov_b32 v74, 0\n\t"
      "v_mov_b32 v75, 0\n\t"
      "v_mov_b32 v76, 0\n\t"
      "v_mov_b32 v77, 0\n\t"
      "v_mov_b32 v78, 0\n\t"
      "v_mov_b32 v79, 0\n\t"
      "buffer_load_dwordx4 v13, s[40:43], s53 offen offset:1024 lds\n\t"
      "v_mov_b32 v80, 0\n\t"
      "v_mov_b32 v81, 0\n\t"
      "v_mov_b32 v82, 0\n\t"
      "v_mov_b32 v83, 0\n\t"
      "v_mov_b32 v84, 0\n\t"
      "v_mov_b32 v85, 0\n\t"
      "v_mov_b32 v86, 0\n\t"
      "v_mov_b32 v87, 0\n\t"
      "v_mov_b32 v88, 0\n\t"
      "v_mov_b32 v89, 0\n\t"
      "v_mov_b32 v90, 0\n\t"
      "v_mov_b32 v91, 0\n\t"
      "v_mov_b32 v92, 0\n\t"
      "v_mov_b32 v93, 0\n\t"
      "v_mov_b32 v94, 0\n\t"
      "v_mov_b32 v95, 0\n\t"
      "s_add_u32 s53, s53, 229376\n\t"
      "buffer_load_dword v18, v16, s[56:59], s60 offen\n\t"
      "v_mov_b32 v96, 0\n\t"
      "v_mov_b32 v97, 0\n\t"
      "v_mov_b32 v98, 0\n\t"
      "v_mov_b32 v99, 0\n\t"
      "v_mov_b32 v100, 0\n\t"
      "v_mov_b32 v101, 0\n\t"
      "v_mov_b32 v102, 0\n\t"
      "v_mov_b32 v103, 0\n\t"
      "v_mov_b32 v104, 0\n\t"
      "v_mov_b32 v105, 0\n\t"
      "v_mov_b32 v106, 0\n\t"
      "v_mov_b32 v107, 0\n\t"
      "v_mov_b32 v108, 0\n\t"
      "v_mov_b32 v109, 0\n\t"
      "v_mov_b32 v110, 0\n\t"
      "v_mov_b32 v111, 0\n\t"
      "s_add_u32 s60, s60, s61\n\t"
      "s_add_u32 s62, s52, 65536\n\t"
      "s_add_u32 m0, s48, 0\n\t"
      "s_nop 0\n\t"
      "buffer_load_dwordx4 v8, s[36:39], s52 offen lds\n\t"
      "v_mov_b32 v112, 0\n\t"
      "v_mov_b32 v113, 0\n\t"
      "v_mov_b32 v114, 0\n\t"
      "v_mov_b32 v115, 0\n\t"
      "v_mov_b32 v116, 0\n\t"
      "v_mov_b32 v117, 0\n\t"
      "v_mov_b32 v118, 0\n\t"
      "v_mov_b32 v119, 0\n\t"
      "v_mov_b32 v120, 0\n\t"
      "v_mov_b32 v121, 0\n\t"
      "v_mov_b32 v122, 0\n\t"
      "v_mov_b32 v123, 0\n\t"
      "v_mov_b32 v124, 0\n\t"
      "v_mov_b32 v125, 0\n\t"
      "v_mov_b32 v126, 0\n\t"
      "v_mov_b32 v127, 0\n\t"
      "buffer_load_dwordx4 v9, s[36:39], s52 offen offset:1024 lds\n\t"
      "v_mov_b32 v128, 0\n\t"
      "v_mov_b32 v129, 0\n\t"
      "v_mov_b32 v130, 0\n\t"
      "v_mov_b32 v131, 0\n\t"
      "v_mov_b32 v132, 0\n\t"
      "v_mov_b32 v133, 0\n\t"
      "v_mov_b32 v134, 0\n\t"
      "v_mov_b32 v135, 0\n\t"
      "v_mov_b32 v136, 0\n\t"
      "v_mov_b32 v137, 0\n\t"
      "v_mov_b32 v138, 0\n\t"
      "v_mov_b32 v139, 0\n\t"
      "v_mov_b32 v140, 0\n\t"
      "v_mov_b32 v141, 0\n\t"
      "v_mov_b32 v142, 0\n\t"
      "v_mov_b32 v143, 0\n\t"
      "buffer_load_dwordx4 v10, s[36:39], s52 offen offset:2048 lds\n\t"
      "v_mov_b32 v144, 0\n\t"
      "v_mov_b32 v145, 0\n\t"
      "v_mov_b32 v146, 0\n\t"
      "v_mov_b32 v147, 0\n\t"
      "v_mov_b32 v148, 0\n\t"
      "v_mov_b32 v149, 0\n\t"
      "v_mov_b32 v150, 0\n\t"
      "v_mov_b32 v151, 0\n\t"
      "v_mov_b32 v152, 0\n\t"
      "v_mov_b32 v153, 0\n\t"
      "v_mov_b32 v154, 0\n\t"
      "v_mov_b32 v155, 0\n\t"
      "v_mov_b32 v156, 0\n\t"
      "v_mov_b32 v157, 0\n\t"
      "v_mov_b32 v158, 0\n\t"
      "v_mov_b32 v159, 0\n\t"
      "buffer_load_dwordx4 v11, s[36:39], s52 offen offset:3072 lds\n\t"
      "v_mov_b32 v160, 0\n\t"
      "v_mov_b32 v161, 0\n\t"
      "v_mov_b32 v162, 0\n\t"
      "v_mov_b32 v163, 0\n\t"
      "v_mov_b32 v164, 0\n\t"
      "v_mov_b32 v165, 0\n\t"
      "v_mov_b32 v166, 0\n\t"
      "v_mov_b32 v167, 0\n\t"
      "v_mov_b32 v168, 0\n\t"
      "v_mov_b32 v169, 0\n\t"
      "v_mov_b32 v170, 0\n\t"
      "v_mov_b32 v171, 0\n\t"
      "v_mov_b32 v172, 0\n\t"
      "v_mov_b32 v173, 0\n\t"
      "v_mov_b32 v174, 0\n\t"
      "v_mov_b32 v175, 0\n\t"
      "s_add_u32 m0, s48, 4096\n\t"
      "s_nop 0\n\t"
      "buffer_load_dwordx4 v8, s[36:39], s62 offen lds\n\t"
      "v_mov_b32 v176, 0\n\t"
      "v_mov_b32 v177, 0\n\t"
      "v_mov_b32 v178, 0\n\t"
      "v_mov_b32 v179, 0\n\t"
      "v_mov_b32 v180, 0\n\t"
      "v_mov_b32 v181, 0\n\t"
      "v_mov_b32 v182, 0\n\t"
      "v_mov_b32 v183, 0\n\t"
      "v_mov_b32 v184, 0\n\t"
      "v_mov_b32 v185, 0\n\t"
      "v_mov_b32 v186, 0\n\t"
      "v_mov_b32 v187, 0\n\t"
      "v_mov_b32 v188, 0\n\t"
      "v_mov_b32 v189, 0\n\t"
      "v_mov_b32 v190, 0\n\t"
      "v_mov_b32 v191, 0\n\t"
      "buffer_load_dwordx4 v9, s[36:39], s62 offen offset:1024 lds\n\t"
      "v_mov_b32 v192, 0\n\t"
      "v_mov_b32 v193, 0\n\t"
      "v_mov_b32 v194, 0\n\t"
      "v_mov_b32 v195, 0\n\t"
      "v_mov_b32 v196, 0\n\t"
      "v_mov_b32 v197, 0\n\t"
      "v_mov_b32 v198, 0\n\t"
      "v_mov_b32 v199, 0\n\t"
      "v_mov_b32 v200, 0\n\t"
      "v_mov_b32 v201, 0\n\t"
      "v_mov_b32 v202, 0\n\t"
      "v_mov_b32 v203, 0\n\t"
      "v_mov_b32 v204, 0\n\t"
      "v_mov_b32 v205, 0\n\t"
      "v_mov_b32 v206, 0\n\t"
      "v_mov_b32 v207, 0\n\t"
      "buffer_load_dwordx4 v10, s[36:39], s62 offen offset:2048 lds\n\t"
      "v_mov_b32 v208, 0\n\t"
      "v_mov_b32 v209, 0\n\t"
      "v_mov_b32 v210, 0\n\t"
      "v_mov_b32 v211, 0\n\t"
      "v_mov_b32 v212, 0\n\t"
      "v_mov_b32 v213, 0\n\t"
      "v_mov_b32 v214, 0\n\t"
      "v_mov_b32 v215, 0\n\t"
      "v_mov_b32 v216, 0\n\t"
      "v_mov_b32 v217, 0\n\t"
      "v_mov_b32 v218, 0\n\t"
      "v_mov_b32 v219, 0\n\t"
      "v_mov_b32 v220, 0\n\t"
      "v_mov_b32 v221, 0\n\t"
      "v_mov_b32 v222, 0\n\t"
      "v_mov_b32 v223, 0\n\t"
      "buffer_load_dwordx4 v11, s[36:39], s62 offen offset:3072 lds\n\t"
      "v_mov_b32 v224, 0\n\t"
      "v_mov_b32 v225, 0\n\t"
      "v_mov_b32 v226, 0\n\t"
      "v_mov_b32 v227, 0\n\t"
      "v_mov_b32 v228, 0\n\t"
      "v_mov_b32 v229, 0\n\t"
      "v_mov_b32 v230, 0\n\t"
      "v_mov_b32 v231, 0\n\t"
      "v_mov_b32 v232, 0\n\t"
      "v_mov_b32 v233, 0\n\t"
      "v_mov_b32 v234, 0\n\t"
      "v_mov_b32 v235, 0\n\t"
      "v_mov_b32 v236, 0\n\t"
      "v_mov_b32 v237, 0\n\t"
      "v_mov_b32 v238, 0\n\t"
      "v_mov_b32 v239, 0\n\t"
      "s_add_u32 s52, s52, 128\n\t"
      "s_add_u32 m0, s50, 0\n\t"
      "s_nop 0\n\t"
      "buffer_load_dwordx4 v14, s[44:47], s54 offen lds\n\t"
      "v_mov_b32 v240, 0\n\t"
      "v_mov_b32 v241, 0\n\t"
      "v_mov_b32 v242, 0\n\t"
      "v_mov_b32 v243, 0\n\t"
      "v_mov_b32 v244, 0\n\t"
      "v_mov_b32 v245, 0\n\t"
      "v_mov_b32 v246, 0\n\t"
      "v_mov_b32 v247, 0\n\t"
      "v_mov_b32 v248, 0\n\t"
      "v_mov_b32 v249, 0\n\t"
      "v_mov_b32 v250, 0\n\t"
      "v_mov_b32 v251, 0\n\t"
      "v_mov_b32 v252, 0\n\t"
      "v_mov_b32 v253, 0\n\t"
      "v_mov_b32 v254, 0\n\t"
      "v_mov_b32 v255, 0\n\t"
      "buffer_load_dwordx4 v15, s[44:47], s54 offen offset:1024 lds\n\t"
      "v_accvgpr_write_b32 a192, 0\n\t"
      "v_accvgpr_write_b32 a193, 0\n\t"
      "v_accvgpr_write_b32 a194, 0\n\t"
      "v_accvgpr_write_b32 a195, 0\n\t"
      "v_accvgpr_write_b32 a196, 0\n\t"
      "v_accvgpr_write_b32 a197, 0\n\t"
      "v_accvgpr_write_b32 a198, 0\n\t"
      "v_accvgpr_write_b32 a199, 0\n\t"
      "v_accvgpr_write_b32 a200, 0\n\t"
      "v_accvgpr_write_b32 a201, 0\n\t"
      "v_accvgpr_write_b32 a202, 0\n\t"
      "v_accvgpr_write_b32 a203, 0\n\t"
      "v_accvgpr_write_b32 a204, 0\n\t"
      "v_accvgpr_write_b32 a205, 0\n\t"
      "v_accvgpr_write_b32 a206, 0\n\t"
      "v_accvgpr_write_b32 a207, 0\n\t"
      "buffer_load_dwordx4 v19, s[44:47], s54 offen offset:2048 lds\n\t"
      "v_accvgpr_write_b32 a208, 0\n\t"
      "v_accvgpr_write_b32 a209, 0\n\t"
      "v_accvgpr_write_b32 a210, 0\n\t"
      "v_accvgpr_write_b32 a211, 0\n\t"
      "v_accvgpr_write_b32 a212, 0\n\t"
      "v_accvgpr_write_b32 a213, 0\n\t"
      "v_accvgpr_write_b32 a214, 0\n\t"
      "v_accvgpr_write_b32 a215, 0\n\t"
      "v_accvgpr_write_b32 a216, 0\n\t"
      "v_accvgpr_write_b32 a217, 0\n\t"
      "v_accvgpr_write_b32 a218, 0\n\t"
      "v_accvgpr_write_b32 a219, 0\n\t"
      "v_accvgpr_write_b32 a220, 0\n\t"
      "v_accvgpr_write_b32 a221, 0\n\t"
      "v_accvgpr_write_b32 a222, 0\n\t"
      "v_accvgpr_write_b32 a223, 0\n\t"
      "buffer_load_dwordx4 v4, s[44:47], s54 offen offset:3072 lds\n\t"
      "v_accvgpr_write_b32 a224, 0\n\t"
      "v_accvgpr_write_b32 a225, 0\n\t"
      "v_accvgpr_write_b32 a226, 0\n\t"
      "v_accvgpr_write_b32 a227, 0\n\t"
      "v_accvgpr_write_b32 a228, 0\n\t"
      "v_accvgpr_write_b32 a229, 0\n\t"
      "v_accvgpr_write_b32 a230, 0\n\t"
      "v_accvgpr_write_b32 a231, 0\n\t"
      "v_accvgpr_write_b32 a232, 0\n\t"
      "v_accvgpr_write_b32 a233, 0\n\t"
      "v_accvgpr_write_b32 a234, 0\n\t"
      "v_accvgpr_write_b32 a235, 0\n\t"
      "v_accvgpr_write_b32 a236, 0\n\t"
      "v_accvgpr_write_b32 a237, 0\n\t"
      "v_accvgpr_write_b32 a238, 0\n\t"
      "v_accvgpr_write_b32 a239, 0\n\t"
      "s_add_u32 m0, s50, 4096\n\t"
      "s_nop 0\n\t"
      "buffer_load_dwordx4 v5, s[44:47], s54 offen lds\n\t"
      "v_accvgpr_write_b32 a240, 0\n\t"
      "v_accvgpr_write_b32 a241, 0\n\t"
      "v_accvgpr_write_b32 a242, 0\n\t"
      "v_accvgpr_write_b32 a243, 0\n\t"
      "v_accvgpr_write_b32 a244, 0\n\t"
      "v_accvgpr_write_b32 a245, 0\n\t"
      "v_accvgpr_write_b32 a246, 0\n\t"
      "v_accvgpr_write_b32 a247, 0\n\t"
      "v_accvgpr_write_b32 a248, 0\n\t"
      "v_accvgpr_write_b32 a249, 0\n\t"
      "v_accvgpr_write_b32 a250, 0\n\t"
      "v_accvgpr_write_b32 a251, 0\n\t"
      "v_accvgpr_write_b32 a252, 0\n\t"
      "v_accvgpr_write_b32 a253, 0\n\t"
      "v_accvgpr_write_b32 a254, 0\n\t"
      "v_accvgpr_write_b32 a255, 0\n\t"
      "s_add_u32 s54, s54, 128\n\t"
      "s_waitcnt vmcnt(16)\n\t"
      "LP0_done_%=:\n\t"
      : "+{v[32:47]}"(raw), "+{v[48:49]}"(ev), "+{v18}"(vj), "=&{v[64:95]}"(c1), "=&{v[96:127]}"(c2), "=&{v[128:159]}"(c3), "=&{v[160:191]}"(c4), "=&{v[192:223]}"(c5), "=&{v[224:255]}"(c6), "=&{a[192:223]}"(d0), "=&{a[224:255]}"(d1) : "{v[4:7]}"(va1), "{v[8:11]}"(va2), "{v[12:15]}"(va3), "{v[16:17]}"(va4), "{v19}"(vw0), "{s[36:51]}"(sa), "{s[56:59]}"(st2), "{s61}"(st2s), "{s80}"(swn) : "memory", "scc", "s52", "s53", "s54", "s60", "s62");
      { const u64 e = (((u64)ev[1] << 32) | ev[0]) + 1; tag = ((u64)rfl((unsigned)(e >> 32)) << 32) | rfl((unsigned)e); }
      f32x2 s2 = {0.f, 0.f};
#pragma unroll
      for (int i = 0; i < 16; ++i) s2 += unpk(raw[i]);
      const float mw = sum8(s2.x + s2.y) * (1.0f / 256.0f);
      asm volatile("" : "+v"(raw));
      f32x2 q2 = {0.f, 0.f};
#pragma unroll
      for (int i = 0; i < 16; ++i) { const f32x2 d = unpk(raw[i]) - mw; q2 += d * d; }
      const float qq = sum8(q2.x + q2.y);
      LAS f32x2* red = (LAS f32x2*)(smem + L_T);
      if (c8 == 0) red[wave * 8 + r8] = (f32x2){mw, qq};
      asm volatile("s_waitcnt lgkmcnt(0)\n\ts_barrier" ::: "memory");
      const f32x2 a0 = red[r8], a1 = red[8 + r8], a2 = red[16 + r8], a3 = red[24 + r8];
      const float mean = 0.25f * ((a0.x + a1.x) + (a2.x + a3.x));
      const float e0 = a0.x - mean, e1 = a1.x - mean, e2 = a2.x - mean, e3 = a3.x - mean;
      const float m2 = (a0.y + a1.y) + (a2.y + a3.y) + 256.0f * ((e0 * e0 + e1 * e1) + (e2 * e2 + e3 * e3));
      const float mb = rbf(mean), var = rbf(m2 * (1.0f / D_));
      const float rs = __builtin_amdgcn_rcpf(rbf(__builtin_amdgcn_sqrtf(rbf(var + 1e-5f))));
      {
        const __amdgpu_buffer_rsrc_t rF = mk_rsrc(fl, FO_END);
        const bool mine = c8 == 0 && (r8 >> 1) == wave;
        __builtin_amdgcn_raw_buffer_store_b32(__float_as_uint(rs), rF, mine ? FO_RS + (8 * b + r8) * 4 : far, 0, 16);
        __builtin_amdgcn_raw_buffer_store_b32(__float_as_uint(mb), rF, mine ? FO_M + (8 * b + r8) * 4 : far, 0, 16);
      }
      asm volatile("" ::: "memory");
      const unsigned slt = rfl(L_T + wn * 576);
      const u32x2 vtag = {(unsigned)tag, (unsigned)(tag >> 32)};
      const unsigned rec = FO_CS + (2 * j + wn) * 4096;
      const unsigned vcsa = rec + p * 576 + fq * 16;
      u32x6 va5;
      va5[0] = rec + p * 576 + fr * 4;
      va5[1] = FO_REC + (4 * (32 * p + (lane >> 2)) + (lane & 3)) * 16; va5[2] = va5[1] + 16 * 64;
      { const unsigned l = lane < 12 ? lane : 0u; va5[3] = FO_REC + (4 * A_BLOCK(l >> 1, j) + 2 * wn + (l & 1)) * 16 + 8; }
      va5[4] = ((wave & 1) ? FO_M : FO_RS) + p * 1024 + lane * 16;
      va5[5] = rec + wm * 2048 + lane * 16;
      u32x8 sa2;
      sa2[0] = sfl[0]; sa2[1] = sfl[1]; sa2[2] = sfl[2]; sa2[3] = sfl[3];
      sa2[4] = (unsigned)tag; sa2[5] = (unsigned)(tag >> 32);
      sa2[6] = rfl((wave & 1) ? L_M : L_RS); sa2[7] = rfl(L_CSP + wn * 4096 + wm * 2048);
      const unsigned swm = rfl(wm);
      const u32x2 swn2 = {swn, rfl((1u << 7) | (p < 6 ? 1u << (6 - p) : 0u))};
      asm volatile(
      "s_cmp_eq_u32 s80, 0\n\t"
      "s_cbranch_scc0 LAA_role1_%=\n\t"
      "s_nop 4\n\t"
      "s_mov_b32 s52, 128\n\t"
      "s_mov_b32 s53, 229376\n\t"
      "s_mov_b32 s54, 128\n\t"
      "s_mov_b32 s63, 0\n\t"
      "s_mov_b32 s60, s61\n\t"
      "s_add_u32 m0, s49, 16384\n\t"
      "s_nop 0\n\t"
      "buffer_load_dwordx4 v12, s[40:43], s53 offen lds\n\t"
      "buffer_load_dwordx4 v13, s[40:43], s53 offen offset:1024 lds\n\t"
      "buffer_load_dwordx4 v14, s[40:43], s53 offen offset:2048 lds\n\t"
      "buffer_load_dwordx4 v15, s[40:43], s53 offen offset:3072 lds\n\t"
      "s_add_u32 s53, s53, 229376\n\t"
      "buffer_load_dword v18, v16, s[56:59], s60 offen\n\t"
      "s_add_u32 s60, s60, s61\n\t"
      "s_add_u32 m0, s49, 32768\n\t"
      "s_nop 0\n\t"
      "buffer_load_dwordx4 v12, s[40:43], s53 offen lds\n\t"
      "buffer_load_dwordx4 v13, s[40:43], s53 offen offset:1024 lds\n\t"
      "buffer_load_dwordx4 v14, s[40:43], s53 offen offset:2048 lds\n\t"
      "buffer_load_dwordx4 v15, s[40:43], s53 offen offset:3072 lds\n\t"
      "s_add_u32 s53, s53, 229376\n\t"
      "buffer_load_dword v18, v16, s[56:59], s60 offen\n\t"
      "s_add_u32 s60, s60, s61\n\t"
      "s_add_u32 s62, s52, 65536\n\t"
      "s_add_u32 m0, s48, 32768\n\t"
      "s_nop 0\n\t"
      "buffer_load_dwordx4 v8, s[36:39], s52 offen lds\n\t"
      "buffer_load_dwordx4 v9, s[36:39], s52 offen offset:1024 lds\n\t"
      "buffer_load_dwordx4 v10, s[36:39], s52 offen offset:2048 lds\n\t"
      "buffer_load_dwordx4 v11, s[36:39], s52 offen offset:3072 lds\n\t"
      "s_add_u32 m0, s48, 36864\n\t"
      "s_nop 0\n\t"
      "buffer_load_dwordx4 v8, s[36:39], s62 offen lds\n\t"
      "buffer_load_dwordx4 v9, s[36:39], s62 offen offset:1024 lds\n\t"
      "buffer_load_dwordx4 v10, s[36:39], s62 offen offset:2048 lds\n\t"
      "buffer_load_dwordx4 v11, s[36:39], s62 offen offset:3072 lds\n\t"
      "s_add_u32 s52, s52, 128\n\t"
      "s_add_u32 m0, s50, 4096\n\t"
      "s_nop 0\n\t"
      "buffer_load_dwordx4 v19, s[44:47], s54 offen lds\n\t"
      "s_add_u32 s54, s54, 128\n\t"
      "s_add_u32 m0, s49, 49152\n\t"
      "s_nop 0\n\t"
      "buffer_load_dwordx4 v12, s[40:43], s53 offen lds\n\t"
      "buffer_load_dwordx4 v13, s[40:43], s53 offen offset:1024 lds\n\t"
      "buffer_load_dwordx4 v14, s[40:43], s53 offen offset:2048 lds\n\t"
      "buffer_load_dwordx4 v15, s[40:43], s53 offen offset:3072 lds\n\t"
      "s_add_u32 s53, s53, 229376\n\t"
      "v_mov_b32 v32, 0\n\t"
      "v_mov_b32 v33, 0\n\t"
      "v_mov_b32 v34, 0\n\t"
      "v_mov_b32 v35, 0\n\t"
      "v_mov_b32 v36, 0\n\t"
      "v_mov_b32 v37, 0\n\t"
      "v_mov_b32 v38, 0\n\t"
      "v_mov_b32 v39, 0\n\t"
      "v_mov_b32 v40, 0\n\t"
      "v_mov_b32 v41, 0\n\t"
      "v_mov_b32 v42, 0\n\t"
      "v_mov_b32 v43, 0\n\t"
      "v_mov_b32 v44, 0\n\t"
      "v_mov_b32 v45, 0\n\t"
      "v_mov_b32 v46, 0\n\t"
      "v_mov_b32 v47, 0\n\t"
      "v_mov_b32 v48, 0\n\t"
      "v_mov_b32 v49, 0\n\t"
      "v_mov_b32 v50, 0\n\t"
      "v_mov_b32 v51, 0\n\t"
      "v_mov_b32 v52, 0\n\t"
      "v_mov_b32 v53, 0\n\t"
      "v_mov_b32 v54, 0\n\t"
      "v_mov_b32 v55, 0\n\t"
      "v_mov_b32 v56, 0\n\t"
      "v_mov_b32 v57, 0\n\t"
      "v_mov_b32 v58, 0\n\t"
      "v_mov_b32 v59, 0\n\t"
      "v_mov_b32 v60, 0\n\t"
      "v_mov_b32 v61, 0\n\t"
      "v_mov_b32 v62, 0\n\t"
      "v_mov_b32 v63, 0\n\t"
      "v_mov_b32 v22, 0x3f803f80\n\t"
      "v_accvgpr_write_b32 a172, v22\n\t"
      "v_accvgpr_write_b32 a173, v22\n\t"
      "v_accvgpr_write_b32 a174, v22\n\t"
      "v_accvgpr_write_b32 a175, v22\n\t"
      "s_waitcnt vmcnt(23)\n\t"
      "s_barrier\n\t"
      "ds_read_b128 a[0:3], v0 offset:0\n\t"
      "ds_read_b128 a[4:7], v0 offset:2048\n\t"
      "ds_read_b128 a[8:11], v0 offset:4096\n\t"
      "ds_read_b128 a[12:15], v0 offset:6144\n\t"
      "ds_read_b128 a[16:19], v0 offset:8192\n\t"
      "ds_read_b128 a[20:23], v0 offset:10240\n\t"
      "ds_read_b128 a[24:27], v0 offset:12288\n\t"
      "ds_read_b128 a[28:31], v0 offset:14336\n\t"
      "ds_read_b64_tr_b16 a[32:33], v2 offset:0\n\t"
      "ds_read_b64_tr_b16 a[34:35], v2 offset:4096\n\t"
      "ds_read_b64_tr_b16 a[36:37], v3 offset:0\n\t"
      "ds_read_b64_tr_b16 a[38:39], v3 offset:4096\n\t"
      "ds_read_b64_tr_b16 a[40:41], v4 offset:0\n\t"
      "ds_read_b64_tr_b16 a[42:43], v4 offset:4096\n\t"
      "ds_read_b64_tr_b16 a[44:45], v5 offset:0\n\t"
      "ds_read_b64_tr_b16 a[46:47], v5 offset:4096\n\t"
      "ds_read_b64_tr_b16 a[48:49], v2 offset:8192\n\t"
      "ds_read_b64_tr_b16 a[50:51], v2 offset:12288\n\t"
      "ds_read_b64_tr_b16 a[52:53], v3 offset:8192\n\t"
      "ds_read_b64_tr_b16 a[54:55], v3 offset:12288\n\t"
      "ds_read_b64_tr_b16 a[56:57], v4 offset:8192\n\t"
      "ds_read_b64_tr_b16 a[58:59], v4 offset:12288\n\t"
      "ds_read_b64_tr_b16 a[60:61], v5 offset:8192\n\t"
      "ds_read_b64_tr_b16 a[62:63], v5 offset:12288\n\t"
      "ds_read_b128 a[64:67], v6 offset:0\n\t"
      "s_mov_b32 s55, 7\n\t"
      "LA_loop_r0_%=:\n\t"
      "s_cmp_eq_u32 s55, s51\n\t"
      "s_cbranch_scc1 LA_cs_r0_%=\n\t"
      "s_cmp_eq_u32 s55, 1\n\t"
      "s_cbranch_scc1 LA_cs_r0_%=\n\t"
      "s_waitcnt vmcnt(19) lgkmcnt(0)\n\t"
      "s_barrier\n\t"
      "v_mfma_f32_16x16x32_bf16 v[32:35], a[32:35], a[0:3], v[32:35]\n\t"
      "ds_read_b128 a[68:71], v1 offset:0\n\t"
      "buffer_load_dword v18, v16, s[56:59], s60 offen\n\t"
      "v_mfma_f32_16x16x32_bf16 v[64:67], a[32:35], a[4:7], v[64:67]\n\t"
      "s_add_u32 s60, s60, s61\n\t"
      "v_mfma_f32_16x16x32_bf16 v[96:99], a[32:35], a[8:11], v[96:99]\n\t"
      "ds_read_b128 a[72:75], v1 offset:2048\n\t"
      "v_mfma_f32_16x16x32_bf16 v[128:131], a[32:35], a[12:15], v[128:131]\n\t"
      "s_add_u32 m0, s49, 0\n\t"
      "v_mfma_f32_16x16x32_bf16 v[160:163], a[32:35], a[16:19], v[160:163]\n\t"
      "ds_read_b128 a[76:79], v1 offset:4096\n\t"
      "buffer_load_dwordx4 v12, s[40:43], s53 offen lds\n\t"
      "v_mfma_f32_16x16x32_bf16 v[192:195], a[32:35], a[20:23], v[192:195]\n\t"
      "v_mfma_f32_16x16x32_bf16 v[224:227], a[32:35], a[24:27], v[224:227]\n\t"
      "v_mfma_f32_16x16x32_bf16 a[192:195], a[32:35], a[28:31], a[192:195]\n\t"
      "ds_read_b128 a[80:83], v1 offset:6144\n\t"
      "v_mfma_f32_16x16x32_bf16 a[196:199], a[36:39], a[28:31], a[196:199]\n\t"
      "v_mfma_f32_16x16x32_bf16 v[228:231], a[36:39], a[24:27], v[228:231]\n\t"
      "ds_read_b128 a[84:87], v1 offset:8192\n\t"
      "v_mfma_f32_16x16x32_bf16 v[196:199], a[36:39], a[20:23], v[196:199]\n\t"
      "v_mfma_f32_16x16x32_bf16 v[164:167], a[36:39], a[16:19], v[164:167]\n\t"
      "v_mfma_f32_16x16x32_bf16 v[132:135], a[36:39], a[12:15], v[132:135]\n\t"
      "ds_read_b128 a[88:91], v1 offset:10240\n\t"
      "v_mfma_f32_16x16x32_bf16 v[100:103], a[36:39], a[8:11], v[100:103]\n\t"
      "v_mfma_f32_16x16x32_bf16 v[68:71], a[36:39], a[4:7], v[68:71]\n\t"
      "ds_read_b128 a[92:95], v1 offset:12288\n\t"
      "v_mfma_f32_16x16x32_bf16 v[36:39], a[36:39], a[0:3], v[36:39]\n\t"
      "v_mfma_f32_16x16x32_bf16 v[40:43], a[40:43], a[0:3], v[40:43]\n\t"
      "ds_read_b128 a[96:99], v1 offset:14336\n\t"
      "buffer_load_dwordx4 v13, s[40:43], s53 offen offset:1024 lds\n\t"
      "v_mfma_f32_16x16x32_bf16 v[72:75], a[40:43], a[4:7], v[72:75]\n\t"
      "v_mfma_f32_16x16x32_bf16 v[104:107], a[40:43], a[8:11], v[104:107]\n\t"
      "v_mfma_f32_16x16x32_bf16 v[136:139], a[40:43], a[12:15], v[136:139]\n\t"
      "ds_read_b64_tr_b16 a[100:101], v2 offset:16384\n\t"
      "v_mfma_f32_16x16x32_bf16 v[168:171], a[40:43], a[16:19], v[168:171]\n\t"
      "v_mfma_f32_16x16x32_bf16 v[200:203], a[40:43], a[20:23], v[200:203]\n\t"
      "ds_read_b64_tr_b16 a[102:103], v2 offset:20480\n\t"
      "v_mfma_f32_16x16x32_bf16 v[232:235], a[40:43], a[24:27], v[232:235]\n\t"
      "v_mfma_f32_16x16x32_bf16 a[200:203], a[40:43], a[28:31], a[200:203]\n\t"
      "v_mfma_f32_16x16x32_bf16 a[204:207], a[44:47], a[28:31], a[204:207]\n\t"
      "ds_read_b64_tr_b16 a[104:105], v3 offset:16384\n\t"
      "v_mfma_f32_16x16x32_bf16 v[236:239], a[44:47], a[24:27], v[236:239]\n\t"
      "v_mfma_f32_16x16x32_bf16 v[204:207], a[44:47], a[20:23], v[204:207]\n\t"
      "ds_read_b64_tr_b16 a[106:107], v3 offset:20480\n\t"
      "v_mfma_f32_16x16x32_bf16 v[172:175], a[44:47], a[16:19], v[172:175]\n\t"
      "v_mfma_f32_16x16x32_bf16 v[140:143], a[44:47], a[12:15], v[140:143]\n\t"
      "buffer_load_dwordx4 v14, s[40:43], s53 offen offset:2048 lds\n\t"
      "v_mfma_f32_16x16x32_bf16 v[108:111], a[44:47], a[8:11], v[108:111]\n\t"
      "ds_read_b64_tr_b16 a[108:109], v4 offset:16384\n\t"
      "v_mfma_f32_16x16x32_bf16 v[76:79], a[44:47], a[4:7], v[76:79]\n\t"
      "v_mfma_f32_16x16x32_bf16 v[44:47], a[44:47], a[0:3], v[44:47]\n\t"
      "ds_read_b64_tr_b16 a[110:111], v4 offset:20480\n\t"
      "v_mfma_f32_16x16x32_bf16 v[48:51], a[48:51], a[0:3], v[48:51]\n\t"
      "v_mfma_f32_16x16x32_bf16 v[80:83], a[48:51], a[4:7], v[80:83]\n\t"
      "ds_read_b64_tr_b16 a[112:113], v5 offset:16384\n\t"
      "v_mfma_f32_16x16x32_bf16 v[112:115], a[48:51], a[8:11], v[112:115]\n\t"
      "v_mfma_f32_16x16x32_bf16 v[144:147], a[48:51], a[12:15], v[144:147]\n\t"
      "v_mfma_f32_16x16x32_bf16 v[176:179], a[48:51], a[16:19], v[176:179]\n\t"
      "ds_read_b64_tr_b16 a[114:115], v5 offset:20480\n\t"
      "v_mfma_f32_16x16x32_bf16 v[208:211], a[48:51], a[20:23], v[208:211]\n\t"
      "v_mfma_f32_16x16x32_bf16 v[240:243], a[48:51], a[24:27], v[240:243]\n\t"
      "ds_read_b64_tr_b16 a[116:117], v2 offset:24576\n\t"
      "v_mfma_f32_16x16x32_bf16 a[208:211], a[48:51], a[28:31], a[208:211]\n\t"
      "v_mfma_f32_16x16x32_bf16 a[212:215], a[52:55], a[28:31], a[212:215]\n\t"
      "buffer_load_dwordx4 v15, s[40:43], s53 offen offset:3072 lds\n\t"
      "v_mfma_f32_16x16x32_bf16 v[244:247], a[52:55], a[24:27], v[244:247]\n\t"
      "ds_read_b64_tr_b16 a[118:119], v2 offset:28672\n\t"
      "s_add_u32 s53, s53, 229376\n\t"
      "v_mfma_f32_16x16x32_bf16 v[212:215], a[52:55], a[20:23], v[212:215]\n\t"
      "v_mfma_f32_16x16x32_bf16 v[180:183], a[52:55], a[16:19], v[180:183]\n\t"
      "ds_read_b64_tr_b16 a[120:121], v3 offset:24576\n\t"
      "v_mfma_f32_16x16x32_bf16 v[148:151], a[52:55], a[12:15], v[148:151]\n\t"
      "v_mfma_f32_16x16x32_bf16 v[116:119], a[52:55], a[8:11], v[116:119]\n\t"
      "ds_read_b64_tr_b16 a[122:123], v3 offset:28672\n\t"
      "v_mfma_f32_16x16x32_bf16 v[84:87], a[52:55], a[4:7], v[84:87]\n\t"
      "v_mfma_f32_16x16x32_bf16 v[52:55], a[52:55], a[0:3], v[52:55]\n\t"
      "v_mfma_f32_16x16x32_bf16 v[56:59], a[56:59], a[0:3], v[56:59]\n\t"
      "ds_read_b64_tr_b16 a[124:125], v4 offset:24576\n\t"
      "v_mfma_f32_16x16x32_bf16 v[88:91], a[56:59], a[4:7], v[88:91]\n\t"
      "v_mfma_f32_16x16x32_bf16 v[120:123], a[56:59], a[8:11], v[120:123]\n\t"
      "ds_read_b64_tr_b16 a[126:127], v4 offset:28672\n\t"
      "v_mfma_f32_16x16x32_bf16 v[152:155], a[56:59], a[12:15], v[152:155]\n\t"
      "v_mfma_f32_16x16x32_bf16 v[184:187], a[56:59], a[16:19], v[184:187]\n\t"
      "v_mfma_f32_16x16x32_bf16 v[216:219], a[56:59], a[20:23], v[216:219]\n\t"
      "ds_read_b64_tr_b16 a[128:129], v5 offset:24576\n\t"
      "v_mfma_f32_16x16x32_bf16 v[248:251], a[56:59], a[24:27], v[248:251]\n\t"
      "v_mfma_f32_16x16x32_bf16 a[216:219], a[56:59], a[28:31], a[216:219]\n\t"
      "ds_read_b64_tr_b16 a[130:131], v5 offset:28672\n\t"
      "v_mfma_f32_16x16x32_bf16 a[220:223], a[60:63], a[28:31], a[220:223]\n\t"
      "v_mfma_f32_16x16x32_bf16 v[252:255], a[60:63], a[24:27], v[252:255]\n\t"
      "v_mfma_f32_16x16x32_bf16 v[220:223], a[60:63], a[20:23], v[220:223]\n\t"
      "ds_read_b128 a[132:135], v7 offset:0\n\t"
      "v_mfma_f32_16x16x32_bf16 v[188:191], a[60:63], a[16:19], v[188:191]\n\t"
      "v_mfma_f32_16x16x32_bf16 v[156:159], a[60:63], a[12:15], v[156:159]\n\t"
      "v_mfma_f32_16x16x32_bf16 v[124:127], a[60:63], a[8:11], v[124:127]\n\t"
      "v_mfma_f32_16x16x32_bf16 v[92:95], a[60:63], a[4:7], v[92:95]\n\t"
      "v_mfma_f32_16x16x32_bf16 v[60:63], a[60:63], a[0:3], v[60:63]\n\t"
      "v_mfma_f32_16x16x32_bf16 a[224:227], a[0:3], a[64:67], a[224:227]\n\t"
      "v_mfma_f32_16x16x32_bf16 a[228:231], a[4:7], a[64:67], a[228:231]\n\t"
      "v_mfma_f32_16x16x32_bf16 a[232:235], a[8:11], a[64:67], a[232:235]\n\t"
      "v_mfma_f32_16x16x32_bf16 a[236:239], a[12:15], a[64:67], a[236:239]\n\t"
      "v_mfma_f32_16x16x32_bf16 a[240:243], a[16:19], a[64:67], a[240:243]\n\t"
      "v_mfma_f32_16x16x32_bf16 a[244:247], a[20:23], a[64:67], a[244:247]\n\t"
      "v_mfma_f32_16x16x32_bf16 a[248:251], a[24:27], a[64:67], a[248:251]\n\t"
      "v_mfma_f32_16x16x32_bf16 a[252:255], a[28:31], a[64:67], a[252:255]\n\t"
      "s_waitcnt vmcnt(9) lgkmcnt(0)\n\t"
      "s_barrier\n\t"
      "v_mfma_f32_16x16x32_bf16 v[32:35], a[100:103], a[68:71], v[32:35]\n\t"
      "ds_read_b128 a[0:3], v0 offset:32768\n\t"
      "buffer_load_dword v18, v16, s[56:59], s60 offen\n\t"
      "v_mfma_f32_16x16x32_bf16 v[64:67], a[100:103], a[72:75], v[64:67]\n\t"
      "s_add_u32 s60, s60, s61\n\t"
      "v_mfma_f32_16x16x32_bf16 v[96:99], a[100:103], a[76:79], v[96:99]\n\t"
      "ds_read_b128 a[4:7], v0 offset:34816\n\t"
      "s_add_u32 s62, s52, 65536\n\t"
      "s_add_u32 m0, s48, 0\n\t"
      "v_mfma_f32_16x16x32_bf16 v[128:131], a[100:103], a[80:83], v[128:131]\n\t"
      "buffer_load_dwordx4 v8, s[36:39], s52 offen lds\n\t"
      "v_mfma_f32_16x16x32_bf16 v[160:163], a[100:103], a[84:87], v[160:163]\n\t"
      "ds_read_b128 a[8:11], v0 offset:36864\n\t"
      "v_mfma_f32_16x16x32_bf16 v[192:195], a[100:103], a[88:91], v[192:195]\n\t"
      "v_mfma_f32_16x16x32_bf16 v[224:227], a[100:103], a[92:95], v[224:227]\n\t"
      "v_mfma_f32_16x16x32_bf16 a[192:195], a[100:103], a[96:99], a[192:195]\n\t"
      "ds_read_b128 a[12:15], v0 offset:38912\n\t"
      "v_mfma_f32_16x16x32_bf16 a[196:199], a[104:107], a[96:99], a[196:199]\n\t"
      "buffer_load_dwordx4 v9, s[36:39], s52 offen offset:1024 lds\n\t"
      "v_mfma_f32_16x16x32_bf16 v[228:231], a[104:107], a[92:95], v[228:231]\n\t"
      "ds_read_b128 a[16:19], v0 offset:40960\n\t"
      "v_mfma_f32_16x16x32_bf16 v[196:199], a[104:107], a[88:91], v[196:199]\n\t"
      "v_mfma_f32_16x16x32_bf16 v[164:167], a[104:107], a[84:87], v[164:167]\n\t"
      "v_mfma_f32_16x16x32_bf16 v[132:135], a[104:107], a[80:83], v[132:135]\n\t"
      "ds_read_b128 a[20:23], v0 offset:43008\n\t"
      "v_mfma_f32_16x16x32_bf16 v[100:103], a[104:107], a[76:79], v[100:103]\n\t"
      "v_mfma_f32_16x16x32_bf16 v[68:71], a[104:107], a[72:75], v[68:71]\n\t"
      "ds_read_b128 a[24:27], v0 offset:45056\n\t"
      "buffer_load_dwordx4 v10, s[36:39], s52 offen offset:2048 lds\n\t"
      "v_mfma_f32_16x16x32_bf16 v[36:39], a[104:107], a[68:71], v[36:39]\n\t"
      "v_mfma_f32_16x16x32_bf16 v[40:43], a[108:111], a[68:71], v[40:43]\n\t"
      "ds_read_b128 a[28:31], v0 offset:47104\n\t"
      "v_mfma_f32_16x16x32_bf16 v[72:75], a[108:111], a[72:75], v[72:75]\n\t"
      "v_mfma_f32_16x16x32_bf16 v[104:107], a[108:111], a[76:79], v[104:107]\n\t"
      "v_mfma_f32_16x16x32_bf16 v[136:139], a[108:111], a[80:83], v[136:139]\n\t"
      "ds_read_b64_tr_b16 a[32:33], v2 offset:32768\n\t"
      "buffer_load_dwordx4 v11, s[36:39], s52 offen offset:3072 lds\n\t"
      "v_mfma_f32_16x16x32_bf16 v[168:171], a[108:111], a[84:87], v[168:171]\n\t"
      "v_mfma_f32_16x16x32_bf16 v[200:203], a[108:111], a[88:91], v[200:203]\n\t"
      "ds_read_b64_tr_b16 a[34:35], v2 offset:36864\n\t"
      "v_mfma_f32_16x16x32_bf16 v[232:235], a[108:111], a[92:95], v[232:235]\n\t"
      "v_mfma_f32_16x16x32_bf16 a[200:203], a[108:111], a[96:99], a[200:203]\n\t"
      "v_mfma_f32_16x16x32_bf16 a[204:207], a[112:115], a[96:99], a[204:207]\n\t"
      "ds_read_b64_tr_b16 a[36:37], v3 offset:32768\n\t"
      "s_add_u32 m0, s48, 4096\n\t"
      "v_mfma_f32_16x16x32_bf16 v[236:239], a[112:115], a[92:95], v[236:239]\n\t"
      "buffer_load_dwordx4 v8, s[36:39], s62 offen lds\n\t"
      "v_mfma_f32_16x16x32_bf16 v[204:207], a[112:115], a[88:91], v[204:207]\n\t"
      "ds_read_b64_tr_b16 a[38:39], v3 offset:36864\n\t"
      "v_mfma_f32_16x16x32_bf16 v[172:175], a[112:115], a[84:87], v[172:175]\n\t"
      "v_mfma_f32_16x16x32_bf16 v[140:143], a[112:115], a[80:83], v[140:143]\n\t"
      "v_mfma_f32_16x16x32_bf16 v[108:111], a[112:115], a[76:79], v[108:111]\n\t"
      "ds_read_b64_tr_b16 a[40:41], v4 offset:32768\n\t"
      "v_mfma_f32_16x16x32_bf16 v[76:79], a[112:115], a[72:75], v[76:79]\n\t"
      "v_mfma_f32_16x16x32_bf16 v[44:47], a[112:115], a[68:71], v[44:47]\n\t"
      "ds_read_b64_tr_b16 a[42:43], v4 offset:36864\n\t"
      "buffer_load_dwordx4 v9, s[36:39], s62 offen offset:1024 lds\n\t"
      "v_mfma_f32_16x16x32_bf16 v[48:51], a[116:119], a[68:71], v[48:51]\n\t"
      "v_mfma_f32_16x16x32_bf16 v[80:83], a[116:119], a[72:75], v[80:83]\n\t"
      "ds_read_b64_tr_b16 a[44:45], v5 offset:32768\n\t"
      "v_mfma_f32_16x16x32_bf16 v[112:115], a[116:119], a[76:79], v[112:115]\n\t"
      "v_mfma_f32_16x16x32_bf16 v[144:147], a[116:119], a[80:83], v[144:147]\n\t"
      "v_mfma_f32_16x16x32_bf16 v[176:179], a[116:119], a[84:87], v[176:179]\n\t"
      "ds_read_b64_tr_b16 a[46:47], v5 offset:36864\n\t"
      "buffer_load_dwordx4 v10, s[36:39], s62 offen offset:2048 lds\n\t"
      "v_mfma_f32_16x16x32_bf16 v[208:211], a[116:119], a[88:91], v[208:211]\n\t"
      "v_mfma_f32_16x16x32_bf16 v[240:243], a[116:119], a[92:95], v[240:243]\n\t"
      "ds_read_b64_tr_b16 a[48:49], v2 offset:40960\n\t"
      "v_mfma_f32_16x16x32_bf16 a[208:211], a[116:119], a[96:99], a[208:211]\n\t"
      "v_mfma_f32_16x16x32_bf16 a[212:215], a[120:123], a[96:99], a[212:215]\n\t"
      "v_mfma_f32_16x16x32_bf16 v[244:247], a[120:123], a[92:95], v[244:247]\n\t"
      "ds_read_b64_tr_b16 a[50:51], v2 offset:45056\n\t"
      "v_mfma_f32_16x16x32_bf16 v[212:215], a[120:123], a[88:91], v[212:215]\n\t"
      "buffer_load_dwordx4 v11, s[36:39], s62 offen offset:3072 lds\n\t"
      "v_mfma_f32_16x16x32_bf16 v[180:183], a[120:123], a[84:87], v[180:183]\n\t"
      "ds_read_b64_tr_b16 a[52:53], v3 offset:40960\n\t"
      "s_add_u32 s52, s52, 128\n\t"
      "v_mfma_f32_16x16x32_bf16 v[148:151], a[120:123], a[80:83], v[148:151]\n\t"
      "v_mfma_f32_16x16x32_bf16 v[116:119], a[120:123], a[76:79], v[116:119]\n\t"
      "ds_read_b64_tr_b16 a[54:55], v3 offset:45056\n\t"
      "v_mfma_f32_16x16x32_bf16 v[84:87], a[120:123], a[72:75], v[84:87]\n\t"
      "v_mfma_f32_16x16x32_bf16 v[52:55], a[120:123], a[68:71], v[52:55]\n\t"
      "s_add_u32 m0, s50, 0\n\t"
      "v_mfma_f32_16x16x32_bf16 v[56:59], a[124:127], a[68:71], v[56:59]\n\t"
      "ds_read_b64_tr_b16 a[56:57], v4 offset:40960\n\t"
      "buffer_load_dwordx4 v19, s[44:47], s54 offen lds\n\t"
      "v_mfma_f32_16x16x32_bf16 v[88:91], a[124:127], a[72:75], v[88:91]\n\t"
      "s_add_u32 s54, s54, 128\n\t"
      "v_mfma_f32_16x16x32_bf16 v[120:123], a[124:127], a[76:79], v[120:123]\n\t"
      "ds_read_b64_tr_b16 a[58:59], v4 offset:45056\n\t"
      "v_mfma_f32_16x16x32_bf16 v[152:155], a[124:127], a[80:83], v[152:155]\n\t"
      "v_mfma_f32_16x16x32_bf16 v[184:187], a[124:127], a[84:87], v[184:187]\n\t"
      "s_add_u32 m0, s49, 16384\n\t"
      "v_mfma_f32_16x16x32_bf16 v[216:219], a[124:127], a[88:91], v[216:219]\n\t"
      "ds_read_b64_tr_b16 a[60:61], v5 offset:40960\n\t"
      "buffer_load_dwordx4 v12, s[40:43], s53 offen lds\n\t"
      "v_mfma_f32_16x16x32_bf16 v[248:251], a[124:127], a[92:95], v[248:251]\n\t"
      "v_mfma_f32_16x16x32_bf16 a[216:219], a[124:127], a[96:99], a[216:219]\n\t"
      "ds_read_b64_tr_b16 a[62:63], v5 offset:45056\n\t"
      "v_mfma_f32_16x16x32_bf16 a[220:223], a[128:131], a[96:99], a[220:223]\n\t"
      "v_mfma_f32_16x16x32_bf16 v[252:255], a[128:131], a[92:95], v[252:255]\n\t"
      "buffer_load_dwordx4 v13, s[40:43], s53 offen offset:1024 lds\n\t"
      "v_mfma_f32_16x16x32_bf16 v[220:223], a[128:131], a[88:91], v[220:223]\n\t"
      "ds_read_b128 a[64:67], v6 offset:4096\n\t"
      "v_mfma_f32_16x16x32_bf16 v[188:191], a[128:131], a[84:87], v[188:191]\n\t"
      "v_mfma_f32_16x16x32_bf16 v[156:159], a[128:131], a[80:83], v[156:159]\n\t"
      "v_mfma_f32_16x16x32_bf16 v[124:127], a[128:131], a[76:79], v[124:127]\n\t"
      "buffer_load_dwordx4 v14, s[40:43], s53 offen offset:2048 lds\n\t"
      "v_mfma_f32_16x16x32_bf16 v[92:95], a[128:131], a[72:75], v[92:95]\n\t"
      "v_mfma_f32_16x16x32_bf16 v[60:63], a[128:131], a[68:71], v[60:63]\n\t"
      "v_mfma_f32_16x16x32_bf16 a[224:227], a[68:71], a[132:135], a[224:227]\n\t"
      "v_mfma_f32_16x16x32_bf16 a[228:231], a[72:75], a[132:135], a[228:231]\n\t"
      "v_mfma_f32_16x16x32_bf16 a[232:235], a[76:79], a[132:135], a[232:235]\n\t"
      "buffer_load_dwordx4 v15, s[40:43], s53 offen offset:3072 lds\n\t"
      "v_mfma_f32_16x16x32_bf16 a[236:239], a[80:83], a[132:135], a[236:239]\n\t"
      "s_add_u32 s53, s53, 229376\n\t"
      "v_mfma_f32_16x16x32_bf16 a[240:243], a[84:87], a[132:135], a[240:243]\n\t"
      "v_mfma_f32_16x16x32_bf16 a[244:247], a[88:91], a[132:135], a[244:247]\n\t"
      "v_mfma_f32_16x16x32_bf16 a[248:251], a[92:95], a[132:135], a[248:251]\n\t"
      "v_mfma_f32_16x16x32_bf16 a[252:255], a[96:99], a[132:135], a[252:255]\n\t"
      "s_waitcnt vmcnt(19) lgkmcnt(0)\n\t"
      "s_barrier\n\t"
      "v_mfma_f32_16x16x32_bf16 v[32:35], a[32:35], a[0:3], v[32:35]\n\t"
      "ds_read_b128 a[68:71], v1 offset:32768\n\t"
      "buffer_load_dword v18, v16, s[56:59], s60 offen\n\t"
      "v_mfma_f32_16x16x32_bf16 v[64:67], a[32:35], a[4:7], v[64:67]\n\t"
      "s_add_u32 s60, s60, s61\n\t"
      "v_mfma_f32_16x16x32_bf16 v[96:99], a[32:35], a[8:11], v[96:99]\n\t"
      "ds_read_b128 a[72:75], v1 offset:34816\n\t"
      "v_mfma_f32_16x16x32_bf16 v[128:131], a[32:35], a[12:15], v[128:131]\n\t"
      "s_add_u32 m0, s49, 32768\n\t"
      "v_mfma_f32_16x16x32_bf16 v[160:163], a[32:35], a[16:19], v[160:163]\n\t"
      "ds_read_b128 a[76:79], v1 offset:36864\n\t"
      "buffer_load_dwordx4 v12, s[40:43], s53 offen lds\n\t"
      "v_mfma_f32_16x16x32_bf16 v[192:195], a[32:35], a[20:23], v[192:195]\n\t"
      "v_mfma_f32_16x16x32_bf16 v[224:227], a[32:35], a[24:27], v[224:227]\n\t"
      "v_mfma_f32_16x16x32_bf16 a[192:195], a[32:35], a[28:31], a[192:195]\n\t"
      "ds_read_b128 a[80:83], v1 offset:38912\n\t"
      "v_mfma_f32_16x16x32_bf16 a[196:199], a[36:39], a[28:31], a[196:199]\n\t"
      "v_mfma_f32_16x16x32_bf16 v[228:231], a[36:39], a[24:27], v[228:231]\n\t"
      "ds_read_b128 a[84:87], v1 offset:40960\n\t"
      "v_mfma_f32_16x16x32_bf16 v[196:199], a[36:39], a[20:23], v[196:199]\n\t"
      "v_mfma_f32_16x16x32_bf16 v[164:167], a[36:39], a[16:19], v[164:167]\n\t"
      "v_mfma_f32_16x16x32_bf16 v[132:135], a[36:39], a[12:15], v[132:135]\n\t"
      "ds_read_b128 a[88:91], v1 offset:43008\n\t"
      "v_mfma_f32_16x16x32_bf16 v[100:103], a[36:39], a[8:11], v[100:103]\n\t"
      "v_mfma_f32_16x16x32_bf16 v[68:71], a[36:39], a[4:7], v[68:71]\n\t"
      "ds_read_b128 a[92:95], v1 offset:45056\n\t"
      "v_mfma_f32_16x16x32_bf16 v[36:39], a[36:39], a[0:3], v[36:39]\n\t"
      "v_mfma_f32_16x16x32_bf16 v[40:43], a[40:43], a[0:3], v[40:43]\n\t"
      "ds_read_b128 a[96:99], v1 offset:47104\n\t"
      "buffer_load_dwordx4 v13, s[40:43], s53 offen offset:1024 lds\n\t"
      "v_mfma_f32_16x16x32_bf16 v[72:75], a[40:43], a[4:7], v[72:75]\n\t"
      "v_mfma_f32_16x16x32_bf16 v[104:107], a[40:43], a[8:11], v[104:107]\n\t"
      "v_mfma_f32_16x16x32_bf16 v[136:139], a[40:43], a[12:15], v[136:139]\n\t"
      "ds_read_b64_tr_b16 a[100:101], v2 offset:49152\n\t"
      "v_mfma_f32_16x16x32_bf16 v[168:171], a[40:43], a[16:19], v[168:171]\n\t"
      "v_mfma_f32_16x16x32_bf16 v[200:203], a[40:43], a[20:23], v[200:203]\n\t"
      "ds_read_b64_tr_b16 a[102:103], v2 offset:53248\n\t"
      "v_mfma_f32_16x16x32_bf16 v[232:235], a[40:43], a[24:27], v[232:235]\n\t"
      "v_mfma_f32_16x16x32_bf16 a[200:203], a[40:43], a[28:31], a[200:203]\n\t"
      "v_mfma_f32_16x16x32_bf16 a[204:207], a[44:47], a[28:31], a[204:207]\n\t"
      "ds_read_b64_tr_b16 a[104:105], v3 offset:49152\n\t"
      "v_mfma_f32_16x16x32_bf16 v[236:239], a[44:47], a[24:27], v[236:239]\n\t"
      "v_mfma_f32_16x16x32_bf16 v[204:207], a[44:47], a[20:23], v[204:207]\n\t"
      "ds_read_b64_tr_b16 a[106:107], v3 offset:53248\n\t"
      "v_mfma_f32_16x16x32_bf16 v[172:175], a[44:47], a[16:19], v[172:175]\n\t"
      "v_mfma_f32_16x16x32_bf16 v[140:143], a[44:47], a[12:15], v[140:143]\n\t"
      "buffer_load_dwordx4 v14, s[40:43], s53 offen offset:2048 lds\n\t"
      "v_mfma_f32_16x16x32_bf16 v[108:111], a[44:47], a[8:11], v[108:111]\n\t"
      "ds_read_b64_tr_b16 a[108:109], v4 offset:49152\n\t"
      "v_mfma_f32_16x16x32_bf16 v[76:79], a[44:47], a[4:7], v[76:79]\n\t"
      "v_mfma_f32_16x16x32_bf16 v[44:47], a[44:47], a[0:3], v[44:47]\n\t"
      "ds_read_b64_tr_b16 a[110:111], v4 offset:53248\n\t"
      "v_mfma_f32_16x16x32_bf16 v[48:51], a[48:51], a[0:3], v[48:51]\n\t"
      "v_mfma_f32_16x16x32_bf16 v[80:83], a[48:51], a[4:7], v[80:83]\n\t"
      "ds_read_b64_tr_b16 a[112:113], v5 offset:49152\n\t"
      "v_mfma_f32_16x16x32_bf16 v[112:115], a[48:51], a[8:11], v[112:115]\n\t"
      "v_mfma_f32_16x16x32_bf16 v[144:147], a[48:51], a[12:15], v[144:147]\n\t"
      "v_mfma_f32_16x16x32_bf16 v[176:179], a[48:51], a[16:19], v[176:179]\n\t"
      "ds_read_b64_tr_b16 a[114:115], v5 offset:53248\n\t"
      "v_mfma_f32_16x16x32_bf16 v[208:211], a[48:51], a[20:23], v[208:211]\n\t"
      "v_mfma_f32_16x16x32_bf16 v[240:243], a[48:51], a[24:27], v[240:243]\n\t"
      "ds_read_b64_tr_b16 a[116:117], v2 offset:57344\n\t"
      "v_mfma_f32_16x16x32_bf16 a[208:211], a[48:51], a[28:31], a[208:211]\n\t"
      "v_mfma_f32_16x16x32_bf16 a[212:215], a[52:55], a[28:31], a[212:215]\n\t"
      "buffer_load_dwordx4 v15, s[40:43], s53 offen offset:3072 lds\n\t"
      "v_mfma_f32_16x16x32_bf16 v[244:247], a[52:55], a[24:27], v[244:247]\n\t"
      "ds_read_b64_tr_b16 a[118:119], v2 offset:61440\n\t"
      "s_add_u32 s53, s53, 229376\n\t"
      "v_mfma_f32_16x16x32_bf16 v[212:215], a[52:55], a[20:23], v[212:215]\n\t"
      "v_mfma_f32_16x16x32_bf16 v[180:183], a[52:55], a[16:19], v[180:183]\n\t"
      "ds_read_b64_tr_b16 a[120:121], v3 offset:57344\n\t"
      "v_mfma_f32_16x16x32_bf16 v[148:151], a[52:55], a[12:15], v[148:151]\n\t"
      "v_mfma_f32_16x16x32_bf16 v[116:119], a[52:55], a[8:11], v[116:119]\n\t"
      "ds_read_b64_tr_b16 a[122:123], v3 offset:61440\n\t"
      "v_mfma_f32_16x16x32_bf16 v[84:87], a[52:55], a[4:7], v[84:87]\n\t"
      "v_mfma_f32_16x16x32_bf16 v[52:55], a[52:55], a[0:3], v[52:55]\n\t"
      "v_mfma_f32_16x16x32_bf16 v[56:59], a[56:59], a[0:3], v[56:59]\n\t"
      "ds_read_b64_tr_b16 a[124:125], v4 offset:57344\n\t"
      "v_mfma_f32_16x16x32_bf16 v[88:91], a[56:59], a[4:7], v[88:91]\n\t"
      "v_mfma_f32_16x16x32_bf16 v[120:123], a[56:59], a[8:11], v[120:123]\n\t"
      "ds_read_b64_tr_b16 a[126:127], v4 offset:61440\n\t"
      "v_mfma_f32_16x16x32_bf16 v[152:155], a[56:59], a[12:15], v[152:155]\n\t"
      "v_mfma_f32_16x16x32_bf16 v[184:187], a[56:59], a[16:19], v[184:187]\n\t"
      "v_mfma_f32_16x16x32_bf16 v[216:219], a[56:59], a[20:23], v[216:219]\n\t"
      "ds_read_b64_tr_b16 a[128:129], v5 offset:57344\n\t"
      "v_mfma_f32_16x16x32_bf16 v[248:251], a[56:59], a[24:27], v[248:251]\n\t"
      "v_mfma_f32_16x16x32_bf16 a[216:219], a[56:59], a[28:31], a[216:219]\n\t"
      "ds_read_b64_tr_b16 a[130:131], v5 offset:61440\n\t"
      "v_mfma_f32_16x16x32_bf16 a[220:223], a[60:63], a[28:31], a[220:223]\n\t"
      "v_mfma_f32_16x16x32_bf16 v[252:255], a[60:63], a[24:27], v[252:255]\n\t"
      "v_mfma_f32_16x16x32_bf16 v[220:223], a[60:63], a[20:23], v[220:223]\n\t"
      "ds_read_b128 a[132:135], v7 offset:4096\n\t"
      "v_mfma_f32_16x16x32_bf16 v[188:191], a[60:63], a[16:19], v[188:191]\n\t"
      "v_mfma_f32_16x16x32_bf16 v[156:159], a[60:63], a[12:15], v[156:159]\n\t"
      "v_mfma_f32_16x16x32_bf16 v[124:127], a[60:63], a[8:11], v[124:127]\n\t"
      "v_mfma_f32_16x16x32_bf16 v[92:95], a[60:63], a[4:7], v[92:95]\n\t"
      "v_mfma_f32_16x16x32_bf16 v[60:63], a[60:63], a[0:3], v[60:63]\n\t"
      "v_mfma_f32_16x16x32_bf16 a[224:227], a[0:3], a[64:67], a[224:227]\n\t"
      "v_mfma_f32_16x16x32_bf16 a[228:231], a[4:7], a[64:67], a[228:231]\n\t"
      "v_mfma_f32_16x16x32_bf16 a[232:235], a[8:11], a[64:67], a[232:235]\n\t"
      "v_mfma_f32_16x16x32_bf16 a[236:239], a[12:15], a[64:67], a[236:239]\n\t"
      "v_mfma_f32_16x16x32_bf16 a[240:243], a[16:19], a[64:67], a[240:243]\n\t"
      "v_mfma_f32_16x16x32_bf16 a[244:247], a[20:23], a[64:67], a[244:247]\n\t"
      "v_mfma_f32_16x16x32_bf16 a[248:251], a[24:27], a[64:67], a[248:251]\n\t"
      "v_mfma_f32_16x16x32_bf16 a[252:255], a[28:31], a[64:67], a[252:255]\n\t"
      "s_waitcnt vmcnt(9) lgkmcnt(0)\n\t"
      "s_bitcmp1_b32 s81, s55\n\t"
      "s_cbranch_scc0 LA_noflNone_r0_%=\n\t"
      "s_not_b32 s74, s68\n\t"
      "s_not_b32 s75, s69\n\t"
      "s_cmp_eq_u32 s63, 1\n\t"
      "s_cselect_b32 s74, s68, s74\n\t"
      "s_cselect_b32 s75, s69, s75\n\t"
      "v_mov_b32 v22, s74\n\t"
      "v_mov_b32 v23, s75\n\t"
      "s_mov_b64 exec, 1\n\t"
      "buffer_store_dwordx4 v[20:23], v17, s[64:67], 0 offen sc1 ; alt\n\t"
      "s_mov_b64 exec, -1\n\t"
      "s_nop 1\n\t"
      "LA_noflNone_r0_%=:\n\t"
      "s_barrier\n\t"
      "v_mfma_f32_16x16x32_bf16 v[32:35], a[100:103], a[68:71], v[32:35]\n\t"
      "ds_read_b128 a[0:3], v0 offset:0\n\t"
      "buffer_load_dword v18, v16, s[56:59], s60 offen\n\t"
      "v_mfma_f32_16x16x32_bf16 v[64:67], a[100:103], a[72:75], v[64:67]\n\t"
      "s_add_u32 s60, s60, s61\n\t"
      "v_mfma_f32_16x16x32_bf16 v[96:99], a[100:103], a[76:79], v[96:99]\n\t"
      "ds_read_b128 a[4:7], v0 offset:2048\n\t"
      "s_add_u32 s62, s52, 65536\n\t"
      "s_add_u32 m0, s48, 32768\n\t"
      "v_mfma_f32_16x16x32_bf16 v[128:131], a[100:103], a[80:83], v[128:131]\n\t"
      "buffer_load_dwordx4 v8, s[36:39], s52 offen lds\n\t"
      "v_mfma_f32_16x16x32_bf16 v[160:163], a[100:103], a[84:87], v[160:163]\n\t"
      "ds_read_b128 a[8:11], v0 offset:4096\n\t"
      "v_mfma_f32_16x16x32_bf16 v[192:195], a[100:103], a[88:91], v[192:195]\n\t"
      "v_mfma_f32_16x16x32_bf16 v[224:227], a[100:103], a[92:95], v[224:227]\n\t"
      "v_mfma_f32_16x16x32_bf16 a[192:195], a[100:103], a[96:99], a[192:195]\n\t"
      "ds_read_b128 a[12:15], v0 offset:6144\n\t"
      "v_mfma_f32_16x16x32_bf16 a[196:199], a[104:107], a[96:99], a[196:199]\n\t"
      "buffer_load_dwordx4 v9, s[36:39], s52 offen offset:1024 lds\n\t"
      "v_mfma_f32_16x16x32_bf16 v[228:231], a[104:107], a[92:95], v[228:231]\n\t"
      "ds_read_b128 a[16:19], v0 offset:8192\n\t"
      "v_mfma_f32_16x16x32_bf16 v[196:199], a[104:107], a[88:91], v[196:199]\n\t"
      "v_mfma_f32_16x16x32_bf16 v[164:167], a[104:107], a[84:87], v[164:167]\n\t"
      "v_mfma_f32_16x16x32_bf16 v[132:135], a[104:107], a[80:83], v[132:135]\n\t"
      "ds_read_b128 a[20:23], v0 offset:10240\n\t"
      "v_mfma_f32_16x16x32_bf16 v[100:103], a[104:107], a[76:79], v[100:103]\n\t"
      "v_mfma_f32_16x16x32_bf16 v[68:71], a[104:107], a[72:75], v[68:71]\n\t"
      "ds_read_b128 a[24:27], v0 offset:12288\n\t"
      "buffer_load_dwordx4 v10, s[36:39], s52 offen offset:2048 lds\n\t"
      "v_mfma_f32_16x16x32_bf16 v[36:39], a[104:107], a[68:71], v[36:39]\n\t"
      "v_mfma_f32_16x16x32_bf16 v[40:43], a[108:111], a[68:71], v[40:43]\n\t"
      "ds_read_b128 a[28:31], v0 offset:14336\n\t"
      "v_mfma_f32_16x16x32_bf16 v[72:75], a[108:111], a[72:75], v[72:75]\n\t"
      "v_mfma_f32_16x16x32_bf16 v[104:107], a[108:111], a[76:79], v[104:107]\n\t"
      "v_mfma_f32_16x16x32_bf16 v[136:139], a[108:111], a[80:83], v[136:139]\n\t"
      "ds_read_b64_tr_b16 a[32:33], v2 offset:0\n\t"
      "buffer_load_dwordx4 v11, s[36:39], s52 offen offset:3072 lds\n\t"
      "v_mfma_f32_16x16x32_bf16 v[168:171], a[108:111], a[84:87], v[168:171]\n\t"
      "v_mfma_f32_16x16x32_bf16 v[200:203], a[108:111], a[88:91], v[200:203]\n\t"
      "ds_read_b64_tr_b16 a[34:35], v2 offset:4096\n\t"
      "v_mfma_f32_16x16x32_bf16 v[232:235], a[108:111], a[92:95], v[232:235]\n\t"
      "v_mfma_f32_16x16x32_bf16 a[200:203], a[108:111], a[96:99], a[200:203]\n\t"
      "v_mfma_f32_16x16x32_bf16 a[204:207], a[112:115], a[96:99], a[204:207]\n\t"
      "ds_read_b64_tr_b16 a[36:37], v3 offset:0\n\t"
      "s_add_u32 m0, s48, 36864\n\t"
      "v_mfma_f32_16x16x32_bf16 v[236:239], a[112:115], a[92:95], v[236:239]\n\t"
      "buffer_load_dwordx4 v8, s[36:39], s62 offen lds\n\t"
      "v_mfma_f32_16x16x32_bf16 v[204:207], a[112:115], a[88:91], v[204:207]\n\t"
      "ds_read_b64_tr_b16 a[38:39], v3 offset:4096\n\t"
      "v_mfma_f32_16x16x32_bf16 v[172:175], a[112:115], a[84:87], v[172:175]\n\t"
      "v_mfma_f32_16x16x32_bf16 v[140:143], a[112:115], a[80:83], v[140:143]\n\t"
      "v_mfma_f32_16x16x32_bf16 v[108:111], a[112:115], a[76:79], v[108:111]\n\t"
      "ds_read_b64_tr_b16 a[40:41], v4 offset:0\n\t"
      "v_mfma_f32_16x16x32_bf16 v[76:79], a[112:115], a[72:75], v[76:79]\n\t"
      "v_mfma_f32_16x16x32_bf16 v[44:47], a[112:115], a[68:71], v[44:47]\n\t"
      "ds_read_b64_tr_b16 a[42:43], v4 offset:4096\n\t"
      "buffer_load_dwordx4 v9, s[36:39], s62 offen offset:1024 lds\n\t"
      "v_mfma_f32_16x16x32_bf16 v[48:51], a[116:119], a[68:71], v[48:51]\n\t"
      "v_mfma_f32_16x16x32_bf16 v[80:83], a[116:119], a[72:75], v[80:83]\n\t"
      "ds_read_b64_tr_b16 a[44:45], v5 offset:0\n\t"
      "v_mfma_f32_16x16x32_bf16 v[112:115], a[116:119], a[76:79], v[112:115]\n\t"
      "v_mfma_f32_16x16x32_bf16 v[144:147], a[116:119], a[80:83], v[144:147]\n\t"
      "v_mfma_f32_16x16x32_bf16 v[176:179], a[116:119], a[84:87], v[176:179]\n\t"
      "ds_read_b64_tr_b16 a[46:47], v5 offset:4096\n\t"
      "buffer_load_dwordx4 v10, s[36:39], s62 offen offset:2048 lds\n\t"
      "v_mfma_f32_16x16x32_bf16 v[208:211], a[116:119], a[88:91], v[208:211]\n\t"
      "v_mfma_f32_16x16x32_bf16 v[240:243], a[116:119], a[92:95], v[240:243]\n\t"
      "ds_read_b64_tr_b16 a[48:49], v2 offset:8192\n\t"
      "v_mfma_f32_16x16x32_bf16 a[208:211], a[116:119], a[96:99], a[208:211]\n\t"
      "v_mfma_f32_16x16x32_bf16 a[212:215], a[120:123], a[96:99], a[212:215]\n\t"
      "v_mfma_f32_16x16x32_bf16 v[244:247], a[120:123], a[92:95], v[244:247]\n\t"
      "ds_read_b64_tr_b16 a[50:51], v2 offset:12288\n\t"
      "v_mfma_f32_16x16x32_bf16 v[212:215], a[120:123], a[88:91], v[212:215]\n\t"
      "buffer_load_dwordx4 v11, s[36:39], s62 offen offset:3072 lds\n\t"
      "v_mfma_f32_16x16x32_bf16 v[180:183], a[120:123], a[84:87], v[180:183]\n\t"
      "ds_read_b64_tr_b16 a[52:53], v3 offset:8192\n\t"
      "s_add_u32 s52, s52, 128\n\t"
      "v_mfma_f32_16x16x32_bf16 v[148:151], a[120:123], a[80:83], v[148:151]\n\t"
      "v_mfma_f32_16x16x32_bf16 v[116:119], a[120:123], a[76:79], v[116:119]\n\t"
      "ds_read_b64_tr_b16 a[54:55], v3 offset:12288\n\t"
      "v_mfma_f32_16x16x32_bf16 v[84:87], a[120:123], a[72:75], v[84:87]\n\t"
      "v_mfma_f32_16x16x32_bf16 v[52:55], a[120:123], a[68:71], v[52:55]\n\t"
      "s_add_u32 m0, s50, 4096\n\t"
      "v_mfma_f32_16x16x32_bf16 v[56:59], a[124:127], a[68:71], v[56:59]\n\t"
      "ds_read_b64_tr_b16 a[56:57], v4 offset:8192\n\t"
      "buffer_load_dwordx4 v19, s[44:47], s54 offen lds\n\t"
      "v_mfma_f32_16x16x32_bf16 v[88:91], a[124:127], a[72:75], v[88:91]\n\t"
      "s_add_u32 s54, s54, 128\n\t"
      "v_mfma_f32_16x16x32_bf16 v[120:123], a[124:127], a[76:79], v[120:123]\n\t"
      "ds_read_b64_tr_b16 a[58:59], v4 offset:12288\n\t"
      "v_mfma_f32_16x16x32_bf16 v[152:155], a[124:127], a[80:83], v[152:155]\n\t"
      "v_mfma_f32_16x16x32_bf16 v[184:187], a[124:127], a[84:87], v[184:187]\n\t"
      "s_add_u32 m0, s49, 49152\n\t"
      "v_mfma_f32_16x16x32_bf16 v[216:219], a[124:127], a[88:91], v[216:219]\n\t"
      "ds_read_b64_tr_b16 a[60:61], v5 offset:8192\n\t"
      "buffer_load_dwordx4 v12, s[40:43], s53 offen lds\n\t"
      "v_mfma_f32_16x16x32_bf16 v[248:251], a[124:127], a[92:95], v[248:251]\n\t"
      "v_mfma_f32_16x16x32_bf16 a[216:219], a[124:127], a[96:99], a[216:219]\n\t"
      "ds_read_b64_tr_b16 a[62:63], v5 offset:12288\n\t"
      "v_mfma_f32_16x16x32_bf16 a[220:223], a[128:131], a[96:99], a[220:223]\n\t"
      "v_mfma_f32_16x16x32_bf16 v[252:255], a[128:131], a[92:95], v[252:255]\n\t"
      "buffer_load_dwordx4 v13, s[40:43], s53 offen offset:1024 lds\n\t"
      "v_mfma_f32_16x16x32_bf16 v[220:223], a[128:131], a[88:91], v[220:223]\n\t"
      "ds_read_b128 a[64:67], v6 offset:0\n\t"
      "v_mfma_f32_16x16x32_bf16 v[188:191], a[128:131], a[84:87], v[188:191]\n\t"
      "v_mfma_f32_16x16x32_bf16 v[156:159], a[128:131], a[80:83], v[156:159]\n\t"
      "v_mfma_f32_16x16x32_bf16 v[124:127], a[128:131], a[76:79], v[124:127]\n\t"
      "buffer_load_dwordx4 v14, s[40:43], s53 offen offset:2048 lds\n\t"
      "v_mfma_f32_16x16x32_bf16 v[92:95], a[128:131], a[72:75], v[92:95]\n\t"
      "v_mfma_f32_16x16x32_bf16 v[60:63], a[128:131], a[68:71], v[60:63]\n\t"
      "v_mfma_f32_16x16x32_bf16 a[224:227], a[68:71], a[132:135], a[224:227]\n\t"
      "v_mfma_f32_16x16x32_bf16 a[228:231], a[72:75], a[132:135], a[228:231]\n\t"
      "v_mfma_f32_16x16x32_bf16 a[232:235], a[76:79], a[132:135], a[232:235]\n\t"
      "buffer_load_dwordx4 v15, s[40:43], s53 offen offset:3072 lds\n\t"
      "v_mfma_f32_16x16x32_bf16 a[236:239], a[80:83], a[132:135], a[236:239]\n\t"
      "s_add_u32 s53, s53, 229376\n\t"
      "s_sub_u32 s55, s55, 1\n\t"
      "v_mfma_f32_16x16x32_bf16 a[240:243], a[84:87], a[132:135], a[240:243]\n\t"
      "v_mfma_f32_16x16x32_bf16 a[244:247], a[88:91], a[132:135], a[244:247]\n\t"
      "v_mfma_f32_16x16x32_bf16 a[248:251], a[92:95], a[132:135], a[248:251]\n\t"
      "v_mfma_f32_16x16x32_bf16 a[252:255], a[96:99], a[132:135], a[252:255]\n\t"
      "s_cmp_lg_u32 s55, 0\n\t"
      "s_cbranch_scc1 LA_loop_r0_%=\n\t"
      "s_branch LA_tail_r0_%=\n\t"
      "LA_cs_r0_%=:\n\t"
      "s_cmp_eq_u32 s73, 0\n\t"
      "s_cbranch_scc0 LA_cs1_r0_%=\n\t"
      "s_waitcnt vmcnt(19) lgkmcnt(0)\n\t"
      "s_barrier\n\t"
      "v_mfma_f32_16x16x32_bf16 v[32:35], a[32:35], a[0:3], v[32:35]\n\t"
      "ds_read_b128 a[68:71], v1 offset:0\n\t"
      "buffer_load_dword v18, v16, s[56:59], s60 offen\n\t"
      "v_mfma_f32_16x16x32_bf16 v[64:67], a[32:35], a[4:7], v[64:67]\n\t"
      "s_add_u32 s60, s60, s61\n\t"
      "v_mfma_f32_16x16x32_bf16 v[96:99], a[32:35], a[8:11], v[96:99]\n\t"
      "ds_read_b128 a[72:75], v1 offset:2048\n\t"
      "v_mfma_f32_16x16x32_bf16 v[128:131], a[32:35], a[12:15], v[128:131]\n\t"
      "s_add_u32 m0, s49, 0\n\t"
      "v_mfma_f32_16x16x32_bf16 v[160:163], a[32:35], a[16:19], v[160:163]\n\t"
      "ds_read_b128 a[76:79], v1 offset:4096\n\t"
      "buffer_load_dwordx4 v12, s[40:43], s53 offen lds\n\t"
      "v_mfma_f32_16x16x32_bf16 v[192:195], a[32:35], a[20:23], v[192:195]\n\t"
      "v_mfma_f32_16x16x32_bf16 v[224:227], a[32:35], a[24:27], v[224:227]\n\t"
      "v_mfma_f32_16x16x32_bf16 a[192:195], a[32:35], a[28:31], a[192:195]\n\t"
      "ds_read_b128 a[80:83], v1 offset:6144\n\t"
      "v_mfma_f32_16x16x32_bf16 a[136:139], a[32:35], a[172:175], 0\n\t"
      "v_mfma_f32_16x16x32_bf16 a[196:199], a[36:39], a[28:31], a[196:199]\n\t"
      "ds_read_b128 a[84:87], v1 offset:8192\n\t"
      "v_mfma_f32_16x16x32_bf16 v[228:231], a[36:39], a[24:27], v[228:231]\n\t"
      "v_mfma_f32_16x16x32_bf16 v[196:199], a[36:39], a[20:23], v[196:199]\n\t"
      "v_mfma_f32_16x16x32_bf16 v[164:167], a[36:39], a[16:19], v[164:167]\n\t"
      "ds_read_b128 a[88:91], v1 offset:10240\n\t"
      "v_mfma_f32_16x16x32_bf16 v[132:135], a[36:39], a[12:15], v[132:135]\n\t"
      "v_mfma_f32_16x16x32_bf16 v[100:103], a[36:39], a[8:11], v[100:103]\n\t"
      "ds_read_b128 a[92:95], v1 offset:12288\n\t"
      "v_mfma_f32_16x16x32_bf16 v[68:71], a[36:39], a[4:7], v[68:71]\n\t"
      "v_mfma_f32_16x16x32_bf16 v[36:39], a[36:39], a[0:3], v[36:39]\n\t"
      "ds_read_b128 a[96:99], v1 offset:14336\n\t"
      "buffer_load_dwordx4 v13, s[40:43], s53 offen offset:1024 lds\n\t"
      "v_mfma_f32_16x16x32_bf16 a[140:143], a[36:39], a[172:175], 0\n\t"
      "v_mfma_f32_16x16x32_bf16 v[40:43], a[40:43], a[0:3], v[40:43]\n\t"
      "v_mfma_f32_16x16x32_bf16 v[72:75], a[40:43], a[4:7], v[72:75]\n\t"
      "ds_read_b64_tr_b16 a[100:101], v2 offset:16384\n\t"
      "v_mfma_f32_16x16x32_bf16 v[104:107], a[40:43], a[8:11], v[104:107]\n\t"
      "v_mfma_f32_16x16x32_bf16 v[136:139], a[40:43], a[12:15], v[136:139]\n\t"
      "ds_read_b64_tr_b16 a[102:103], v2 offset:20480\n\t"
      "v_mfma_f32_16x16x32_bf16 v[168:171], a[40:43], a[16:19], v[168:171]\n\t"
      "v_mfma_f32_16x16x32_bf16 v[200:203], a[40:43], a[20:23], v[200:203]\n\t"
      "v_mfma_f32_16x16x32_bf16 v[232:235], a[40:43], a[24:27], v[232:235]\n\t"
      "ds_read_b64_tr_b16 a[104:105], v3 offset:16384\n\t"
      "v_mfma_f32_16x16x32_bf16 a[200:203], a[40:43], a[28:31], a[200:203]\n\t"
      "v_mfma_f32_16x16x32_bf16 a[144:147], a[40:43], a[172:175], 0\n\t"
      "ds_read_b64_tr_b16 a[106:107], v3 offset:20480\n\t"
      "v_mfma_f32_16x16x32_bf16 a[204:207], a[44:47], a[28:31], a[204:207]\n\t"
      "v_mfma_f32_16x16x32_bf16 v[236:239], a[44:47], a[24:27], v[236:239]\n\t"
      "buffer_load_dwordx4 v14, s[40:43], s53 offen offset:2048 lds\n\t"
      "v_mfma_f32_16x16x32_bf16 v[204:207], a[44:47], a[20:23], v[204:207]\n\t"
      "ds_read_b64_tr_b16 a[108:109], v4 offset:16384\n\t"
      "v_mfma_f32_16x16x32_bf16 v[172:175], a[44:47], a[16:19], v[172:175]\n\t"
      "v_mfma_f32_16x16x32_bf16 v[140:143], a[44:47], a[12:15], v[140:143]\n\t"
      "ds_read_b64_tr_b16 a[110:111], v4 offset:20480\n\t"
      "v_mfma_f32_16x16x32_bf16 v[108:111], a[44:47], a[8:11], v[108:111]\n\t"
      "v_mfma_f32_16x16x32_bf16 v[76:79], a[44:47], a[4:7], v[76:79]\n\t"
      "ds_read_b64_tr_b16 a[112:113], v5 offset:16384\n\t"
      "v_mfma_f32_16x16x32_bf16 v[44:47], a[44:47], a[0:3], v[44:47]\n\t"
      "v_mfma_f32_16x16x32_bf16 a[148:151], a[44:47], a[172:175], 0\n\t"
      "v_mfma_f32_16x16x32_bf16 v[48:51], a[48:51], a[0:3], v[48:51]\n\t"
      "ds_read_b64_tr_b16 a[114:115], v5 offset:20480\n\t"
      "v_mfma_f32_16x16x32_bf16 v[80:83], a[48:51], a[4:7], v[80:83]\n\t"
      "v_mfma_f32_16x16x32_bf16 v[112:115], a[48:51], a[8:11], v[112:115]\n\t"
      "ds_read_b64_tr_b16 a[116:117], v2 offset:24576\n\t"
      "v_mfma_f32_16x16x32_bf16 v[144:147], a[48:51], a[12:15], v[144:147]\n\t"
      "v_mfma_f32_16x16x32_bf16 v[176:179], a[48:51], a[16:19], v[176:179]\n\t"
      "buffer_load_dwordx4 v15, s[40:43], s53 offen offset:3072 lds\n\t"
      "v_mfma_f32_16x16x32_bf16 v[208:211], a[48:51], a[20:23], v[208:211]\n\t"
      "ds_read_b64_tr_b16 a[118:119], v2 offset:28672\n\t"
      "s_add_u32 s53, s53, 229376\n\t"
      "v_mfma_f32_16x16x32_bf16 v[240:243], a[48:51], a[24:27], v[240:243]\n\t"
      "v_mfma_f32_16x16x32_bf16 a[208:211], a[48:51], a[28:31], a[208:211]\n\t"
      "ds_read_b64_tr_b16 a[120:121], v3 offset:24576\n\t"
      "v_mfma_f32_16x16x32_bf16 a[152:155], a[48:51], a[172:175], 0\n\t"
      "v_mfma_f32_16x16x32_bf16 a[212:215], a[52:55], a[28:31], a[212:215]\n\t"
      "ds_read_b64_tr_b16 a[122:123], v3 offset:28672\n\t"
      "v_mfma_f32_16x16x32_bf16 v[244:247], a[52:55], a[24:27], v[244:247]\n\t"
      "v_mfma_f32_16x16x32_bf16 v[212:215], a[52:55], a[20:23], v[212:215]\n\t"
      "v_mfma_f32_16x16x32_bf16 v[180:183], a[52:55], a[16:19], v[180:183]\n\t"
      "ds_read_b64_tr_b16 a[124:125], v4 offset:24576\n\t"
      "v_mfma_f32_16x16x32_bf16 v[148:151], a[52:55], a[12:15], v[148:151]\n\t"
      "v_mfma_f32_16x16x32_bf16 v[116:119], a[52:55], a[8:11], v[116:119]\n\t"
      "ds_read_b64_tr_b16 a[126:127], v4 offset:28672\n\t"
      "v_mfma_f32_16x16x32_bf16 v[84:87], a[52:55], a[4:7], v[84:87]\n\t"
      "v_mfma_f32_16x16x32_bf16 v[52:55], a[52:55], a[0:3], v[52:55]\n\t"
      "v_mfma_f32_16x16x32_bf16 v[56:59], a[56:59], a[0:3], v[56:59]\n\t"
      "ds_read_b64_tr_b16 a[128:129], v5 offset:24576\n\t"
      "v_mfma_f32_16x16x32_bf16 v[88:91], a[56:59], a[4:7], v[88:91]\n\t"
      "v_mfma_f32_16x16x32_bf16 v[120:123], a[56:59], a[8:11], v[120:123]\n\t"
      "ds_read_b64_tr_b16 a[130:131], v5 offset:28672\n\t"
      "v_mfma_f32_16x16x32_bf16 v[152:155], a[56:59], a[12:15], v[152:155]\n\t"
      "v_mfma_f32_16x16x32_bf16 v[184:187], a[56:59], a[16:19], v[184:187]\n\t"
      "v_mfma_f32_16x16x32_bf16 v[216:219], a[56:59], a[20:23], v[216:219]\n\t"
      "ds_read_b128 a[132:135], v7 offset:0\n\t"
      "v_mfma_f32_16x16x32_bf16 v[248:251], a[56:59], a[24:27], v[248:251]\n\t"
      "v_mfma_f32_16x16x32_bf16 a[216:219], a[56:59], a[28:31], a[216:219]\n\t"
      "v_mfma_f32_16x16x32_bf16 a[220:223], a[60:63], a[28:31], a[220:223]\n\t"
      "v_mfma_f32_16x16x32_bf16 v[252:255], a[60:63], a[24:27], v[252:255]\n\t"
      "v_mfma_f32_16x16x32_bf16 v[220:223], a[60:63], a[20:23], v[220:223]\n\t"
      "v_mfma_f32_16x16x32_bf16 v[188:191], a[60:63], a[16:19], v[188:191]\n\t"
      "v_mfma_f32_16x16x32_bf16 v[156:159], a[60:63], a[12:15], v[156:159]\n\t"
      "v_mfma_f32_16x16x32_bf16 v[124:127], a[60:63], a[8:11], v[124:127]\n\t"
      "v_mfma_f32_16x16x32_bf16 v[92:95], a[60:63], a[4:7], v[92:95]\n\t"
      "v_mfma_f32_16x16x32_bf16 v[60:63], a[60:63], a[0:3], v[60:63]\n\t"
      "v_mfma_f32_16x16x32_bf16 a[224:227], a[0:3], a[64:67], a[224:227]\n\t"
      "v_mfma_f32_16x16x32_bf16 a[228:231], a[4:7], a[64:67], a[228:231]\n\t"
      "v_mfma_f32_16x16x32_bf16 a[232:235], a[8:11], a[64:67], a[232:235]\n\t"
      "v_mfma_f32_16x16x32_bf16 a[236:239], a[12:15], a[64:67], a[236:239]\n\t"
      "v_mfma_f32_16x16x32_bf16 a[240:243], a[16:19], a[64:67], a[240:243]\n\t"
      "v_mfma_f32_16x16x32_bf16 a[244:247], a[20:23], a[64:67], a[244:247]\n\t"
      "v_mfma_f32_16x16x32_bf16 a[248:251], a[24:27], a[64:67], a[248:251]\n\t"
      "v_mfma_f32_16x16x32_bf16 a[252:255], a[28:31], a[64:67], a[252:255]\n\t"
      "s_waitcnt vmcnt(9) lgkmcnt(0)\n\t"
      "s_barrier\n\t"
      "v_mfma_f32_16x16x32_bf16 v[32:35], a[100:103], a[68:71], v[32:35]\n\t"
      "ds_read_b128 a[0:3], v0 offset:32768\n\t"
      "buffer_load_dword v18, v16, s[56:59], s60 offen\n\t"
      "v_mfma_f32_16x16x32_bf16 v[64:67], a[100:103], a[72:75], v[64:67]\n\t"
      "s_add_u32 s60, s60, s61\n\t"
      "v_mfma_f32_16x16x32_bf16 v[96:99], a[100:103], a[76:79], v[96:99]\n\t"
      "ds_read_b128 a[4:7], v0 offset:34816\n\t"
      "s_add_u32 s62, s52, 65536\n\t"
      "s_add_u32 m0, s48, 0\n\t"
      "v_mfma_f32_16x16x32_bf16 v[128:131], a[100:103], a[80:83], v[128:131]\n\t"
      "buffer_load_dwordx4 v8, s[36:39], s52 offen lds\n\t"
      "v_mfma_f32_16x16x32_bf16 v[160:163], a[100:103], a[84:87], v[160:163]\n\t"
      "ds_read_b128 a[8:11], v0 offset:36864\n\t"
      "v_mfma_f32_16x16x32_bf16 v[192:195], a[100:103], a[88:91], v[192:195]\n\t"
      "v_mfma_f32_16x16x32_bf16 v[224:227], a[100:103], a[92:95], v[224:227]\n\t"
      "v_mfma_f32_16x16x32_bf16 a[192:195], a[100:103], a[96:99], a[192:195]\n\t"
      "ds_read_b128 a[12:15], v0 offset:38912\n\t"
      "v_mfma_f32_16x16x32_bf16 a[136:139], a[100:103], a[172:175], a[136:139]\n\t"
      "buffer_load_dwordx4 v9, s[36:39], s52 offen offset:1024 lds\n\t"
      "v_mfma_f32_16x16x32_bf16 a[196:199], a[104:107], a[96:99], a[196:199]\n\t"
      "ds_read_b128 a[16:19], v0 offset:40960\n\t"
      "v_mfma_f32_16x16x32_bf16 v[228:231], a[104:107], a[92:95], v[228:231]\n\t"
      "v_mfma_f32_16x16x32_bf16 v[196:199], a[104:107], a[88:91], v[196:199]\n\t"
      "v_mfma_f32_16x16x32_bf16 v[164:167], a[104:107], a[84:87], v[164:167]\n\t"
      "ds_read_b128 a[20:23], v0 offset:43008\n\t"
      "v_mfma_f32_16x16x32_bf16 v[132:135], a[104:107], a[80:83], v[132:135]\n\t"
      "v_mfma_f32_16x16x32_bf16 v[100:103], a[104:107], a[76:79], v[100:103]\n\t"
      "ds_read_b128 a[24:27], v0 offset:45056\n\t"
      "buffer_load_dwordx4 v10, s[36:39], s52 offen offset:2048 lds\n\t"
      "v_mfma_f32_16x16x32_bf16 v[68:71], a[104:107], a[72:75], v[68:71]\n\t"
      "v_mfma_f32_16x16x32_bf16 v[36:39], a[104:107], a[68:71], v[36:39]\n\t"
      "ds_read_b128 a[28:31], v0 offset:47104\n\t"
      "v_mfma_f32_16x16x32_bf16 a[140:143], a[104:107], a[172:175], a[140:143]\n\t"
      "v_mfma_f32_16x16x32_bf16 v[40:43], a[108:111], a[68:71], v[40:43]\n\t"
      "v_mfma_f32_16x16x32_bf16 v[72:75], a[108:111], a[72:75], v[72:75]\n\t"
      "ds_read_b64_tr_b16 a[32:33], v2 offset:32768\n\t"
      "buffer_load_dwordx4 v11, s[36:39], s52 offen offset:3072 lds\n\t"
      "v_mfma_f32_16x16x32_bf16 v[104:107], a[108:111], a[76:79], v[104:107]\n\t"
      "v_mfma_f32_16x16x32_bf16 v[136:139], a[108:111], a[80:83], v[136:139]\n\t"
      "ds_read_b64_tr_b16 a[34:35], v2 offset:36864\n\t"
      "v_mfma_f32_16x16x32_bf16 v[168:171], a[108:111], a[84:87], v[168:171]\n\t"
      "v_mfma_f32_16x16x32_bf16 v[200:203], a[108:111], a[88:91], v[200:203]\n\t"
      "v_mfma_f32_16x16x32_bf16 v[232:235], a[108:111], a[92:95], v[232:235]\n\t"
      "ds_read_b64_tr_b16 a[36:37], v3 offset:32768\n\t"
      "s_add_u32 m0, s48, 4096\n\t"
      "v_mfma_f32_16x16x32_bf16 a[200:203], a[108:111], a[96:99], a[200:203]\n\t"
      "buffer_load_dwordx4 v8, s[36:39], s62 offen lds\n\t"
      "v_mfma_f32_16x16x32_bf16 a[144:147], a[108:111], a[172:175], a[144:147]\n\t"
      "ds_read_b64_tr_b16 a[38:39], v3 offset:36864\n\t"
      "v_mfma_f32_16x16x32_bf16 a[204:207], a[112:115], a[96:99], a[204:207]\n\t"
      "v_mfma_f32_16x16x32_bf16 v[236:239], a[112:115], a[92:95], v[236:239]\n\t"
      "v_mfma_f32_16x16x32_bf16 v[204:207], a[112:115], a[88:91], v[204:207]\n\t"
      "ds_read_b64_tr_b16 a[40:41], v4 offset:32768\n\t"
      "v_mfma_f32_16x16x32_bf16 v[172:175], a[112:115], a[84:87], v[172:175]\n\t"
      "v_mfma_f32_16x16x32_bf16 v[140:143], a[112:115], a[80:83], v[140:143]\n\t"
      "ds_read_b64_tr_b16 a[42:43], v4 offset:36864\n\t"
      "buffer_load_dwordx4 v9, s[36:39], s62 offen offset:1024 lds\n\t"
      "v_mfma_f32_16x16x32_bf16 v[108:111], a[112:115], a[76:79], v[108:111]\n\t"
      "v_mfma_f32_16x16x32_bf16 v[76:79], a[112:115], a[72:75], v[76:79]\n\t"
      "ds_read_b64_tr_b16 a[44:45], v5 offset:32768\n\t"
      "v_mfma_f32_16x16x32_bf16 v[44:47], a[112:115], a[68:71], v[44:47]\n\t"
      "v_mfma_f32_16x16x32_bf16 a[148:151], a[112:115], a[172:175], a[148:151]\n\t"
      "v_mfma_f32_16x16x32_bf16 v[48:51], a[116:119], a[68:71], v[48:51]\n\t"
      "ds_read_b64_tr_b16 a[46:47], v5 offset:36864\n\t"
      "buffer_load_dwordx4 v10, s[36:39], s62 offen offset:2048 lds\n\t"
      "v_mfma_f32_16x16x32_bf16 v[80:83], a[116:119], a[72:75], v[80:83]\n\t"
      "v_mfma_f32_16x16x32_bf16 v[112:115], a[116:119], a[76:79], v[112:115]\n\t"
      "ds_read_b64_tr_b16 a[48:49], v2 offset:40960\n\t"
      "v_mfma_f32_16x16x32_bf16 v[144:147], a[116:119], a[80:83], v[144:147]\n\t"
      "v_mfma_f32_16x16x32_bf16 v[176:179], a[116:119], a[84:87], v[176:179]\n\t"
      "v_mfma_f32_16x16x32_bf16 v[208:211], a[116:119], a[88:91], v[208:211]\n\t"
      "ds_read_b64_tr_b16 a[50:51], v2 offset:45056\n\t"
      "v_mfma_f32_16x16x32_bf16 v[240:243], a[116:119], a[92:95], v[240:243]\n\t"
      "buffer_load_dwordx4 v11, s[36:39], s62 offen offset:3072 lds\n\t"
      "v_mfma_f32_16x16x32_bf16 a[208:211], a[116:119], a[96:99], a[208:211]\n\t"
      "ds_read_b64_tr_b16 a[52:53], v3 offset:40960\n\t"
      "s_add_u32 s52, s52, 128\n\t"
      "v_mfma_f32_16x16x32_bf16 a[152:155], a[116:119], a[172:175], a[152:155]\n\t"
      "v_mfma_f32_16x16x32_bf16 a[212:215], a[120:123], a[96:99], a[212:215]\n\t"
      "ds_read_b64_tr_b16 a[54:55], v3 offset:45056\n\t"
      "v_mfma_f32_16x16x32_bf16 v[244:247], a[120:123], a[92:95], v[244:247]\n\t"
      "v_mfma_f32_16x16x32_bf16 v[212:215], a[120:123], a[88:91], v[212:215]\n\t"
      "s_add_u32 m0, s50, 0\n\t"
      "v_mfma_f32_16x16x32_bf16 v[180:183], a[120:123], a[84:87], v[180:183]\n\t"
      "ds_read_b64_tr_b16 a[56:57], v4 offset:40960\n\t"
      "buffer_load_dwordx4 v19, s[44:47], s54 offen lds\n\t"
      "v_mfma_f32_16x16x32_bf16 v[148:151], a[120:123], a[80:83], v[148:151]\n\t"
      "s_add_u32 s54, s54, 128\n\t"
      "v_mfma_f32_16x16x32_bf16 v[116:119], a[120:123], a[76:79], v[116:119]\n\t"
      "ds_read_b64_tr_b16 a[58:59], v4 offset:45056\n\t"
      "v_mfma_f32_16x16x32_bf16 v[84:87], a[120:123], a[72:75], v[84:87]\n\t"
      "v_mfma_f32_16x16x32_bf16 v[52:55], a[120:123], a[68:71], v[52:55]\n\t"
      "s_add_u32 m0, s49, 16384\n\t"
      "v_mfma_f32_16x16x32_bf16 v[56:59], a[124:127], a[68:71], v[56:59]\n\t"
      "ds_read_b64_tr_b16 a[60:61], v5 offset:40960\n\t"
      "buffer_load_dwordx4 v12, s[40:43], s53 offen lds\n\t"
      "v_mfma_f32_16x16x32_bf16 v[88:91], a[124:127], a[72:75], v[88:91]\n\t"
      "v_mfma_f32_16x16x32_bf16 v[120:123], a[124:127], a[76:79], v[120:123]\n\t"
      "ds_read_b64_tr_b16 a[62:63], v5 offset:45056\n\t"
      "v_mfma_f32_16x16x32_bf16 v[152:155], a[124:127], a[80:83], v[152:155]\n\t"
      "v_mfma_f32_16x16x32_bf16 v[184:187], a[124:127], a[84:87], v[184:187]\n\t"
      "buffer_load_dwordx4 v13, s[40:43], s53 offen offset:1024 lds\n\t"
      "v_mfma_f32_16x16x32_bf16 v[216:219], a[124:127], a[88:91], v[216:219]\n\t"
      "ds_read_b128 a[64:67], v6 offset:4096\n\t"
      "v_mfma_f32_16x16x32_bf16 v[248:251], a[124:127], a[92:95], v[248:251]\n\t"
      "v_mfma_f32_16x16x32_bf16 a[216:219], a[124:127], a[96:99], a[216:219]\n\t"
      "v_mfma_f32_16x16x32_bf16 a[220:223], a[128:131], a[96:99], a[220:223]\n\t"
      "buffer_load_dwordx4 v14, s[40:43], s53 offen offset:2048 lds\n\t"
      "v_mfma_f32_16x16x32_bf16 v[252:255], a[128:131], a[92:95], v[252:255]\n\t"
      "v_mfma_f32_16x16x32_bf16 v[220:223], a[128:131], a[88:91], v[220:223]\n\t"
      "v_mfma_f32_16x16x32_bf16 v[188:191], a[128:131], a[84:87], v[188:191]\n\t"
      "v_mfma_f32_16x16x32_bf16 v[156:159], a[128:131], a[80:83], v[156:159]\n\t"
      "v_mfma_f32_16x16x32_bf16 v[124:127], a[128:131], a[76:79], v[124:127]\n\t"
      "buffer_load_dwordx4 v15, s[40:43], s53 offen offset:3072 lds\n\t"
      "v_mfma_f32_16x16x32_bf16 v[92:95], a[128:131], a[72:75], v[92:95]\n\t"
      "s_add_u32 s53, s53, 229376\n\t"
      "v_mfma_f32_16x16x32_bf16 v[60:63], a[128:131], a[68:71], v[60:63]\n\t"
      "v_mfma_f32_16x16x32_bf16 a[224:227], a[68:71], a[132:135], a[224:227]\n\t"
      "v_mfma_f32_16x16x32_bf16 a[228:231], a[72:75], a[132:135], a[228:231]\n\t"
      "v_mfma_f32_16x16x32_bf16 a[232:235], a[76:79], a[132:135], a[232:235]\n\t"
      "v_mfma_f32_16x16x32_bf16 a[236:239], a[80:83], a[132:135], a[236:239]\n\t"
      "v_mfma_f32_16x16x32_bf16 a[240:243], a[84:87], a[132:135], a[240:243]\n\t"
      "v_mfma_f32_16x16x32_bf16 a[244:247], a[88:91], a[132:135], a[244:247]\n\t"
      "v_mfma_f32_16x16x32_bf16 a[248:251], a[92:95], a[132:135], a[248:251]\n\t"
      "v_mfma_f32_16x16x32_bf16 a[252:255], a[96:99], a[132:135], a[252:255]\n\t"
      "s_waitcnt vmcnt(19) lgkmcnt(0)\n\t"
      "s_barrier\n\t"
      "v_mfma_f32_16x16x32_bf16 v[32:35], a[32:35], a[0:3], v[32:35]\n\t"
      "ds_read_b128 a[68:71], v1 offset:32768\n\t"
      "buffer_load_dword v18, v16, s[56:59], s60 offen\n\t"
      "v_mfma_f32_16x16x32_bf16 v[64:67], a[32:35], a[4:7], v[64:67]\n\t"
      "s_add_u32 s60, s60, s61\n\t"
      "v_mfma_f32_16x16x32_bf16 v[96:99], a[32:35], a[8:11], v[96:99]\n\t"
      "ds_read_b128 a[72:75], v1 offset:34816\n\t"
      "v_mfma_f32_16x16x32_bf16 v[128:131], a[32:35], a[12:15], v[128:131]\n\t"
      "s_add_u32 m0, s49, 32768\n\t"
      "v_mfma_f32_16x16x32_bf16 v[160:163], a[32:35], a[16:19], v[160:163]\n\t"
      "ds_read_b128 a[76:79], v1 offset:36864\n\t"
      "buffer_load_dwordx4 v12, s[40:43], s53 offen lds\n\t"
      "v_mfma_f32_16x16x32_bf16 v[192:195], a[32:35], a[20:23], v[192:195]\n\t"
      "v_mfma_f32_16x16x32_bf16 v[224:227], a[32:35], a[24:27], v[224:227]\n\t"
      "v_mfma_f32_16x16x32_bf16 a[192:195], a[32:35], a[28:31], a[192:195]\n\t"
      "ds_read_b128 a[80:83], v1 offset:38912\n\t"
      "v_mfma_f32_16x16x32_bf16 a[136:139], a[32:35], a[172:175], a[136:139]\n\t"
      "v_mfma_f32_16x16x32_bf16 a[196:199], a[36:39], a[28:31], a[196:199]\n\t"
      "ds_read_b128 a[84:87], v1 offset:40960\n\t"
      "v_mfma_f32_16x16x32_bf16 v[228:231], a[36:39], a[24:27], v[228:231]\n\t"
      "v_mfma_f32_16x16x32_bf16 v[196:199], a[36:39], a[20:23], v[196:199]\n\t"
      "v_mfma_f32_16x16x32_bf16 v[164:167], a[36:39], a[16:19], v[164:167]\n\t"
      "ds_read_b128 a[88:91], v1 offset:43008\n\t"
      "v_mfma_f32_16x16x32_bf16 v[132:135], a[36:39], a[12:15], v[132:135]\n\t"
      "v_mfma_f32_16x16x32_bf16 v[100:103], a[36:39], a[8:11], v[100:103]\n\t"
      "ds_read_b128 a[92:95], v1 offset:45056\n\t"
      "v_mfma_f32_16x16x32_bf16 v[68:71], a[36:39], a[4:7], v[68:71]\n\t"
      "v_mfma_f32_16x16x32_bf16 v[36:39], a[36:39], a[0:3], v[36:39]\n\t"
      "ds_read_b128 a[96:99], v1 offset:47104\n\t"
      "buffer_load_dwordx4 v13, s[40:43], s53 offen offset:1024 lds\n\t"
      "v_mfma_f32_16x16x32_bf16 a[140:143], a[36:39], a[172:175], a[140:143]\n\t"
      "v_mfma_f32_16x16x32_bf16 v[40:43], a[40:43], a[0:3], v[40:43]\n\t"
      "v_mfma_f32_16x16x32_bf16 v[72:75], a[40:43], a[4:7], v[72:75]\n\t"
      "ds_read_b64_tr_b16 a[100:101], v2 offset:49152\n\t"
      "v_mfma_f32_16x16x32_bf16 v[104:107], a[40:43], a[8:11], v[104:107]\n\t"
      "v_mfma_f32_16x16x32_bf16 v[136:139], a[40:43], a[12:15], v[136:139]\n\t"
      "ds_read_b64_tr_b16 a[102:103], v2 offset:53248\n\t"
      "v_mfma_f32_16x16x32_bf16 v[168:171], a[40:43], a[16:19], v[168:171]\n\t"
      "v_mfma_f32_16x16x32_bf16 v[200:203], a[40:43], a[20:23], v[200:203]\n\t"
      "v_mfma_f32_16x16x32_bf16 v[232:235], a[40:43], a[24:27], v[232:235]\n\t"
      "ds_read_b64_tr_b16 a[104:105], v3 offset:49152\n\t"
      "v_mfma_f32_16x16x32_bf16 a[200:203], a[40:43], a[28:31], a[200:203]\n\t"
      "v_mfma_f32_16x16x32_bf16 a[144:147], a[40:43], a[172:175], a[144:147]\n\t"
      "ds_read_b64_tr_b16 a[106:107], v3 offset:53248\n\t"
      "v_mfma_f32_16x16x32_bf16 a[204:207], a[44:47], a[28:31], a[204:207]\n\t"
      "v_mfma_f32_16x16x32_bf16 v[236:239], a[44:47], a[24:27], v[236:239]\n\t"
      "buffer_load_dwordx4 v14, s[40:43], s53 offen offset:2048 lds\n\t"
      "v_mfma_f32_16x16x32_bf16 v[204:207], a[44:47], a[20:23], v[204:207]\n\t"
      "ds_read_b64_tr_b16 a[108:109], v4 offset:49152\n\t"
      "v_mfma_f32_16x16x32_bf16 v[172:175], a[44:47], a[16:19], v[172:175]\n\t"
      "v_mfma_f32_16x16x32_bf16 v[140:143], a[44:47], a[12:15], v[140:143]\n\t"
      "ds_read_b64_tr_b16 a[110:111], v4 offset:53248\n\t"
      "v_mfma_f32_16x16x32_bf16 v[108:111], a[44:47], a[8:11], v[108:111]\n\t"
      "v_mfma_f32_16x16x32_bf16 v[76:79], a[44:47], a[4:7], v[76:79]\n\t"
      "ds_read_b64_tr_b16 a[112:113], v5 offset:49152\n\t"
      "v_mfma_f32_16x16x32_bf16 v[44:47], a[44:47], a[0:3], v[44:47]\n\t"
      "v_mfma_f32_16x16x32_bf16 a[148:151], a[44:47], a[172:175], a[148:151]\n\t"
      "v_mfma_f32_16x16x32_bf16 v[48:51], a[48:51], a[0:3], v[48:51]\n\t"
      "ds_read_b64_tr_b16 a[114:115], v5 offset:53248\n\t"
      "v_mfma_f32_16x16x32_bf16 v[80:83], a[48:51], a[4:7], v[80:83]\n\t"
      "v_mfma_f32_16x16x32_bf16 v[112:115], a[48:51], a[8:11], v[112:115]\n\t"
      "ds_read_b64_tr_b16 a[116:117], v2 offset:57344\n\t"
      "v_mfma_f32_16x16x32_bf16 v[144:147], a[48:51], a[12:15], v[144:147]\n\t"
      "v_mfma_f32_16x16x32_bf16 v[176:179], a[48:51], a[16:19], v[176:179]\n\t"
      "buffer_load_dwordx4 v15, s[40:43], s53 offen offset:3072 lds\n\t"
      "v_mfma_f32_16x16x32_bf16 v[208:211], a[48:51], a[20:23], v[208:211]\n\t"
      "ds_read_b64_tr_b16 a[118:119], v2 offset:61440\n\t"
      "s_add_u32 s53, s53, 229376\n\t"
      "v_mfma_f32_16x16x32_bf16 v[240:243], a[48:51], a[24:27], v[240:243]\n\t"
      "v_mfma_f32_16x16x32_bf16 a[208:211], a[48:51], a[28:31], a[208:211]\n\t"
      "ds_read_b64_tr_b16 a[120:121], v3 offset:57344\n\t"
      "v_mfma_f32_16x16x32_bf16 a[152:155], a[48:51], a[172:175], a[152:155]\n\t"
      "v_mfma_f32_16x16x32_bf16 a[212:215], a[52:55], a[28:31], a[212:215]\n\t"
      "ds_read_b64_tr_b16 a[122:123], v3 offset:61440\n\t"
      "v_mfma_f32_16x16x32_bf16 v[244:247], a[52:55], a[24:27], v[244:247]\n\t"
      "v_mfma_f32_16x16x32_bf16 v[212:215], a[52:55], a[20:23], v[212:215]\n\t"
      "v_mfma_f32_16x16x32_bf16 v[180:183], a[52:55], a[16:19], v[180:183]\n\t"
      "ds_read_b64_tr_b16 a[124:125], v4 offset:57344\n\t"
      "v_mfma_f32_16x16x32_bf16 v[148:151], a[52:55], a[12:15], v[148:151]\n\t"
      "v_mfma_f32_16x16x32_bf16 v[116:119], a[52:55], a[8:11], v[116:119]\n\t"
      "ds_read_b64_tr_b16 a[126:127], v4 offset:61440\n\t"
      "v_mfma_f32_16x16x32_bf16 v[84:87], a[52:55], a[4:7], v[84:87]\n\t"
      "v_mfma_f32_16x16x32_bf16 v[52:55], a[52:55], a[0:3], v[52:55]\n\t"
      "v_mfma_f32_16x16x32_bf16 v[56:59], a[56:59], a[0:3], v[56:59]\n\t"
      "ds_read_b64_tr_b16 a[128:129], v5 offset:57344\n\t"
      "v_mfma_f32_16x16x32_bf16 v[88:91], a[56:59], a[4:7], v[88:91]\n\t"
      "v_mfma_f32_16x16x32_bf16 v[120:123], a[56:59], a[8:11], v[120:123]\n\t"
      "ds_read_b64_tr_b16 a[130:131], v5 offset:61440\n\t"
      "v_mfma_f32_16x16x32_bf16 v[152:155], a[56:59], a[12:15], v[152:155]\n\t"
      "v_mfma_f32_16x16x32_bf16 v[184:187], a[56:59], a[16:19], v[184:187]\n\t"
      "v_mfma_f32_16x16x32_bf16 v[216:219], a[56:59], a[20:23], v[216:219]\n\t"
      "ds_read_b128 a[132:135], v7 offset:4096\n\t"
      "v_mfma_f32_16x16x32_bf16 v[248:251], a[56:59], a[24:27], v[248:251]\n\t"
      "v_mfma_f32_16x16x32_bf16 a[216:219], a[56:59], a[28:31], a[216:219]\n\t"
      "v_mfma_f32_16x16x32_bf16 a[220:223], a[60:63], a[28:31], a[220:223]\n\t"
      "v_mfma_f32_16x16x32_bf16 v[252:255], a[60:63], a[24:27], v[252:255]\n\t"
      "v_mfma_f32_16x16x32_bf16 v[220:223], a[60:63], a[20:23], v[220:223]\n\t"
      "v_mfma_f32_16x16x32_bf16 v[188:191], a[60:63], a[16:19], v[188:191]\n\t"
      "v_mfma_f32_16x16x32_bf16 v[156:159], a[60:63], a[12:15], v[156:159]\n\t"
      "v_mfma_f32_16x16x32_bf16 v[124:127], a[60:63], a[8:11], v[124:127]\n\t"
      "v_mfma_f32_16x16x32_bf16 v[92:95], a[60:63], a[4:7], v[92:95]\n\t"
      "v_mfma_f32_16x16x32_bf16 v[60:63], a[60:63], a[0:3], v[60:63]\n\t"
      "v_mfma_f32_16x16x32_bf16 a[224:227], a[0:3], a[64:67], a[224:227]\n\t"
      "v_mfma_f32_16x16x32_bf16 a[228:231], a[4:7], a[64:67], a[228:231]\n\t"
      "v_mfma_f32_16x16x32_bf16 a[232:235], a[8:11], a[64:67], a[232:235]\n\t"
      "v_mfma_f32_16x16x32_bf16 a[236:239], a[12:15], a[64:67], a[236:239]\n\t"
      "v_mfma_f32_16x16x32_bf16 a[240:243], a[16:19], a[64:67], a[240:243]\n\t"
      "v_mfma_f32_16x16x32_bf16 a[244:247], a[20:23], a[64:67], a[244:247]\n\t"
      "v_mfma_f32_16x16x32_bf16 a[248:251], a[24:27], a[64:67], a[248:251]\n\t"
      "v_mfma_f32_16x16x32_bf16 a[252:255], a[28:31], a[64:67], a[252:255]\n\t"
      "s_waitcnt vmcnt(9) lgkmcnt(0)\n\t"
      "s_bitcmp1_b32 s81, s55\n\t"
      "s_cbranch_scc0 LA_nofl0_r0_%=\n\t"
      "s_not_b32 s74, s68\n\t"
      "s_not_b32 s75, s69\n\t"
      "s_cmp_eq_u32 s63, 1\n\t"
      "s_cselect_b32 s74, s68, s74\n\t"
      "s_cselect_b32 s75, s69, s75\n\t"
      "v_mov_b32 v22, s74\n\t"
      "v_mov_b32 v23, s75\n\t"
      "s_mov_b64 exec, 1\n\t"
      "buffer_store_dwordx4 v[20:23], v17, s[64:67], 0 offen sc1 ; alt\n\t"
      "s_mov_b64 exec, -1\n\t"
      "s_nop 1\n\t"
      "LA_nofl0_r0_%=:\n\t"
      "s_barrier\n\t"
      "v_mfma_f32_16x16x32_bf16 v[32:35], a[100:103], a[68:71], v[32:35]\n\t"
      "ds_read_b128 a[0:3], v0 offset:0\n\t"
      "buffer_load_dword v18, v16, s[56:59], s60 offen\n\t"
      "v_mfma_f32_16x16x32_bf16 v[64:67], a[100:103], a[72:75], v[64:67]\n\t"
      "s_add_u32 s60, s60, s61\n\t"
      "v_mfma_f32_16x16x32_bf16 v[96:99], a[100:103], a[76:79], v[96:99]\n\t"
      "ds_read_b128 a[4:7], v0 offset:2048\n\t"
      "s_add_u32 s62, s52, 65536\n\t"
      "s_add_u32 m0, s48, 32768\n\t"
      "v_mfma_f32_16x16x32_bf16 v[128:131], a[100:103], a[80:83], v[128:131]\n\t"
      "buffer_load_dwordx4 v8, s[36:39], s52 offen lds\n\t"
      "v_mfma_f32_16x16x32_bf16 v[160:163], a[100:103], a[84:87], v[160:163]\n\t"
      "ds_read_b128 a[8:11], v0 offset:4096\n\t"
      "v_mfma_f32_16x16x32_bf16 v[192:195], a[100:103], a[88:91], v[192:195]\n\t"
      "v_mfma_f32_16x16x32_bf16 v[224:227], a[100:103], a[92:95], v[224:227]\n\t"
      "v_mfma_f32_16x16x32_bf16 a[192:195], a[100:103], a[96:99], a[192:195]\n\t"
      "ds_read_b128 a[12:15], v0 offset:6144\n\t"
      "v_mfma_f32_16x16x32_bf16 a[136:139], a[100:103], a[172:175], a[136:139]\n\t"
      "buffer_load_dwordx4 v9, s[36:39], s52 offen offset:1024 lds\n\t"
      "v_mfma_f32_16x16x32_bf16 a[196:199], a[104:107], a[96:99], a[196:199]\n\t"
      "ds_read_b128 a[16:19], v0 offset:8192\n\t"
      "v_mfma_f32_16x16x32_bf16 v[228:231], a[104:107], a[92:95], v[228:231]\n\t"
      "v_mfma_f32_16x16x32_bf16 v[196:199], a[104:107], a[88:91], v[196:199]\n\t"
      "v_mfma_f32_16x16x32_bf16 v[164:167], a[104:107], a[84:87], v[164:167]\n\t"
      "ds_read_b128 a[20:23], v0 offset:10240\n\t"
      "v_mfma_f32_16x16x32_bf16 v[132:135], a[104:107], a[80:83], v[132:135]\n\t"
      "v_mfma_f32_16x16x32_bf16 v[100:103], a[104:107], a[76:79], v[100:103]\n\t"
      "ds_read_b128 a[24:27], v0 offset:12288\n\t"
      "buffer_load_dwordx4 v10, s[36:39], s52 offen offset:2048 lds\n\t"
      "v_mfma_f32_16x16x32_bf16 v[68:71], a[104:107], a[72:75], v[68:71]\n\t"
      "v_mfma_f32_16x16x32_bf16 v[36:39], a[104:107], a[68:71], v[36:39]\n\t"
      "ds_read_b128 a[28:31], v0 offset:14336\n\t"
      "v_mfma_f32_16x16x32_bf16 a[140:143], a[104:107], a[172:175], a[140:143]\n\t"
      "v_mfma_f32_16x16x32_bf16 v[40:43], a[108:111], a[68:71], v[40:43]\n\t"
      "v_mfma_f32_16x16x32_bf16 v[72:75], a[108:111], a[72:75], v[72:75]\n\t"
      "ds_read_b64_tr_b16 a[32:33], v2 offset:0\n\t"
      "buffer_load_dwordx4 v11, s[36:39], s52 offen offset:3072 lds\n\t"
      "v_mfma_f32_16x16x32_bf16 v[104:107], a[108:111], a[76:79], v[104:107]\n\t"
      "v_mfma_f32_16x16x32_bf16 v[136:139], a[108:111], a[80:83], v[136:139]\n\t"
      "ds_read_b64_tr_b16 a[34:35], v2 offset:4096\n\t"
      "v_mfma_f32_16x16x32_bf16 v[168:171], a[108:111], a[84:87], v[168:171]\n\t"
      "v_mfma_f32_16x16x32_bf16 v[200:203], a[108:111], a[88:91], v[200:203]\n\t"
      "v_mfma_f32_16x16x32_bf16 v[232:235], a[108:111], a[92:95], v[232:235]\n\t"
      "ds_read_b64_tr_b16 a[36:37], v3 offset:0\n\t"
      "s_add_u32 m0, s48, 36864\n\t"
      "v_mfma_f32_16x16x32_bf16 a[200:203], a[108:111], a[96:99], a[200:203]\n\t"
      "buffer_load_dwordx4 v8, s[36:39], s62 offen lds\n\t"
      "v_mfma_f32_16x16x32_bf16 a[144:147], a[108:111], a[172:175], a[144:147]\n\t"
      "ds_read_b64_tr_b16 a[38:39], v3 offset:4096\n\t"
      "v_mfma_f32_16x16x32_bf16 a[204:207], a[112:115], a[96:99], a[204:207]\n\t"
      "v_mfma_f32_16x16x32_bf16 v[236:239], a[112:115], a[92:95], v[236:239]\n\t"
      "v_mfma_f32_16x16x32_bf16 v[204:207], a[112:115], a[88:91], v[204:207]\n\t"
      "ds_read_b64_tr_b16 a[40:41], v4 offset:0\n\t"
      "v_mfma_f32_16x16x32_bf16 v[172:175], a[112:115], a[84:87], v[172:175]\n\t"
      "v_mfma_f32_16x16x32_bf16 v[140:143], a[112:115], a[80:83], v[140:143]\n\t"
      "ds_read_b64_tr_b16 a[42:43], v4 offset:4096\n\t"
      "buffer_load_dwordx4 v9, s[36:39], s62 offen offset:1024 lds\n\t"
      "v_mfma_f32_16x16x32_bf16 v[108:111], a[112:115], a[76:79], v[108:111]\n\t"
      "v_mfma_f32_16x16x32_bf16 v[76:79], a[112:115], a[72:75], v[76:79]\n\t"
      "ds_read_b64_tr_b16 a[44:45], v5 offset:0\n\t"
      "v_mfma_f32_16x16x32_bf16 v[44:47], a[112:115], a[68:71], v[44:47]\n\t"
      "v_mfma_f32_16x16x32_bf16 a[148:151], a[112:115], a[172:175], a[148:151]\n\t"
      "v_mfma_f32_16x16x32_bf16 v[48:51], a[116:119], a[68:71], v[48:51]\n\t"
      "ds_read_b64_tr_b16 a[46:47], v5 offset:4096\n\t"
      "buffer_load_dwordx4 v10, s[36:39], s62 offen offset:2048 lds\n\t"
      "v_mfma_f32_16x16x32_bf16 v[80:83], a[116:119], a[72:75], v[80:83]\n\t"
      "v_mfma_f32_16x16x32_bf16 v[112:115], a[116:119], a[76:79], v[112:115]\n\t"
      "ds_read_b64_tr_b16 a[48:49], v2 offset:8192\n\t"
      "v_mfma_f32_16x16x32_bf16 v[144:147], a[116:119], a[80:83], v[144:147]\n\t"
      "v_mfma_f32_16x16x32_bf16 v[176:179], a[116:119], a[84:87], v[176:179]\n\t"
      "v_mfma_f32_16x16x32_bf16 v[208:211], a[116:119], a[88:91], v[208:211]\n\t"
      "ds_read_b64_tr_b16 a[50:51], v2 offset:12288\n\t"
      "v_mfma_f32_16x16x32_bf16 v[240:243], a[116:119], a[92:95], v[240:243]\n\t"
      "buffer_load_dwordx4 v11, s[36:39], s62 offen offset:3072 lds\n\t"
      "v_mfma_f32_16x16x32_bf16 a[208:211], a[116:119], a[96:99], a[208:211]\n\t"
      "ds_read_b64_tr_b16 a[52:53], v3 offset:8192\n\t"
      "s_add_u32 s52, s52, 128\n\t"
      "v_mfma_f32_16x16x32_bf16 a[152:155], a[116:119], a[172:175], a[152:155]\n\t"
      "v_mfma_f32_16x16x32_bf16 a[212:215], a[120:123], a[96:99], a[212:215]\n\t"
      "ds_read_b64_tr_b16 a[54:55], v3 offset:12288\n\t"
      "v_mfma_f32_16x16x32_bf16 v[244:247], a[120:123], a[92:95], v[244:247]\n\t"
      "v_mfma_f32_16x16x32_bf16 v[212:215], a[120:123], a[88:91], v[212:215]\n\t"
      "s_add_u32 m0, s50, 4096\n\t"
      "v_mfma_f32_16x16x32_bf16 v[180:183], a[120:123], a[84:87], v[180:183]\n\t"
      "ds_read_b64_tr_b16 a[56:57], v4 offset:8192\n\t"
      "buffer_load_dwordx4 v19, s[44:47], s54 offen lds\n\t"
      "v_mfma_f32_16x16x32_bf16 v[148:151], a[120:123], a[80:83], v[148:151]\n\t"
      "s_add_u32 s54, s54, 128\n\t"
      "v_mfma_f32_16x16x32_bf16 v[116:119], a[120:123], a[76:79], v[116:119]\n\t"
      "ds_read_b64_tr_b16 a[58:59], v4 offset:12288\n\t"
      "v_mfma_f32_16x16x32_bf16 v[84:87], a[120:123], a[72:75], v[84:87]\n\t"
      "v_mfma_f32_16x16x32_bf16 v[52:55], a[120:123], a[68:71], v[52:55]\n\t"
      "s_add_u32 m0, s49, 49152\n\t"
      "v_mfma_f32_16x16x32_bf16 v[56:59], a[124:127], a[68:71], v[56:59]\n\t"
      "ds_read_b64_tr_b16 a[60:61], v5 offset:8192\n\t"
      "buffer_load_dwordx4 v12, s[40:43], s53 offen lds\n\t"
      "v_mfma_f32_16x16x32_bf16 v[88:91], a[124:127], a[72:75], v[88:91]\n\t"
      "v_mfma_f32_16x16x32_bf16 v[120:123], a[124:127], a[76:79], v[120:123]\n\t"
      "ds_read_b64_tr_b16 a[62:63], v5 offset:12288\n\t"
      "v_mfma_f32_16x16x32_bf16 v[152:155], a[124:127], a[80:83], v[152:155]\n\t"
      "v_mfma_f32_16x16x32_bf16 v[184:187], a[124:127], a[84:87], v[184:187]\n\t"
      "buffer_load_dwordx4 v13, s[40:43], s53 offen offset:1024 lds\n\t"
      "v_mfma_f32_16x16x32_bf16 v[216:219], a[124:127], a[88:91], v[216:219]\n\t"
      "ds_read_b128 a[64:67], v6 offset:0\n\t"
      "v_mfma_f32_16x16x32_bf16 v[248:251], a[124:127], a[92:95], v[248:251]\n\t"
      "v_mfma_f32_16x16x32_bf16 a[216:219], a[124:127], a[96:99], a[216:219]\n\t"
      "v_mfma_f32_16x16x32_bf16 a[220:223], a[128:131], a[96:99], a[220:223]\n\t"
      "buffer_load_dwordx4 v14, s[40:43], s53 offen offset:2048 lds\n\t"
      "v_mfma_f32_16x16x32_bf16 v[252:255], a[128:131], a[92:95], v[252:255]\n\t"
      "v_mfma_f32_16x16x32_bf16 v[220:223], a[128:131], a[88:91], v[220:223]\n\t"
      "v_mfma_f32_16x16x32_bf16 v[188:191], a[128:131], a[84:87], v[188:191]\n\t"
      "v_mfma_f32_16x16x32_bf16 v[156:159], a[128:131], a[80:83], v[156:159]\n\t"
      "v_mfma_f32_16x16x32_bf16 v[124:127], a[128:131], a[76:79], v[124:127]\n\t"
      "buffer_load_dwordx4 v15, s[40:43], s53 offen offset:3072 lds\n\t"
      "v_mfma_f32_16x16x32_bf16 v[92:95], a[128:131], a[72:75], v[92:95]\n\t"
      "s_add_u32 s53, s53, 229376\n\t"
      "v_mfma_f32_16x16x32_bf16 v[60:63], a[128:131], a[68:71], v[60:63]\n\t"
      "v_mfma_f32_16x16x32_bf16 a[224:227], a[68:71], a[132:135], a[224:227]\n\t"
      "v_mfma_f32_16x16x32_bf16 a[228:231], a[72:75], a[132:135], a[228:231]\n\t"
      "v_mfma_f32_16x16x32_bf16 a[232:235], a[76:79], a[132:135], a[232:235]\n\t"
      "v_mfma_f32_16x16x32_bf16 a[236:239], a[80:83], a[132:135], a[236:239]\n\t"
      "s_sub_u32 s55, s55, 1\n\t"
      "v_mfma_f32_16x16x32_bf16 a[240:243], a[84:87], a[132:135], a[240:243]\n\t"
      "v_mfma_f32_16x16x32_bf16 a[244:247], a[88:91], a[132:135], a[244:247]\n\t"
      "v_mfma_f32_16x16x32_bf16 a[248:251], a[92:95], a[132:135], a[248:251]\n\t"
      "v_mfma_f32_16x16x32_bf16 a[252:255], a[96:99], a[132:135], a[252:255]\n\t"
      "s_cmp_eq_u32 s55, 0\n\t"
      "s_cbranch_scc1 LA_nopub0_r0_%=\n\t"
      "s_nop 7\n\t"
      "s_mov_b32 exec_lo, 0x00010001\n\t"
      "s_mov_b32 exec_hi, 0x00010001\n\t"
      "buffer_store_dwordx4 a[136:139], v25, s[64:67], 0 offen offset:0 sc1\n\t"
      "buffer_store_dwordx4 a[140:143], v25, s[64:67], 0 offen offset:64 sc1\n\t"
      "buffer_store_dwordx4 a[144:147], v25, s[64:67], 0 offen offset:128 sc1\n\t"
      "buffer_store_dwordx4 a[148:151], v25, s[64:67], 0 offen offset:192 sc1\n\t"
      "buffer_store_dwordx4 a[152:155], v25, s[64:67], 0 offen offset:256 sc1\n\t"
      "s_mov_b64 exec, 0xffff\n\t"
      "s_mov_b64 exec, -1\n\t"
      "s_mov_b32 s63, 1\n\t"
      "LA_nopub0_r0_%=:\n\t"
      "s_cmp_lg_u32 s55, 0\n\t"
      "s_cbranch_scc1 LA_loop_r0_%=\n\t"
      "s_branch LA_tail_r0_%=\n\t"
      "LA_cs1_r0_%=:\n\t"
      "s_waitcnt vmcnt(19) lgkmcnt(0)\n\t"
      "s_barrier\n\t"
      "v_mfma_f32_16x16x32_bf16 v[32:35], a[32:35], a[0:3], v[32:35]\n\t"
      "ds_read_b128 a[68:71], v1 offset:0\n\t"
      "buffer_load_dword v18, v16, s[56:59], s60 offen\n\t"
      "v_mfma_f32_16x16x32_bf16 v[64:67], a[32:35], a[4:7], v[64:67]\n\t"
      "s_add_u32 s60, s60, s61\n\t"
      "v_mfma_f32_16x16x32_bf16 v[96:99], a[32:35], a[8:11], v[96:99]\n\t"
      "ds_read_b128 a[72:75], v1 offset:2048\n\t"
      "v_mfma_f32_16x16x32_bf16 v[128:131], a[32:35], a[12:15], v[128:131]\n\t"
      "s_add_u32 m0, s49, 0\n\t"
      "v_mfma_f32_16x16x32_bf16 v[160:163], a[32:35], a[16:19], v[160:163]\n\t"
      "ds_read_b128 a[76:79], v1 offset:4096\n\t"
      "buffer_load_dwordx4 v12, s[40:43], s53 offen lds\n\t"
      "v_mfma_f32_16x16x32_bf16 v[192:195], a[32:35], a[20:23], v[192:195]\n\t"
      "v_mfma_f32_16x16x32_bf16 v[224:227], a[32:35], a[24:27], v[224:227]\n\t"
      "v_mfma_f32_16x16x32_bf16 a[192:195], a[32:35], a[28:31], a[192:195]\n\t"
      "ds_read_b128 a[80:83], v1 offset:6144\n\t"
      "v_mfma_f32_16x16x32_bf16 a[196:199], a[36:39], a[28:31], a[196:199]\n\t"
      "v_mfma_f32_16x16x32_bf16 v[228:231], a[36:39], a[24:27], v[228:231]\n\t"
      "ds_read_b128 a[84:87], v1 offset:8192\n\t"
      "v_mfma_f32_16x16x32_bf16 v[196:199], a[36:39], a[20:23], v[196:199]\n\t"
      "v_mfma_f32_16x16x32_bf16 v[164:167], a[36:39], a[16:19], v[164:167]\n\t"
      "v_mfma_f32_16x16x32_bf16 v[132:135], a[36:39], a[12:15], v[132:135]\n\t"
      "ds_read_b128 a[88:91], v1 offset:10240\n\t"
      "v_mfma_f32_16x16x32_bf16 v[100:103], a[36:39], a[8:11], v[100:103]\n\t"
      "v_mfma_f32_16x16x32_bf16 v[68:71], a[36:39], a[4:7], v[68:71]\n\t"
      "ds_read_b128 a[92:95], v1 offset:12288\n\t"
      "v_mfma_f32_16x16x32_bf16 v[36:39], a[36:39], a[0:3], v[36:39]\n\t"
      "v_mfma_f32_16x16x32_bf16 v[40:43], a[40:43], a[0:3], v[40:43]\n\t"
      "ds_read_b128 a[96:99], v1 offset:14336\n\t"
      "buffer_load_dwordx4 v13, s[40:43], s53 offen offset:1024 lds\n\t"
      "v_mfma_f32_16x16x32_bf16 v[72:75], a[40:43], a[4:7], v[72:75]\n\t"
      "v_mfma_f32_16x16x32_bf16 v[104:107], a[40:43], a[8:11], v[104:107]\n\t"
      "v_mfma_f32_16x16x32_bf16 v[136:139], a[40:43], a[12:15], v[136:139]\n\t"
      "ds_read_b64_tr_b16 a[100:101], v2 offset:16384\n\t"
      "v_mfma_f32_16x16x32_bf16 v[168:171], a[40:43], a[16:19], v[168:171]\n\t"
      "v_mfma_f32_16x16x32_bf16 v[200:203], a[40:43], a[20:23], v[200:203]\n\t"
      "ds_read_b64_tr_b16 a[102:103], v2 offset:20480\n\t"
      "v_mfma_f32_16x16x32_bf16 v[232:235], a[40:43], a[24:27], v[232:235]\n\t"
      "v_mfma_f32_16x16x32_bf16 a[200:203], a[40:43], a[28:31], a[200:203]\n\t"
      "v_mfma_f32_16x16x32_bf16 a[204:207], a[44:47], a[28:31], a[204:207]\n\t"
      "ds_read_b64_tr_b16 a[104:105], v3 offset:16384\n\t"
      "v_mfma_f32_16x16x32_bf16 v[236:239], a[44:47], a[24:27], v[236:239]\n\t"
      "v_mfma_f32_16x16x32_bf16 v[204:207], a[44:47], a[20:23], v[204:207]\n\t"
      "ds_read_b64_tr_b16 a[106:107], v3 offset:20480\n\t"
      "v_mfma_f32_16x16x32_bf16 v[172:175], a[44:47], a[16:19], v[172:175]\n\t"
      "v_mfma_f32_16x16x32_bf16 v[140:143], a[44:47], a[12:15], v[140:143]\n\t"
      "buffer_load_dwordx4 v14, s[40:43], s53 offen offset:2048 lds\n\t"
      "v_mfma_f32_16x16x32_bf16 v[108:111], a[44:47], a[8:11], v[108:111]\n\t"
      "ds_read_b64_tr_b16 a[108:109], v4 offset:16384\n\t"
      "v_mfma_f32_16x16x32_bf16 v[76:79], a[44:47], a[4:7], v[76:79]\n\t"
      "v_mfma_f32_16x16x32_bf16 v[44:47], a[44:47], a[0:3], v[44:47]\n\t"
      "ds_read_b64_tr_b16 a[110:111], v4 offset:20480\n\t"
      "v_mfma_f32_16x16x32_bf16 v[48:51], a[48:51], a[0:3], v[48:51]\n\t"
      "v_mfma_f32_16x16x32_bf16 v[80:83], a[48:51], a[4:7], v[80:83]\n\t"
      "ds_read_b64_tr_b16 a[112:113], v5 offset:16384\n\t"
      "v_mfma_f32_16x16x32_bf16 v[112:115], a[48:51], a[8:11], v[112:115]\n\t"
      "v_mfma_f32_16x16x32_bf16 v[144:147], a[48:51], a[12:15], v[144:147]\n\t"
      "v_mfma_f32_16x16x32_bf16 v[176:179], a[48:51], a[16:19], v[176:179]\n\t"
      "ds_read_b64_tr_b16 a[114:115], v5 offset:20480\n\t"
      "v_mfma_f32_16x16x32_bf16 v[208:211], a[48:51], a[20:23], v[208:211]\n\t"
      "v_mfma_f32_16x16x32_bf16 v[240:243], a[48:51], a[24:27], v[240:243]\n\t"
      "ds_read_b64_tr_b16 a[116:117], v2 offset:24576\n\t"
      "v_mfma_f32_16x16x32_bf16 a[208:211], a[48:51], a[28:31], a[208:211]\n\t"
      "v_mfma_f32_16x16x32_bf16 a[212:215], a[52:55], a[28:31], a[212:215]\n\t"
      "buffer_load_dwordx4 v15, s[40:43], s53 offen offset:3072 lds\n\t"
      "v_mfma_f32_16x16x32_bf16 v[244:247], a[52:55], a[24:27], v[244:247]\n\t"
      "ds_read_b64_tr_b16 a[118:119], v2 offset:28672\n\t"
      "s_add_u32 s53, s53, 229376\n\t"
      "v_mfma_f32_16x16x32_bf16 v[212:215], a[52:55], a[20:23], v[212:215]\n\t"
      "v_mfma_f32_16x16x32_bf16 v[180:183], a[52:55], a[16:19], v[180:183]\n\t"
      "ds_read_b64_tr_b16 a[120:121], v3 offset:24576\n\t"
      "v_mfma_f32_16x16x32_bf16 v[148:151], a[52:55], a[12:15], v[148:151]\n\t"
      "v_mfma_f32_16x16x32_bf16 v[116:119], a[52:55], a[8:11], v[116:119]\n\t"
      "ds_read_b64_tr_b16 a[122:123], v3 offset:28672\n\t"
      "v_mfma_f32_16x16x32_bf16 v[84:87], a[52:55], a[4:7], v[84:87]\n\t"
      "v_mfma_f32_16x16x32_bf16 v[52:55], a[52:55], a[0:3], v[52:55]\n\t"
      "v_mfma_f32_16x16x32_bf16 a[156:159], a[52:55], a[172:175], 0\n\t"
      "ds_read_b64_tr_b16 a[124:125], v4 offset:24576\n\t"
      "v_mfma_f32_16x16x32_bf16 v[56:59], a[56:59], a[0:3], v[56:59]\n\t"
      "v_mfma_f32_16x16x32_bf16 v[88:91], a[56:59], a[4:7], v[88:91]\n\t"
      "ds_read_b64_tr_b16 a[126:127], v4 offset:28672\n\t"
      "v_mfma_f32_16x16x32_bf16 v[120:123], a[56:59], a[8:11], v[120:123]\n\t"
      "v_mfma_f32_16x16x32_bf16 v[152:155], a[56:59], a[12:15], v[152:155]\n\t"
      "v_mfma_f32_16x16x32_bf16 v[184:187], a[56:59], a[16:19], v[184:187]\n\t"
      "ds_read_b64_tr_b16 a[128:129], v5 offset:24576\n\t"
      "v_mfma_f32_16x16x32_bf16 v[216:219], a[56:59], a[20:23], v[216:219]\n\t"
      "v_mfma_f32_16x16x32_bf16 v[248:251], a[56:59], a[24:27], v[248:251]\n\t"
      "ds_read_b64_tr_b16 a[130:131], v5 offset:28672\n\t"
      "v_mfma_f32_16x16x32_bf16 a[216:219], a[56:59], a[28:31], a[216:219]\n\t"
      "v_mfma_f32_16x16x32_bf16 a[160:163], a[56:59], a[172:175], 0\n\t"
      "v_mfma_f32_16x16x32_bf16 a[220:223], a[60:63], a[28:31], a[220:223]\n\t"
      "ds_read_b128 a[132:135], v7 offset:0\n\t"
      "v_mfma_f32_16x16x32_bf16 v[252:255], a[60:63], a[24:27], v[252:255]\n\t"
      "v_mfma_f32_16x16x32_bf16 v[220:223], a[60:63], a[20:23], v[220:223]\n\t"
      "v_mfma_f32_16x16x32_bf16 v[188:191], a[60:63], a[16:19], v[188:191]\n\t"
      "v_mfma_f32_16x16x32_bf16 v[156:159], a[60:63], a[12:15], v[156:159]\n\t"
      "v_mfma_f32_16x16x32_bf16 v[124:127], a[60:63], a[8:11], v[124:127]\n\t"
      "v_mfma_f32_16x16x32_bf16 v[92:95], a[60:63], a[4:7], v[92:95]\n\t"
      "v_mfma_f32_16x16x32_bf16 v[60:63], a[60:63], a[0:3], v[60:63]\n\t"
      "v_mfma_f32_16x16x32_bf16 a[164:167], a[60:63], a[172:175], 0\n\t"
      "v_mfma_f32_16x16x32_bf16 a[224:227], a[0:3], a[64:67], a[224:227]\n\t"
      "v_mfma_f32_16x16x32_bf16 a[228:231], a[4:7], a[64:67], a[228:231]\n\t"
      "v_mfma_f32_16x16x32_bf16 a[232:235], a[8:11], a[64:67], a[232:235]\n\t"
      "v_mfma_f32_16x16x32_bf16 a[236:239], a[12:15], a[64:67], a[236:239]\n\t"
      "v_mfma_f32_16x16x32_bf16 a[240:243], a[16:19], a[64:67], a[240:243]\n\t"
      "v_mfma_f32_16x16x32_bf16 a[244:247], a[20:23], a[64:67], a[244:247]\n\t"
      "v_mfma_f32_16x16x32_bf16 a[248:251], a[24:27], a[64:67], a[248:251]\n\t"
      "v_mfma_f32_16x16x32_bf16 a[252:255], a[28:31], a[64:67], a[252:255]\n\t"
      "v_mfma_f32_16x16x32_bf16 a[168:171], a[172:175], a[64:67], 0\n\t"
      "s_waitcnt vmcnt(9) lgkmcnt(0)\n\t"
      "s_barrier\n\t"
      "v_mfma_f32_16x16x32_bf16 v[32:35], a[100:103], a[68:71], v[32:35]\n\t"
      "ds_read_b128 a[0:3], v0 offset:32768\n\t"
      "buffer_load_dword v18, v16, s[56:59], s60 offen\n\t"
      "v_mfma_f32_16x16x32_bf16 v[64:67], a[100:103], a[72:75], v[64:67]\n\t"
      "s_add_u32 s60, s60, s61\n\t"
      "v_mfma_f32_16x16x32_bf16 v[96:99], a[100:103], a[76:79], v[96:99]\n\t"
      "ds_read_b128 a[4:7], v0 offset:34816\n\t"
      "s_add_u32 s62, s52, 65536\n\t"
      "s_add_u32 m0, s48, 0\n\t"
      "v_mfma_f32_16x16x32_bf16 v[128:131], a[100:103], a[80:83], v[128:131]\n\t"
      "buffer_load_dwordx4 v8, s[36:39], s52 offen lds\n\t"
      "v_mfma_f32_16x16x32_bf16 v[160:163], a[100:103], a[84:87], v[160:163]\n\t"
      "ds_read_b128 a[8:11], v0 offset:36864\n\t"
      "v_mfma_f32_16x16x32_bf16 v[192:195], a[100:103], a[88:91], v[192:195]\n\t"
      "v_mfma_f32_16x16x32_bf16 v[224:227], a[100:103], a[92:95], v[224:227]\n\t"
      "v_mfma_f32_16x16x32_bf16 a[192:195], a[100:103], a[96:99], a[192:195]\n\t"
      "ds_read_b128 a[12:15], v0 offset:38912\n\t"
      "v_mfma_f32_16x16x32_bf16 a[196:199], a[104:107], a[96:99], a[196:199]\n\t"
      "buffer_load_dwordx4 v9, s[36:39], s52 offen offset:1024 lds\n\t"
      "v_mfma_f32_16x16x32_bf16 v[228:231], a[104:107], a[92:95], v[228:231]\n\t"
      "ds_read_b128 a[16:19], v0 offset:40960\n\t"
      "v_mfma_f32_16x16x32_bf16 v[196:199], a[104:107], a[88:91], v[196:199]\n\t"
      "v_mfma_f32_16x16x32_bf16 v[164:167], a[104:107], a[84:87], v[164:167]\n\t"
      "v_mfma_f32_16x16x32_bf16 v[132:135], a[104:107], a[80:83], v[132:135]\n\t"
      "ds_read_b128 a[20:23], v0 offset:43008\n\t"
      "v_mfma_f32_16x16x32_bf16 v[100:103], a[104:107], a[76:79], v[100:103]\n\t"
      "v_mfma_f32_16x16x32_bf16 v[68:71], a[104:107], a[72:75], v[68:71]\n\t"
      "ds_read_b128 a[24:27], v0 offset:45056\n\t"
      "buffer_load_dwordx4 v10, s[36:39], s52 offen offset:2048 lds\n\t"
      "v_mfma_f32_16x16x32_bf16 v[36:39], a[104:107], a[68:71], v[36:39]\n\t"
      "v_mfma_f32_16x16x32_bf16 v[40:43], a[108:111], a[68:71], v[40:43]\n\t"
      "ds_read_b128 a[28:31], v0 offset:47104\n\t"
      "v_mfma_f32_16x16x32_bf16 v[72:75], a[108:111], a[72:75], v[72:75]\n\t"
      "v_mfma_f32_16x16x32_bf16 v[104:107], a[108:111], a[76:79], v[104:107]\n\t"
      "v_mfma_f32_16x16x32_bf16 v[136:139], a[108:111], a[80:83], v[136:139]\n\t"
      "ds_read_b64_tr_b16 a[32:33], v2 offset:32768\n\t"
      "buffer_load_dwordx4 v11, s[36:39], s52 offen offset:3072 lds\n\t"
      "v_mfma_f32_16x16x32_bf16 v[168:171], a[108:111], a[84:87], v[168:171]\n\t"
      "v_mfma_f32_16x16x32_bf16 v[200:203], a[108:111], a[88:91], v[200:203]\n\t"
      "ds_read_b64_tr_b16 a[34:35], v2 offset:36864\n\t"
      "v_mfma_f32_16x16x32_bf16 v[232:235], a[108:111], a[92:95], v[232:235]\n\t"
      "v_mfma_f32_16x16x32_bf16 a[200:203], a[108:111], a[96:99], a[200:203]\n\t"
      "v_mfma_f32_16x16x32_bf16 a[204:207], a[112:115], a[96:99], a[204:207]\n\t"
      "ds_read_b64_tr_b16 a[36:37], v3 offset:32768\n\t"
      "s_add_u32 m0, s48, 4096\n\t"
      "v_mfma_f32_16x16x32_bf16 v[236:239], a[112:115], a[92:95], v[236:239]\n\t"
      "buffer_load_dwordx4 v8, s[36:39], s62 offen lds\n\t"
      "v_mfma_f32_16x16x32_bf16 v[204:207], a[112:115], a[88:91], v[204:207]\n\t"
      "ds_read_b64_tr_b16 a[38:39], v3 offset:36864\n\t"
      "v_mfma_f32_16x16x32_bf16 v[172:175], a[112:115], a[84:87], v[172:175]\n\t"
      "v_mfma_f32_16x16x32_bf16 v[140:143], a[112:115], a[80:83], v[140:143]\n\t"
      "v_mfma_f32_16x16x32_bf16 v[108:111], a[112:115], a[76:79], v[108:111]\n\t"
      "ds_read_b64_tr_b16 a[40:41], v4 offset:32768\n\t"
      "v_mfma_f32_16x16x32_bf16 v[76:79], a[112:115], a[72:75], v[76:79]\n\t"
      "v_mfma_f32_16x16x32_bf16 v[44:47], a[112:115], a[68:71], v[44:47]\n\t"
      "ds_read_b64_tr_b16 a[42:43], v4 offset:36864\n\t"
      "buffer_load_dwordx4 v9, s[36:39], s62 offen offset:1024 lds\n\t"
      "v_mfma_f32_16x16x32_bf16 v[48:51], a[116:119], a[68:71], v[48:51]\n\t"
      "v_mfma_f32_16x16x32_bf16 v[80:83], a[116:119], a[72:75], v[80:83]\n\t"
      "ds_read_b64_tr_b16 a[44:45], v5 offset:32768\n\t"
      "v_mfma_f32_16x16x32_bf16 v[112:115], a[116:119], a[76:79], v[112:115]\n\t"
      "v_mfma_f32_16x16x32_bf16 v[144:147], a[116:119], a[80:83], v[144:147]\n\t"
      "v_mfma_f32_16x16x32_bf16 v[176:179], a[116:119], a[84:87], v[176:179]\n\t"
      "ds_read_b64_tr_b16 a[46:47], v5 offset:36864\n\t"
      "buffer_load_dwordx4 v10, s[36:39], s62 offen offset:2048 lds\n\t"
      "v_mfma_f32_16x16x32_bf16 v[208:211], a[116:119], a[88:91], v[208:211]\n\t"
      "v_mfma_f32_16x16x32_bf16 v[240:243], a[116:119], a[92:95], v[240:243]\n\t"
      "ds_read_b64_tr_b16 a[48:49], v2 offset:40960\n\t"
      "v_mfma_f32_16x16x32_bf16 a[208:211], a[116:119], a[96:99], a[208:211]\n\t"
      "v_mfma_f32_16x16x32_bf16 a[212:215], a[120:123], a[96:99], a[212:215]\n\t"
      "v_mfma_f32_16x16x32_bf16 v[244:247], a[120:123], a[92:95], v[244:247]\n\t"
      "ds_read_b64_tr_b16 a[50:51], v2 offset:45056\n\t"
      "v_mfma_f32_16x16x32_bf16 v[212:215], a[120:123], a[88:91], v[212:215]\n\t"
      "buffer_load_dwordx4 v11, s[36:39], s62 offen offset:3072 lds\n\t"
      "v_mfma_f32_16x16x32_bf16 v[180:183], a[120:123], a[84:87], v[180:183]\n\t"
      "ds_read_b64_tr_b16 a[52:53], v3 offset:40960\n\t"
      "s_add_u32 s52, s52, 128\n\t"
      "v_mfma_f32_16x16x32_bf16 v[148:151], a[120:123], a[80:83], v[148:151]\n\t"
      "v_mfma_f32_16x16x32_bf16 v[116:119], a[120:123], a[76:79], v[116:119]\n\t"
      "ds_read_b64_tr_b16 a[54:55], v3 offset:45056\n\t"
      "v_mfma_f32_16x16x32_bf16 v[84:87], a[120:123], a[72:75], v[84:87]\n\t"
      "v_mfma_f32_16x16x32_bf16 v[52:55], a[120:123], a[68:71], v[52:55]\n\t"
      "s_add_u32 m0, s50, 0\n\t"
      "v_mfma_f32_16x16x32_bf16 a[156:159], a[120:123], a[172:175], a[156:159]\n\t"
      "ds_read_b64_tr_b16 a[56:57], v4 offset:40960\n\t"
      "buffer_load_dwordx4 v19, s[44:47], s54 offen lds\n\t"
      "v_mfma_f32_16x16x32_bf16 v[56:59], a[124:127], a[68:71], v[56:59]\n\t"
      "s_add_u32 s54, s54, 128\n\t"
      "v_mfma_f32_16x16x32_bf16 v[88:91], a[124:127], a[72:75], v[88:91]\n\t"
      "ds_read_b64_tr_b16 a[58:59], v4 offset:45056\n\t"
      "v_mfma_f32_16x16x32_bf16 v[120:123], a[124:127], a[76:79], v[120:123]\n\t"
      "v_mfma_f32_16x16x32_bf16 v[152:155], a[124:127], a[80:83], v[152:155]\n\t"
      "s_add_u32 m0, s49, 16384\n\t"
      "v_mfma_f32_16x16x32_bf16 v[184:187], a[124:127], a[84:87], v[184:187]\n\t"
      "ds_read_b64_tr_b16 a[60:61], v5 offset:40960\n\t"
      "buffer_load_dwordx4 v12, s[40:43], s53 offen lds\n\t"
      "v_mfma_f32_16x16x32_bf16 v[216:219], a[124:127], a[88:91], v[216:219]\n\t"
      "v_mfma_f32_16x16x32_bf16 v[248:251], a[124:127], a[92:95], v[248:251]\n\t"
      "ds_read_b64_tr_b16 a[62:63], v5 offset:45056\n\t"
      "v_mfma_f32_16x16x32_bf16 a[216:219], a[124:127], a[96:99], a[216:219]\n\t"
      "v_mfma_f32_16x16x32_bf16 a[160:163], a[124:127], a[172:175], a[160:163]\n\t"
      "buffer_load_dwordx4 v13, s[40:43], s53 offen offset:1024 lds\n\t"
      "v_mfma_f32_16x16x32_bf16 a[220:223], a[128:131], a[96:99], a[220:223]\n\t"
      "ds_read_b128 a[64:67], v6 offset:4096\n\t"
      "v_mfma_f32_16x16x32_bf16 v[252:255], a[128:131], a[92:95], v[252:255]\n\t"
      "v_mfma_f32_16x16x32_bf16 v[220:223], a[128:131], a[88:91], v[220:223]\n\t"
      "v_mfma_f32_16x16x32_bf16 v[188:191], a[128:131], a[84:87], v[188:191]\n\t"
      "buffer_load_dwordx4 v14, s[40:43], s53 offen offset:2048 lds\n\t"
      "v_mfma_f32_16x16x32_bf16 v[156:159], a[128:131], a[80:83], v[156:159]\n\t"
      "v_mfma_f32_16x16x32_bf16 v[124:127], a[128:131], a[76:79], v[124:127]\n\t"
      "v_mfma_f32_16x16x32_bf16 v[92:95], a[128:131], a[72:75], v[92:95]\n\t"
      "v_mfma_f32_16x16x32_bf16 v[60:63], a[128:131], a[68:71], v[60:63]\n\t"
      "v_mfma_f32_16x16x32_bf16 a[164:167], a[128:131], a[172:175], a[164:167]\n\t"
      "buffer_load_dwordx4 v15, s[40:43], s53 offen offset:3072 lds\n\t"
      "v_mfma_f32_16x16x32_bf16 a[224:227], a[68:71], a[132:135], a[224:227]\n\t"
      "s_add_u32 s53, s53, 229376\n\t"
      "v_mfma_f32_16x16x32_bf16 a[228:231], a[72:75], a[132:135], a[228:231]\n\t"
      "v_mfma_f32_16x16x32_bf16 a[232:235], a[76:79], a[132:135], a[232:235]\n\t"
      "v_mfma_f32_16x16x32_bf16 a[236:239], a[80:83], a[132:135], a[236:239]\n\t"
      "v_mfma_f32_16x16x32_bf16 a[240:243], a[84:87], a[132:135], a[240:243]\n\t"
      "v_mfma_f32_16x16x32_bf16 a[244:247], a[88:91], a[132:135], a[244:247]\n\t"
      "v_mfma_f32_16x16x32_bf16 a[248:251], a[92:95], a[132:135], a[248:251]\n\t"
      "v_mfma_f32_16x16x32_bf16 a[252:255], a[96:99], a[132:135], a[252:255]\n\t"
      "v_mfma_f32_16x16x32_bf16 a[168:171], a[172:175], a[132:135], a[168:171]\n\t"
      "s_waitcnt vmcnt(19) lgkmcnt(0)\n\t"
      "s_barrier\n\t"
      "v_mfma_f32_16x16x32_bf16 v[32:35], a[32:35], a[0:3], v[32:35]\n\t"
      "ds_read_b128 a[68:71], v1 offset:32768\n\t"
      "buffer_load_dword v18, v16, s[56:59], s60 offen\n\t"
      "v_mfma_f32_16x16x32_bf16 v[64:67], a[32:35], a[4:7], v[64:67]\n\t"
      "s_add_u32 s60, s60, s61\n\t"
      "v_mfma_f32_16x16x32_bf16 v[96:99], a[32:35], a[8:11], v[96:99]\n\t"
      "ds_read_b128 a[72:75], v1 offset:34816\n\t"
      "v_mfma_f32_16x16x32_bf16 v[128:131], a[32:35], a[12:15], v[128:131]\n\t"
      "s_add_u32 m0, s49, 32768\n\t"
      "v_mfma_f32_16x16x32_bf16 v[160:163], a[32:35], a[16:19], v[160:163]\n\t"
      "ds_read_b128 a[76:79], v1 offset:36864\n\t"
      "buffer_load_dwordx4 v12, s[40:43], s53 offen lds\n\t"
      "v_mfma_f32_16x16x32_bf16 v[192:195], a[32:35], a[20:23], v[192:195]\n\t"
      "v_mfma_f32_16x16x32_bf16 v[224:227], a[32:35], a[24:27], v[224:227]\n\t"
      "v_mfma_f32_16x16x32_bf16 a[192:195], a[32:35], a[28:31], a[192:195]\n\t"
      "ds_read_b128 a[80:83], v1 offset:38912\n\t"
      "v_mfma_f32_16x16x32_bf16 a[196:199], a[36:39], a[28:31], a[196:199]\n\t"
      "v_mfma_f32_16x16x32_bf16 v[228:231], a[36:39], a[24:27], v[228:231]\n\t"
      "ds_read_b128 a[84:87], v1 offset:40960\n\t"
      "v_mfma_f32_16x16x32_bf16 v[196:199], a[36:39], a[20:23], v[196:199]\n\t"
      "v_mfma_f32_16x16x32_bf16 v[164:167], a[36:39], a[16:19], v[164:167]\n\t"
      "v_mfma_f32_16x16x32_bf16 v[132:135], a[36:39], a[12:15], v[132:135]\n\t"
      "ds_read_b128 a[88:91], v1 offset:43008\n\t"
      "v_mfma_f32_16x16x32_bf16 v[100:103], a[36:39], a[8:11], v[100:103]\n\t"
      "v_mfma_f32_16x16x32_bf16 v[68:71], a[36:39], a[4:7], v[68:71]\n\t"
      "ds_read_b128 a[92:95], v1 offset:45056\n\t"
      "v_mfma_f32_16x16x32_bf16 v[36:39], a[36:39], a[0:3], v[36:39]\n\t"
      "v_mfma_f32_16x16x32_bf16 v[40:43], a[40:43], a[0:3], v[40:43]\n\t"
      "ds_read_b128 a[96:99], v1 offset:47104\n\t"
      "buffer_load_dwordx4 v13, s[40:43], s53 offen offset:1024 lds\n\t"
      "v_mfma_f32_16x16x32_bf16 v[72:75], a[40:43], a[4:7], v[72:75]\n\t"
      "v_mfma_f32_16x16x32_bf16 v[104:107], a[40:43], a[8:11], v[104:107]\n\t"
      "v_mfma_f32_16x16x32_bf16 v[136:139], a[40:43], a[12:15], v[136:139]\n\t"
      "ds_read_b64_tr_b16 a[100:101], v2 offset:49152\n\t"
      "v_mfma_f32_16x16x32_bf16 v[168:171], a[40:43], a[16:19], v[168:171]\n\t"
      "v_mfma_f32_16x16x32_bf16 v[200:203], a[40:43], a[20:23], v[200:203]\n\t"
      "ds_read_b64_tr_b16 a[102:103], v2 offset:53248\n\t"
      "v_mfma_f32_16x16x32_bf16 v[232:235], a[40:43], a[24:27], v[232:235]\n\t"
      "v_mfma_f32_16x16x32_bf16 a[200:203], a[40:43], a[28:31], a[200:203]\n\t"
      "v_mfma_f32_16x16x32_bf16 a[204:207], a[44:47], a[28:31], a[204:207]\n\t"
      "ds_read_b64_tr_b16 a[104:105], v3 offset:49152\n\t"
      "v_mfma_f32_16x16x32_bf16 v[236:239], a[44:47], a[24:27], v[236:239]\n\t"
      "v_mfma_f32_16x16x32_bf16 v[204:207], a[44:47], a[20:23], v[204:207]\n\t"
      "ds_read_b64_tr_b16 a[106:107], v3 offset:53248\n\t"
      "v_mfma_f32_16x16x32_bf16 v[172:175], a[44:47], a[16:19], v[172:175]\n\t"
      "v_mfma_f32_16x16x32_bf16 v[140:143], a[44:47], a[12:15], v[140:143]\n\t"
      "buffer_load_dwordx4 v14, s[40:43], s53 offen offset:2048 lds\n\t"
      "v_mfma_f32_16x16x32_bf16 v[108:111], a[44:47], a[8:11], v[108:111]\n\t"
      "ds_read_b64_tr_b16 a[108:109], v4 offset:49152\n\t"
      "v_mfma_f32_16x16x32_bf16 v[76:79], a[44:47], a[4:7], v[76:79]\n\t"
      "v_mfma_f32_16x16x32_bf16 v[44:47], a[44:47], a[0:3], v[44:47]\n\t"
      "ds_read_b64_tr_b16 a[110:111], v4 offset:53248\n\t"
      "v_mfma_f32_16x16x32_bf16 v[48:51], a[48:51], a[0:3], v[48:51]\n\t"
      "v_mfma_f32_16x16x32_bf16 v[80:83], a[48:51], a[4:7], v[80:83]\n\t"
      "ds_read_b64_tr_b16 a[112:113], v5 offset:49152\n\t"
      "v_mfma_f32_16x16x32_bf16 v[112:115], a[48:51], a[8:11], v[112:115]\n\t"
      "v_mfma_f32_16x16x32_bf16 v[144:147], a[48:51], a[12:15], v[144:147]\n\t"
      "v_mfma_f32_16x16x32_bf16 v[176:179], a[48:51], a[16:19], v[176:179]\n\t"
      "ds_read_b64_tr_b16 a[114:115], v5 offset:53248\n\t"
      "v_mfma_f32_16x16x32_bf16 v[208:211], a[48:51], a[20:23], v[208:211]\n\t"
      "v_mfma_f32_16x16x32_bf16 v[240:243], a[48:51], a[24:27], v[240:243]\n\t"
      "ds_read_b64_tr_b16 a[116:117], v2 offset:57344\n\t"
      "v_mfma_f32_16x16x32_bf16 a[208:211], a[48:51], a[28:31], a[208:211]\n\t"
      "v_mfma_f32_16x16x32_bf16 a[212:215], a[52:55], a[28:31], a[212:215]\n\t"
      "buffer_load_dwordx4 v15, s[40:43], s53 offen offset:3072 lds\n\t"
      "v_mfma_f32_16x16x32_bf16 v[244:247], a[52:55], a[24:27], v[244:247]\n\t"
      "ds_read_b64_tr_b16 a[118:119], v2 offset:61440\n\t"
      "s_add_u32 s53, s53, 229376\n\t"
      "v_mfma_f32_16x16x32_bf16 v[212:215], a[52:55], a[20:23], v[212:215]\n\t"
      "v_mfma_f32_16x16x32_bf16 v[180:183], a[52:55], a[16:19], v[180:183]\n\t"
      "ds_read_b64_tr_b16 a[120:121], v3 offset:57344\n\t"
      "v_mfma_f32_16x16x32_bf16 v[148:151], a[52:55], a[12:15], v[148:151]\n\t"
      "v_mfma_f32_16x16x32_bf16 v[116:119], a[52:55], a[8:11], v[116:119]\n\t"
      "ds_read_b64_tr_b16 a[122:123], v3 offset:61440\n\t"
      "v_mfma_f32_16x16x32_bf16 v[84:87], a[52:55], a[4:7], v[84:87]\n\t"
      "v_mfma_f32_16x16x32_bf16 v[52:55], a[52:55], a[0:3], v[52:55]\n\t"
      "v_mfma_f32_16x16x32_bf16 a[156:159], a[52:55], a[172:175], a[156:159]\n\t"
      "ds_read_b64_tr_b16 a[124:125], v4 offset:57344\n\t"
      "v_mfma_f32_16x16x32_bf16 v[56:59], a[56:59], a[0:3], v[56:59]\n\t"
      "v_mfma_f32_16x16x32_bf16 v[88:91], a[56:59], a[4:7], v[88:91]\n\t"
      "ds_read_b64_tr_b16 a[126:127], v4 offset:61440\n\t"
      "v_mfma_f32_16x16x32_bf16 v[120:123], a[56:59], a[8:11], v[120:123]\n\t"
      "v_mfma_f32_16x16x32_bf16 v[152:155], a[56:59], a[12:15], v[152:155]\n\t"
      "v_mfma_f32_16x16x32_bf16 v[184:187], a[56:59], a[16:19], v[184:187]\n\t"
      "ds_read_b64_tr_b16 a[128:129], v5 offset:57344\n\t"
      "v_mfma_f32_16x16x32_bf16 v[216:219], a[56:59], a[20:23], v[216:219]\n\t"
      "v_mfma_f32_16x16x32_bf16 v[248:251], a[56:59], a[24:27], v[248:251]\n\t"
      "ds_read_b64_tr_b16 a[130:131], v5 offset:61440\n\t"
      "v_mfma_f32_16x16x32_bf16 a[216:219], a[56:59], a[28:31], a[216:219]\n\t"
      "v_mfma_f32_16x16x32_bf16 a[160:163], a[56:59], a[172:175], a[160:163]\n\t"
      "v_mfma_f32_16x16x32_bf16 a[220:223], a[60:63], a[28:31], a[220:223]\n\t"
      "ds_read_b128 a[132:135], v7 offset:4096\n\t"
      "v_mfma_f32_16x16x32_bf16 v[252:255], a[60:63], a[24:27], v[252:255]\n\t"
      "v_mfma_f32_16x16x32_bf16 v[220:223], a[60:63], a[20:23], v[220:223]\n\t"
      "v_mfma_f32_16x16x32_bf16 v[188:191], a[60:63], a[16:19], v[188:191]\n\t"
      "v_mfma_f32_16x16x32_bf16 v[156:159], a[60:63], a[12:15], v[156:159]\n\t"
      "v_mfma_f32_16x16x32_bf16 v[124:127], a[60:63], a[8:11], v[124:127]\n\t"
      "v_mfma_f32_16x16x32_bf16 v[92:95], a[60:63], a[4:7], v[92:95]\n\t"
      "v_mfma_f32_16x16x32_bf16 v[60:63], a[60:63], a[0:3], v[60:63]\n\t"
      "v_mfma_f32_16x16x32_bf16 a[164:167], a[60:63], a[172:175], a[164:167]\n\t"
      "v_mfma_f32_16x16x32_bf16 a[224:227], a[0:3], a[64:67], a[224:227]\n\t"
      "v_mfma_f32_16x16x32_bf16 a[228:231], a[4:7], a[64:67], a[228:231]\n\t"
      "v_mfma_f32_16x16x32_bf16 a[232:235], a[8:11], a[64:67], a[232:235]\n\t"
      "v_mfma_f32_16x16x32_bf16 a[236:239], a[12:15], a[64:67], a[236:239]\n\t"
      "v_mfma_f32_16x16x32_bf16 a[240:243], a[16:19], a[64:67], a[240:243]\n\t"
      "v_mfma_f32_16x16x32_bf16 a[244:247], a[20:23], a[64:67], a[244:247]\n\t"
      "v_mfma_f32_16x16x32_bf16 a[248:251], a[24:27], a[64:67], a[248:251]\n\t"
      "v_mfma_f32_16x16x32_bf16 a[252:255], a[28:31], a[64:67], a[252:255]\n\t"
      "v_mfma_f32_16x16x32_bf16 a[168:171], a[172:175], a[64:67], a[168:171]\n\t"
      "s_waitcnt vmcnt(9) lgkmcnt(0)\n\t"
      "s_bitcmp1_b32 s81, s55\n\t"
      "s_cbranch_scc0 LA_nofl1_r0_%=\n\t"
      "s_not_b32 s74, s68\n\t"
      "s_not_b32 s75, s69\n\t"
      "s_cmp_eq_u32 s63, 1\n\t"
      "s_cselect_b32 s74, s68, s74\n\t"
      "s_cselect_b32 s75, s69, s75\n\t"
      "v_mov_b32 v22, s74\n\t"
      "v_mov_b32 v23, s75\n\t"
      "s_mov_b64 exec, 1\n\t"
      "buffer_store_dwordx4 v[20:23], v17, s[64:67], 0 offen sc1 ; alt\n\t"
      "s_mov_b64 exec, -1\n\t"
      "s_nop 1\n\t"
      "LA_nofl1_r0_%=:\n\t"
      "s_barrier\n\t"
      "v_mfma_f32_16x16x32_bf16 v[32:35], a[100:103], a[68:71], v[32:35]\n\t"
      "ds_read_b128 a[0:3], v0 offset:0\n\t"
      "buffer_load_dword v18, v16, s[56:59], s60 offen\n\t"
      "v_mfma_f32_16x16x32_bf16 v[64:67], a[100:103], a[72:75], v[64:67]\n\t"
      "s_add_u32 s60, s60, s61\n\t"
      "v_mfma_f32_16x16x32_bf16 v[96:99], a[100:103], a[76:79], v[96:99]\n\t"
      "ds_read_b128 a[4:7], v0 offset:2048\n\t"
      "s_add_u32 s62, s52, 65536\n\t"
      "s_add_u32 m0, s48, 32768\n\t"
      "v_mfma_f32_16x16x32_bf16 v[128:131], a[100:103], a[80:83], v[128:131]\n\t"
      "buffer_load_dwordx4 v8, s[36:39], s52 offen lds\n\t"
      "v_mfma_f32_16x16x32_bf16 v[160:163], a[100:103], a[84:87], v[160:163]\n\t"
      "ds_read_b128 a[8:11], v0 offset:4096\n\t"
      "v_mfma_f32_16x16x32_bf16 v[192:195], a[100:103], a[88:91], v[192:195]\n\t"
      "v_mfma_f32_16x16x32_bf16 v[224:227], a[100:103], a[92:95], v[224:227]\n\t"
      "v_mfma_f32_16x16x32_bf16 a[192:195], a[100:103], a[96:99], a[192:195]\n\t"
      "ds_read_b128 a[12:15], v0 offset:6144\n\t"
      "v_mfma_f32_16x16x32_bf16 a[196:199], a[104:107], a[96:99], a[196:199]\n\t"
      "buffer_load_dwordx4 v9, s[36:39], s52 offen offset:1024 lds\n\t"
      "v_mfma_f32_16x16x32_bf16 v[228:231], a[104:107], a[92:95], v[228:231]\n\t"
      "ds_read_b128 a[16:19], v0 offset:8192\n\t"
      "v_mfma_f32_16x16x32_bf16 v[196:199], a[104:107], a[88:91], v[196:199]\n\t"
      "v_mfma_f32_16x16x32_bf16 v[164:167], a[104:107], a[84:87], v[164:167]\n\t"
      "v_mfma_f32_16x16x32_bf16 v[132:135], a[104:107], a[80:83], v[132:135]\n\t"
      "ds_read_b128 a[20:23], v0 offset:10240\n\t"
      "v_mfma_f32_16x16x32_bf16 v[100:103], a[104:107], a[76:79], v[100:103]\n\t"
      "v_mfma_f32_16x16x32_bf16 v[68:71], a[104:107], a[72:75], v[68:71]\n\t"
      "ds_read_b128 a[24:27], v0 offset:12288\n\t"
      "buffer_load_dwordx4 v10, s[36:39], s52 offen offset:2048 lds\n\t"
      "v_mfma_f32_16x16x32_bf16 v[36:39], a[104:107], a[68:71], v[36:39]\n\t"
      "v_mfma_f32_16x16x32_bf16 v[40:43], a[108:111], a[68:71], v[40:43]\n\t"
      "ds_read_b128 a[28:31], v0 offset:14336\n\t"
      "v_mfma_f32_16x16x32_bf16 v[72:75], a[108:111], a[72:75], v[72:75]\n\t"
      "v_mfma_f32_16x16x32_bf16 v[104:107], a[108:111], a[76:79], v[104:107]\n\t"
      "v_mfma_f32_16x16x32_bf16 v[136:139], a[108:111], a[80:83], v[136:139]\n\t"
      "ds_read_b64_tr_b16 a[32:33], v2 offset:0\n\t"
      "buffer_load_dwordx4 v11, s[36:39], s52 offen offset:3072 lds\n\t"
      "v_mfma_f32_16x16x32_bf16 v[168:171], a[108:111], a[84:87], v[168:171]\n\t"
      "v_mfma_f32_16x16x32_bf16 v[200:203], a[108:111], a[88:91], v[200:203]\n\t"
      "ds_read_b64_tr_b16 a[34:35], v2 offset:4096\n\t"
      "v_mfma_f32_16x16x32_bf16 v[232:235], a[108:111], a[92:95], v[232:235]\n\t"
      "v_mfma_f32_16x16x32_bf16 a[200:203], a[108:111], a[96:99], a[200:203]\n\t"
      "v_mfma_f32_16x16x32_bf16 a[204:207], a[112:115], a[96:99], a[204:207]\n\t"
      "ds_read_b64_tr_b16 a[36:37], v3 offset:0\n\t"
      "s_add_u32 m0, s48, 36864\n\t"
      "v_mfma_f32_16x16x32_bf16 v[236:239], a[112:115], a[92:95], v[236:239]\n\t"
      "buffer_load_dwordx4 v8, s[36:39], s62 offen lds\n\t"
      "v_mfma_f32_16x16x32_bf16 v[204:207], a[112:115], a[88:91], v[204:207]\n\t"
      "ds_read_b64_tr_b16 a[38:39], v3 offset:4096\n\t"
      "v_mfma_f32_16x16x32_bf16 v[172:175], a[112:115], a[84:87], v[172:175]\n\t"
      "v_mfma_f32_16x16x32_bf16 v[140:143], a[112:115], a[80:83], v[140:143]\n\t"
      "v_mfma_f32_16x16x32_bf16 v[108:111], a[112:115], a[76:79], v[108:111]\n\t"
      "ds_read_b64_tr_b16 a[40:41], v4 offset:0\n\t"
      "v_mfma_f32_16x16x32_bf16 v[76:79], a[112:115], a[72:75], v[76:79]\n\t"
      "v_mfma_f32_16x16x32_bf16 v[44:47], a[112:115], a[68:71], v[44:47]\n\t"
      "ds_read_b64_tr_b16 a[42:43], v4 offset:4096\n\t"
      "buffer_load_dwordx4 v9, s[36:39], s62 offen offset:1024 lds\n\t"
      "v_mfma_f32_16x16x32_bf16 v[48:51], a[116:119], a[68:71], v[48:51]\n\t"
      "v_mfma_f32_16x16x32_bf16 v[80:83], a[116:119], a[72:75], v[80:83]\n\t"
      "ds_read_b64_tr_b16 a[44:45], v5 offset:0\n\t"
      "v_mfma_f32_16x16x32_bf16 v[112:115], a[116:119], a[76:79], v[112:115]\n\t"
      "v_mfma_f32_16x16x32_bf16 v[144:147], a[116:119], a[80:83], v[144:147]\n\t"
      "v_mfma_f32_16x16x32_bf16 v[176:179], a[116:119], a[84:87], v[176:179]\n\t"
      "ds_read_b64_tr_b16 a[46:47], v5 offset:4096\n\t"
      "buffer_load_dwordx4 v10, s[36:39], s62 offen offset:2048 lds\n\t"
      "v_mfma_f32_16x16x32_bf16 v[208:211], a[116:119], a[88:91], v[208:211]\n\t"
      "v_mfma_f32_16x16x32_bf16 v[240:243], a[116:119], a[92:95], v[240:243]\n\t"
      "ds_read_b64_tr_b16 a[48:49], v2 offset:8192\n\t"
      "v_mfma_f32_16x16x32_bf16 a[208:211], a[116:119], a[96:99], a[208:211]\n\t"
      "v_mfma_f32_16x16x32_bf16 a[212:215], a[120:123], a[96:99], a[212:215]\n\t"
      "v_mfma_f32_16x16x32_bf16 v[244:247], a[120:123], a[92:95], v[244:247]\n\t"
      "ds_read_b64_tr_b16 a[50:51], v2 offset:12288\n\t"
      "v_mfma_f32_16x16x32_bf16 v[212:215], a[120:123], a[88:91], v[212:215]\n\t"
      "buffer_load_dwordx4 v11, s[36:39], s62 offen offset:3072 lds\n\t"
      "v_mfma_f32_16x16x32_bf16 v[180:183], a[120:123], a[84:87], v[180:183]\n\t"
      "ds_read_b64_tr_b16 a[52:53], v3 offset:8192\n\t"
      "s_add_u32 s52, s52, 128\n\t"
      "v_mfma_f32_16x16x32_bf16 v[148:151], a[120:123], a[80:83], v[148:151]\n\t"
      "v_mfma_f32_16x16x32_bf16 v[116:119], a[120:123], a[76:79], v[116:119]\n\t"
      "ds_read_b64_tr_b16 a[54:55], v3 offset:12288\n\t"
      "v_mfma_f32_16x16x32_bf16 v[84:87], a[120:123], a[72:75], v[84:87]\n\t"
      "v_mfma_f32_16x16x32_bf16 v[52:55], a[120:123], a[68:71], v[52:55]\n\t"
      "s_add_u32 m0, s50, 4096\n\t"
      "v_mfma_f32_16x16x32_bf16 a[156:159], a[120:123], a[172:175], a[156:159]\n\t"
      "ds_read_b64_tr_b16 a[56:57], v4 offset:8192\n\t"
      "buffer_load_dwordx4 v19, s[44:47], s54 offen lds\n\t"
      "v_mfma_f32_16x16x32_bf16 v[56:59], a[124:127], a[68:71], v[56:59]\n\t"
      "s_add_u32 s54, s54, 128\n\t"
      "v_mfma_f32_16x16x32_bf16 v[88:91], a[124:127], a[72:75], v[88:91]\n\t"
      "ds_read_b64_tr_b16 a[58:59], v4 offset:12288\n\t"
      "v_mfma_f32_16x16x32_bf16 v[120:123], a[124:127], a[76:79], v[120:123]\n\t"
      "v_mfma_f32_16x16x32_bf16 v[152:155], a[124:127], a[80:83], v[152:155]\n\t"
      "s_add_u32 m0, s49, 49152\n\t"
      "v_mfma_f32_16x16x32_bf16 v[184:187], a[124:127], a[84:87], v[184:187]\n\t"
      "ds_read_b64_tr_b16 a[60:61], v5 offset:8192\n\t"
      "buffer_load_dwordx4 v12, s[40:43], s53 offen lds\n\t"
      "v_mfma_f32_16x16x32_bf16 v[216:219], a[124:127], a[88:91], v[216:219]\n\t"
      "v_mfma_f32_16x16x32_bf16 v[248:251], a[124:127], a[92:95], v[248:251]\n\t"
      "ds_read_b64_tr_b16 a[62:63], v5 offset:12288\n\t"
      "v_mfma_f32_16x16x32_bf16 a[216:219], a[124:127], a[96:99], a[216:219]\n\t"
      "v_mfma_f32_16x16x32_bf16 a[160:163], a[124:127], a[172:175], a[160:163]\n\t"
      "buffer_load_dwordx4 v13, s[40:43], s53 offen offset:1024 lds\n\t"
      "v_mfma_f32_16x16x32_bf16 a[220:223], a[128:131], a[96:99], a[220:223]\n\t"
      "ds_read_b128 a[64:67], v6 offset:0\n\t"
      "v_mfma_f32_16x16x32_bf16 v[252:255], a[128:131], a[92:95], v[252:255]\n\t"
      "v_mfma_f32_16x16x32_bf16 v[220:223], a[128:131], a[88:91], v[220:223]\n\t"
      "v_mfma_f32_16x16x32_bf16 v[188:191], a[128:131], a[84:87], v[188:191]\n\t"
      "buffer_load_dwordx4 v14, s[40:43], s53 offen offset:2048 lds\n\t"
      "v_mfma_f32_16x16x32_bf16 v[156:159], a[128:131], a[80:83], v[156:159]\n\t"
      "v_mfma_f32_16x16x32_bf16 v[124:127], a[128:131], a[76:79], v[124:127]\n\t"
      "v_mfma_f32_16x16x32_bf16 v[92:95], a[128:131], a[72:75], v[92:95]\n\t"
      "v_mfma_f32_16x16x32_bf16 v[60:63], a[128:131], a[68:71], v[60:63]\n\t"
      "v_mfma_f32_16x16x32_bf16 a[164:167], a[128:131], a[172:175], a[164:167]\n\t"
      "buffer_load_dwordx4 v15, s[40:43], s53 offen offset:3072 lds\n\t"
      "v_mfma_f32_16x16x32_bf16 a[224:227], a[68:71], a[132:135], a[224:227]\n\t"
      "s_add_u32 s53, s53, 229376\n\t"
      "v_mfma_f32_16x16x32_bf16 a[228:231], a[72:75], a[132:135], a[228:231]\n\t"
      "v_mfma_f32_16x16x32_bf16 a[232:235], a[76:79], a[132:135], a[232:235]\n\t"
      "v_mfma_f32_16x16x32_bf16 a[236:239], a[80:83], a[132:135], a[236:239]\n\t"
      "v_mfma_f32_16x16x32_bf16 a[240:243], a[84:87], a[132:135], a[240:243]\n\t"
      "s_sub_u32 s55, s55, 1\n\t"
      "v_mfma_f32_16x16x32_bf16 a[244:247], a[88:91], a[132:135], a[244:247]\n\t"
      "v_mfma_f32_16x16x32_bf16 a[248:251], a[92:95], a[132:135], a[248:251]\n\t"
      "v_mfma_f32_16x16x32_bf16 a[252:255], a[96:99], a[132:135], a[252:255]\n\t"
      "v_mfma_f32_16x16x32_bf16 a[168:171], a[172:175], a[132:135], a[168:171]\n\t"
      "s_cmp_eq_u32 s55, 0\n\t"
      "s_cbranch_scc1 LA_nopub1_r0_%=\n\t"
      "s_nop 7\n\t"
      "s_mov_b32 exec_lo, 0x00010001\n\t"
      "s_mov_b32 exec_hi, 0x00010001\n\t"
      "buffer_store_dwordx4 a[156:159], v25, s[64:67], 0 offen offset:320 sc1\n\t"
      "buffer_store_dwordx4 a[160:163], v25, s[64:67], 0 offen offset:384 sc1\n\t"
      "buffer_store_dwordx4 a[164:167], v25, s[64:67], 0 offen offset:448 sc1\n\t"
      "s_mov_b64 exec, 0xffff\n\t"
      "buffer_store_dword a168, v26, s[64:67], 0 offen offset:512 sc1\n\t"
      "s_mov_b64 exec, -1\n\t"
      "s_mov_b32 s63, 1\n\t"
      "LA_nopub1_r0_%=:\n\t"
      "s_cmp_lg_u32 s55, 0\n\t"
      "s_cbranch_scc1 LA_loop_r0_%=\n\t"
      "LA_tail_r0_%=:\n\t"
      "s_cmp_eq_u32 s73, 0\n\t"
      "s_cbranch_scc0 LA_tail1_r0_%=\n\t"
      "s_waitcnt vmcnt(19) lgkmcnt(0)\n\t"
      "s_barrier\n\t"
      "v_mfma_f32_16x16x32_bf16 v[32:35], a[32:35], a[0:3], v[32:35]\n\t"
      "ds_read_b128 a[68:71], v1 offset:0\n\t"
      "v_mfma_f32_16x16x32_bf16 v[64:67], a[32:35], a[4:7], v[64:67]\n\t"
      "v_mfma_f32_16x16x32_bf16 v[96:99], a[32:35], a[8:11], v[96:99]\n\t"
      "ds_read_b128 a[72:75], v1 offset:2048\n\t"
      "v_mfma_f32_16x16x32_bf16 v[128:131], a[32:35], a[12:15], v[128:131]\n\t"
      "v_mfma_f32_16x16x32_bf16 v[160:163], a[32:35], a[16:19], v[160:163]\n\t"
      "ds_read_b128 a[76:79], v1 offset:4096\n\t"
      "v_mfma_f32_16x16x32_bf16 v[192:195], a[32:35], a[20:23], v[192:195]\n\t"
      "v_mfma_f32_16x16x32_bf16 v[224:227], a[32:35], a[24:27], v[224:227]\n\t"
      "v_mfma_f32_16x16x32_bf16 a[192:195], a[32:35], a[28:31], a[192:195]\n\t"
      "ds_read_b128 a[80:83], v1 offset:6144\n\t"
      "v_mfma_f32_16x16x32_bf16 a[136:139], a[32:35], a[172:175], a[136:139]\n\t"
      "v_mfma_f32_16x16x32_bf16 a[196:199], a[36:39], a[28:31], a[196:199]\n\t"
      "ds_read_b128 a[84:87], v1 offset:8192\n\t"
      "v_mfma_f32_16x16x32_bf16 v[228:231], a[36:39], a[24:27], v[228:231]\n\t"
      "v_mfma_f32_16x16x32_bf16 v[196:199], a[36:39], a[20:23], v[196:199]\n\t"
      "v_mfma_f32_16x16x32_bf16 v[164:167], a[36:39], a[16:19], v[164:167]\n\t"
      "ds_read_b128 a[88:91], v1 offset:10240\n\t"
      "v_mfma_f32_16x16x32_bf16 v[132:135], a[36:39], a[12:15], v[132:135]\n\t"
      "v_mfma_f32_16x16x32_bf16 v[100:103], a[36:39], a[8:11], v[100:103]\n\t"
      "ds_read_b128 a[92:95], v1 offset:12288\n\t"
      "v_mfma_f32_16x16x32_bf16 v[68:71], a[36:39], a[4:7], v[68:71]\n\t"
      "v_mfma_f32_16x16x32_bf16 v[36:39], a[36:39], a[0:3], v[36:39]\n\t"
      "ds_read_b128 a[96:99], v1 offset:14336\n\t"
      "v_mfma_f32_16x16x32_bf16 a[140:143], a[36:39], a[172:175], a[140:143]\n\t"
      "v_mfma_f32_16x16x32_bf16 v[40:43], a[40:43], a[0:3], v[40:43]\n\t"
      "v_mfma_f32_16x16x32_bf16 v[72:75], a[40:43], a[4:7], v[72:75]\n\t"
      "ds_read_b64_tr_b16 a[100:101], v2 offset:16384\n\t"
      "v_mfma_f32_16x16x32_bf16 v[104:107], a[40:43], a[8:11], v[104:107]\n\t"
      "v_mfma_f32_16x16x32_bf16 v[136:139], a[40:43], a[12:15], v[136:139]\n\t"
      "ds_read_b64_tr_b16 a[102:103], v2 offset:20480\n\t"
      "v_mfma_f32_16x16x32_bf16 v[168:171], a[40:43], a[16:19], v[168:171]\n\t"
      "v_mfma_f32_16x16x32_bf16 v[200:203], a[40:43], a[20:23], v[200:203]\n\t"
      "v_mfma_f32_16x16x32_bf16 v[232:235], a[40:43], a[24:27], v[232:235]\n\t"
      "ds_read_b64_tr_b16 a[104:105], v3 offset:16384\n\t"
      "v_mfma_f32_16x16x32_bf16 a[200:203], a[40:43], a[28:31], a[200:203]\n\t"
      "v_mfma_f32_16x16x32_bf16 a[144:147], a[40:43], a[172:175], a[144:147]\n\t"
      "ds_read_b64_tr_b16 a[106:107], v3 offset:20480\n\t"
      "v_mfma_f32_16x16x32_bf16 a[204:207], a[44:47], a[28:31], a[204:207]\n\t"
      "v_mfma_f32_16x16x32_bf16 v[236:239], a[44:47], a[24:27], v[236:239]\n\t"
      "v_mfma_f32_16x16x32_bf16 v[204:207], a[44:47], a[20:23], v[204:207]\n\t"
      "ds_read_b64_tr_b16 a[108:109], v4 offset:16384\n\t"
      "v_mfma_f32_16x16x32_bf16 v[172:175], a[44:47], a[16:19], v[172:175]\n\t"
      "v_mfma_f32_16x16x32_bf16 v[140:143], a[44:47], a[12:15], v[140:143]\n\t"
      "ds_read_b64_tr_b16 a[110:111], v4 offset:20480\n\t"
      "v_mfma_f32_16x16x32_bf16 v[108:111], a[44:47], a[8:11], v[108:111]\n\t"
      "v_mfma_f32_16x16x32_bf16 v[76:79], a[44:47], a[4:7], v[76:79]\n\t"
      "ds_read_b64_tr_b16 a[112:113], v5 offset:16384\n\t"
      "v_mfma_f32_16x16x32_bf16 v[44:47], a[44:47], a[0:3], v[44:47]\n\t"
      "v_mfma_f32_16x16x32_bf16 a[148:151], a[44:47], a[172:175], a[148:151]\n\t"
      "v_mfma_f32_16x16x32_bf16 v[48:51], a[48:51], a[0:3], v[48:51]\n\t"
      "ds_read_b64_tr_b16 a[114:115], v5 offset:20480\n\t"
      "v_mfma_f32_16x16x32_bf16 v[80:83], a[48:51], a[4:7], v[80:83]\n\t"
      "v_mfma_f32_16x16x32_bf16 v[112:115], a[48:51], a[8:11], v[112:115]\n\t"
      "ds_read_b64_tr_b16 a[116:117], v2 offset:24576\n\t"
      "v_mfma_f32_16x16x32_bf16 v[144:147], a[48:51], a[12:15], v[144:147]\n\t"
      "v_mfma_f32_16x16x32_bf16 v[176:179], a[48:51], a[16:19], v[176:179]\n\t"
      "v_mfma_f32_16x16x32_bf16 v[208:211], a[48:51], a[20:23], v[208:211]\n\t"
      "ds_read_b64_tr_b16 a[118:119], v2 offset:28672\n\t"
      "v_mfma_f32_16x16x32_bf16 v[240:243], a[48:51], a[24:27], v[240:243]\n\t"
      "v_mfma_f32_16x16x32_bf16 a[208:211], a[48:51], a[28:31], a[208:211]\n\t"
      "ds_read_b64_tr_b16 a[120:121], v3 offset:24576\n\t"
      "v_mfma_f32_16x16x32_bf16 a[152:155], a[48:51], a[172:175], a[152:155]\n\t"
      "v_mfma_f32_16x16x32_bf16 a[212:215], a[52:55], a[28:31], a[212:215]\n\t"
      "ds_read_b64_tr_b16 a[122:123], v3 offset:28672\n\t"
      "v_mfma_f32_16x16x32_bf16 v[244:247], a[52:55], a[24:27], v[244:247]\n\t"
      "v_mfma_f32_16x16x32_bf16 v[212:215], a[52:55], a[20:23], v[212:215]\n\t"
      "v_mfma_f32_16x16x32_bf16 v[180:183], a[52:55], a[16:19], v[180:183]\n\t"
      "ds_read_b64_tr_b16 a[124:125], v4 offset:24576\n\t"
      "v_mfma_f32_16x16x32_bf16 v[148:151], a[52:55], a[12:15], v[148:151]\n\t"
      "v_mfma_f32_16x16x32_bf16 v[116:119], a[52:55], a[8:11], v[116:119]\n\t"
      "ds_read_b64_tr_b16 a[126:127], v4 offset:28672\n\t"
      "v_mfma_f32_16x16x32_bf16 v[84:87], a[52:55], a[4:7], v[84:87]\n\t"
      "v_mfma_f32_16x16x32_bf16 v[52:55], a[52:55], a[0:3], v[52:55]\n\t"
      "v_mfma_f32_16x16x32_bf16 v[56:59], a[56:59], a[0:3], v[56:59]\n\t"
      "ds_read_b64_tr_b16 a[128:129], v5 offset:24576\n\t"
      "v_mfma_f32_16x16x32_bf16 v[88:91], a[56:59], a[4:7], v[88:91]\n\t"
      "v_mfma_f32_16x16x32_bf16 v[120:123], a[56:59], a[8:11], v[120:123]\n\t"
      "ds_read_b64_tr_b16 a[130:131], v5 offset:28672\n\t"
      "v_mfma_f32_16x16x32_bf16 v[152:155], a[56:59], a[12:15], v[152:155]\n\t"
      "v_mfma_f32_16x16x32_bf16 v[184:187], a[56:59], a[16:19], v[184:187]\n\t"
      "v_mfma_f32_16x16x32_bf16 v[216:219], a[56:59], a[20:23], v[216:219]\n\t"
      "ds_read_b128 a[132:135], v7 offset:0\n\t"
      "v_mfma_f32_16x16x32_bf16 v[248:251], a[56:59], a[24:27], v[248:251]\n\t"
      "v_mfma_f32_16x16x32_bf16 a[216:219], a[56:59], a[28:31], a[216:219]\n\t"
      "v_mfma_f32_16x16x32_bf16 a[220:223], a[60:63], a[28:31], a[220:223]\n\t"
      "v_mfma_f32_16x16x32_bf16 v[252:255], a[60:63], a[24:27], v[252:255]\n\t"
      "v_mfma_f32_16x16x32_bf16 v[220:223], a[60:63], a[20:23], v[220:223]\n\t"
      "v_mfma_f32_16x16x32_bf16 v[188:191], a[60:63], a[16:19], v[188:191]\n\t"
      "v_mfma_f32_16x16x32_bf16 v[156:159], a[60:63], a[12:15], v[156:159]\n\t"
      "v_mfma_f32_16x16x32_bf16 v[124:127], a[60:63], a[8:11], v[124:127]\n\t"
      "v_mfma_f32_16x16x32_bf16 v[92:95], a[60:63], a[4:7], v[92:95]\n\t"
      "v_mfma_f32_16x16x32_bf16 v[60:63], a[60:63], a[0:3], v[60:63]\n\t"
      "v_mfma_f32_16x16x32_bf16 a[224:227], a[0:3], a[64:67], a[224:227]\n\t"
      "v_mfma_f32_16x16x32_bf16 a[228:231], a[4:7], a[64:67], a[228:231]\n\t"
      "v_mfma_f32_16x16x32_bf16 a[232:235], a[8:11], a[64:67], a[232:235]\n\t"
      "v_mfma_f32_16x16x32_bf16 a[236:239], a[12:15], a[64:67], a[236:239]\n\t"
      "v_mfma_f32_16x16x32_bf16 a[240:243], a[16:19], a[64:67], a[240:243]\n\t"
      "v_mfma_f32_16x16x32_bf16 a[244:247], a[20:23], a[64:67], a[244:247]\n\t"
      "v_mfma_f32_16x16x32_bf16 a[248:251], a[24:27], a[64:67], a[248:251]\n\t"
      "v_mfma_f32_16x16x32_bf16 a[252:255], a[28:31], a[64:67], a[252:255]\n\t"
      "s_waitcnt vmcnt(4) lgkmcnt(0)\n\t"
      "s_barrier\n\t"
      "v_mfma_f32_16x16x32_bf16 v[32:35], a[100:103], a[68:71], v[32:35]\n\t"
      "ds_read_b128 a[0:3], v0 offset:32768\n\t"
      "v_mfma_f32_16x16x32_bf16 v[64:67], a[100:103], a[72:75], v[64:67]\n\t"
      "v_mfma_f32_16x16x32_bf16 v[96:99], a[100:103], a[76:79], v[96:99]\n\t"
      "ds_read_b128 a[4:7], v0 offset:34816\n\t"
      "buffer_load_dwordx2 v[8:9], v27, s[64:67], 0 offen sc1\n\t"
      "buffer_load_dwordx2 v[10:11], v28, s[64:67], 0 offen sc1\n\t"
      "buffer_load_dwordx2 v[12:13], v29, s[64:67], 0 offen sc1\n\t"
      "v_mfma_f32_16x16x32_bf16 v[128:131], a[100:103], a[80:83], v[128:131]\n\t"
      "v_mfma_f32_16x16x32_bf16 v[160:163], a[100:103], a[84:87], v[160:163]\n\t"
      "ds_read_b128 a[8:11], v0 offset:36864\n\t"
      "v_mfma_f32_16x16x32_bf16 v[192:195], a[100:103], a[88:91], v[192:195]\n\t"
      "v_mfma_f32_16x16x32_bf16 v[224:227], a[100:103], a[92:95], v[224:227]\n\t"
      "v_mfma_f32_16x16x32_bf16 a[192:195], a[100:103], a[96:99], a[192:195]\n\t"
      "ds_read_b128 a[12:15], v0 offset:38912\n\t"
      "v_mfma_f32_16x16x32_bf16 a[136:139], a[100:103], a[172:175], a[136:139]\n\t"
      "v_mfma_f32_16x16x32_bf16 a[196:199], a[104:107], a[96:99], a[196:199]\n\t"
      "ds_read_b128 a[16:19], v0 offset:40960\n\t"
      "v_mfma_f32_16x16x32_bf16 v[228:231], a[104:107], a[92:95], v[228:231]\n\t"
      "v_mfma_f32_16x16x32_bf16 v[196:199], a[104:107], a[88:91], v[196:199]\n\t"
      "v_mfma_f32_16x16x32_bf16 v[164:167], a[104:107], a[84:87], v[164:167]\n\t"
      "ds_read_b128 a[20:23], v0 offset:43008\n\t"
      "v_mfma_f32_16x16x32_bf16 v[132:135], a[104:107], a[80:83], v[132:135]\n\t"
      "v_mfma_f32_16x16x32_bf16 v[100:103], a[104:107], a[76:79], v[100:103]\n\t"
      "ds_read_b128 a[24:27], v0 offset:45056\n\t"
      "v_mfma_f32_16x16x32_bf16 v[68:71], a[104:107], a[72:75], v[68:71]\n\t"
      "v_mfma_f32_16x16x32_bf16 v[36:39], a[104:107], a[68:71], v[36:39]\n\t"
      "ds_read_b128 a[28:31], v0 offset:47104\n\t"
      "v_mfma_f32_16x16x32_bf16 a[140:143], a[104:107], a[172:175], a[140:143]\n\t"
      "v_mfma_f32_16x16x32_bf16 v[40:43], a[108:111], a[68:71], v[40:43]\n\t"
      "v_mfma_f32_16x16x32_bf16 v[72:75], a[108:111], a[72:75], v[72:75]\n\t"
      "ds_read_b64_tr_b16 a[32:33], v2 offset:32768\n\t"
      "v_mfma_f32_16x16x32_bf16 v[104:107], a[108:111], a[76:79], v[104:107]\n\t"
      "v_mfma_f32_16x16x32_bf16 v[136:139], a[108:111], a[80:83], v[136:139]\n\t"
      "ds_read_b64_tr_b16 a[34:35], v2 offset:36864\n\t"
      "v_mfma_f32_16x16x32_bf16 v[168:171], a[108:111], a[84:87], v[168:171]\n\t"
      "v_mfma_f32_16x16x32_bf16 v[200:203], a[108:111], a[88:91], v[200:203]\n\t"
      "v_mfma_f32_16x16x32_bf16 v[232:235], a[108:111], a[92:95], v[232:235]\n\t"
      "ds_read_b64_tr_b16 a[36:37], v3 offset:32768\n\t"
      "v_mfma_f32_16x16x32_bf16 a[200:203], a[108:111], a[96:99], a[200:203]\n\t"
      "v_mfma_f32_16x16x32_bf16 a[144:147], a[108:111], a[172:175], a[144:147]\n\t"
      "ds_read_b64_tr_b16 a[38:39], v3 offset:36864\n\t"
      "v_mfma_f32_16x16x32_bf16 a[204:207], a[112:115], a[96:99], a[204:207]\n\t"
      "v_mfma_f32_16x16x32_bf16 v[236:239], a[112:115], a[92:95], v[236:239]\n\t"
      "v_mfma_f32_16x16x32_bf16 v[204:207], a[112:115], a[88:91], v[204:207]\n\t"
      "ds_read_b64_tr_b16 a[40:41], v4 offset:32768\n\t"
      "v_mfma_f32_16x16x32_bf16 v[172:175], a[112:115], a[84:87], v[172:175]\n\t"
      "v_mfma_f32_16x16x32_bf16 v[140:143], a[112:115], a[80:83], v[140:143]\n\t"
      "ds_read_b64_tr_b16 a[42:43], v4 offset:36864\n\t"
      "v_mfma_f32_16x16x32_bf16 v[108:111], a[112:115], a[76:79], v[108:111]\n\t"
      "v_mfma_f32_16x16x32_bf16 v[76:79], a[112:115], a[72:75], v[76:79]\n\t"
      "ds_read_b64_tr_b16 a[44:45], v5 offset:32768\n\t"
      "v_mfma_f32_16x16x32_bf16 v[44:47], a[112:115], a[68:71], v[44:47]\n\t"
      "v_mfma_f32_16x16x32_bf16 a[148:151], a[112:115], a[172:175], a[148:151]\n\t"
      "v_mfma_f32_16x16x32_bf16 v[48:51], a[116:119], a[68:71], v[48:51]\n\t"
      "ds_read_b64_tr_b16 a[46:47], v5 offset:36864\n\t"
      "v_mfma_f32_16x16x32_bf16 v[80:83], a[116:119], a[72:75], v[80:83]\n\t"
      "v_mfma_f32_16x16x32_bf16 v[112:115], a[116:119], a[76:79], v[112:115]\n\t"
      "ds_read_b64_tr_b16 a[48:49], v2 offset:40960\n\t"
      "v_mfma_f32_16x16x32_bf16 v[144:147], a[116:119], a[80:83], v[144:147]\n\t"
      "v_mfma_f32_16x16x32_bf16 v[176:179], a[116:119], a[84:87], v[176:179]\n\t"
      "v_mfma_f32_16x16x32_bf16 v[208:211], a[116:119], a[88:91], v[208:211]\n\t"
      "ds_read_b64_tr_b16 a[50:51], v2 offset:45056\n\t"
      "v_mfma_f32_16x16x32_bf16 v[240:243], a[116:119], a[92:95], v[240:243]\n\t"
      "v_mfma_f32_16x16x32_bf16 a[208:211], a[116:119], a[96:99], a[208:211]\n\t"
      "ds_read_b64_tr_b16 a[52:53], v3 offset:40960\n\t"
      "v_mfma_f32_16x16x32_bf16 a[152:155], a[116:119], a[172:175], a[152:155]\n\t"
      "v_mfma_f32_16x16x32_bf16 a[212:215], a[120:123], a[96:99], a[212:215]\n\t"
      "ds_read_b64_tr_b16 a[54:55], v3 offset:45056\n\t"
      "v_mfma_f32_16x16x32_bf16 v[244:247], a[120:123], a[92:95], v[244:247]\n\t"
      "v_mfma_f32_16x16x32_bf16 v[212:215], a[120:123], a[88:91], v[212:215]\n\t"
      "v_mfma_f32_16x16x32_bf16 v[180:183], a[120:123], a[84:87], v[180:183]\n\t"
      "ds_read_b64_tr_b16 a[56:57], v4 offset:40960\n\t"
      "v_mfma_f32_16x16x32_bf16 v[148:151], a[120:123], a[80:83], v[148:151]\n\t"
      "v_mfma_f32_16x16x32_bf16 v[116:119], a[120:123], a[76:79], v[116:119]\n\t"
      "ds_read_b64_tr_b16 a[58:59], v4 offset:45056\n\t"
      "v_mfma_f32_16x16x32_bf16 v[84:87], a[120:123], a[72:75], v[84:87]\n\t"
      "v_mfma_f32_16x16x32_bf16 v[52:55], a[120:123], a[68:71], v[52:55]\n\t"
      "v_mfma_f32_16x16x32_bf16 v[56:59], a[124:127], a[68:71], v[56:59]\n\t"
      "ds_read_b64_tr_b16 a[60:61], v5 offset:40960\n\t"
      "v_mfma_f32_16x16x32_bf16 v[88:91], a[124:127], a[72:75], v[88:91]\n\t"
      "v_mfma_f32_16x16x32_bf16 v[120:123], a[124:127], a[76:79], v[120:123]\n\t"
      "ds_read_b64_tr_b16 a[62:63], v5 offset:45056\n\t"
      "v_mfma_f32_16x16x32_bf16 v[152:155], a[124:127], a[80:83], v[152:155]\n\t"
      "v_mfma_f32_16x16x32_bf16 v[184:187], a[124:127], a[84:87], v[184:187]\n\t"
      "v_mfma_f32_16x16x32_bf16 v[216:219], a[124:127], a[88:91], v[216:219]\n\t"
      "ds_read_b128 a[64:67], v6 offset:4096\n\t"
      "v_mfma_f32_16x16x32_bf16 v[248:251], a[124:127], a[92:95], v[248:251]\n\t"
      "v_mfma_f32_16x16x32_bf16 a[216:219], a[124:127], a[96:99], a[216:219]\n\t"
      "v_mfma_f32_16x16x32_bf16 a[220:223], a[128:131], a[96:99], a[220:223]\n\t"
      "v_mfma_f32_16x16x32_bf16 v[252:255], a[128:131], a[92:95], v[252:255]\n\t"
      "v_mfma_f32_16x16x32_bf16 v[220:223], a[128:131], a[88:91], v[220:223]\n\t"
      "v_mfma_f32_16x16x32_bf16 v[188:191], a[128:131], a[84:87], v[188:191]\n\t"
      "v_mfma_f32_16x16x32_bf16 v[156:159], a[128:131], a[80:83], v[156:159]\n\t"
      "v_mfma_f32_16x16x32_bf16 v[124:127], a[128:131], a[76:79], v[124:127]\n\t"
      "v_mfma_f32_16x16x32_bf16 v[92:95], a[128:131], a[72:75], v[92:95]\n\t"
      "v_mfma_f32_16x16x32_bf16 v[60:63], a[128:131], a[68:71], v[60:63]\n\t"
      "v_mfma_f32_16x16x32_bf16 a[224:227], a[68:71], a[132:135], a[224:227]\n\t"
      "v_mfma_f32_16x16x32_bf16 a[228:231], a[72:75], a[132:135], a[228:231]\n\t"
      "v_mfma_f32_16x16x32_bf16 a[232:235], a[76:79], a[132:135], a[232:235]\n\t"
      "v_mfma_f32_16x16x32_bf16 a[236:239], a[80:83], a[132:135], a[236:239]\n\t"
      "v_mfma_f32_16x16x32_bf16 a[240:243], a[84:87], a[132:135], a[240:243]\n\t"
      "v_mfma_f32_16x16x32_bf16 a[244:247], a[88:91], a[132:135], a[244:247]\n\t"
      "v_mfma_f32_16x16x32_bf16 a[248:251], a[92:95], a[132:135], a[248:251]\n\t"
      "v_mfma_f32_16x16x32_bf16 a[252:255], a[96:99], a[132:135], a[252:255]\n\t"
      "s_waitcnt vmcnt(0) lgkmcnt(0)\n\t"
      "s_not_b32 s74, s68\n\t"
      "s_not_b32 s75, s69\n\t"
      "s_cmp_eq_u32 s63, 1\n\t"
      "s_cselect_b32 s74, s68, s74\n\t"
      "s_cselect_b32 s75, s69, s75\n\t"
      "v_mov_b32 v22, s74\n\t"
      "v_mov_b32 v23, s75\n\t"
      "s_mov_b64 exec, 1\n\t"
      "buffer_store_dwordx4 v[20:23], v17, s[64:67], 0 offen sc1\n\t"
      "s_mov_b64 exec, -1\n\t"
      "s_nop 1\n\t"
      "s_mov_b32 s72, 0x10000\n\t"
      "LPA0a_chk_r0_%=:\n\t"
      "v_cmp_ne_u64 vcc, v[8:9], s[68:69]\n\t"
      "s_nop 4\n\t"
      "s_cbranch_vccz LPA0a_ok_r0_%=\n\t"
      "s_sub_u32 s72, s72, 1\n\t"
      "s_cmp_eq_u32 s72, 0\n\t"
      "s_cbranch_scc1 LPA0a_ok_r0_%=\n\t"
      "s_sleep 1\n\t"
      "buffer_load_dwordx2 v[8:9], v27, s[64:67], 0 offen sc1 ; alt\n\t"
      "s_waitcnt vmcnt(0)\n\t"
      "s_branch LPA0a_chk_r0_%=\n\t"
      "LPA0a_ok_r0_%=:\n\t"
      "s_mov_b32 s72, 0x10000\n\t"
      "LPA1a_chk_r0_%=:\n\t"
      "v_cmp_ne_u64 vcc, v[10:11], s[68:69]\n\t"
      "s_nop 4\n\t"
      "s_cbranch_vccz LPA1a_ok_r0_%=\n\t"
      "s_sub_u32 s72, s72, 1\n\t"
      "s_cmp_eq_u32 s72, 0\n\t"
      "s_cbranch_scc1 LPA1a_ok_r0_%=\n\t"
      "s_sleep 1\n\t"
      "buffer_load_dwordx2 v[10:11], v28, s[64:67], 0 offen sc1 ; alt\n\t"
      "s_waitcnt vmcnt(0)\n\t"
      "s_branch LPA1a_chk_r0_%=\n\t"
      "LPA1a_ok_r0_%=:\n\t"
      "s_mov_b32 s72, 0x10000\n\t"
      "LPCa_chk_r0_%=:\n\t"
      "v_cmp_ne_u64 vcc, v[12:13], s[68:69]\n\t"
      "s_nop 4\n\t"
      "s_cbranch_vccz LPCa_ok_r0_%=\n\t"
      "s_sub_u32 s72, s72, 1\n\t"
      "s_cmp_eq_u32 s72, 0\n\t"
      "s_cbranch_scc1 LPCa_ok_r0_%=\n\t"
      "s_sleep 1\n\t"
      "buffer_load_dwordx2 v[12:13], v29, s[64:67], 0 offen sc1 ; alt\n\t"
      "s_waitcnt vmcnt(0)\n\t"
      "s_branch LPCa_chk_r0_%=\n\t"
      "LPCa_ok_r0_%=:\n\t"
      "s_barrier\n\t"
      "v_mfma_f32_16x16x32_bf16 v[32:35], a[32:35], a[0:3], v[32:35]\n\t"
      "ds_read_b128 a[68:71], v1 offset:32768\n\t"
      "v_mfma_f32_16x16x32_bf16 v[64:67], a[32:35], a[4:7], v[64:67]\n\t"
      "v_mfma_f32_16x16x32_bf16 v[96:99], a[32:35], a[8:11], v[96:99]\n\t"
      "ds_read_b128 a[72:75], v1 offset:34816\n\t"
      "s_mov_b32 m0, s70\n\t"
      "v_mfma_f32_16x16x32_bf16 v[128:131], a[32:35], a[12:15], v[128:131]\n\t"
      "buffer_load_dwordx4 v30, s[64:67], 0 offen sc1 lds\n\t"
      "v_mfma_f32_16x16x32_bf16 v[160:163], a[32:35], a[16:19], v[160:163]\n\t"
      "ds_read_b128 a[76:79], v1 offset:36864\n\t"
      "v_mfma_f32_16x16x32_bf16 v[192:195], a[32:35], a[20:23], v[192:195]\n\t"
      "v_mfma_f32_16x16x32_bf16 v[224:227], a[32:35], a[24:27], v[224:227]\n\t"
      "s_mov_b32 m0, s71\n\t"
      "v_mfma_f32_16x16x32_bf16 a[192:195], a[32:35], a[28:31], a[192:195]\n\t"
      "ds_read_b128 a[80:83], v1 offset:38912\n\t"
      "buffer_load_dwordx4 v31, s[64:67], 0 offen sc1 lds\n\t"
      "v_mfma_f32_16x16x32_bf16 a[136:139], a[32:35], a[172:175], a[136:139]\n\t"
      "v_mfma_f32_16x16x32_bf16 a[196:199], a[36:39], a[28:31], a[196:199]\n\t"
      "ds_read_b128 a[84:87], v1 offset:40960\n\t"
      "v_mfma_f32_16x16x32_bf16 v[228:231], a[36:39], a[24:27], v[228:231]\n\t"
      "v_mfma_f32_16x16x32_bf16 v[196:199], a[36:39], a[20:23], v[196:199]\n\t"
      "v_mfma_f32_16x16x32_bf16 v[164:167], a[36:39], a[16:19], v[164:167]\n\t"
      "ds_read_b128 a[88:91], v1 offset:43008\n\t"
      "buffer_load_dwordx4 v31, s[64:67], 0 offen offset:1024 sc1 lds\n\t"
      "v_mfma_f32_16x16x32_bf16 v[132:135], a[36:39], a[12:15], v[132:135]\n\t"
      "v_mfma_f32_16x16x32_bf16 v[100:103], a[36:39], a[8:11], v[100:103]\n\t"
      "ds_read_b128 a[92:95], v1 offset:45056\n\t"
      "v_mfma_f32_16x16x32_bf16 v[68:71], a[36:39], a[4:7], v[68:71]\n\t"
      "v_mfma_f32_16x16x32_bf16 v[36:39], a[36:39], a[0:3], v[36:39]\n\t"
      "ds_read_b128 a[96:99], v1 offset:47104\n\t"
      "v_mfma_f32_16x16x32_bf16 a[140:143], a[36:39], a[172:175], a[140:143]\n\t"
      "v_mfma_f32_16x16x32_bf16 v[40:43], a[40:43], a[0:3], v[40:43]\n\t"
      "v_mfma_f32_16x16x32_bf16 v[72:75], a[40:43], a[4:7], v[72:75]\n\t"
      "ds_read_b64_tr_b16 a[100:101], v2 offset:49152\n\t"
      "v_mfma_f32_16x16x32_bf16 v[104:107], a[40:43], a[8:11], v[104:107]\n\t"
      "v_mfma_f32_16x16x32_bf16 v[136:139], a[40:43], a[12:15], v[136:139]\n\t"
      "ds_read_b64_tr_b16 a[102:103], v2 offset:53248\n\t"
      "v_mfma_f32_16x16x32_bf16 v[168:171], a[40:43], a[16:19], v[168:171]\n\t"
      "v_mfma_f32_16x16x32_bf16 v[200:203], a[40:43], a[20:23], v[200:203]\n\t"
      "v_mfma_f32_16x16x32_bf16 v[232:235], a[40:43], a[24:27], v[232:235]\n\t"
      "ds_read_b64_tr_b16 a[104:105], v3 offset:49152\n\t"
      "v_mfma_f32_16x16x32_bf16 a[200:203], a[40:43], a[28:31], a[200:203]\n\t"
      "v_mfma_f32_16x16x32_bf16 a[144:147], a[40:43], a[172:175], a[144:147]\n\t"
      "ds_read_b64_tr_b16 a[106:107], v3 offset:53248\n\t"
      "v_mfma_f32_16x16x32_bf16 a[204:207], a[44:47], a[28:31], a[204:207]\n\t"
      "v_mfma_f32_16x16x32_bf16 v[236:239], a[44:47], a[24:27], v[236:239]\n\t"
      "v_mfma_f32_16x16x32_bf16 v[204:207], a[44:47], a[20:23], v[204:207]\n\t"
      "ds_read_b64_tr_b16 a[108:109], v4 offset:49152\n\t"
      "v_mfma_f32_16x16x32_bf16 v[172:175], a[44:47], a[16:19], v[172:175]\n\t"
      "v_mfma_f32_16x16x32_bf16 v[140:143], a[44:47], a[12:15], v[140:143]\n\t"
      "ds_read_b64_tr_b16 a[110:111], v4 offset:53248\n\t"
      "v_mfma_f32_16x16x32_bf16 v[108:111], a[44:47], a[8:11], v[108:111]\n\t"
      "v_mfma_f32_16x16x32_bf16 v[76:79], a[44:47], a[4:7], v[76:79]\n\t"
      "ds_read_b64_tr_b16 a[112:113], v5 offset:49152\n\t"
      "v_mfma_f32_16x16x32_bf16 v[44:47], a[44:47], a[0:3], v[44:47]\n\t"
      "v_mfma_f32_16x16x32_bf16 a[148:151], a[44:47], a[172:175], a[148:151]\n\t"
      "v_mfma_f32_16x16x32_bf16 v[48:51], a[48:51], a[0:3], v[48:51]\n\t"
      "ds_read_b64_tr_b16 a[114:115], v5 offset:53248\n\t"
      "v_mfma_f32_16x16x32_bf16 v[80:83], a[48:51], a[4:7], v[80:83]\n\t"
      "v_mfma_f32_16x16x32_bf16 v[112:115], a[48:51], a[8:11], v[112:115]\n\t"
      "ds_read_b64_tr_b16 a[116:117], v2 offset:57344\n\t"
      "v_mfma_f32_16x16x32_bf16 v[144:147], a[48:51], a[12:15], v[144:147]\n\t"
      "v_mfma_f32_16x16x32_bf16 v[176:179], a[48:51], a[16:19], v[176:179]\n\t"
      "v_mfma_f32_16x16x32_bf16 v[208:211], a[48:51], a[20:23], v[208:211]\n\t"
      "ds_read_b64_tr_b16 a[118:119], v2 offset:61440\n\t"
      "v_mfma_f32_16x16x32_bf16 v[240:243], a[48:51], a[24:27], v[240:243]\n\t"
      "v_mfma_f32_16x16x32_bf16 a[208:211], a[48:51], a[28:31], a[208:211]\n\t"
      "ds_read_b64_tr_b16 a[120:121], v3 offset:57344\n\t"
      "v_mfma_f32_16x16x32_bf16 a[152:155], a[48:51], a[172:175], a[152:155]\n\t"
      "v_mfma_f32_16x16x32_bf16 a[212:215], a[52:55], a[28:31], a[212:215]\n\t"
      "ds_read_b64_tr_b16 a[122:123], v3 offset:61440\n\t"
      "v_mfma_f32_16x16x32_bf16 v[244:247], a[52:55], a[24:27], v[244:247]\n\t"
      "v_mfma_f32_16x16x32_bf16 v[212:215], a[52:55], a[20:23], v[212:215]\n\t"
      "v_mfma_f32_16x16x32_bf16 v[180:183], a[52:55], a[16:19], v[180:183]\n\t"
      "ds_read_b64_tr_b16 a[124:125], v4 offset:57344\n\t"
      "v_mfma_f32_16x16x32_bf16 v[148:151], a[52:55], a[12:15], v[148:151]\n\t"
      "v_mfma_f32_16x16x32_bf16 v[116:119], a[52:55], a[8:11], v[116:119]\n\t"
      "ds_read_b64_tr_b16 a[126:127], v4 offset:61440\n\t"
      "v_mfma_f32_16x16x32_bf16 v[84:87], a[52:55], a[4:7], v[84:87]\n\t"
      "v_mfma_f32_16x16x32_bf16 v[52:55], a[52:55], a[0:3], v[52:55]\n\t"
      "v_mfma_f32_16x16x32_bf16 v[56:59], a[56:59], a[0:3], v[56:59]\n\t"
      "ds_read_b64_tr_b16 a[128:129], v5 offset:57344\n\t"
      "v_mfma_f32_16x16x32_bf16 v[88:91], a[56:59], a[4:7], v[88:91]\n\t"
      "v_mfma_f32_16x16x32_bf16 v[120:123], a[56:59], a[8:11], v[120:123]\n\t"
      "ds_read_b64_tr_b16 a[130:131], v5 offset:61440\n\t"
      "v_mfma_f32_16x16x32_bf16 v[152:155], a[56:59], a[12:15], v[152:155]\n\t"
      "v_mfma_f32_16x16x32_bf16 v[184:187], a[56:59], a[16:19], v[184:187]\n\t"
      "v_mfma_f32_16x16x32_bf16 v[216:219], a[56:59], a[20:23], v[216:219]\n\t"
      "ds_read_b128 a[132:135], v7 offset:4096\n\t"
      "v_mfma_f32_16x16x32_bf16 v[248:251], a[56:59], a[24:27], v[248:251]\n\t"
      "v_mfma_f32_16x16x32_bf16 a[216:219], a[56:59], a[28:31], a[216:219]\n\t"
      "v_mfma_f32_16x16x32_bf16 a[220:223], a[60:63], a[28:31], a[220:223]\n\t"
      "v_mfma_f32_16x16x32_bf16 v[252:255], a[60:63], a[24:27], v[252:255]\n\t"
      "v_mfma_f32_16x16x32_bf16 v[220:223], a[60:63], a[20:23], v[220:223]\n\t"
      "v_mfma_f32_16x16x32_bf16 v[188:191], a[60:63], a[16:19], v[188:191]\n\t"
      "v_mfma_f32_16x16x32_bf16 v[156:159], a[60:63], a[12:15], v[156:159]\n\t"
      "v_mfma_f32_16x16x32_bf16 v[124:127], a[60:63], a[8:11], v[124:127]\n\t"
      "v_mfma_f32_16x16x32_bf16 v[92:95], a[60:63], a[4:7], v[92:95]\n\t"
      "v_mfma_f32_16x16x32_bf16 v[60:63], a[60:63], a[0:3], v[60:63]\n\t"
      "v_mfma_f32_16x16x32_bf16 a[224:227], a[0:3], a[64:67], a[224:227]\n\t"
      "v_mfma_f32_16x16x32_bf16 a[228:231], a[4:7], a[64:67], a[228:231]\n\t"
      "v_mfma_f32_16x16x32_bf16 a[232:235], a[8:11], a[64:67], a[232:235]\n\t"
      "v_mfma_f32_16x16x32_bf16 a[236:239], a[12:15], a[64:67], a[236:239]\n\t"
      "v_mfma_f32_16x16x32_bf16 a[240:243], a[16:19], a[64:67], a[240:243]\n\t"
      "v_mfma_f32_16x16x32_bf16 a[244:247], a[20:23], a[64:67], a[244:247]\n\t"
      "v_mfma_f32_16x16x32_bf16 a[248:251], a[24:27], a[64:67], a[248:251]\n\t"
      "v_mfma_f32_16x16x32_bf16 a[252:255], a[28:31], a[64:67], a[252:255]\n\t"
      "s_waitcnt lgkmcnt(0)\n\t"
      "v_mfma_f32_16x16x32_bf16 v[32:35], a[100:103], a[68:71], v[32:35]\n\t"
      "v_mfma_f32_16x16x32_bf16 v[64:67], a[100:103], a[72:75], v[64:67]\n\t"
      "v_mfma_f32_16x16x32_bf16 v[96:99], a[100:103], a[76:79], v[96:99]\n\t"
      "v_mfma_f32_16x16x32_bf16 v[128:131], a[100:103], a[80:83], v[128:131]\n\t"
      "v_mfma_f32_16x16x32_bf16 v[160:163], a[100:103], a[84:87], v[160:163]\n\t"
      "v_mfma_f32_16x16x32_bf16 v[192:195], a[100:103], a[88:91], v[192:195]\n\t"
      "v_mfma_f32_16x16x32_bf16 v[224:227], a[100:103], a[92:95], v[224:227]\n\t"
      "v_mfma_f32_16x16x32_bf16 a[192:195], a[100:103], a[96:99], a[192:195]\n\t"
      "v_mfma_f32_16x16x32_bf16 a[136:139], a[100:103], a[172:175], a[136:139]\n\t"
      "v_mfma_f32_16x16x32_bf16 a[196:199], a[104:107], a[96:99], a[196:199]\n\t"
      "v_mfma_f32_16x16x32_bf16 v[228:231], a[104:107], a[92:95], v[228:231]\n\t"
      "v_mfma_f32_16x16x32_bf16 v[196:199], a[104:107], a[88:91], v[196:199]\n\t"
      "v_mfma_f32_16x16x32_bf16 v[164:167], a[104:107], a[84:87], v[164:167]\n\t"
      "v_mfma_f32_16x16x32_bf16 v[132:135], a[104:107], a[80:83], v[132:135]\n\t"
      "v_mfma_f32_16x16x32_bf16 v[100:103], a[104:107], a[76:79], v[100:103]\n\t"
      "v_mfma_f32_16x16x32_bf16 v[68:71], a[104:107], a[72:75], v[68:71]\n\t"
      "v_mfma_f32_16x16x32_bf16 v[36:39], a[104:107], a[68:71], v[36:39]\n\t"
      "v_mfma_f32_16x16x32_bf16 a[140:143], a[104:107], a[172:175], a[140:143]\n\t"
      "v_mfma_f32_16x16x32_bf16 v[40:43], a[108:111], a[68:71], v[40:43]\n\t"
      "v_mfma_f32_16x16x32_bf16 v[72:75], a[108:111], a[72:75], v[72:75]\n\t"
      "v_mfma_f32_16x16x32_bf16 v[104:107], a[108:111], a[76:79], v[104:107]\n\t"
      "v_mfma_f32_16x16x32_bf16 v[136:139], a[108:111], a[80:83], v[136:139]\n\t"
      "v_mfma_f32_16x16x32_bf16 v[168:171], a[108:111], a[84:87], v[168:171]\n\t"
      "v_mfma_f32_16x16x32_bf16 v[200:203], a[108:111], a[88:91], v[200:203]\n\t"
      "v_mfma_f32_16x16x32_bf16 v[232:235], a[108:111], a[92:95], v[232:235]\n\t"
      "v_mfma_f32_16x16x32_bf16 a[200:203], a[108:111], a[96:99], a[200:203]\n\t"
      "v_mfma_f32_16x16x32_bf16 a[144:147], a[108:111], a[172:175], a[144:147]\n\t"
      "v_mfma_f32_16x16x32_bf16 a[204:207], a[112:115], a[96:99], a[204:207]\n\t"
      "v_mfma_f32_16x16x32_bf16 v[236:239], a[112:115], a[92:95], v[236:239]\n\t"
      "v_mfma_f32_16x16x32_bf16 v[204:207], a[112:115], a[88:91], v[204:207]\n\t"
      "v_mfma_f32_16x16x32_bf16 v[172:175], a[112:115], a[84:87], v[172:175]\n\t"
      "v_mfma_f32_16x16x32_bf16 v[140:143], a[112:115], a[80:83], v[140:143]\n\t"
      "v_mfma_f32_16x16x32_bf16 v[108:111], a[112:115], a[76:79], v[108:111]\n\t"
      "v_mfma_f32_16x16x32_bf16 v[76:79], a[112:115], a[72:75], v[76:79]\n\t"
      "v_mfma_f32_16x16x32_bf16 v[44:47], a[112:115], a[68:71], v[44:47]\n\t"
      "v_mfma_f32_16x16x32_bf16 a[148:151], a[112:115], a[172:175], a[148:151]\n\t"
      "v_mfma_f32_16x16x32_bf16 v[48:51], a[116:119], a[68:71], v[48:51]\n\t"
      "v_mfma_f32_16x16x32_bf16 v[80:83], a[116:119], a[72:75], v[80:83]\n\t"
      "v_mfma_f32_16x16x32_bf16 v[112:115], a[116:119], a[76:79], v[112:115]\n\t"
      "v_mfma_f32_16x16x32_bf16 v[144:147], a[116:119], a[80:83], v[144:147]\n\t"
      "v_mfma_f32_16x16x32_bf16 v[176:179], a[116:119], a[84:87], v[176:179]\n\t"
      "v_mfma_f32_16x16x32_bf16 v[208:211], a[116:119], a[88:91], v[208:211]\n\t"
      "v_mfma_f32_16x16x32_bf16 v[240:243], a[116:119], a[92:95], v[240:243]\n\t"
      "v_mfma_f32_16x16x32_bf16 a[208:211], a[116:119], a[96:99], a[208:211]\n\t"
      "v_mfma_f32_16x16x32_bf16 a[152:155], a[116:119], a[172:175], a[152:155]\n\t"
      "v_mfma_f32_16x16x32_bf16 a[212:215], a[120:123], a[96:99], a[212:215]\n\t"
      "v_mfma_f32_16x16x32_bf16 v[244:247], a[120:123], a[92:95], v[244:247]\n\t"
      "v_mfma_f32_16x16x32_bf16 v[212:215], a[120:123], a[88:91], v[212:215]\n\t"
      "v_mfma_f32_16x16x32_bf16 v[180:183], a[120:123], a[84:87], v[180:183]\n\t"
      "v_mfma_f32_16x16x32_bf16 v[148:151], a[120:123], a[80:83], v[148:151]\n\t"
      "v_mfma_f32_16x16x32_bf16 v[116:119], a[120:123], a[76:79], v[116:119]\n\t"
      "v_mfma_f32_16x16x32_bf16 v[84:87], a[120:123], a[72:75], v[84:87]\n\t"
      "v_mfma_f32_16x16x32_bf16 v[52:55], a[120:123], a[68:71], v[52:55]\n\t"
      "v_mfma_f32_16x16x32_bf16 v[56:59], a[124:127], a[68:71], v[56:59]\n\t"
      "v_mfma_f32_16x16x32_bf16 v[88:91], a[124:127], a[72:75], v[88:91]\n\t"
      "v_mfma_f32_16x16x32_bf16 v[120:123], a[124:127], a[76:79], v[120:123]\n\t"
      "v_mfma_f32_16x16x32_bf16 v[152:155], a[124:127], a[80:83], v[152:155]\n\t"
      "v_mfma_f32_16x16x32_bf16 v[184:187], a[124:127], a[84:87], v[184:187]\n\t"
      "v_mfma_f32_16x16x32_bf16 v[216:219], a[124:127], a[88:91], v[216:219]\n\t"
      "v_mfma_f32_16x16x32_bf16 v[248:251], a[124:127], a[92:95], v[248:251]\n\t"
      "v_mfma_f32_16x16x32_bf16 a[216:219], a[124:127], a[96:99], a[216:219]\n\t"
      "v_mfma_f32_16x16x32_bf16 a[220:223], a[128:131], a[96:99], a[220:223]\n\t"
      "v_mfma_f32_16x16x32_bf16 v[252:255], a[128:131], a[92:95], v[252:255]\n\t"
      "v_mfma_f32_16x16x32_bf16 v[220:223], a[128:131], a[88:91], v[220:223]\n\t"
      "v_mfma_f32_16x16x32_bf16 v[188:191], a[128:131], a[84:87], v[188:191]\n\t"
      "v_mfma_f32_16x16x32_bf16 v[156:159], a[128:131], a[80:83], v[156:159]\n\t"
      "v_mfma_f32_16x16x32_bf16 v[124:127], a[128:131], a[76:79], v[124:127]\n\t"
      "v_mfma_f32_16x16x32_bf16 v[92:95], a[128:131], a[72:75], v[92:95]\n\t"
      "v_mfma_f32_16x16x32_bf16 v[60:63], a[128:131], a[68:71], v[60:63]\n\t"
      "v_mfma_f32_16x16x32_bf16 a[224:227], a[68:71], a[132:135], a[224:227]\n\t"
      "v_mfma_f32_16x16x32_bf16 a[228:231], a[72:75], a[132:135], a[228:231]\n\t"
      "v_mfma_f32_16x16x32_bf16 a[232:235], a[76:79], a[132:135], a[232:235]\n\t"
      "v_mfma_f32_16x16x32_bf16 a[236:239], a[80:83], a[132:135], a[236:239]\n\t"
      "v_mfma_f32_16x16x32_bf16 a[240:243], a[84:87], a[132:135], a[240:243]\n\t"
      "v_mfma_f32_16x16x32_bf16 a[244:247], a[88:91], a[132:135], a[244:247]\n\t"
      "v_mfma_f32_16x16x32_bf16 a[248:251], a[92:95], a[132:135], a[248:251]\n\t"
      "v_mfma_f32_16x16x32_bf16 a[252:255], a[96:99], a[132:135], a[252:255]\n\t"
      "s_nop 7\n\t"
      "v_mbcnt_lo_u32_b32 v22, -1, 0\n\t"
      "v_mbcnt_hi_u32_b32 v22, -1, v22\n\t"
      "v_and_b32 v23, 0x30, v22\n\t"
      "v_add_u32 v23, s82, v23\n\t"
      "v_lshlrev_b32 v22, 2, v22\n\t"
      "v_add_u32 v22, s82, v22\n\t"
      "s_mov_b32 exec_lo, 0x00010001\n\t"
      "s_mov_b32 exec_hi, 0x00010001\n\t"
      "ds_write_b128 v23, a[136:139] offset:0\n\t"
      "ds_write_b128 v23, a[140:143] offset:64\n\t"
      "ds_write_b128 v23, a[144:147] offset:128\n\t"
      "ds_write_b128 v23, a[148:151] offset:192\n\t"
      "ds_write_b128 v23, a[152:155] offset:256\n\t"
      "s_mov_b64 exec, 0xffff\n\t"
      "s_mov_b64 exec, -1\n\t"
      "s_branch LA_end_r0_%=\n\t"
      "LA_tail1_r0_%=:\n\t"
      "s_waitcnt vmcnt(19) lgkmcnt(0)\n\t"
      "s_barrier\n\t"
      "v_mfma_f32_16x16x32_bf16 v[32:35], a[32:35], a[0:3], v[32:35]\n\t"
      "ds_read_b128 a[68:71], v1 offset:0\n\t"
      "v_mfma_f32_16x16x32_bf16 v[64:67], a[32:35], a[4:7], v[64:67]\n\t"
      "v_mfma_f32_16x16x32_bf16 v[96:99], a[32:35], a[8:11], v[96:99]\n\t"
      "ds_read_b128 a[72:75], v1 offset:2048\n\t"
      "v_mfma_f32_16x16x32_bf16 v[128:131], a[32:35], a[12:15], v[128:131]\n\t"
      "v_mfma_f32_16x16x32_bf16 v[160:163], a[32:35], a[16:19], v[160:163]\n\t"
      "ds_read_b128 a[76:79], v1 offset:4096\n\t"
      "v_mfma_f32_16x16x32_bf16 v[192:195], a[32:35], a[20:23], v[192:195]\n\t"
      "v_mfma_f32_16x16x32_bf16 v[224:227], a[32:35], a[24:27], v[224:227]\n\t"
      "v_mfma_f32_16x16x32_bf16 a[192:195], a[32:35], a[28:31], a[192:195]\n\t"
      "ds_read_b128 a[80:83], v1 offset:6144\n\t"
      "v_mfma_f32_16x16x32_bf16 a[196:199], a[36:39], a[28:31], a[196:199]\n\t"
      "v_mfma_f32_16x16x32_bf16 v[228:231], a[36:39], a[24:27], v[228:231]\n\t"
      "ds_read_b128 a[84:87], v1 offset:8192\n\t"
      "v_mfma_f32_16x16x32_bf16 v[196:199], a[36:39], a[20:23], v[196:199]\n\t"
      "v_mfma_f32_16x16x32_bf16 v[164:167], a[36:39], a[16:19], v[164:167]\n\t"
      "v_mfma_f32_16x16x32_bf16 v[132:135], a[36:39], a[12:15], v[132:135]\n\t"
      "ds_read_b128 a[88:91], v1 offset:10240\n\t"
      "v_mfma_f32_16x16x32_bf16 v[100:103], a[36:39], a[8:11], v[100:103]\n\t"
      "v_mfma_f32_16x16x32_bf16 v[68:71], a[36:39], a[4:7], v[68:71]\n\t"
      "ds_read_b128 a[92:95], v1 offset:12288\n\t"
      "v_mfma_f32_16x16x32_bf16 v[36:39], a[36:39], a[0:3], v[36:39]\n\t"
      "v_mfma_f32_16x16x32_bf16 v[40:43], a[40:43], a[0:3], v[40:43]\n\t"
      "ds_read_b128 a[96:99], v1 offset:14336\n\t"
      "v_mfma_f32_16x16x32_bf16 v[72:75], a[40:43], a[4:7], v[72:75]\n\t"
      "v_mfma_f32_16x16x32_bf16 v[104:107], a[40:43], a[8:11], v[104:107]\n\t"
      "v_mfma_f32_16x16x32_bf16 v[136:139], a[40:43], a[12:15], v[136:139]\n\t"
      "ds_read_b64_tr_b16 a[100:101], v2 offset:16384\n\t"
      "v_mfma_f32_16x16x32_bf16 v[168:171], a[40:43], a[16:19], v[168:171]\n\t"
      "v_mfma_f32_16x16x32_bf16 v[200:203], a[40:43], a[20:23], v[200:203]\n\t"
      "ds_read_b64_tr_b16 a[102:103], v2 offset:20480\n\t"
      "v_mfma_f32_16x16x32_bf16 v[232:235], a[40:43], a[24:27], v[232:235]\n\t"
      "v_mfma_f32_16x16x32_bf16 a[200:203], a[40:43], a[28:31], a[200:203]\n\t"
      "v_mfma_f32_16x16x32_bf16 a[204:207], a[44:47], a[28:31], a[204:207]\n\t"
      "ds_read_b64_tr_b16 a[104:105], v3 offset:16384\n\t"
      "v_mfma_f32_16x16x32_bf16 v[236:239], a[44:47], a[24:27], v[236:239]\n\t"
      "v_mfma_f32_16x16x32_bf16 v[204:207], a[44:47], a[20:23], v[204:207]\n\t"
      "ds_read_b64_tr_b16 a[106:107], v3 offset:20480\n\t"
      "v_mfma_f32_16x16x32_bf16 v[172:175], a[44:47], a[16:19], v[172:175]\n\t"
      "v_mfma_f32_16x16x32_bf16 v[140:143], a[44:47], a[12:15], v[140:143]\n\t"
      "v_mfma_f32_16x16x32_bf16 v[108:111], a[44:47], a[8:11], v[108:111]\n\t"
      "ds_read_b64_tr_b16 a[108:109], v4 offset:16384\n\t"
      "v_mfma_f32_16x16x32_bf16 v[76:79], a[44:47], a[4:7], v[76:79]\n\t"
      "v_mfma_f32_16x16x32_bf16 v[44:47], a[44:47], a[0:3], v[44:47]\n\t"
      "ds_read_b64_tr_b16 a[110:111], v4 offset:20480\n\t"
      "v_mfma_f32_16x16x32_bf16 v[48:51], a[48:51], a[0:3], v[48:51]\n\t"
      "v_mfma_f32_16x16x32_bf16 v[80:83], a[48:51], a[4:7], v[80:83]\n\t"
      "ds_read_b64_tr_b16 a[112:113], v5 offset:16384\n\t"
      "v_mfma_f32_16x16x32_bf16 v[112:115], a[48:51], a[8:11], v[112:115]\n\t"
      "v_mfma_f32_16x16x32_bf16 v[144:147], a[48:51], a[12:15], v[144:147]\n\t"
      "v_mfma_f32_16x16x32_bf16 v[176:179], a[48:51], a[16:19], v[176:179]\n\t"
      "ds_read_b64_tr_b16 a[114:115], v5 offset:20480\n\t"
      "v_mfma_f32_16x16x32_bf16 v[208:211], a[48:51], a[20:23], v[208:211]\n\t"
      "v_mfma_f32_16x16x32_bf16 v[240:243], a[48:51], a[24:27], v[240:243]\n\t"
      "ds_read_b64_tr_b16 a[116:117], v2 offset:24576\n\t"
      "v_mfma_f32_16x16x32_bf16 a[208:211], a[48:51], a[28:31], a[208:211]\n\t"
      "v_mfma_f32_16x16x32_bf16 a[212:215], a[52:55], a[28:31], a[212:215]\n\t"
      "v_mfma_f32_16x16x32_bf16 v[244:247], a[52:55], a[24:27], v[244:247]\n\t"
      "ds_read_b64_tr_b16 a[118:119], v2 offset:28672\n\t"
      "v_mfma_f32_16x16x32_bf16 v[212:215], a[52:55], a[20:23], v[212:215]\n\t"
      "v_mfma_f32_16x16x32_bf16 v[180:183], a[52:55], a[16:19], v[180:183]\n\t"
      "ds_read_b64_tr_b16 a[120:121], v3 offset:24576\n\t"
      "v_mfma_f32_16x16x32_bf16 v[148:151], a[52:55], a[12:15], v[148:151]\n\t"
      "v_mfma_f32_16x16x32_bf16 v[116:119], a[52:55], a[8:11], v[116:119]\n\t"
      "ds_read_b64_tr_b16 a[122:123], v3 offset:28672\n\t"
      "v_mfma_f32_16x16x32_bf16 v[84:87], a[52:55], a[4:7], v[84:87]\n\t"
      "v_mfma_f32_16x16x32_bf16 v[52:55], a[52:55], a[0:3], v[52:55]\n\t"
      "v_mfma_f32_16x16x32_bf16 a[156:159], a[52:55], a[172:175], a[156:159]\n\t"
      "ds_read_b64_tr_b16 a[124:125], v4 offset:24576\n\t"
      "v_mfma_f32_16x16x32_bf16 v[56:59], a[56:59], a[0:3], v[56:59]\n\t"
      "v_mfma_f32_16x16x32_bf16 v[88:91], a[56:59], a[4:7], v[88:91]\n\t"
      "ds_read_b64_tr_b16 a[126:127], v4 offset:28672\n\t"
      "v_mfma_f32_16x16x32_bf16 v[120:123], a[56:59], a[8:11], v[120:123]\n\t"
      "v_mfma_f32_16x16x32_bf16 v[152:155], a[56:59], a[12:15], v[152:155]\n\t"
      "v_mfma_f32_16x16x32_bf16 v[184:187], a[56:59], a[16:19], v[184:187]\n\t"
      "ds_read_b64_tr_b16 a[128:129], v5 offset:24576\n\t"
      "v_mfma_f32_16x16x32_bf16 v[216:219], a[56:59], a[20:23], v[216:219]\n\t"
      "v_mfma_f32_16x16x32_bf16 v[248:251], a[56:59], a[24:27], v[248:251]\n\t"
      "ds_read_b64_tr_b16 a[130:131], v5 offset:28672\n\t"
      "v_mfma_f32_16x16x32_bf16 a[216:219], a[56:59], a[28:31], a[216:219]\n\t"
      "v_mfma_f32_16x16x32_bf16 a[160:163], a[56:59], a[172:175], a[160:163]\n\t"
      "v_mfma_f32_16x16x32_bf16 a[220:223], a[60:63], a[28:31], a[220:223]\n\t"
      "ds_read_b128 a[132:135], v7 offset:0\n\t"
      "v_mfma_f32_16x16x32_bf16 v[252:255], a[60:63], a[24:27], v[252:255]\n\t"
      "v_mfma_f32_16x16x32_bf16 v[220:223], a[60:63], a[20:23], v[220:223]\n\t"
      "v_mfma_f32_16x16x32_bf16 v[188:191], a[60:63], a[16:19], v[188:191]\n\t"
      "v_mfma_f32_16x16x32_bf16 v[156:159], a[60:63], a[12:15], v[156:159]\n\t"
      "v_mfma_f32_16x16x32_bf16 v[124:127], a[60:63], a[8:11], v[124:127]\n\t"
      "v_mfma_f32_16x16x32_bf16 v[92:95], a[60:63], a[4:7], v[92:95]\n\t"
      "v_mfma_f32_16x16x32_bf16 v[60:63], a[60:63], a[0:3], v[60:63]\n\t"
      "v_mfma_f32_16x16x32_bf16 a[164:167], a[60:63], a[172:175], a[164:167]\n\t"
      "v_mfma_f32_16x16x32_bf16 a[224:227], a[0:3], a[64:67], a[224:227]\n\t"
      "v_mfma_f32_16x16x32_bf16 a[228:231], a[4:7], a[64:67], a[228:231]\n\t"
      "v_mfma_f32_16x16x32_bf16 a[232:235], a[8:11], a[64:67], a[232:235]\n\t"
      "v_mfma_f32_16x16x32_bf16 a[236:239], a[12:15], a[64:67], a[236:239]\n\t"
      "v_mfma_f32_16x16x32_bf16 a[240:243], a[16:19], a[64:67], a[240:243]\n\t"
      "v_mfma_f32_16x16x32_bf16 a[244:247], a[20:23], a[64:67], a[244:247]\n\t"
      "v_mfma_f32_16x16x32_bf16 a[248:251], a[24:27], a[64:67], a[248:251]\n\t"
      "v_mfma_f32_16x16x32_bf16 a[252:255], a[28:31], a[64:67], a[252:255]\n\t"
      "v_mfma_f32_16x16x32_bf16 a[168:171], a[172:175], a[64:67], a[168:171]\n\t"
      "s_waitcnt vmcnt(4) lgkmcnt(0)\n\t"
      "s_barrier\n\t"
      "v_mfma_f32_16x16x32_bf16 v[32:35], a[100:103], a[68:71], v[32:35]\n\t"
      "ds_read_b128 a[0:3], v0 offset:32768\n\t"
      "v_mfma_f32_16x16x32_bf16 v[64:67], a[100:103], a[72:75], v[64:67]\n\t"
      "v_mfma_f32_16x16x32_bf16 v[96:99], a[100:103], a[76:79], v[96:99]\n\t"
      "ds_read_b128 a[4:7], v0 offset:34816\n\t"
      "buffer_load_dwordx2 v[8:9], v27, s[64:67], 0 offen sc1\n\t"
      "buffer_load_dwordx2 v[10:11], v28, s[64:67], 0 offen sc1\n\t"
      "buffer_load_dwordx2 v[12:13], v29, s[64:67], 0 offen sc1\n\t"
      "v_mfma_f32_16x16x32_bf16 v[128:131], a[100:103], a[80:83], v[128:131]\n\t"
      "v_mfma_f32_16x16x32_bf16 v[160:163], a[100:103], a[84:87], v[160:163]\n\t"
      "ds_read_b128 a[8:11], v0 offset:36864\n\t"
      "v_mfma_f32_16x16x32_bf16 v[192:195], a[100:103], a[88:91], v[192:195]\n\t"
      "v_mfma_f32_16x16x32_bf16 v[224:227], a[100:103], a[92:95], v[224:227]\n\t"
      "v_mfma_f32_16x16x32_bf16 a[192:195], a[100:103], a[96:99], a[192:195]\n\t"
      "ds_read_b128 a[12:15], v0 offset:38912\n\t"
      "v_mfma_f32_16x16x32_bf16 a[196:199], a[104:107], a[96:99], a[196:199]\n\t"
      "v_mfma_f32_16x16x32_bf16 v[228:231], a[104:107], a[92:95], v[228:231]\n\t"
      "ds_read_b128 a[16:19], v0 offset:40960\n\t"
      "v_mfma_f32_16x16x32_bf16 v[196:199], a[104:107], a[88:91], v[196:199]\n\t"
      "v_mfma_f32_16x16x32_bf16 v[164:167], a[104:107], a[84:87], v[164:167]\n\t"
      "v_mfma_f32_16x16x32_bf16 v[132:135], a[104:107], a[80:83], v[132:135]\n\t"
      "ds_read_b128 a[20:23], v0 offset:43008\n\t"
      "v_mfma_f32_16x16x32_bf16 v[100:103], a[104:107], a[76:79], v[100:103]\n\t"
      "v_mfma_f32_16x16x32_bf16 v[68:71], a[104:107], a[72:75], v[68:71]\n\t"
      "ds_read_b128 a[24:27], v0 offset:45056\n\t"
      "v_mfma_f32_16x16x32_bf16 v[36:39], a[104:107], a[68:71], v[36:39]\n\t"
      "v_mfma_f32_16x16x32_bf16 v[40:43], a[108:111], a[68:71], v[40:43]\n\t"
      "ds_read_b128 a[28:31], v0 offset:47104\n\t"
      "v_mfma_f32_16x16x32_bf16 v[72:75], a[108:111], a[72:75], v[72:75]\n\t"
      "v_mfma_f32_16x16x32_bf16 v[104:107], a[108:111], a[76:79], v[104:107]\n\t"
      "v_mfma_f32_16x16x32_bf16 v[136:139], a[108:111], a[80:83], v[136:139]\n\t"
      "ds_read_b64_tr_b16 a[32:33], v2 offset:32768\n\t"
      "v_mfma_f32_16x16x32_bf16 v[168:171], a[108:111], a[84:87], v[168:171]\n\t"
      "v_mfma_f32_16x16x32_bf16 v[200:203], a[108:111], a[88:91], v[200:203]\n\t"
      "ds_read_b64_tr_b16 a[34:35], v2 offset:36864\n\t"
      "v_mfma_f32_16x16x32_bf16 v[232:235], a[108:111], a[92:95], v[232:235]\n\t"
      "v_mfma_f32_16x16x32_bf16 a[200:203], a[108:111], a[96:99], a[200:203]\n\t"
      "v_mfma_f32_16x16x32_bf16 a[204:207], a[112:115], a[96:99], a[204:207]\n\t"
      "ds_read_b64_tr_b16 a[36:37], v3 offset:32768\n\t"
      "v_mfma_f32_16x16x32_bf16 v[236:239], a[112:115], a[92:95], v[236:239]\n\t"
      "v_mfma_f32_16x16x32_bf16 v[204:207], a[112:115], a[88:91], v[204:207]\n\t"
      "ds_read_b64_tr_b16 a[38:39], v3 offset:36864\n\t"
      "v_mfma_f32_16x16x32_bf16 v[172:175], a[112:115], a[84:87], v[172:175]\n\t"
      "v_mfma_f32_16x16x32_bf16 v[140:143], a[112:115], a[80:83], v[140:143]\n\t"
      "v_mfma_f32_16x16x32_bf16 v[108:111], a[112:115], a[76:79], v[108:111]\n\t"
      "ds_read_b64_tr_b16 a[40:41], v4 offset:32768\n\t"
      "v_mfma_f32_16x16x32_bf16 v[76:79], a[112:115], a[72:75], v[76:79]\n\t"
      "v_mfma_f32_16x16x32_bf16 v[44:47], a[112:115], a[68:71], v[44:47]\n\t"
      "ds_read_b64_tr_b16 a[42:43], v4 offset:36864\n\t"
      "v_mfma_f32_16x16x32_bf16 v[48:51], a[116:119], a[68:71], v[48:51]\n\t"
      "v_mfma_f32_16x16x32_bf16 v[80:83], a[116:119], a[72:75], v[80:83]\n\t"
      "ds_read_b64_tr_b16 a[44:45], v5 offset:32768\n\t"
      "v_mfma_f32_16x16x32_bf16 v[112:115], a[116:119], a[76:79], v[112:115]\n\t"
      "v_mfma_f32_16x16x32_bf16 v[144:147], a[116:119], a[80:83], v[144:147]\n\t"
      "v_mfma_f32_16x16x32_bf16 v[176:179], a[116:119], a[84:87], v[176:179]\n\t"
      "ds_read_b64_tr_b16 a[46:47], v5 offset:36864\n\t"
      "v_mfma_f32_16x16x32_bf16 v[208:211], a[116:119], a[88:91], v[208:211]\n\t"
      "v_mfma_f32_16x16x32_bf16 v[240:243], a[116:119], a[92:95], v[240:243]\n\t"
      "ds_read_b64_tr_b16 a[48:49], v2 offset:40960\n\t"
      "v_mfma_f32_16x16x32_bf16 a[208:211], a[116:119], a[96:99], a[208:211]\n\t"
      "v_mfma_f32_16x16x32_bf16 a[212:215], a[120:123], a[96:99], a[212:215]\n\t"
      "v_mfma_f32_16x16x32_bf16 v[244:247], a[120:123], a[92:95], v[244:247]\n\t"
      "ds_read_b64_tr_b16 a[50:51], v2 offset:45056\n\t"
      "v_mfma_f32_16x16x32_bf16 v[212:215], a[120:123], a[88:91], v[212:215]\n\t"
      "v_mfma_f32_16x16x32_bf16 v[180:183], a[120:123], a[84:87], v[180:183]\n\t"
      "ds_read_b64_tr_b16 a[52:53], v3 offset:40960\n\t"
      "v_mfma_f32_16x16x32_bf16 v[148:151], a[120:123], a[80:83], v[148:151]\n\t"
      "v_mfma_f32_16x16x32_bf16 v[116:119], a[120:123], a[76:79], v[116:119]\n\t"
      "ds_read_b64_tr_b16 a[54:55], v3 offset:45056\n\t"
      "v_mfma_f32_16x16x32_bf16 v[84:87], a[120:123], a[72:75], v[84:87]\n\t"
      "v_mfma_f32_16x16x32_bf16 v[52:55], a[120:123], a[68:71], v[52:55]\n\t"
      "v_mfma_f32_16x16x32_bf16 a[156:159], a[120:123], a[172:175], a[156:159]\n\t"
      "ds_read_b64_tr_b16 a[56:57], v4 offset:40960\n\t"
      "v_mfma_f32_16x16x32_bf16 v[56:59], a[124:127], a[68:71], v[56:59]\n\t"
      "v_mfma_f32_16x16x32_bf16 v[88:91], a[124:127], a[72:75], v[88:91]\n\t"
      "ds_read_b64_tr_b16 a[58:59], v4 offset:45056\n\t"
      "v_mfma_f32_16x16x32_bf16 v[120:123], a[124:127], a[76:79], v[120:123]\n\t"
      "v_mfma_f32_16x16x32_bf16 v[152:155], a[124:127], a[80:83], v[152:155]\n\t"
      "v_mfma_f32_16x16x32_bf16 v[184:187], a[124:127], a[84:87], v[184:187]\n\t"
      "ds_read_b64_tr_b16 a[60:61], v5 offset:40960\n\t"
      "v_mfma_f32_16x16x32_bf16 v[216:219], a[124:127], a[88:91], v[216:219]\n\t"
      "v_mfma_f32_16x16x32_bf16 v[248:251], a[124:127], a[92:95], v[248:251]\n\t"
      "ds_read_b64_tr_b16 a[62:63], v5 offset:45056\n\t"
      "v_mfma_f32_16x16x32_bf16 a[216:219], a[124:127], a[96:99], a[216:219]\n\t"
      "v_mfma_f32_16x16x32_bf16 a[160:163], a[124:127], a[172:175], a[160:163]\n\t"
      "v_mfma_f32_16x16x32_bf16 a[220:223], a[128:131], a[96:99], a[220:223]\n\t"
      "ds_read_b128 a[64:67], v6 offset:4096\n\t"
      "v_mfma_f32_16x16x32_bf16 v[252:255], a[128:131], a[92:95], v[252:255]\n\t"
      "v_mfma_f32_16x16x32_bf16 v[220:223], a[128:131], a[88:91], v[220:223]\n\t"
      "v_mfma_f32_16x16x32_bf16 v[188:191], a[128:131], a[84:87], v[188:191]\n\t"
      "v_mfma_f32_16x16x32_bf16 v[156:159], a[128:131], a[80:83], v[156:159]\n\t"
      "v_mfma_f32_16x16x32_bf16 v[124:127], a[128:131], a[76:79], v[124:127]\n\t"
      "v_mfma_f32_16x16x32_bf16 v[92:95], a[128:131], a[72:75], v[92:95]\n\t"
      "v_mfma_f32_16x16x32_bf16 v[60:63], a[128:131], a[68:71], v[60:63]\n\t"
      "v_mfma_f32_16x16x32_bf16 a[164:167], a[128:131], a[172:175], a[164:167]\n\t"
      "v_mfma_f32_16x16x32_bf16 a[224:227], a[68:71], a[132:135], a[224:227]\n\t"
      "v_mfma_f32_16x16x32_bf16 a[228:231], a[72:75], a[132:135], a[228:231]\n\t"
      "v_mfma_f32_16x16x32_bf16 a[232:235], a[76:79], a[132:135], a[232:235]\n\t"
      "v_mfma_f32_16x16x32_bf16 a[236:239], a[80:83], a[132:135], a[236:239]\n\t"
      "v_mfma_f32_16x16x32_bf16 a[240:243], a[84:87], a[132:135], a[240:243]\n\t"
      "v_mfma_f32_16x16x32_bf16 a[244:247], a[88:91], a[132:135], a[244:247]\n\t"
      "v_mfma_f32_16x16x32_bf16 a[248:251], a[92:95], a[132:135], a[248:251]\n\t"
      "v_mfma_f32_16x16x32_bf16 a[252:255], a[96:99], a[132:135], a[252:255]\n\t"
      "v_mfma_f32_16x16x32_bf16 a[168:171], a[172:175], a[132:135], a[168:171]\n\t"
      "s_waitcnt vmcnt(0) lgkmcnt(0)\n\t"
      "s_not_b32 s74, s68\n\t"
      "s_not_b32 s75, s69\n\t"
      "s_cmp_eq_u32 s63, 1\n\t"
      "s_cselect_b32 s74, s68, s74\n\t"
      "s_cselect_b32 s75, s69, s75\n\t"
      "v_mov_b32 v22, s74\n\t"
      "v_mov_b32 v23, s75\n\t"
      "s_mov_b64 exec, 1\n\t"
      "buffer_store_dwordx4 v[20:23], v17, s[64:67], 0 offen sc1\n\t"
      "s_mov_b64 exec, -1\n\t"
      "s_nop 1\n\t"
      "s_mov_b32 s72, 0x10000\n\t"
      "LPA0b_chk_r0_%=:\n\t"
      "v_cmp_ne_u64 vcc, v[8:9], s[68:69]\n\t"
      "s_nop 4\n\t"
      "s_cbranch_vccz LPA0b_ok_r0_%=\n\t"
      "s_sub_u32 s72, s72, 1\n\t"
      "s_cmp_eq_u32 s72, 0\n\t"
      "s_cbranch_scc1 LPA0b_ok_r0_%=\n\t"
      "s_sleep 1\n\t"
      "buffer_load_dwordx2 v[8:9], v27, s[64:67], 0 offen sc1 ; alt\n\t"
      "s_waitcnt vmcnt(0)\n\t"
      "s_branch LPA0b_chk_r0_%=\n\t"
      "LPA0b_ok_r0_%=:\n\t"
      "s_mov_b32 s72, 0x10000\n\t"
      "LPA1b_chk_r0_%=:\n\t"
      "v_cmp_ne_u64 vcc, v[10:11], s[68:69]\n\t"
      "s_nop 4\n\t"
      "s_cbranch_vccz LPA1b_ok_r0_%=\n\t"
      "s_sub_u32 s72, s72, 1\n\t"
      "s_cmp_eq_u32 s72, 0\n\t"
      "s_cbranch_scc1 LPA1b_ok_r0_%=\n\t"
      "s_sleep 1\n\t"
      "buffer_load_dwordx2 v[10:11], v28, s[64:67], 0 offen sc1 ; alt\n\t"
      "s_waitcnt vmcnt(0)\n\t"
      "s_branch LPA1b_chk_r0_%=\n\t"
      "LPA1b_ok_r0_%=:\n\t"
      "s_mov_b32 s72, 0x10000\n\t"
      "LPCb_chk_r0_%=:\n\t"
      "v_cmp_ne_u64 vcc, v[12:13], s[68:69]\n\t"
      "s_nop 4\n\t"
      "s_cbranch_vccz LPCb_ok_r0_%=\n\t"
      "s_sub_u32 s72, s72, 1\n\t"
      "s_cmp_eq_u32 s72, 0\n\t"
      "s_cbranch_scc1 LPCb_ok_r0_%=\n\t"
      "s_sleep 1\n\t"
      "buffer_load_dwordx2 v[12:13], v29, s[64:67], 0 offen sc1 ; alt\n\t"
      "s_waitcnt vmcnt(0)\n\t"
      "s_branch LPCb_chk_r0_%=\n\t"
      "LPCb_ok_r0_%=:\n\t"
      "s_barrier\n\t"
      "v_mfma_f32_16x16x32_bf16 v[32:35], a[32:35], a[0:3], v[32:35]\n\t"
      "ds_read_b128 a[68:71], v1 offset:32768\n\t"
      "v_mfma_f32_16x16x32_bf16 v[64:67], a[32:35], a[4:7], v[64:67]\n\t"
      "v_mfma_f32_16x16x32_bf16 v[96:99], a[32:35], a[8:11], v[96:99]\n\t"
      "ds_read_b128 a[72:75], v1 offset:34816\n\t"
      "s_mov_b32 m0, s70\n\t"
      "v_mfma_f32_16x16x32_bf16 v[128:131], a[32:35], a[12:15], v[128:131]\n\t"
      "buffer_load_dwordx4 v30, s[64:67], 0 offen sc1 lds\n\t"
      "v_mfma_f32_16x16x32_bf16 v[160:163], a[32:35], a[16:19], v[160:163]\n\t"
      "ds_read_b128 a[76:79], v1 offset:36864\n\t"
      "v_mfma_f32_16x16x32_bf16 v[192:195], a[32:35], a[20:23], v[192:195]\n\t"
      "v_mfma_f32_16x16x32_bf16 v[224:227], a[32:35], a[24:27], v[224:227]\n\t"
      "s_mov_b32 m0, s71\n\t"
      "v_mfma_f32_16x16x32_bf16 a[192:195], a[32:35], a[28:31], a[192:195]\n\t"
      "ds_read_b128 a[80:83], v1 offset:38912\n\t"
      "buffer_load_dwordx4 v31, s[64:67], 0 offen sc1 lds\n\t"
      "v_mfma_f32_16x16x32_bf16 a[196:199], a[36:39], a[28:31], a[196:199]\n\t"
      "v_mfma_f32_16x16x32_bf16 v[228:231], a[36:39], a[24:27], v[228:231]\n\t"
      "ds_read_b128 a[84:87], v1 offset:40960\n\t"
      "v_mfma_f32_16x16x32_bf16 v[196:199], a[36:39], a[20:23], v[196:199]\n\t"
      "v_mfma_f32_16x16x32_bf16 v[164:167], a[36:39], a[16:19], v[164:167]\n\t"
      "v_mfma_f32_16x16x32_bf16 v[132:135], a[36:39], a[12:15], v[132:135]\n\t"
      "ds_read_b128 a[88:91], v1 offset:43008\n\t"
      "buffer_load_dwordx4 v31, s[64:67], 0 offen offset:1024 sc1 lds\n\t"
      "v_mfma_f32_16x16x32_bf16 v[100:103], a[36:39], a[8:11], v[100:103]\n\t"
      "v_mfma_f32_16x16x32_bf16 v[68:71], a[36:39], a[4:7], v[68:71]\n\t"
      "ds_read_b128 a[92:95], v1 offset:45056\n\t"
      "v_mfma_f32_16x16x32_bf16 v[36:39], a[36:39], a[0:3], v[36:39]\n\t"
      "v_mfma_f32_16x16x32_bf16 v[40:43], a[40:43], a[0:3], v[40:43]\n\t"
      "ds_read_b128 a[96:99], v1 offset:47104\n\t"
      "v_mfma_f32_16x16x32_bf16 v[72:75], a[40:43], a[4:7], v[72:75]\n\t"
      "v_mfma_f32_16x16x32_bf16 v[104:107], a[40:43], a[8:11], v[104:107]\n\t"
      "v_mfma_f32_16x16x32_bf16 v[136:139], a[40:43], a[12:15], v[136:139]\n\t"
      "ds_read_b64_tr_b16 a[100:101], v2 offset:49152\n\t"
      "v_mfma_f32_16x16x32_bf16 v[168:171], a[40:43], a[16:19], v[168:171]\n\t"
      "v_mfma_f32_16x16x32_bf16 v[200:203], a[40:43], a[20:23], v[200:203]\n\t"
      "ds_read_b64_tr_b16 a[102:103], v2 offset:53248\n\t"
      "v_mfma_f32_16x16x32_bf16 v[232:235], a[40:43], a[24:27], v[232:235]\n\t"
      "v_mfma_f32_16x16x32_bf16 a[200:203], a[40:43], a[28:31], a[200:203]\n\t"
      "v_mfma_f32_16x16x32_bf16 a[204:207], a[44:47], a[28:31], a[204:207]\n\t"
      "ds_read_b64_tr_b16 a[104:105], v3 offset:49152\n\t"
      "v_mfma_f32_16x16x32_bf16 v[236:239], a[44:47], a[24:27], v[236:239]\n\t"
      "v_mfma_f32_16x16x32_bf16 v[204:207], a[44:47], a[20:23], v[204:207]\n\t"
      "ds_read_b64_tr_b16 a[106:107], v3 offset:53248\n\t"
      "v_mfma_f32_16x16x32_bf16 v[172:175], a[44:47], a[16:19], v[172:175]\n\t"
      "v_mfma_f32_16x16x32_bf16 v[140:143], a[44:47], a[12:15], v[140:143]\n\t"
      "v_mfma_f32_16x16x32_bf16 v[108:111], a[44:47], a[8:11], v[108:111]\n\t"
      "ds_read_b64_tr_b16 a[108:109], v4 offset:49152\n\t"
      "v_mfma_f32_16x16x32_bf16 v[76:79], a[44:47], a[4:7], v[76:79]\n\t"
      "v_mfma_f32_16x16x32_bf16 v[44:47], a[44:47], a[0:3], v[44:47]\n\t"
      "ds_read_b64_tr_b16 a[110:111], v4 offset:53248\n\t"
      "v_mfma_f32_16x16x32_bf16 v[48:51], a[48:51], a[0:3], v[48:51]\n\t"
      "v_mfma_f32_16x16x32_bf16 v[80:83], a[48:51], a[4:7], v[80:83]\n\t"
      "ds_read_b64_tr_b16 a[112:113], v5 offset:49152\n\t"
      "v_mfma_f32_16x16x32_bf16 v[112:115], a[48:51], a[8:11], v[112:115]\n\t"
      "v_mfma_f32_16x16x32_bf16 v[144:147], a[48:51], a[12:15], v[144:147]\n\t"
      "v_mfma_f32_16x16x32_bf16 v[176:179], a[48:51], a[16:19], v[176:179]\n\t"
      "ds_read_b64_tr_b16 a[114:115], v5 offset:53248\n\t"
      "v_mfma_f32_16x16x32_bf16 v[208:211], a[48:51], a[20:23], v[208:211]\n\t"
      "v_mfma_f32_16x16x32_bf16 v[240:243], a[48:51], a[24:27], v[240:243]\n\t"
      "ds_read_b64_tr_b16 a[116:117], v2 offset:57344\n\t"
      "v_mfma_f32_16x16x32_bf16 a[208:211], a[48:51], a[28:31], a[208:211]\n\t"
      "v_mfma_f32_16x16x32_bf16 a[212:215], a[52:55], a[28:31], a[212:215]\n\t"
      "v_mfma_f32_16x16x32_bf16 v[244:247], a[52:55], a[24:27], v[244:247]\n\t"
      "ds_read_b64_tr_b16 a[118:119], v2 offset:61440\n\t"
      "v_mfma_f32_16x16x32_bf16 v[212:215], a[52:55], a[20:23], v[212:215]\n\t"
      "v_mfma_f32_16x16x32_bf16 v[180:183], a[52:55], a[16:19], v[180:183]\n\t"
      "ds_read_b64_tr_b16 a[120:121], v3 offset:57344\n\t"
      "v_mfma_f32_16x16x32_bf16 v[148:151], a[52:55], a[12:15], v[148:151]\n\t"
      "v_mfma_f32_16x16x32_bf16 v[116:119], a[52:55], a[8:11], v[116:119]\n\t"
      "ds_read_b64_tr_b16 a[122:123], v3 offset:61440\n\t"
      "v_mfma_f32_16x16x32_bf16 v[84:87], a[52:55], a[4:7], v[84:87]\n\t"
      "v_mfma_f32_16x16x32_bf16 v[52:55], a[52:55], a[0:3], v[52:55]\n\t"
      "v_mfma_f32_16x16x32_bf16 a[156:159], a[52:55], a[172:175], a[156:159]\n\t"
      "ds_read_b64_tr_b16 a[124:125], v4 offset:57344\n\t"
      "v_mfma_f32_16x16x32_bf16 v[56:59], a[56:59], a[0:3], v[56:59]\n\t"
      "v_mfma_f32_16x16x32_bf16 v[88:91], a[56:59], a[4:7], v[88:91]\n\t"
      "ds_read_b64_tr_b16 a[126:127], v4 offset:61440\n\t"
      "v_mfma_f32_16x16x32_bf16 v[120:123], a[56:59], a[8:11], v[120:123]\n\t"
      "v_mfma_f32_16x16x32_bf16 v[152:155], a[56:59], a[12:15], v[152:155]\n\t"
      "v_mfma_f32_16x16x32_bf16 v[184:187], a[56:59], a[16:19], v[184:187]\n\t"
      "ds_read_b64_tr_b16 a[128:129], v5 offset:57344\n\t"
      "v_mfma_f32_16x16x32_bf16 v[216:219], a[56:59], a[20:23], v[216:219]\n\t"
      "v_mfma_f32_16x16x32_bf16 v[248:251], a[56:59], a[24:27], v[248:251]\n\t"
      "ds_read_b64_tr_b16 a[130:131], v5 offset:61440\n\t"
      "v_mfma_f32_16x16x32_bf16 a[216:219], a[56:59], a[28:31], a[216:219]\n\t"
      "v_mfma_f32_16x16x32_bf16 a[160:163], a[56:59], a[172:175], a[160:163]\n\t"
      "v_mfma_f32_16x16x32_bf16 a[220:223], a[60:63], a[28:31], a[220:223]\n\t"
      "ds_read_b128 a[132:135], v7 offset:4096\n\t"
      "v_mfma_f32_16x16x32_bf16 v[252:255], a[60:63], a[24:27], v[252:255]\n\t"
      "v_mfma_f32_16x16x32_bf16 v[220:223], a[60:63], a[20:23], v[220:223]\n\t"
      "v_mfma_f32_16x16x32_bf16 v[188:191], a[60:63], a[16:19], v[188:191]\n\t"
      "v_mfma_f32_16x16x32_bf16 v[156:159], a[60:63], a[12:15], v[156:159]\n\t"
      "v_mfma_f32_16x16x32_bf16 v[124:127], a[60:63], a[8:11], v[124:127]\n\t"
      "v_mfma_f32_16x16x32_bf16 v[92:95], a[60:63], a[4:7], v[92:95]\n\t"
      "v_mfma_f32_16x16x32_bf16 v[60:63], a[60:63], a[0:3], v[60:63]\n\t"
      "v_mfma_f32_16x16x32_bf16 a[164:167], a[60:63], a[172:175], a[164:167]\n\t"
      "v_mfma_f32_16x16x32_bf16 a[224:227], a[0:3], a[64:67], a[224:227]\n\t"
      "v_mfma_f32_16x16x32_bf16 a[228:231], a[4:7], a[64:67], a[228:231]\n\t"
      "v_mfma_f32_16x16x32_bf16 a[232:235], a[8:11], a[64:67], a[232:235]\n\t"
      "v_mfma_f32_16x16x32_bf16 a[236:239], a[12:15], a[64:67], a[236:239]\n\t"
      "v_mfma_f32_16x16x32_bf16 a[240:243], a[16:19], a[64:67], a[240:243]\n\t"
      "v_mfma_f32_16x16x32_bf16 a[244:247], a[20:23], a[64:67], a[244:247]\n\t"
      "v_mfma_f32_16x16x32_bf16 a[248:251], a[24:27], a[64:67], a[248:251]\n\t"
      "v_mfma_f32_16x16x32_bf16 a[252:255], a[28:31], a[64:67], a[252:255]\n\t"
      "v_mfma_f32_16x16x32_bf16 a[168:171], a[172:175], a[64:67], a[168:171]\n\t"
      "s_waitcnt lgkmcnt(0)\n\t"
      "v_mfma_f32_16x16x32_bf16 v[32:35], a[100:103], a[68:71], v[32:35]\n\t"
      "v_mfma_f32_16x16x32_bf16 v[64:67], a[100:103], a[72:75], v[64:67]\n\t"
      "v_mfma_f32_16x16x32_bf16 v[96:99], a[100:103], a[76:79], v[96:99]\n\t"
      "v_mfma_f32_16x16x32_bf16 v[128:131], a[100:103], a[80:83], v[128:131]\n\t"
      "v_mfma_f32_16x16x32_bf16 v[160:163], a[100:103], a[84:87], v[160:163]\n\t"
      "v_mfma_f32_16x16x32_bf16 v[192:195], a[100:103], a[88:91], v[192:195]\n\t"
      "v_mfma_f32_16x16x32_bf16 v[224:227], a[100:103], a[92:95], v[224:227]\n\t"
      "v_mfma_f32_16x16x32_bf16 a[192:195], a[100:103], a[96:99], a[192:195]\n\t"
      "v_mfma_f32_16x16x32_bf16 a[196:199], a[104:107], a[96:99], a[196:199]\n\t"
      "v_mfma_f32_16x16x32_bf16 v[228:231], a[104:107], a[92:95], v[228:231]\n\t"
      "v_mfma_f32_16x16x32_bf16 v[196:199], a[104:107], a[88:91], v[196:199]\n\t"
      "v_mfma_f32_16x16x32_bf16 v[164:167], a[104:107], a[84:87], v[164:167]\n\t"
      "v_mfma_f32_16x16x32_bf16 v[132:135], a[104:107], a[80:83], v[132:135]\n\t"
      "v_mfma_f32_16x16x32_bf16 v[100:103], a[104:107], a[76:79], v[100:103]\n\t"
      "v_mfma_f32_16x16x32_bf16 v[68:71], a[104:107], a[72:75], v[68:71]\n\t"
      "v_mfma_f32_16x16x32_bf16 v[36:39], a[104:107], a[68:71], v[36:39]\n\t"
      "v_mfma_f32_16x16x32_bf16 v[40:43], a[108:111], a[68:71], v[40:43]\n\t"
      "v_mfma_f32_16x16x32_bf16 v[72:75], a[108:111], a[72:75], v[72:75]\n\t"
      "v_mfma_f32_16x16x32_bf16 v[104:107], a[108:111], a[76:79], v[104:107]\n\t"
      "v_mfma_f32_16x16x32_bf16 v[136:139], a[108:111], a[80:83], v[136:139]\n\t"
      "v_mfma_f32_16x16x32_bf16 v[168:171], a[108:111], a[84:87], v[168:171]\n\t"
      "v_mfma_f32_16x16x32_bf16 v[200:203], a[108:111], a[88:91], v[200:203]\n\t"
      "v_mfma_f32_16x16x32_bf16 v[232:235], a[108:111], a[92:95], v[232:235]\n\t"
      "v_mfma_f32_16x16x32_bf16 a[200:203], a[108:111], a[96:99], a[200:203]\n\t"
      "v_mfma_f32_16x16x32_bf16 a[204:207], a[112:115], a[96:99], a[204:207]\n\t"
      "v_mfma_f32_16x16x32_bf16 v[236:239], a[112:115], a[92:95], v[236:239]\n\t"
      "v_mfma_f32_16x16x32_bf16 v[204:207], a[112:115], a[88:91], v[204:207]\n\t"
      "v_mfma_f32_16x16x32_bf16 v[172:175], a[112:115], a[84:87], v[172:175]\n\t"
      "v_mfma_f32_16x16x32_bf16 v[140:143], a[112:115], a[80:83], v[140:143]\n\t"
      "v_mfma_f32_16x16x32_bf16 v[108:111], a[112:115], a[76:79], v[108:111]\n\t"
      "v_mfma_f32_16x16x32_bf16 v[76:79], a[112:115], a[72:75], v[76:79]\n\t"
      "v_mfma_f32_16x16x32_bf16 v[44:47], a[112:115], a[68:71], v[44:47]\n\t"
      "v_mfma_f32_16x16x32_bf16 v[48:51], a[116:119], a[68:71], v[48:51]\n\t"
      "v_mfma_f32_16x16x32_bf16 v[80:83], a[116:119], a[72:75], v[80:83]\n\t"
      "v_mfma_f32_16x16x32_bf16 v[112:115], a[116:119], a[76:79], v[112:115]\n\t"
      "v_mfma_f32_16x16x32_bf16 v[144:147], a[116:119], a[80:83], v[144:147]\n\t"
      "v_mfma_f32_16x16x32_bf16 v[176:179], a[116:119], a[84:87], v[176:179]\n\t"
      "v_mfma_f32_16x16x32_bf16 v[208:211], a[116:119], a[88:91], v[208:211]\n\t"
      "v_mfma_f32_16x16x32_bf16 v[240:243], a[116:119], a[92:95], v[240:243]\n\t"
      "v_mfma_f32_16x16x32_bf16 a[208:211], a[116:119], a[96:99], a[208:211]\n\t"
      "v_mfma_f32_16x16x32_bf16 a[212:215], a[120:123], a[96:99], a[212:215]\n\t"
      "v_mfma_f32_16x16x32_bf16 v[244:247], a[120:123], a[92:95], v[244:247]\n\t"
      "v_mfma_f32_16x16x32_bf16 v[212:215], a[120:123], a[88:91], v[212:215]\n\t"
      "v_mfma_f32_16x16x32_bf16 v[180:183], a[120:123], a[84:87], v[180:183]\n\t"
      "v_mfma_f32_16x16x32_bf16 v[148:151], a[120:123], a[80:83], v[148:151]\n\t"
      "v_mfma_f32_16x16x32_bf16 v[116:119], a[120:123], a[76:79], v[116:119]\n\t"
      "v_mfma_f32_16x16x32_bf16 v[84:87], a[120:123], a[72:75], v[84:87]\n\t"
      "v_mfma_f32_16x16x32_bf16 v[52:55], a[120:123], a[68:71], v[52:55]\n\t"
      "v_mfma_f32_16x16x32_bf16 a[156:159], a[120:123], a[172:175], a[156:159]\n\t"
      "v_mfma_f32_16x16x32_bf16 v[56:59], a[124:127], a[68:71], v[56:59]\n\t"
      "v_mfma_f32_16x16x32_bf16 v[88:91], a[124:127], a[72:75], v[88:91]\n\t"
      "v_mfma_f32_16x16x32_bf16 v[120:123], a[124:127], a[76:79], v[120:123]\n\t"
      "v_mfma_f32_16x16x32_bf16 v[152:155], a[124:127], a[80:83], v[152:155]\n\t"
      "v_mfma_f32_16x16x32_bf16 v[184:187], a[124:127], a[84:87], v[184:187]\n\t"
      "v_mfma_f32_16x16x32_bf16 v[216:219], a[124:127], a[88:91], v[216:219]\n\t"
      "v_mfma_f32_16x16x32_bf16 v[248:251], a[124:127], a[92:95], v[248:251]\n\t"
      "v_mfma_f32_16x16x32_bf16 a[216:219], a[124:127], a[96:99], a[216:219]\n\t"
      "v_mfma_f32_16x16x32_bf16 a[160:163], a[124:127], a[172:175], a[160:163]\n\t"
      "v_mfma_f32_16x16x32_bf16 a[220:223], a[128:131], a[96:99], a[220:223]\n\t"
      "v_mfma_f32_16x16x32_bf16 v[252:255], a[128:131], a[92:95], v[252:255]\n\t"
      "v_mfma_f32_16x16x32_bf16 v[220:223], a[128:131], a[88:91], v[220:223]\n\t"
      "v_mfma_f32_16x16x32_bf16 v[188:191], a[128:131], a[84:87], v[188:191]\n\t"
      "v_mfma_f32_16x16x32_bf16 v[156:159], a[128:131], a[80:83], v[156:159]\n\t"
      "v_mfma_f32_16x16x32_bf16 v[124:127], a[128:131], a[76:79], v[124:127]\n\t"
      "v_mfma_f32_16x16x32_bf16 v[92:95], a[128:131], a[72:75], v[92:95]\n\t"
      "v_mfma_f32_16x16x32_bf16 v[60:63], a[128:131], a[68:71], v[60:63]\n\t"
      "v_mfma_f32_16x16x32_bf16 a[164:167], a[128:131], a[172:175], a[164:167]\n\t"
      "v_mfma_f32_16x16x32_bf16 a[224:227], a[68:71], a[132:135], a[224:227]\n\t"
      "v_mfma_f32_16x16x32_bf16 a[228:231], a[72:75], a[132:135], a[228:231]\n\t"
      "v_mfma_f32_16x16x32_bf16 a[232:235], a[76:79], a[132:135], a[232:235]\n\t"
      "v_mfma_f32_16x16x32_bf16 a[236:239], a[80:83], a[132:135], a[236:239]\n\t"
      "v_mfma_f32_16x16x32_bf16 a[240:243], a[84:87], a[132:135], a[240:243]\n\t"
      "v_mfma_f32_16x16x32_bf16 a[244:247], a[88:91], a[132:135], a[244:247]\n\t"
      "v_mfma_f32_16x16x32_bf16 a[248:251], a[92:95], a[132:135], a[248:251]\n\t"
      "v_mfma_f32_16x16x32_bf16 a[252:255], a[96:99], a[132:135], a[252:255]\n\t"
      "v_mfma_f32_16x16x32_bf16 a[168:171], a[172:175], a[132:135], a[168:171]\n\t"
      "s_nop 7\n\t"
      "v_mbcnt_lo_u32_b32 v22, -1, 0\n\t"
      "v_mbcnt_hi_u32_b32 v22, -1, v22\n\t"
      "v_and_b32 v23, 0x30, v22\n\t"
      "v_add_u32 v23, s82, v23\n\t"
      "v_lshlrev_b32 v22, 2, v22\n\t"
      "v_add_u32 v22, s82, v22\n\t"
      "s_mov_b32 exec_lo, 0x00010001\n\t"
      "s_mov_b32 exec_hi, 0x00010001\n\t"
      "ds_write_b128 v23, a[156:159] offset:320\n\t"
      "ds_write_b128 v23, a[160:163] offset:384\n\t"
      "ds_write_b128 v23, a[164:167] offset:448\n\t"
      "s_mov_b64 exec, 0xffff\n\t"
      "ds_write_b32 v22, a168 offset:512\n\t"
      "s_mov_b64 exec, -1\n\t"
      "LA_end_r0_%=:\n\t"
      "s_waitcnt vmcnt(0) lgkmcnt(0)\n\t"
      "s_barrier\n\t"
      "s_nop 15\n\t"
      "s_branch LAA_done_%=\n\t"
      "LAA_role1_%=:\n\t"
      "s_nop 4\n\t"
      "s_mov_b32 s52, 128\n\t"
      "s_mov_b32 s53, 229376\n\t"
      "s_mov_b32 s54, 128\n\t"
      "s_mov_b32 s63, 0\n\t"
      "s_mov_b32 s60, s61\n\t"
      "s_add_u32 m0, s49, 8192\n\t"
      "s_nop 0\n\t"
      "buffer_load_dwordx4 v12, s[40:43], s53 offen lds\n\t"
      "buffer_load_dwordx4 v13, s[40:43], s53 offen offset:1024 lds\n\t"
      "s_add_u32 s53, s53, 229376\n\t"
      "buffer_load_dword v18, v16, s[56:59], s60 offen\n\t"
      "s_add_u32 s60, s60, s61\n\t"
      "s_add_u32 m0, s49, 16384\n\t"
      "s_nop 0\n\t"
      "buffer_load_dwordx4 v12, s[40:43], s53 offen lds\n\t"
      "buffer_load_dwordx4 v13, s[40:43], s53 offen offset:1024 lds\n\t"
      "s_add_u32 s53, s53, 229376\n\t"
      "buffer_load_dword v18, v16, s[56:59], s60 offen\n\t"
      "s_add_u32 s60, s60, s61\n\t"
      "s_add_u32 s62, s52, 65536\n\t"
      "s_add_u32 m0, s48, 32768\n\t"
      "s_nop 0\n\t"
      "buffer_load_dwordx4 v8, s[36:39], s52 offen lds\n\t"
      "buffer_load_dwordx4 v9, s[36:39], s52 offen offset:1024 lds\n\t"
      "buffer_load_dwordx4 v10, s[36:39], s52 offen offset:2048 lds\n\t"
      "buffer_load_dwordx4 v11, s[36:39], s52 offen offset:3072 lds\n\t"
      "s_add_u32 m0, s48, 36864\n\t"
      "s_nop 0\n\t"
      "buffer_load_dwordx4 v8, s[36:39], s62 offen lds\n\t"
      "buffer_load_dwordx4 v9, s[36:39], s62 offen offset:1024 lds\n\t"
      "buffer_load_dwordx4 v10, s[36:39], s62 offen offset:2048 lds\n\t"
      "buffer_load_dwordx4 v11, s[36:39], s62 offen offset:3072 lds\n\t"
      "s_add_u32 s52, s52, 128\n\t"
      "s_add_u32 m0, s50, 20480\n\t"
      "s_nop 0\n\t"
      "buffer_load_dwordx4 v14, s[44:47], s54 offen lds\n\t"
      "buffer_load_dwordx4 v15, s[44:47], s54 offen offset:1024 lds\n\t"
      "buffer_load_dwordx4 v19, s[44:47], s54 offen offset:2048 lds\n\t"
      "buffer_load_dwordx4 v4, s[44:47], s54 offen offset:3072 lds\n\t"
      "s_add_u32 m0, s50, 24576\n\t"
      "s_nop 0\n\t"
      "buffer_load_dwordx4 v5, s[44:47], s54 offen lds\n\t"
      "s_add_u32 s54, s54, 128\n\t"
      "s_add_u32 m0, s49, 24576\n\t"
      "s_nop 0\n\t"
      "buffer_load_dwordx4 v12, s[40:43], s53 offen lds\n\t"
      "buffer_load_dwordx4 v13, s[40:43], s53 offen offset:1024 lds\n\t"
      "s_add_u32 s53, s53, 229376\n\t"
      "v_mov_b32 v32, 0\n\t"
      "v_mov_b32 v33, 0\n\t"
      "v_mov_b32 v34, 0\n\t"
      "v_mov_b32 v35, 0\n\t"
      "v_mov_b32 v36, 0\n\t"
      "v_mov_b32 v37, 0\n\t"
      "v_mov_b32 v38, 0\n\t"
      "v_mov_b32 v39, 0\n\t"
      "v_mov_b32 v40, 0\n\t"
      "v_mov_b32 v41, 0\n\t"
      "v_mov_b32 v42, 0\n\t"
      "v_mov_b32 v43, 0\n\t"
      "v_mov_b32 v44, 0\n\t"
      "v_mov_b32 v45, 0\n\t"
      "v_mov_b32 v46, 0\n\t"
      "v_mov_b32 v47, 0\n\t"
      "v_mov_b32 v48, 0\n\t"
      "v_mov_b32 v49, 0\n\t"
      "v_mov_b32 v50, 0\n\t"
      "v_mov_b32 v51, 0\n\t"
      "v_mov_b32 v52, 0\n\t"
      "v_mov_b32 v53, 0\n\t"
      "v_mov_b32 v54, 0\n\t"
      "v_mov_b32 v55, 0\n\t"
      "v_mov_b32 v56, 0\n\t"
      "v_mov_b32 v57, 0\n\t"
      "v_mov_b32 v58, 0\n\t"
      "v_mov_b32 v59, 0\n\t"
      "v_mov_b32 v60, 0\n\t"
      "v_mov_b32 v61, 0\n\t"
      "v_mov_b32 v62, 0\n\t"
      "v_mov_b32 v63, 0\n\t"
      "v_mov_b32 v22, 0x3f803f80\n\t"
      "v_accvgpr_write_b32 a172, v22\n\t"
      "v_accvgpr_write_b32 a173, v22\n\t"
      "v_accvgpr_write_b32 a174, v22\n\t"
      "v_accvgpr_write_b32 a175, v22\n\t"
      "s_waitcnt vmcnt(21)\n\t"
      "s_barrier\n\t"
      "ds_read_b128 a[0:3], v0 offset:0\n\t"
      "ds_read_b128 a[4:7], v0 offset:2048\n\t"
      "ds_read_b128 a[8:11], v0 offset:4096\n\t"
      "ds_read_b128 a[12:15], v0 offset:6144\n\t"
      "ds_read_b128 a[16:19], v0 offset:8192\n\t"
      "ds_read_b128 a[20:23], v0 offset:10240\n\t"
      "ds_read_b128 a[24:27], v0 offset:12288\n\t"
      "ds_read_b128 a[28:31], v0 offset:14336\n\t"
      "ds_read_b64_tr_b16 a[32:33], v2 offset:0\n\t"
      "ds_read_b64_tr_b16 a[34:35], v2 offset:2048\n\t"
      "ds_read_b64_tr_b16 a[36:37], v3 offset:0\n\t"
      "ds_read_b64_tr_b16 a[38:39], v3 offset:2048\n\t"
      "ds_read_b64_tr_b16 a[40:41], v2 offset:4096\n\t"
      "ds_read_b64_tr_b16 a[42:43], v2 offset:6144\n\t"
      "ds_read_b64_tr_b16 a[44:45], v3 offset:4096\n\t"
      "ds_read_b64_tr_b16 a[46:47], v3 offset:6144\n\t"
      "ds_read_b128 a[48:51], v6 offset:0\n\t"
      "ds_read_b128 a[52:55], v6 offset:2048\n\t"
      "ds_read_b128 a[56:59], v6 offset:4096\n\t"
      "ds_read_b128 a[60:63], v6 offset:6144\n\t"
      "ds_read_b128 a[64:67], v6 offset:8192\n\t"
      "s_mov_b32 s55, 7\n\t"
      "LA_loop_r1_%=:\n\t"
      "s_cmp_eq_u32 s55, s51\n\t"
      "s_cbranch_scc1 LA_cs_r1_%=\n\t"
      "s_cmp_eq_u32 s55, 1\n\t"
      "s_cbranch_scc1 LA_cs_r1_%=\n\t"
      "s_waitcnt vmcnt(19) lgkmcnt(0)\n\t"
      "s_barrier\n\t"
      "v_mfma_f32_16x16x32_bf16 v[32:35], a[32:35], a[0:3], v[32:35]\n\t"
      "ds_read_b128 a[68:71], v1 offset:0\n\t"
      "buffer_load_dword v18, v16, s[56:59], s60 offen\n\t"
      "v_mfma_f32_16x16x32_bf16 v[48:51], a[32:35], a[4:7], v[48:51]\n\t"
      "s_add_u32 s60, s60, s61\n\t"
      "v_mfma_f32_16x16x32_bf16 v[64:67], a[32:35], a[8:11], v[64:67]\n\t"
      "ds_read_b128 a[72:75], v1 offset:2048\n\t"
      "v_mfma_f32_16x16x32_bf16 v[80:83], a[32:35], a[12:15], v[80:83]\n\t"
      "s_add_u32 m0, s49, 0\n\t"
      "v_mfma_f32_16x16x32_bf16 v[96:99], a[32:35], a[16:19], v[96:99]\n\t"
      "buffer_load_dwordx4 v12, s[40:43], s53 offen lds\n\t"
      "v_mfma_f32_16x16x32_bf16 v[112:115], a[32:35], a[20:23], v[112:115]\n\t"
      "ds_read_b128 a[76:79], v1 offset:4096\n\t"
      "v_mfma_f32_16x16x32_bf16 v[128:131], a[32:35], a[24:27], v[128:131]\n\t"
      "v_mfma_f32_16x16x32_bf16 v[144:147], a[32:35], a[28:31], v[144:147]\n\t"
      "v_mfma_f32_16x16x32_bf16 v[148:151], a[36:39], a[28:31], v[148:151]\n\t"
      "ds_read_b128 a[80:83], v1 offset:6144\n\t"
      "v_mfma_f32_16x16x32_bf16 v[132:135], a[36:39], a[24:27], v[132:135]\n\t"
      "v_mfma_f32_16x16x32_bf16 v[116:119], a[36:39], a[20:23], v[116:119]\n\t"
      "v_mfma_f32_16x16x32_bf16 v[100:103], a[36:39], a[16:19], v[100:103]\n\t"
      "ds_read_b128 a[84:87], v1 offset:8192\n\t"
      "v_mfma_f32_16x16x32_bf16 v[84:87], a[36:39], a[12:15], v[84:87]\n\t"
      "v_mfma_f32_16x16x32_bf16 v[68:71], a[36:39], a[8:11], v[68:71]\n\t"
      "v_mfma_f32_16x16x32_bf16 v[52:55], a[36:39], a[4:7], v[52:55]\n\t"
      "ds_read_b128 a[88:91], v1 offset:10240\n\t"
      "v_mfma_f32_16x16x32_bf16 v[36:39], a[36:39], a[0:3], v[36:39]\n\t"
      "v_mfma_f32_16x16x32_bf16 v[40:43], a[40:43], a[0:3], v[40:43]\n\t"
      "v_mfma_f32_16x16x32_bf16 v[56:59], a[40:43], a[4:7], v[56:59]\n\t"
      "ds_read_b128 a[92:95], v1 offset:12288\n\t"
      "v_mfma_f32_16x16x32_bf16 v[72:75], a[40:43], a[8:11], v[72:75]\n\t"
      "v_mfma_f32_16x16x32_bf16 v[88:91], a[40:43], a[12:15], v[88:91]\n\t"
      "v_mfma_f32_16x16x32_bf16 v[104:107], a[40:43], a[16:19], v[104:107]\n\t"
      "ds_read_b128 a[96:99], v1 offset:14336\n\t"
      "v_mfma_f32_16x16x32_bf16 v[120:123], a[40:43], a[20:23], v[120:123]\n\t"
      "v_mfma_f32_16x16x32_bf16 v[136:139], a[40:43], a[24:27], v[136:139]\n\t"
      "v_mfma_f32_16x16x32_bf16 v[152:155], a[40:43], a[28:31], v[152:155]\n\t"
      "ds_read_b64_tr_b16 a[100:101], v2 offset:8192\n\t"
      "v_mfma_f32_16x16x32_bf16 v[156:159], a[44:47], a[28:31], v[156:159]\n\t"
      "v_mfma_f32_16x16x32_bf16 v[140:143], a[44:47], a[24:27], v[140:143]\n\t"
      "v_mfma_f32_16x16x32_bf16 v[124:127], a[44:47], a[20:23], v[124:127]\n\t"
      "ds_read_b64_tr_b16 a[102:103], v2 offset:10240\n\t"
      "v_mfma_f32_16x16x32_bf16 v[108:111], a[44:47], a[16:19], v[108:111]\n\t"
      "v_mfma_f32_16x16x32_bf16 v[92:95], a[44:47], a[12:15], v[92:95]\n\t"
      "v_mfma_f32_16x16x32_bf16 v[76:79], a[44:47], a[8:11], v[76:79]\n\t"
      "ds_read_b64_tr_b16 a[104:105], v3 offset:8192\n\t"
      "v_mfma_f32_16x16x32_bf16 v[60:63], a[44:47], a[4:7], v[60:63]\n\t"
      "v_mfma_f32_16x16x32_bf16 v[44:47], a[44:47], a[0:3], v[44:47]\n\t"
      "ds_read_b64_tr_b16 a[106:107], v3 offset:10240\n\t"
      "v_mfma_f32_16x16x32_bf16 v[160:163], a[0:3], a[48:51], v[160:163]\n\t"
      "v_mfma_f32_16x16x32_bf16 v[164:167], a[4:7], a[48:51], v[164:167]\n\t"
      "v_mfma_f32_16x16x32_bf16 v[168:171], a[8:11], a[48:51], v[168:171]\n\t"
      "ds_read_b64_tr_b16 a[108:109], v2 offset:12288\n\t"
      "v_mfma_f32_16x16x32_bf16 v[172:175], a[12:15], a[48:51], v[172:175]\n\t"
      "v_mfma_f32_16x16x32_bf16 v[176:179], a[16:19], a[48:51], v[176:179]\n\t"
      "v_mfma_f32_16x16x32_bf16 v[180:183], a[20:23], a[48:51], v[180:183]\n\t"
      "ds_read_b64_tr_b16 a[110:111], v2 offset:14336\n\t"
      "v_mfma_f32_16x16x32_bf16 v[184:187], a[24:27], a[48:51], v[184:187]\n\t"
      "v_mfma_f32_16x16x32_bf16 v[188:191], a[28:31], a[48:51], v[188:191]\n\t"
      "v_mfma_f32_16x16x32_bf16 v[220:223], a[28:31], a[52:55], v[220:223]\n\t"
      "ds_read_b64_tr_b16 a[112:113], v3 offset:12288\n\t"
      "buffer_load_dwordx4 v13, s[40:43], s53 offen offset:1024 lds\n\t"
      "v_mfma_f32_16x16x32_bf16 v[216:219], a[24:27], a[52:55], v[216:219]\n\t"
      "s_add_u32 s53, s53, 229376\n\t"
      "v_mfma_f32_16x16x32_bf16 v[212:215], a[20:23], a[52:55], v[212:215]\n\t"
      "v_mfma_f32_16x16x32_bf16 v[208:211], a[16:19], a[52:55], v[208:211]\n\t"
      "ds_read_b64_tr_b16 a[114:115], v3 offset:14336\n\t"
      "v_mfma_f32_16x16x32_bf16 v[204:207], a[12:15], a[52:55], v[204:207]\n\t"
      "v_mfma_f32_16x16x32_bf16 v[200:203], a[8:11], a[52:55], v[200:203]\n\t"
      "v_mfma_f32_16x16x32_bf16 v[196:199], a[4:7], a[52:55], v[196:199]\n\t"
      "ds_read_b128 a[116:119], v7 offset:0\n\t"
      "v_mfma_f32_16x16x32_bf16 v[192:195], a[0:3], a[52:55], v[192:195]\n\t"
      "v_mfma_f32_16x16x32_bf16 v[224:227], a[0:3], a[56:59], v[224:227]\n\t"
      "v_mfma_f32_16x16x32_bf16 v[228:231], a[4:7], a[56:59], v[228:231]\n\t"
      "ds_read_b128 a[120:123], v7 offset:2048\n\t"
      "v_mfma_f32_16x16x32_bf16 v[232:235], a[8:11], a[56:59], v[232:235]\n\t"
      "v_mfma_f32_16x16x32_bf16 v[236:239], a[12:15], a[56:59], v[236:239]\n\t"
      "v_mfma_f32_16x16x32_bf16 v[240:243], a[16:19], a[56:59], v[240:243]\n\t"
      "ds_read_b128 a[124:127], v7 offset:4096\n\t"
      "v_mfma_f32_16x16x32_bf16 v[244:247], a[20:23], a[56:59], v[244:247]\n\t"
      "v_mfma_f32_16x16x32_bf16 v[248:251], a[24:27], a[56:59], v[248:251]\n\t"
      "v_mfma_f32_16x16x32_bf16 v[252:255], a[28:31], a[56:59], v[252:255]\n\t"
      "ds_read_b128 a[128:131], v7 offset:6144\n\t"
      "v_mfma_f32_16x16x32_bf16 a[220:223], a[28:31], a[60:63], a[220:223]\n\t"
      "v_mfma_f32_16x16x32_bf16 a[216:219], a[24:27], a[60:63], a[216:219]\n\t"
      "v_mfma_f32_16x16x32_bf16 a[212:215], a[20:23], a[60:63], a[212:215]\n\t"
      "ds_read_b128 a[132:135], v7 offset:8192\n\t"
      "v_mfma_f32_16x16x32_bf16 a[208:211], a[16:19], a[60:63], a[208:211]\n\t"
      "v_mfma_f32_16x16x32_bf16 a[204:207], a[12:15], a[60:63], a[204:207]\n\t"
      "v_mfma_f32_16x16x32_bf16 a[200:203], a[8:11], a[60:63], a[200:203]\n\t"
      "v_mfma_f32_16x16x32_bf16 a[196:199], a[4:7], a[60:63], a[196:199]\n\t"
      "v_mfma_f32_16x16x32_bf16 a[192:195], a[0:3], a[60:63], a[192:195]\n\t"
      "v_mfma_f32_16x16x32_bf16 a[224:227], a[0:3], a[64:67], a[224:227]\n\t"
      "v_mfma_f32_16x16x32_bf16 a[228:231], a[4:7], a[64:67], a[228:231]\n\t"
      "v_mfma_f32_16x16x32_bf16 a[232:235], a[8:11], a[64:67], a[232:235]\n\t"
      "v_mfma_f32_16x16x32_bf16 a[236:239], a[12:15], a[64:67], a[236:239]\n\t"
      "v_mfma_f32_16x16x32_bf16 a[240:243], a[16:19], a[64:67], a[240:243]\n\t"
      "v_mfma_f32_16x16x32_bf16 a[244:247], a[20:23], a[64:67], a[244:247]\n\t"
      "v_mfma_f32_16x16x32_bf16 a[248:251], a[24:27], a[64:67], a[248:251]\n\t"
      "v_mfma_f32_16x16x32_bf16 a[252:255], a[28:31], a[64:67], a[252:255]\n\t"
      "s_waitcnt vmcnt(5) lgkmcnt(0)\n\t"
      "s_barrier\n\t"
      "v_mfma_f32_16x16x32_bf16 v[32:35], a[100:103], a[68:71], v[32:35]\n\t"
      "ds_read_b128 a[0:3], v0 offset:32768\n\t"
      "buffer_load_dword v18, v16, s[56:59], s60 offen\n\t"
      "v_mfma_f32_16x16x32_bf16 v[48:51], a[100:103], a[72:75], v[48:51]\n\t"
      "s_add_u32 s60, s60, s61\n\t"
      "v_mfma_f32_16x16x32_bf16 v[64:67], a[100:103], a[76:79], v[64:67]\n\t"
      "ds_read_b128 a[4:7], v0 offset:34816\n\t"
      "s_add_u32 s62, s52, 65536\n\t"
      "s_add_u32 m0, s48, 0\n\t"
      "v_mfma_f32_16x16x32_bf16 v[80:83], a[100:103], a[80:83], v[80:83]\n\t"
      "buffer_load_dwordx4 v8, s[36:39], s52 offen lds\n\t"
      "v_mfma_f32_16x16x32_bf16 v[96:99], a[100:103], a[84:87], v[96:99]\n\t"
      "v_mfma_f32_16x16x32_bf16 v[112:115], a[100:103], a[88:91], v[112:115]\n\t"
      "ds_read_b128 a[8:11], v0 offset:36864\n\t"
      "v_mfma_f32_16x16x32_bf16 v[128:131], a[100:103], a[92:95], v[128:131]\n\t"
      "buffer_load_dwordx4 v9, s[36:39], s52 offen offset:1024 lds\n\t"
      "v_mfma_f32_16x16x32_bf16 v[144:147], a[100:103], a[96:99], v[144:147]\n\t"
      "v_mfma_f32_16x16x32_bf16 v[148:151], a[104:107], a[96:99], v[148:151]\n\t"
      "ds_read_b128 a[12:15], v0 offset:38912\n\t"
      "v_mfma_f32_16x16x32_bf16 v[132:135], a[104:107], a[92:95], v[132:135]\n\t"
      "v_mfma_f32_16x16x32_bf16 v[116:119], a[104:107], a[88:91], v[116:119]\n\t"
      "buffer_load_dwordx4 v10, s[36:39], s52 offen offset:2048 lds\n\t"
      "v_mfma_f32_16x16x32_bf16 v[100:103], a[104:107], a[84:87], v[100:103]\n\t"
      "ds_read_b128 a[16:19], v0 offset:40960\n\t"
      "v_mfma_f32_16x16x32_bf16 v[84:87], a[104:107], a[80:83], v[84:87]\n\t"
      "v_mfma_f32_16x16x32_bf16 v[68:71], a[104:107], a[76:79], v[68:71]\n\t"
      "v_mfma_f32_16x16x32_bf16 v[52:55], a[104:107], a[72:75], v[52:55]\n\t"
      "ds_read_b128 a[20:23], v0 offset:43008\n\t"
      "buffer_load_dwordx4 v11, s[36:39], s52 offen offset:3072 lds\n\t"
      "v_mfma_f32_16x16x32_bf16 v[36:39], a[104:107], a[68:71], v[36:39]\n\t"
      "v_mfma_f32_16x16x32_bf16 v[40:43], a[108:111], a[68:71], v[40:43]\n\t"
      "v_mfma_f32_16x16x32_bf16 v[56:59], a[108:111], a[72:75], v[56:59]\n\t"
      "ds_read_b128 a[24:27], v0 offset:45056\n\t"
      "s_add_u32 m0, s48, 4096\n\t"
      "v_mfma_f32_16x16x32_bf16 v[72:75], a[108:111], a[76:79], v[72:75]\n\t"
      "buffer_load_dwordx4 v8, s[36:39], s62 offen lds\n\t"
      "v_mfma_f32_16x16x32_bf16 v[88:91], a[108:111], a[80:83], v[88:91]\n\t"
      "v_mfma_f32_16x16x32_bf16 v[104:107], a[108:111], a[84:87], v[104:107]\n\t"
      "ds_read_b128 a[28:31], v0 offset:47104\n\t"
      "v_mfma_f32_16x16x32_bf16 v[120:123], a[108:111], a[88:91], v[120:123]\n\t"
      "buffer_load_dwordx4 v9, s[36:39], s62 offen offset:1024 lds\n\t"
      "v_mfma_f32_16x16x32_bf16 v[136:139], a[108:111], a[92:95], v[136:139]\n\t"
      "v_mfma_f32_16x16x32_bf16 v[152:155], a[108:111], a[96:99], v[152:155]\n\t"
      "ds_read_b64_tr_b16 a[32:33], v2 offset:16384\n\t"
      "v_mfma_f32_16x16x32_bf16 v[156:159], a[112:115], a[96:99], v[156:159]\n\t"
      "v_mfma_f32_16x16x32_bf16 v[140:143], a[112:115], a[92:95], v[140:143]\n\t"
      "buffer_load_dwordx4 v10, s[36:39], s62 offen offset:2048 lds\n\t"
      "v_mfma_f32_16x16x32_bf16 v[124:127], a[112:115], a[88:91], v[124:127]\n\t"
      "ds_read_b64_tr_b16 a[34:35], v2 offset:18432\n\t"
      "v_mfma_f32_16x16x32_bf16 v[108:111], a[112:115], a[84:87], v[108:111]\n\t"
      "v_mfma_f32_16x16x32_bf16 v[92:95], a[112:115], a[80:83], v[92:95]\n\t"
      "v_mfma_f32_16x16x32_bf16 v[76:79], a[112:115], a[76:79], v[76:79]\n\t"
      "ds_read_b64_tr_b16 a[36:37], v3 offset:16384\n\t"
      "buffer_load_dwordx4 v11, s[36:39], s62 offen offset:3072 lds\n\t"
      "v_mfma_f32_16x16x32_bf16 v[60:63], a[112:115], a[72:75], v[60:63]\n\t"
      "s_add_u32 s52, s52, 128\n\t"
      "v_mfma_f32_16x16x32_bf16 v[44:47], a[112:115], a[68:71], v[44:47]\n\t"
      "ds_read_b64_tr_b16 a[38:39], v3 offset:18432\n\t"
      "v_mfma_f32_16x16x32_bf16 v[160:163], a[68:71], a[116:119], v[160:163]\n\t"
      "s_add_u32 m0, s50, 0\n\t"
      "v_mfma_f32_16x16x32_bf16 v[164:167], a[72:75], a[116:119], v[164:167]\n\t"
      "buffer_load_dwordx4 v14, s[44:47], s54 offen lds\n\t"
      "v_mfma_f32_16x16x32_bf16 v[168:171], a[76:79], a[116:119], v[168:171]\n\t"
      "ds_read_b64_tr_b16 a[40:41], v2 offset:20480\n\t"
      "v_mfma_f32_16x16x32_bf16 v[172:175], a[80:83], a[116:119], v[172:175]\n\t"
      "v_mfma_f32_16x16x32_bf16 v[176:179], a[84:87], a[116:119], v[176:179]\n\t"
      "buffer_load_dwordx4 v15, s[44:47], s54 offen offset:1024 lds\n\t"
      "v_mfma_f32_16x16x32_bf16 v[180:183], a[88:91], a[116:119], v[180:183]\n\t"
      "ds_read_b64_tr_b16 a[42:43], v2 offset:22528\n\t"
      "v_mfma_f32_16x16x32_bf16 v[184:187], a[92:95], a[116:119], v[184:187]\n\t"
      "v_mfma_f32_16x16x32_bf16 v[188:191], a[96:99], a[116:119], v[188:191]\n\t"
      "v_mfma_f32_16x16x32_bf16 v[220:223], a[96:99], a[120:123], v[220:223]\n\t"
      "ds_read_b64_tr_b16 a[44:45], v3 offset:20480\n\t"
      "buffer_load_dwordx4 v19, s[44:47], s54 offen offset:2048 lds\n\t"
      "v_mfma_f32_16x16x32_bf16 v[216:219], a[92:95], a[120:123], v[216:219]\n\t"
      "v_mfma_f32_16x16x32_bf16 v[212:215], a[88:91], a[120:123], v[212:215]\n\t"
      "v_mfma_f32_16x16x32_bf16 v[208:211], a[84:87], a[120:123], v[208:211]\n\t"
      "ds_read_b64_tr_b16 a[46:47], v3 offset:22528\n\t"
      "v_mfma_f32_16x16x32_bf16 v[204:207], a[80:83], a[120:123], v[204:207]\n\t"
      "buffer_load_dwordx4 v4, s[44:47], s54 offen offset:3072 lds\n\t"
      "v_mfma_f32_16x16x32_bf16 v[200:203], a[76:79], a[120:123], v[200:203]\n\t"
      "v_mfma_f32_16x16x32_bf16 v[196:199], a[72:75], a[120:123], v[196:199]\n\t"
      "ds_read_b128 a[48:51], v6 offset:20480\n\t"
      "v_mfma_f32_16x16x32_bf16 v[192:195], a[68:71], a[120:123], v[192:195]\n\t"
      "s_add_u32 m0, s50, 4096\n\t"
      "v_mfma_f32_16x16x32_bf16 v[224:227], a[68:71], a[124:127], v[224:227]\n\t"
      "buffer_load_dwordx4 v5, s[44:47], s54 offen lds\n\t"
      "v_mfma_f32_16x16x32_bf16 v[228:231], a[72:75], a[124:127], v[228:231]\n\t"
      "ds_read_b128 a[52:55], v6 offset:22528\n\t"
      "s_add_u32 s54, s54, 128\n\t"
      "v_mfma_f32_16x16x32_bf16 v[232:235], a[76:79], a[124:127], v[232:235]\n\t"
      "v_mfma_f32_16x16x32_bf16 v[236:239], a[80:83], a[124:127], v[236:239]\n\t"
      "v_mfma_f32_16x16x32_bf16 v[240:243], a[84:87], a[124:127], v[240:243]\n\t"
      "ds_read_b128 a[56:59], v6 offset:24576\n\t"
      "s_add_u32 m0, s49, 8192\n\t"
      "v_mfma_f32_16x16x32_bf16 v[244:247], a[88:91], a[124:127], v[244:247]\n\t"
      "buffer_load_dwordx4 v12, s[40:43], s53 offen lds\n\t"
      "v_mfma_f32_16x16x32_bf16 v[248:251], a[92:95], a[124:127], v[248:251]\n\t"
      "v_mfma_f32_16x16x32_bf16 v[252:255], a[96:99], a[124:127], v[252:255]\n\t"
      "ds_read_b128 a[60:63], v6 offset:26624\n\t"
      "v_mfma_f32_16x16x32_bf16 a[220:223], a[96:99], a[128:131], a[220:223]\n\t"
      "v_mfma_f32_16x16x32_bf16 a[216:219], a[92:95], a[128:131], a[216:219]\n\t"
      "v_mfma_f32_16x16x32_bf16 a[212:215], a[88:91], a[128:131], a[212:215]\n\t"
      "ds_read_b128 a[64:67], v6 offset:28672\n\t"
      "v_mfma_f32_16x16x32_bf16 a[208:211], a[84:87], a[128:131], a[208:211]\n\t"
      "v_mfma_f32_16x16x32_bf16 a[204:207], a[80:83], a[128:131], a[204:207]\n\t"
      "v_mfma_f32_16x16x32_bf16 a[200:203], a[76:79], a[128:131], a[200:203]\n\t"
      "v_mfma_f32_16x16x32_bf16 a[196:199], a[72:75], a[128:131], a[196:199]\n\t"
      "v_mfma_f32_16x16x32_bf16 a[192:195], a[68:71], a[128:131], a[192:195]\n\t"
      "v_mfma_f32_16x16x32_bf16 a[224:227], a[68:71], a[132:135], a[224:227]\n\t"
      "v_mfma_f32_16x16x32_bf16 a[228:231], a[72:75], a[132:135], a[228:231]\n\t"
      "v_mfma_f32_16x16x32_bf16 a[232:235], a[76:79], a[132:135], a[232:235]\n\t"
      "buffer_load_dwordx4 v13, s[40:43], s53 offen offset:1024 lds\n\t"
      "v_mfma_f32_16x16x32_bf16 a[236:239], a[80:83], a[132:135], a[236:239]\n\t"
      "s_add_u32 s53, s53, 229376\n\t"
      "v_mfma_f32_16x16x32_bf16 a[240:243], a[84:87], a[132:135], a[240:243]\n\t"
      "v_mfma_f32_16x16x32_bf16 a[244:247], a[88:91], a[132:135], a[244:247]\n\t"
      "v_mfma_f32_16x16x32_bf16 a[248:251], a[92:95], a[132:135], a[248:251]\n\t"
      "v_mfma_f32_16x16x32_bf16 a[252:255], a[96:99], a[132:135], a[252:255]\n\t"
      "s_waitcnt vmcnt(19) lgkmcnt(0)\n\t"
      "s_barrier\n\t"
      "v_mfma_f32_16x16x32_bf16 v[32:35], a[32:35], a[0:3], v[32:35]\n\t"
      "ds_read_b128 a[68:71], v1 offset:32768\n\t"
      "buffer_load_dword v18, v16, s[56:59], s60 offen\n\t"
      "v_mfma_f32_16x16x32_bf16 v[48:51], a[32:35], a[4:7], v[48:51]\n\t"
      "s_add_u32 s60, s60, s61\n\t"
      "v_mfma_f32_16x16x32_bf16 v[64:67], a[32:35], a[8:11], v[64:67]\n\t"
      "ds_read_b128 a[72:75], v1 offset:34816\n\t"
      "v_mfma_f32_16x16x32_bf16 v[80:83], a[32:35], a[12:15], v[80:83]\n\t"
      "s_add_u32 m0, s49, 16384\n\t"
      "v_mfma_f32_16x16x32_bf16 v[96:99], a[32:35], a[16:19], v[96:99]\n\t"
      "buffer_load_dwordx4 v12, s[40:43], s53 offen lds\n\t"
      "v_mfma_f32_16x16x32_bf16 v[112:115], a[32:35], a[20:23], v[112:115]\n\t"
      "ds_read_b128 a[76:79], v1 offset:36864\n\t"
      "v_mfma_f32_16x16x32_bf16 v[128:131], a[32:35], a[24:27], v[128:131]\n\t"
      "v_mfma_f32_16x16x32_bf16 v[144:147], a[32:35], a[28:31], v[144:147]\n\t"
      "v_mfma_f32_16x16x32_bf16 v[148:151], a[36:39], a[28:31], v[148:151]\n\t"
      "ds_read_b128 a[80:83], v1 offset:38912\n\t"
      "v_mfma_f32_16x16x32_bf16 v[132:135], a[36:39], a[24:27], v[132:135]\n\t"
      "v_mfma_f32_16x16x32_bf16 v[116:119], a[36:39], a[20:23], v[116:119]\n\t"
      "v_mfma_f32_16x16x32_bf16 v[100:103], a[36:39], a[16:19], v[100:103]\n\t"
      "ds_read_b128 a[84:87], v1 offset:40960\n\t"
      "v_mfma_f32_16x16x32_bf16 v[84:87], a[36:39], a[12:15], v[84:87]\n\t"
      "v_mfma_f32_16x16x32_bf16 v[68:71], a[36:39], a[8:11], v[68:71]\n\t"
      "v_mfma_f32_16x16x32_bf16 v[52:55], a[36:39], a[4:7], v[52:55]\n\t"
      "ds_read_b128 a[88:91], v1 offset:43008\n\t"
      "v_mfma_f32_16x16x32_bf16 v[36:39], a[36:39], a[0:3], v[36:39]\n\t"
      "v_mfma_f32_16x16x32_bf16 v[40:43], a[40:43], a[0:3], v[40:43]\n\t"
      "v_mfma_f32_16x16x32_bf16 v[56:59], a[40:43], a[4:7], v[56:59]\n\t"
      "ds_read_b128 a[92:95], v1 offset:45056\n\t"
      "v_mfma_f32_16x16x32_bf16 v[72:75], a[40:43], a[8:11], v[72:75]\n\t"
      "v_mfma_f32_16x16x32_bf16 v[88:91], a[40:43], a[12:15], v[88:91]\n\t"
      "v_mfma_f32_16x16x32_bf16 v[104:107], a[40:43], a[16:19], v[104:107]\n\t"
      "ds_read_b128 a[96:99], v1 offset:47104\n\t"
      "v_mfma_f32_16x16x32_bf16 v[120:123], a[40:43], a[20:23], v[120:123]\n\t"
      "v_mfma_f32_16x16x32_bf16 v[136:139], a[40:43], a[24:27], v[136:139]\n\t"
      "v_mfma_f32_16x16x32_bf16 v[152:155], a[40:43], a[28:31], v[152:155]\n\t"
      "ds_read_b64_tr_b16 a[100:101], v2 offset:24576\n\t"
      "v_mfma_f32_16x16x32_bf16 v[156:159], a[44:47], a[28:31], v[156:159]\n\t"
      "v_mfma_f32_16x16x32_bf16 v[140:143], a[44:47], a[24:27], v[140:143]\n\t"
      "v_mfma_f32_16x16x32_bf16 v[124:127], a[44:47], a[20:23], v[124:127]\n\t"
      "ds_read_b64_tr_b16 a[102:103], v2 offset:26624\n\t"
      "v_mfma_f32_16x16x32_bf16 v[108:111], a[44:47], a[16:19], v[108:111]\n\t"
      "v_mfma_f32_16x16x32_bf16 v[92:95], a[44:47], a[12:15], v[92:95]\n\t"
      "v_mfma_f32_16x16x32_bf16 v[76:79], a[44:47], a[8:11], v[76:79]\n\t"
      "ds_read_b64_tr_b16 a[104:105], v3 offset:24576\n\t"
      "v_mfma_f32_16x16x32_bf16 v[60:63], a[44:47], a[4:7], v[60:63]\n\t"
      "v_mfma_f32_16x16x32_bf16 v[44:47], a[44:47], a[0:3], v[44:47]\n\t"
      "ds_read_b64_tr_b16 a[106:107], v3 offset:26624\n\t"
      "v_mfma_f32_16x16x32_bf16 v[160:163], a[0:3], a[48:51], v[160:163]\n\t"
      "v_mfma_f32_16x16x32_bf16 v[164:167], a[4:7], a[48:51], v[164:167]\n\t"
      "v_mfma_f32_16x16x32_bf16 v[168:171], a[8:11], a[48:51], v[168:171]\n\t"
      "ds_read_b64_tr_b16 a[108:109], v2 offset:28672\n\t"
      "v_mfma_f32_16x16x32_bf16 v[172:175], a[12:15], a[48:51], v[172:175]\n\t"
      "v_mfma_f32_16x16x32_bf16 v[176:179], a[16:19], a[48:51], v[176:179]\n\t"
      "v_mfma_f32_16x16x32_bf16 v[180:183], a[20:23], a[48:51], v[180:183]\n\t"
      "ds_read_b64_tr_b16 a[110:111], v2 offset:30720\n\t"
      "v_mfma_f32_16x16x32_bf16 v[184:187], a[24:27], a[48:51], v[184:187]\n\t"
      "v_mfma_f32_16x16x32_bf16 v[188:191], a[28:31], a[48:51], v[188:191]\n\t"
      "v_mfma_f32_16x16x32_bf16 v[220:223], a[28:31], a[52:55], v[220:223]\n\t"
      "ds_read_b64_tr_b16 a[112:113], v3 offset:28672\n\t"
      "buffer_load_dwordx4 v13, s[40:43], s53 offen offset:1024 lds\n\t"
      "v_mfma_f32_16x16x32_bf16 v[216:219], a[24:27], a[52:55], v[216:219]\n\t"
      "s_add_u32 s53, s53, 229376\n\t"
      "v_mfma_f32_16x16x32_bf16 v[212:215], a[20:23], a[52:55], v[212:215]\n\t"
      "v_mfma_f32_16x16x32_bf16 v[208:211], a[16:19], a[52:55], v[208:211]\n\t"
      "ds_read_b64_tr_b16 a[114:115], v3 offset:30720\n\t"
      "v_mfma_f32_16x16x32_bf16 v[204:207], a[12:15], a[52:55], v[204:207]\n\t"
      "v_mfma_f32_16x16x32_bf16 v[200:203], a[8:11], a[52:55], v[200:203]\n\t"
      "v_mfma_f32_16x16x32_bf16 v[196:199], a[4:7], a[52:55], v[196:199]\n\t"
      "ds_read_b128 a[116:119], v7 offset:20480\n\t"
      "v_mfma_f32_16x16x32_bf16 v[192:195], a[0:3], a[52:55], v[192:195]\n\t"
      "v_mfma_f32_16x16x32_bf16 v[224:227], a[0:3], a[56:59], v[224:227]\n\t"
      "v_mfma_f32_16x16x32_bf16 v[228:231], a[4:7], a[56:59], v[228:231]\n\t"
      "ds_read_b128 a[120:123], v7 offset:22528\n\t"
      "v_mfma_f32_16x16x32_bf16 v[232:235], a[8:11], a[56:59], v[232:235]\n\t"
      "v_mfma_f32_16x16x32_bf16 v[236:239], a[12:15], a[56:59], v[236:239]\n\t"
      "v_mfma_f32_16x16x32_bf16 v[240:243], a[16:19], a[56:59], v[240:243]\n\t"
      "ds_read_b128 a[124:127], v7 offset:24576\n\t"
      "v_mfma_f32_16x16x32_bf16 v[244:247], a[20:23], a[56:59], v[244:247]\n\t"
      "v_mfma_f32_16x16x32_bf16 v[248:251], a[24:27], a[56:59], v[248:251]\n\t"
      "v_mfma_f32_16x16x32_bf16 v[252:255], a[28:31], a[56:59], v[252:255]\n\t"
      "ds_read_b128 a[128:131], v7 offset:26624\n\t"
      "v_mfma_f32_16x16x32_bf16 a[220:223], a[28:31], a[60:63], a[220:223]\n\t"
      "v_mfma_f32_16x16x32_bf16 a[216:219], a[24:27], a[60:63], a[216:219]\n\t"
      "v_mfma_f32_16x16x32_bf16 a[212:215], a[20:23], a[60:63], a[212:215]\n\t"
      "ds_read_b128 a[132:135], v7 offset:28672\n\t"
      "v_mfma_f32_16x16x32_bf16 a[208:211], a[16:19], a[60:63], a[208:211]\n\t"
      "v_mfma_f32_16x16x32_bf16 a[204:207], a[12:15], a[60:63], a[204:207]\n\t"
      "v_mfma_f32_16x16x32_bf16 a[200:203], a[8:11], a[60:63], a[200:203]\n\t"
      "v_mfma_f32_16x16x32_bf16 a[196:199], a[4:7], a[60:63], a[196:199]\n\t"
      "v_mfma_f32_16x16x32_bf16 a[192:195], a[0:3], a[60:63], a[192:195]\n\t"
      "v_mfma_f32_16x16x32_bf16 a[224:227], a[0:3], a[64:67], a[224:227]\n\t"
      "v_mfma_f32_16x16x32_bf16 a[228:231], a[4:7], a[64:67], a[228:231]\n\t"
      "v_mfma_f32_16x16x32_bf16 a[232:235], a[8:11], a[64:67], a[232:235]\n\t"
      "v_mfma_f32_16x16x32_bf16 a[236:239], a[12:15], a[64:67], a[236:239]\n\t"
      "v_mfma_f32_16x16x32_bf16 a[240:243], a[16:19], a[64:67], a[240:243]\n\t"
      "v_mfma_f32_16x16x32_bf16 a[244:247], a[20:23], a[64:67], a[244:247]\n\t"
      "v_mfma_f32_16x16x32_bf16 a[248:251], a[24:27], a[64:67], a[248:251]\n\t"
      "v_mfma_f32_16x16x32_bf16 a[252:255], a[28:31], a[64:67], a[252:255]\n\t"
      "s_waitcnt vmcnt(5) lgkmcnt(0)\n\t"
      "s_bitcmp1_b32 s81, s55\n\t"
      "s_cbranch_scc0 LA_noflNone_r1_%=\n\t"
      "s_not_b32 s74, s68\n\t"
      "s_not_b32 s75, s69\n\t"
      "s_cmp_eq_u32 s63, 1\n\t"
      "s_cselect_b32 s74, s68, s74\n\t"
      "s_cselect_b32 s75, s69, s75\n\t"
      "v_mov_b32 v22, s74\n\t"
      "v_mov_b32 v23, s75\n\t"
      "s_mov_b64 exec, 1\n\t"
      "buffer_store_dwordx4 v[20:23], v17, s[64:67], 0 offen sc1 ; alt\n\t"
      "s_mov_b64 exec, -1\n\t"
      "s_nop 1\n\t"
      "LA_noflNone_r1_%=:\n\t"
      "s_barrier\n\t"
      "v_mfma_f32_16x16x32_bf16 v[32:35], a[100:103], a[68:71], v[32:35]\n\t"
      "ds_read_b128 a[0:3], v0 offset:0\n\t"
      "buffer_load_dword v18, v16, s[56:59], s60 offen\n\t"
      "v_mfma_f32_16x16x32_bf16 v[48:51], a[100:103], a[72:75], v[48:51]\n\t"
      "s_add_u32 s60, s60, s61\n\t"
      "v_mfma_f32_16x16x32_bf16 v[64:67], a[100:103], a[76:79], v[64:67]\n\t"
      "ds_read_b128 a[4:7], v0 offset:2048\n\t"
      "s_add_u32 s62, s52, 65536\n\t"
      "s_add_u32 m0, s48, 32768\n\t"
      "v_mfma_f32_16x16x32_bf16 v[80:83], a[100:103], a[80:83], v[80:83]\n\t"
      "buffer_load_dwordx4 v8, s[36:39], s52 offen lds\n\t"
      "v_mfma_f32_16x16x32_bf16 v[96:99], a[100:103], a[84:87], v[96:99]\n\t"
      "v_mfma_f32_16x16x32_bf16 v[112:115], a[100:103], a[88:91], v[112:115]\n\t"
      "ds_read_b128 a[8:11], v0 offset:4096\n\t"
      "v_mfma_f32_16x16x32_bf16 v[128:131], a[100:103], a[92:95], v[128:131]\n\t"
      "buffer_load_dwordx4 v9, s[36:39], s52 offen offset:1024 lds\n\t"
      "v_mfma_f32_16x16x32_bf16 v[144:147], a[100:103], a[96:99], v[144:147]\n\t"
      "v_mfma_f32_16x16x32_bf16 v[148:151], a[104:107], a[96:99], v[148:151]\n\t"
      "ds_read_b128 a[12:15], v0 offset:6144\n\t"
      "v_mfma_f32_16x16x32_bf16 v[132:135], a[104:107], a[92:95], v[132:135]\n\t"
      "v_mfma_f32_16x16x32_bf16 v[116:119], a[104:107], a[88:91], v[116:119]\n\t"
      "buffer_load_dwordx4 v10, s[36:39], s52 offen offset:2048 lds\n\t"
      "v_mfma_f32_16x16x32_bf16 v[100:103], a[104:107], a[84:87], v[100:103]\n\t"
      "ds_read_b128 a[16:19], v0 offset:8192\n\t"
      "v_mfma_f32_16x16x32_bf16 v[84:87], a[104:107], a[80:83], v[84:87]\n\t"
      "v_mfma_f32_16x16x32_bf16 v[68:71], a[104:107], a[76:79], v[68:71]\n\t"
      "v_mfma_f32_16x16x32_bf16 v[52:55], a[104:107], a[72:75], v[52:55]\n\t"
      "ds_read_b128 a[20:23], v0 offset:10240\n\t"
      "buffer_load_dwordx4 v11, s[36:39], s52 offen offset:3072 lds\n\t"
      "v_mfma_f32_16x16x32_bf16 v[36:39], a[104:107], a[68:71], v[36:39]\n\t"
      "v_mfma_f32_16x16x32_bf16 v[40:43], a[108:111], a[68:71], v[40:43]\n\t"
      "v_mfma_f32_16x16x32_bf16 v[56:59], a[108:111], a[72:75], v[56:59]\n\t"
      "ds_read_b128 a[24:27], v0 offset:12288\n\t"
      "s_add_u32 m0, s48, 36864\n\t"
      "v_mfma_f32_16x16x32_bf16 v[72:75], a[108:111], a[76:79], v[72:75]\n\t"
      "buffer_load_dwordx4 v8, s[36:39], s62 offen lds\n\t"
      "v_mfma_f32_16x16x32_bf16 v[88:91], a[108:111], a[80:83], v[88:91]\n\t"
      "v_mfma_f32_16x16x32_bf16 v[104:107], a[108:111], a[84:87], v[104:107]\n\t"
      "ds_read_b128 a[28:31], v0 offset:14336\n\t"
      "v_mfma_f32_16x16x32_bf16 v[120:123], a[108:111], a[88:91], v[120:123]\n\t"
      "buffer_load_dwordx4 v9, s[36:39], s62 offen offset:1024 lds\n\t"
      "v_mfma_f32_16x16x32_bf16 v[136:139], a[108:111], a[92:95], v[136:139]\n\t"
      "v_mfma_f32_16x16x32_bf16 v[152:155], a[108:111], a[96:99], v[152:155]\n\t"
      "ds_read_b64_tr_b16 a[32:33], v2 offset:0\n\t"
      "v_mfma_f32_16x16x32_bf16 v[156:159], a[112:115], a[96:99], v[156:159]\n\t"
      "v_mfma_f32_16x16x32_bf16 v[140:143], a[112:115], a[92:95], v[140:143]\n\t"
      "buffer_load_dwordx4 v10, s[36:39], s62 offen offset:2048 lds\n\t"
      "v_mfma_f32_16x16x32_bf16 v[124:127], a[112:115], a[88:91], v[124:127]\n\t"
      "ds_read_b64_tr_b16 a[34:35], v2 offset:2048\n\t"
      "v_mfma_f32_16x16x32_bf16 v[108:111], a[112:115], a[84:87], v[108:111]\n\t"
      "v_mfma_f32_16x16x32_bf16 v[92:95], a[112:115], a[80:83], v[92:95]\n\t"
      "v_mfma_f32_16x16x32_bf16 v[76:79], a[112:115], a[76:79], v[76:79]\n\t"
      "ds_read_b64_tr_b16 a[36:37], v3 offset:0\n\t"
      "buffer_load_dwordx4 v11, s[36:39], s62 offen offset:3072 lds\n\t"
      "v_mfma_f32_16x16x32_bf16 v[60:63], a[112:115], a[72:75], v[60:63]\n\t"
      "s_add_u32 s52, s52, 128\n\t"
      "v_mfma_f32_16x16x32_bf16 v[44:47], a[112:115], a[68:71], v[44:47]\n\t"
      "ds_read_b64_tr_b16 a[38:39], v3 offset:2048\n\t"
      "v_mfma_f32_16x16x32_bf16 v[160:163], a[68:71], a[116:119], v[160:163]\n\t"
      "s_add_u32 m0, s50, 20480\n\t"
      "v_mfma_f32_16x16x32_bf16 v[164:167], a[72:75], a[116:119], v[164:167]\n\t"
      "buffer_load_dwordx4 v14, s[44:47], s54 offen lds\n\t"
      "v_mfma_f32_16x16x32_bf16 v[168:171], a[76:79], a[116:119], v[168:171]\n\t"
      "ds_read_b64_tr_b16 a[40:41], v2 offset:4096\n\t"
      "v_mfma_f32_16x16x32_bf16 v[172:175], a[80:83], a[116:119], v[172:175]\n\t"
      "v_mfma_f32_16x16x32_bf16 v[176:179], a[84:87], a[116:119], v[176:179]\n\t"
      "buffer_load_dwordx4 v15, s[44:47], s54 offen offset:1024 lds\n\t"
      "v_mfma_f32_16x16x32_bf16 v[180:183], a[88:91], a[116:119], v[180:183]\n\t"
      "ds_read_b64_tr_b16 a[42:43], v2 offset:6144\n\t"
      "v_mfma_f32_16x16x32_bf16 v[184:187], a[92:95], a[116:119], v[184:187]\n\t"
      "v_mfma_f32_16x16x32_bf16 v[188:191], a[96:99], a[116:119], v[188:191]\n\t"
      "v_mfma_f32_16x16x32_bf16 v[220:223], a[96:99], a[120:123], v[220:223]\n\t"
      "ds_read_b64_tr_b16 a[44:45], v3 offset:4096\n\t"
      "buffer_load_dwordx4 v19, s[44:47], s54 offen offset:2048 lds\n\t"
      "v_mfma_f32_16x16x32_bf16 v[216:219], a[92:95], a[120:123], v[216:219]\n\t"
      "v_mfma_f32_16x16x32_bf16 v[212:215], a[88:91], a[120:123], v[212:215]\n\t"
      "v_mfma_f32_16x16x32_bf16 v[208:211], a[84:87], a[120:123], v[208:211]\n\t"
      "ds_read_b64_tr_b16 a[46:47], v3 offset:6144\n\t"
      "v_mfma_f32_16x16x32_bf16 v[204:207], a[80:83], a[120:123], v[204:207]\n\t"
      "buffer_load_dwordx4 v4, s[44:47], s54 offen offset:3072 lds\n\t"
      "v_mfma_f32_16x16x32_bf16 v[200:203], a[76:79], a[120:123], v[200:203]\n\t"
      "v_mfma_f32_16x16x32_bf16 v[196:199], a[72:75], a[120:123], v[196:199]\n\t"
      "ds_read_b128 a[48:51], v6 offset:0\n\t"
      "v_mfma_f32_16x16x32_bf16 v[192:195], a[68:71], a[120:123], v[192:195]\n\t"
      "s_add_u32 m0, s50, 24576\n\t"
      "v_mfma_f32_16x16x32_bf16 v[224:227], a[68:71], a[124:127], v[224:227]\n\t"
      "buffer_load_dwordx4 v5, s[44:47], s54 offen lds\n\t"
      "v_mfma_f32_16x16x32_bf16 v[228:231], a[72:75], a[124:127], v[228:231]\n\t"
      "ds_read_b128 a[52:55], v6 offset:2048\n\t"
      "s_add_u32 s54, s54, 128\n\t"
      "v_mfma_f32_16x16x32_bf16 v[232:235], a[76:79], a[124:127], v[232:235]\n\t"
      "v_mfma_f32_16x16x32_bf16 v[236:239], a[80:83], a[124:127], v[236:239]\n\t"
      "v_mfma_f32_16x16x32_bf16 v[240:243], a[84:87], a[124:127], v[240:243]\n\t"
      "ds_read_b128 a[56:59], v6 offset:4096\n\t"
      "s_add_u32 m0, s49, 24576\n\t"
      "v_mfma_f32_16x16x32_bf16 v[244:247], a[88:91], a[124:127], v[244:247]\n\t"
      "buffer_load_dwordx4 v12, s[40:43], s53 offen lds\n\t"
      "v_mfma_f32_16x16x32_bf16 v[248:251], a[92:95], a[124:127], v[248:251]\n\t"
      "v_mfma_f32_16x16x32_bf16 v[252:255], a[96:99], a[124:127], v[252:255]\n\t"
      "ds_read_b128 a[60:63], v6 offset:6144\n\t"
      "v_mfma_f32_16x16x32_bf16 a[220:223], a[96:99], a[128:131], a[220:223]\n\t"
      "v_mfma_f32_16x16x32_bf16 a[216:219], a[92:95], a[128:131], a[216:219]\n\t"
      "v_mfma_f32_16x16x32_bf16 a[212:215], a[88:91], a[128:131], a[212:215]\n\t"
      "ds_read_b128 a[64:67], v6 offset:8192\n\t"
      "v_mfma_f32_16x16x32_bf16 a[208:211], a[84:87], a[128:131], a[208:211]\n\t"
      "v_mfma_f32_16x16x32_bf16 a[204:207], a[80:83], a[128:131], a[204:207]\n\t"
      "v_mfma_f32_16x16x32_bf16 a[200:203], a[76:79], a[128:131], a[200:203]\n\t"
      "v_mfma_f32_16x16x32_bf16 a[196:199], a[72:75], a[128:131], a[196:199]\n\t"
      "v_mfma_f32_16x16x32_bf16 a[192:195], a[68:71], a[128:131], a[192:195]\n\t"
      "v_mfma_f32_16x16x32_bf16 a[224:227], a[68:71], a[132:135], a[224:227]\n\t"
      "v_mfma_f32_16x16x32_bf16 a[228:231], a[72:75], a[132:135], a[228:231]\n\t"
      "v_mfma_f32_16x16x32_bf16 a[232:235], a[76:79], a[132:135], a[232:235]\n\t"
      "buffer_load_dwordx4 v13, s[40:43], s53 offen offset:1024 lds\n\t"
      "v_mfma_f32_16x16x32_bf16 a[236:239], a[80:83], a[132:135], a[236:239]\n\t"
      "s_add_u32 s53, s53, 229376\n\t"
      "s_sub_u32 s55, s55, 1\n\t"
      "v_mfma_f32_16x16x32_bf16 a[240:243], a[84:87], a[132:135], a[240:243]\n\t"
      "v_mfma_f32_16x16x32_bf16 a[244:247], a[88:91], a[132:135], a[244:247]\n\t"
      "v_mfma_f32_16x16x32_bf16 a[248:251], a[92:95], a[132:135], a[248:251]\n\t"
      "v_mfma_f32_16x16x32_bf16 a[252:255], a[96:99], a[132:135], a[252:255]\n\t"
      "s_cmp_lg_u32 s55, 0\n\t"
      "s_cbranch_scc1 LA_loop_r1_%=\n\t"
      "s_branch LA_tail_r1_%=\n\t"
      "LA_cs_r1_%=:\n\t"
      "s_cmp_eq_u32 s73, 0\n\t"
      "s_cbranch_scc0 LA_cs1_r1_%=\n\t"
      "s_waitcnt vmcnt(19) lgkmcnt(0)\n\t"
      "s_barrier\n\t"
      "v_mfma_f32_16x16x32_bf16 v[32:35], a[32:35], a[0:3], v[32:35]\n\t"
      "ds_read_b128 a[68:71], v1 offset:0\n\t"
      "buffer_load_dword v18, v16, s[56:59], s60 offen\n\t"
      "v_mfma_f32_16x16x32_bf16 v[48:51], a[32:35], a[4:7], v[48:51]\n\t"
      "s_add_u32 s60, s60, s61\n\t"
      "v_mfma_f32_16x16x32_bf16 v[64:67], a[32:35], a[8:11], v[64:67]\n\t"
      "ds_read_b128 a[72:75], v1 offset:2048\n\t"
      "v_mfma_f32_16x16x32_bf16 v[80:83], a[32:35], a[12:15], v[80:83]\n\t"
      "s_add_u32 m0, s49, 0\n\t"
      "v_mfma_f32_16x16x32_bf16 v[96:99], a[32:35], a[16:19], v[96:99]\n\t"
      "buffer_load_dwordx4 v12, s[40:43], s53 offen lds\n\t"
      "v_mfma_f32_16x16x32_bf16 v[112:115], a[32:35], a[20:23], v[112:115]\n\t"
      "ds_read_b128 a[76:79], v1 offset:4096\n\t"
      "v_mfma_f32_16x16x32_bf16 v[128:131], a[32:35], a[24:27], v[128:131]\n\t"
      "v_mfma_f32_16x16x32_bf16 v[144:147], a[32:35], a[28:31], v[144:147]\n\t"
      "v_mfma_f32_16x16x32_bf16 a[136:139], a[32:35], a[172:175], 0\n\t"
      "ds_read_b128 a[80:83], v1 offset:6144\n\t"
      "v_mfma_f32_16x16x32_bf16 v[148:151], a[36:39], a[28:31], v[148:151]\n\t"
      "v_mfma_f32_16x16x32_bf16 v[132:135], a[36:39], a[24:27], v[132:135]\n\t"
      "v_mfma_f32_16x16x32_bf16 v[116:119], a[36:39], a[20:23], v[116:119]\n\t"
      "ds_read_b128 a[84:87], v1 offset:8192\n\t"
      "v_mfma_f32_16x16x32_bf16 v[100:103], a[36:39], a[16:19], v[100:103]\n\t"
      "v_mfma_f32_16x16x32_bf16 v[84:87], a[36:39], a[12:15], v[84:87]\n\t"
      "v_mfma_f32_16x16x32_bf16 v[68:71], a[36:39], a[8:11], v[68:71]\n\t"
      "ds_read_b128 a[88:91], v1 offset:10240\n\t"
      "v_mfma_f32_16x16x32_bf16 v[52:55], a[36:39], a[4:7], v[52:55]\n\t"
      "v_mfma_f32_16x16x32_bf16 v[36:39], a[36:39], a[0:3], v[36:39]\n\t"
      "v_mfma_f32_16x16x32_bf16 a[140:143], a[36:39], a[172:175], 0\n\t"
      "ds_read_b128 a[92:95], v1 offset:12288\n\t"
      "v_mfma_f32_16x16x32_bf16 v[40:43], a[40:43], a[0:3], v[40:43]\n\t"
      "v_mfma_f32_16x16x32_bf16 v[56:59], a[40:43], a[4:7], v[56:59]\n\t"
      "v_mfma_f32_16x16x32_bf16 v[72:75], a[40:43], a[8:11], v[72:75]\n\t"
      "ds_read_b128 a[96:99], v1 offset:14336\n\t"
      "v_mfma_f32_16x16x32_bf16 v[88:91], a[40:43], a[12:15], v[88:91]\n\t"
      "v_mfma_f32_16x16x32_bf16 v[104:107], a[40:43], a[16:19], v[104:107]\n\t"
      "v_mfma_f32_16x16x32_bf16 v[120:123], a[40:43], a[20:23], v[120:123]\n\t"
      "ds_read_b64_tr_b16 a[100:101], v2 offset:8192\n\t"
      "v_mfma_f32_16x16x32_bf16 v[136:139], a[40:43], a[24:27], v[136:139]\n\t"
      "v_mfma_f32_16x16x32_bf16 v[152:155], a[40:43], a[28:31], v[152:155]\n\t"
      "v_mfma_f32_16x16x32_bf16 a[144:147], a[40:43], a[172:175], 0\n\t"
      "ds_read_b64_tr_b16 a[102:103], v2 offset:10240\n\t"
      "v_mfma_f32_16x16x32_bf16 v[156:159], a[44:47], a[28:31], v[156:159]\n\t"
      "v_mfma_f32_16x16x32_bf16 v[140:143], a[44:47], a[24:27], v[140:143]\n\t"
      "v_mfma_f32_16x16x32_bf16 v[124:127], a[44:47], a[20:23], v[124:127]\n\t"
      "ds_read_b64_tr_b16 a[104:105], v3 offset:8192\n\t"
      "v_mfma_f32_16x16x32_bf16 v[108:111], a[44:47], a[16:19], v[108:111]\n\t"
      "v_mfma_f32_16x16x32_bf16 v[92:95], a[44:47], a[12:15], v[92:95]\n\t"
      "ds_read_b64_tr_b16 a[106:107], v3 offset:10240\n\t"
      "v_mfma_f32_16x16x32_bf16 v[76:79], a[44:47], a[8:11], v[76:79]\n\t"
      "v_mfma_f32_16x16x32_bf16 v[60:63], a[44:47], a[4:7], v[60:63]\n\t"
      "v_mfma_f32_16x16x32_bf16 v[44:47], a[44:47], a[0:3], v[44:47]\n\t"
      "ds_read_b64_tr_b16 a[108:109], v2 offset:12288\n\t"
      "v_mfma_f32_16x16x32_bf16 a[148:151], a[44:47], a[172:175], 0\n\t"
      "v_mfma_f32_16x16x32_bf16 v[160:163], a[0:3], a[48:51], v[160:163]\n\t"
      "v_mfma_f32_16x16x32_bf16 v[164:167], a[4:7], a[48:51], v[164:167]\n\t"
      "ds_read_b64_tr_b16 a[110:111], v2 offset:14336\n\t"
      "v_mfma_f32_16x16x32_bf16 v[168:171], a[8:11], a[48:51], v[168:171]\n\t"
      "v_mfma_f32_16x16x32_bf16 v[172:175], a[12:15], a[48:51], v[172:175]\n\t"
      "v_mfma_f32_16x16x32_bf16 v[176:179], a[16:19], a[48:51], v[176:179]\n\t"
      "ds_read_b64_tr_b16 a[112:113], v3 offset:12288\n\t"
      "buffer_load_dwordx4 v13, s[40:43], s53 offen offset:1024 lds\n\t"
      "v_mfma_f32_16x16x32_bf16 v[180:183], a[20:23], a[48:51], v[180:183]\n\t"
      "s_add_u32 s53, s53, 229376\n\t"
      "v_mfma_f32_16x16x32_bf16 v[184:187], a[24:27], a[48:51], v[184:187]\n\t"
      "v_mfma_f32_16x16x32_bf16 v[188:191], a[28:31], a[48:51], v[188:191]\n\t"
      "ds_read_b64_tr_b16 a[114:115], v3 offset:14336\n\t"
      "v_mfma_f32_16x16x32_bf16 a[152:155], a[172:175], a[48:51], 0\n\t"
      "v_mfma_f32_16x16x32_bf16 v[220:223], a[28:31], a[52:55], v[220:223]\n\t"
      "v_mfma_f32_16x16x32_bf16 v[216:219], a[24:27], a[52:55], v[216:219]\n\t"
      "ds_read_b128 a[116:119], v7 offset:0\n\t"
      "v_mfma_f32_16x16x32_bf16 v[212:215], a[20:23], a[52:55], v[212:215]\n\t"
      "v_mfma_f32_16x16x32_bf16 v[208:211], a[16:19], a[52:55], v[208:211]\n\t"
      "v_mfma_f32_16x16x32_bf16 v[204:207], a[12:15], a[52:55], v[204:207]\n\t"
      "ds_read_b128 a[120:123], v7 offset:2048\n\t"
      "v_mfma_f32_16x16x32_bf16 v[200:203], a[8:11], a[52:55], v[200:203]\n\t"
      "v_mfma_f32_16x16x32_bf16 v[196:199], a[4:7], a[52:55], v[196:199]\n\t"
      "v_mfma_f32_16x16x32_bf16 v[192:195], a[0:3], a[52:55], v[192:195]\n\t"
      "ds_read_b128 a[124:127], v7 offset:4096\n\t"
      "v_mfma_f32_16x16x32_bf16 v[224:227], a[0:3], a[56:59], v[224:227]\n\t"
      "v_mfma_f32_16x16x32_bf16 v[228:231], a[4:7], a[56:59], v[228:231]\n\t"
      "v_mfma_f32_16x16x32_bf16 v[232:235], a[8:11], a[56:59], v[232:235]\n\t"
      "ds_read_b128 a[128:131], v7 offset:6144\n\t"
      "v_mfma_f32_16x16x32_bf16 v[236:239], a[12:15], a[56:59], v[236:239]\n\t"
      "v_mfma_f32_16x16x32_bf16 v[240:243], a[16:19], a[56:59], v[240:243]\n\t"
      "v_mfma_f32_16x16x32_bf16 v[244:247], a[20:23], a[56:59], v[244:247]\n\t"
      "ds_read_b128 a[132:135], v7 offset:8192\n\t"
      "v_mfma_f32_16x16x32_bf16 v[248:251], a[24:27], a[56:59], v[248:251]\n\t"
      "v_mfma_f32_16x16x32_bf16 v[252:255], a[28:31], a[56:59], v[252:255]\n\t"
      "v_mfma_f32_16x16x32_bf16 a[220:223], a[28:31], a[60:63], a[220:223]\n\t"
      "v_mfma_f32_16x16x32_bf16 a[216:219], a[24:27], a[60:63], a[216:219]\n\t"
      "v_mfma_f32_16x16x32_bf16 a[212:215], a[20:23], a[60:63], a[212:215]\n\t"
      "v_mfma_f32_16x16x32_bf16 a[208:211], a[16:19], a[60:63], a[208:211]\n\t"
      "v_mfma_f32_16x16x32_bf16 a[204:207], a[12:15], a[60:63], a[204:207]\n\t"
      "v_mfma_f32_16x16x32_bf16 a[200:203], a[8:11], a[60:63], a[200:203]\n\t"
      "v_mfma_f32_16x16x32_bf16 a[196:199], a[4:7], a[60:63], a[196:199]\n\t"
      "v_mfma_f32_16x16x32_bf16 a[192:195], a[0:3], a[60:63], a[192:195]\n\t"
      "v_mfma_f32_16x16x32_bf16 a[224:227], a[0:3], a[64:67], a[224:227]\n\t"
      "v_mfma_f32_16x16x32_bf16 a[228:231], a[4:7], a[64:67], a[228:231]\n\t"
      "v_mfma_f32_16x16x32_bf16 a[232:235], a[8:11], a[64:67], a[232:235]\n\t"
      "v_mfma_f32_16x16x32_bf16 a[236:239], a[12:15], a[64:67], a[236:239]\n\t"
      "v_mfma_f32_16x16x32_bf16 a[240:243], a[16:19], a[64:67], a[240:243]\n\t"
      "v_mfma_f32_16x16x32_bf16 a[244:247], a[20:23], a[64:67], a[244:247]\n\t"
      "v_mfma_f32_16x16x32_bf16 a[248:251], a[24:27], a[64:67], a[248:251]\n\t"
      "v_mfma_f32_16x16x32_bf16 a[252:255], a[28:31], a[64:67], a[252:255]\n\t"
      "s_waitcnt vmcnt(5) lgkmcnt(0)\n\t"
      "s_barrier\n\t"
      "v_mfma_f32_16x16x32_bf16 v[32:35], a[100:103], a[68:71], v[32:35]\n\t"
      "ds_read_b128 a[0:3], v0 offset:32768\n\t"
      "buffer_load_dword v18, v16, s[56:59], s60 offen\n\t"
      "v_mfma_f32_16x16x32_bf16 v[48:51], a[100:103], a[72:75], v[48:51]\n\t"
      "s_add_u32 s60, s60, s61\n\t"
      "v_mfma_f32_16x16x32_bf16 v[64:67], a[100:103], a[76:79], v[64:67]\n\t"
      "ds_read_b128 a[4:7], v0 offset:34816\n\t"
      "s_add_u32 s62, s52, 65536\n\t"
      "s_add_u32 m0, s48, 0\n\t"
      "v_mfma_f32_16x16x32_bf16 v[80:83], a[100:103], a[80:83], v[80:83]\n\t"
      "buffer_load_dwordx4 v8, s[36:39], s52 offen lds\n\t"
      "v_mfma_f32_16x16x32_bf16 v[96:99], a[100:103], a[84:87], v[96:99]\n\t"
      "v_mfma_f32_16x16x32_bf16 v[112:115], a[100:103], a[88:91], v[112:115]\n\t"
      "ds_read_b128 a[8:11], v0 offset:36864\n\t"
      "v_mfma_f32_16x16x32_bf16 v[128:131], a[100:103], a[92:95], v[128:131]\n\t"
      "buffer_load_dwordx4 v9, s[36:39], s52 offen offset:1024 lds\n\t"
      "v_mfma_f32_16x16x32_bf16 v[144:147], a[100:103], a[96:99], v[144:147]\n\t"
      "v_mfma_f32_16x16x32_bf16 a[136:139], a[100:103], a[172:175], a[136:139]\n\t"
      "ds_read_b128 a[12:15], v0 offset:38912\n\t"
      "v_mfma_f32_16x16x32_bf16 v[148:151], a[104:107], a[96:99], v[148:151]\n\t"
      "v_mfma_f32_16x16x32_bf16 v[132:135], a[104:107], a[92:95], v[132:135]\n\t"
      "buffer_load_dwordx4 v10, s[36:39], s52 offen offset:2048 lds\n\t"
      "v_mfma_f32_16x16x32_bf16 v[116:119], a[104:107], a[88:91], v[116:119]\n\t"
      "ds_read_b128 a[16:19], v0 offset:40960\n\t"
      "v_mfma_f32_16x16x32_bf16 v[100:103], a[104:107], a[84:87], v[100:103]\n\t"
      "v_mfma_f32_16x16x32_bf16 v[84:87], a[104:107], a[80:83], v[84:87]\n\t"
      "v_mfma_f32_16x16x32_bf16 v[68:71], a[104:107], a[76:79], v[68:71]\n\t"
      "ds_read_b128 a[20:23], v0 offset:43008\n\t"
      "buffer_load_dwordx4 v11, s[36:39], s52 offen offset:3072 lds\n\t"
      "v_mfma_f32_16x16x32_bf16 v[52:55], a[104:107], a[72:75], v[52:55]\n\t"
      "v_mfma_f32_16x16x32_bf16 v[36:39], a[104:107], a[68:71], v[36:39]\n\t"
      "v_mfma_f32_16x16x32_bf16 a[140:143], a[104:107], a[172:175], a[140:143]\n\t"
      "ds_read_b128 a[24:27], v0 offset:45056\n\t"
      "s_add_u32 m0, s48, 4096\n\t"
      "v_mfma_f32_16x16x32_bf16 v[40:43], a[108:111], a[68:71], v[40:43]\n\t"
      "buffer_load_dwordx4 v8, s[36:39], s62 offen lds\n\t"
      "v_mfma_f32_16x16x32_bf16 v[56:59], a[108:111], a[72:75], v[56:59]\n\t"
      "v_mfma_f32_16x16x32_bf16 v[72:75], a[108:111], a[76:79], v[72:75]\n\t"
      "ds_read_b128 a[28:31], v0 offset:47104\n\t"
      "v_mfma_f32_16x16x32_bf16 v[88:91], a[108:111], a[80:83], v[88:91]\n\t"
      "buffer_load_dwordx4 v9, s[36:39], s62 offen offset:1024 lds\n\t"
      "v_mfma_f32_16x16x32_bf16 v[104:107], a[108:111], a[84:87], v[104:107]\n\t"
      "v_mfma_f32_16x16x32_bf16 v[120:123], a[108:111], a[88:91], v[120:123]\n\t"
      "ds_read_b64_tr_b16 a[32:33], v2 offset:16384\n\t"
      "v_mfma_f32_16x16x32_bf16 v[136:139], a[108:111], a[92:95], v[136:139]\n\t"
      "v_mfma_f32_16x16x32_bf16 v[152:155], a[108:111], a[96:99], v[152:155]\n\t"
      "buffer_load_dwordx4 v10, s[36:39], s62 offen offset:2048 lds\n\t"
      "v_mfma_f32_16x16x32_bf16 a[144:147], a[108:111], a[172:175], a[144:147]\n\t"
      "ds_read_b64_tr_b16 a[34:35], v2 offset:18432\n\t"
      "v_mfma_f32_16x16x32_bf16 v[156:159], a[112:115], a[96:99], v[156:159]\n\t"
      "v_mfma_f32_16x16x32_bf16 v[140:143], a[112:115], a[92:95], v[140:143]\n\t"
      "v_mfma_f32_16x16x32_bf16 v[124:127], a[112:115], a[88:91], v[124:127]\n\t"
      "ds_read_b64_tr_b16 a[36:37], v3 offset:16384\n\t"
      "buffer_load_dwordx4 v11, s[36:39], s62 offen offset:3072 lds\n\t"
      "v_mfma_f32_16x16x32_bf16 v[108:111], a[112:115], a[84:87], v[108:111]\n\t"
      "s_add_u32 s52, s52, 128\n\t"
      "v_mfma_f32_16x16x32_bf16 v[92:95], a[112:115], a[80:83], v[92:95]\n\t"
      "ds_read_b64_tr_b16 a[38:39], v3 offset:18432\n\t"
      "v_mfma_f32_16x16x32_bf16 v[76:79], a[112:115], a[76:79], v[76:79]\n\t"
      "s_add_u32 m0, s50, 0\n\t"
      "v_mfma_f32_16x16x32_bf16 v[60:63], a[112:115], a[72:75], v[60:63]\n\t"
      "buffer_load_dwordx4 v14, s[44:47], s54 offen lds\n\t"
      "v_mfma_f32_16x16x32_bf16 v[44:47], a[112:115], a[68:71], v[44:47]\n\t"
      "ds_read_b64_tr_b16 a[40:41], v2 offset:20480\n\t"
      "v_mfma_f32_16x16x32_bf16 a[148:151], a[112:115], a[172:175], a[148:151]\n\t"
      "v_mfma_f32_16x16x32_bf16 v[160:163], a[68:71], a[116:119], v[160:163]\n\t"
      "buffer_load_dwordx4 v15, s[44:47], s54 offen offset:1024 lds\n\t"
      "v_mfma_f32_16x16x32_bf16 v[164:167], a[72:75], a[116:119], v[164:167]\n\t"
      "ds_read_b64_tr_b16 a[42:43], v2 offset:22528\n\t"
      "v_mfma_f32_16x16x32_bf16 v[168:171], a[76:79], a[116:119], v[168:171]\n\t"
      "v_mfma_f32_16x16x32_bf16 v[172:175], a[80:83], a[116:119], v[172:175]\n\t"
      "v_mfma_f32_16x16x32_bf16 v[176:179], a[84:87], a[116:119], v[176:179]\n\t"
      "ds_read_b64_tr_b16 a[44:45], v3 offset:20480\n\t"
      "buffer_load_dwordx4 v19, s[44:47], s54 offen offset:2048 lds\n\t"
      "v_mfma_f32_16x16x32_bf16 v[180:183], a[88:91], a[116:119], v[180:183]\n\t"
      "v_mfma_f32_16x16x32_bf16 v[184:187], a[92:95], a[116:119], v[184:187]\n\t"
      "v_mfma_f32_16x16x32_bf16 v[188:191], a[96:99], a[116:119], v[188:191]\n\t"
      "ds_read_b64_tr_b16 a[46:47], v3 offset:22528\n\t"
      "v_mfma_f32_16x16x32_bf16 a[152:155], a[172:175], a[116:119], a[152:155]\n\t"
      "buffer_load_dwordx4 v4, s[44:47], s54 offen offset:3072 lds\n\t"
      "v_mfma_f32_16x16x32_bf16 v[220:223], a[96:99], a[120:123], v[220:223]\n\t"
      "v_mfma_f32_16x16x32_bf16 v[216:219], a[92:95], a[120:123], v[216:219]\n\t"
      "ds_read_b128 a[48:51], v6 offset:20480\n\t"
      "v_mfma_f32_16x16x32_bf16 v[212:215], a[88:91], a[120:123], v[212:215]\n\t"
      "s_add_u32 m0, s50, 4096\n\t"
      "v_mfma_f32_16x16x32_bf16 v[208:211], a[84:87], a[120:123], v[208:211]\n\t"
      "buffer_load_dwordx4 v5, s[44:47], s54 offen lds\n\t"
      "v_mfma_f32_16x16x32_bf16 v[204:207], a[80:83], a[120:123], v[204:207]\n\t"
      "ds_read_b128 a[52:55], v6 offset:22528\n\t"
      "s_add_u32 s54, s54, 128\n\t"
      "v_mfma_f32_16x16x32_bf16 v[200:203], a[76:79], a[120:123], v[200:203]\n\t"
      "v_mfma_f32_16x16x32_bf16 v[196:199], a[72:75], a[120:123], v[196:199]\n\t"
      "v_mfma_f32_16x16x32_bf16 v[192:195], a[68:71], a[120:123], v[192:195]\n\t"
      "ds_read_b128 a[56:59], v6 offset:24576\n\t"
      "s_add_u32 m0, s49, 8192\n\t"
      "v_mfma_f32_16x16x32_bf16 v[224:227], a[68:71], a[124:127], v[224:227]\n\t"
      "buffer_load_dwordx4 v12, s[40:43], s53 offen lds\n\t"
      "v_mfma_f32_16x16x32_bf16 v[228:231], a[72:75], a[124:127], v[228:231]\n\t"
      "v_mfma_f32_16x16x32_bf16 v[232:235], a[76:79], a[124:127], v[232:235]\n\t"
      "ds_read_b128 a[60:63], v6 offset:26624\n\t"
      "v_mfma_f32_16x16x32_bf16 v[236:239], a[80:83], a[124:127], v[236:239]\n\t"
      "v_mfma_f32_16x16x32_bf16 v[240:243], a[84:87], a[124:127], v[240:243]\n\t"
      "v_mfma_f32_16x16x32_bf16 v[244:247], a[88:91], a[124:127], v[244:247]\n\t"
      "ds_read_b128 a[64:67], v6 offset:28672\n\t"
      "v_mfma_f32_16x16x32_bf16 v[248:251], a[92:95], a[124:127], v[248:251]\n\t"
      "v_mfma_f32_16x16x32_bf16 v[252:255], a[96:99], a[124:127], v[252:255]\n\t"
      "v_mfma_f32_16x16x32_bf16 a[220:223], a[96:99], a[128:131], a[220:223]\n\t"
      "v_mfma_f32_16x16x32_bf16 a[216:219], a[92:95], a[128:131], a[216:219]\n\t"
      "v_mfma_f32_16x16x32_bf16 a[212:215], a[88:91], a[128:131], a[212:215]\n\t"
      "v_mfma_f32_16x16x32_bf16 a[208:211], a[84:87], a[128:131], a[208:211]\n\t"
      "v_mfma_f32_16x16x32_bf16 a[204:207], a[80:83], a[128:131], a[204:207]\n\t"
      "v_mfma_f32_16x16x32_bf16 a[200:203], a[76:79], a[128:131], a[200:203]\n\t"
      "buffer_load_dwordx4 v13, s[40:43], s53 offen offset:1024 lds\n\t"
      "v_mfma_f32_16x16x32_bf16 a[196:199], a[72:75], a[128:131], a[196:199]\n\t"
      "s_add_u32 s53, s53, 229376\n\t"
      "v_mfma_f32_16x16x32_bf16 a[192:195], a[68:71], a[128:131], a[192:195]\n\t"
      "v_mfma_f32_16x16x32_bf16 a[224:227], a[68:71], a[132:135], a[224:227]\n\t"
      "v_mfma_f32_16x16x32_bf16 a[228:231], a[72:75], a[132:135], a[228:231]\n\t"
      "v_mfma_f32_16x16x32_bf16 a[232:235], a[76:79], a[132:135], a[232:235]\n\t"
      "v_mfma_f32_16x16x32_bf16 a[236:239], a[80:83], a[132:135], a[236:239]\n\t"
      "v_mfma_f32_16x16x32_bf16 a[240:243], a[84:87], a[132:135], a[240:243]\n\t"
      "v_mfma_f32_16x16x32_bf16 a[244:247], a[88:91], a[132:135], a[244:247]\n\t"
      "v_mfma_f32_16x16x32_bf16 a[248:251], a[92:95], a[132:135], a[248:251]\n\t"
      "v_mfma_f32_16x16x32_bf16 a[252:255], a[96:99], a[132:135], a[252:255]\n\t"
      "s_waitcnt vmcnt(19) lgkmcnt(0)\n\t"
      "s_barrier\n\t"
      "v_mfma_f32_16x16x32_bf16 v[32:35], a[32:35], a[0:3], v[32:35]\n\t"
      "ds_read_b128 a[68:71], v1 offset:32768\n\t"
      "buffer_load_dword v18, v16, s[56:59], s60 offen\n\t"
      "v_mfma_f32_16x16x32_bf16 v[48:51], a[32:35], a[4:7], v[48:51]\n\t"
      "s_add_u32 s60, s60, s61\n\t"
      "v_mfma_f32_16x16x32_bf16 v[64:67], a[32:35], a[8:11], v[64:67]\n\t"
      "ds_read_b128 a[72:75], v1 offset:34816\n\t"
      "v_mfma_f32_16x16x32_bf16 v[80:83], a[32:35], a[12:15], v[80:83]\n\t"
      "s_add_u32 m0, s49, 16384\n\t"
      "v_mfma_f32_16x16x32_bf16 v[96:99], a[32:35], a[16:19], v[96:99]\n\t"
      "buffer_load_dwordx4 v12, s[40:43], s53 offen lds\n\t"
      "v_mfma_f32_16x16x32_bf16 v[112:115], a[32:35], a[20:23], v[112:115]\n\t"
      "ds_read_b128 a[76:79], v1 offset:36864\n\t"
      "v_mfma_f32_16x16x32_bf16 v[128:131], a[32:35], a[24:27], v[128:131]\n\t"
      "v_mfma_f32_16x16x32_bf16 v[144:147], a[32:35], a[28:31], v[144:147]\n\t"
      "v_mfma_f32_16x16x32_bf16 a[136:139], a[32:35], a[172:175], a[136:139]\n\t"
      "ds_read_b128 a[80:83], v1 offset:38912\n\t"
      "v_mfma_f32_16x16x32_bf16 v[148:151], a[36:39], a[28:31], v[148:151]\n\t"
      "v_mfma_f32_16x16x32_bf16 v[132:135], a[36:39], a[24:27], v[132:135]\n\t"
      "v_mfma_f32_16x16x32_bf16 v[116:119], a[36:39], a[20:23], v[116:119]\n\t"
      "ds_read_b128 a[84:87], v1 offset:40960\n\t"
      "v_mfma_f32_16x16x32_bf16 v[100:103], a[36:39], a[16:19], v[100:103]\n\t"
      "v_mfma_f32_16x16x32_bf16 v[84:87], a[36:39], a[12:15], v[84:87]\n\t"
      "v_mfma_f32_16x16x32_bf16 v[68:71], a[36:39], a[8:11], v[68:71]\n\t"
      "ds_read_b128 a[88:91], v1 offset:43008\n\t"
      "v_mfma_f32_16x16x32_bf16 v[52:55], a[36:39], a[4:7], v[52:55]\n\t"
      "v_mfma_f32_16x16x32_bf16 v[36:39], a[36:39], a[0:3], v[36:39]\n\t"
      "v_mfma_f32_16x16x32_bf16 a[140:143], a[36:39], a[172:175], a[140:143]\n\t"
      "ds_read_b128 a[92:95], v1 offset:45056\n\t"
      "v_mfma_f32_16x16x32_bf16 v[40:43], a[40:43], a[0:3], v[40:43]\n\t"
      "v_mfma_f32_16x16x32_bf16 v[56:59], a[40:43], a[4:7], v[56:59]\n\t"
      "v_mfma_f32_16x16x32_bf16 v[72:75], a[40:43], a[8:11], v[72:75]\n\t"
      "ds_read_b128 a[96:99], v1 offset:47104\n\t"
      "v_mfma_f32_16x16x32_bf16 v[88:91], a[40:43], a[12:15], v[88:91]\n\t"
      "v_mfma_f32_16x16x32_bf16 v[104:107], a[40:43], a[16:19], v[104:107]\n\t"
      "v_mfma_f32_16x16x32_bf16 v[120:123], a[40:43], a[20:23], v[120:123]\n\t"
      "ds_read_b64_tr_b16 a[100:101], v2 offset:24576\n\t"
      "v_mfma_f32_16x16x32_bf16 v[136:139], a[40:43], a[24:27], v[136:139]\n\t"
      "v_mfma_f32_16x16x32_bf16 v[152:155], a[40:43], a[28:31], v[152:155]\n\t"
      "v_mfma_f32_16x16x32_bf16 a[144:147], a[40:43], a[172:175], a[144:147]\n\t"
      "ds_read_b64_tr_b16 a[102:103], v2 offset:26624\n\t"
      "v_mfma_f32_16x16x32_bf16 v[156:159], a[44:47], a[28:31], v[156:159]\n\t"
      "v_mfma_f32_16x16x32_bf16 v[140:143], a[44:47], a[24:27], v[140:143]\n\t"
      "v_mfma_f32_16x16x32_bf16 v[124:127], a[44:47], a[20:23], v[124:127]\n\t"
      "ds_read_b64_tr_b16 a[104:105], v3 offset:24576\n\t"
      "v_mfma_f32_16x16x32_bf16 v[108:111], a[44:47], a[16:19], v[108:111]\n\t"
      "v_mfma_f32_16x16x32_bf16 v[92:95], a[44:47], a[12:15], v[92:95]\n\t"
      "ds_read_b64_tr_b16 a[106:107], v3 offset:26624\n\t"
      "v_mfma_f32_16x16x32_bf16 v[76:79], a[44:47], a[8:11], v[76:79]\n\t"
      "v_mfma_f32_16x16x32_bf16 v[60:63], a[44:47], a[4:7], v[60:63]\n\t"
      "v_mfma_f32_16x16x32_bf16 v[44:47], a[44:47], a[0:3], v[44:47]\n\t"
      "ds_read_b64_tr_b16 a[108:109], v2 offset:28672\n\t"
      "v_mfma_f32_16x16x32_bf16 a[148:151], a[44:47], a[172:175], a[148:151]\n\t"
      "v_mfma_f32_16x16x32_bf16 v[160:163], a[0:3], a[48:51], v[160:163]\n\t"
      "v_mfma_f32_16x16x32_bf16 v[164:167], a[4:7], a[48:51], v[164:167]\n\t"
      "ds_read_b64_tr_b16 a[110:111], v2 offset:30720\n\t"
      "v_mfma_f32_16x16x32_bf16 v[168:171], a[8:11], a[48:51], v[168:171]\n\t"
      "v_mfma_f32_16x16x32_bf16 v[172:175], a[12:15], a[48:51], v[172:175]\n\t"
      "v_mfma_f32_16x16x32_bf16 v[176:179], a[16:19], a[48:51], v[176:179]\n\t"
      "ds_read_b64_tr_b16 a[112:113], v3 offset:28672\n\t"
      "buffer_load_dwordx4 v13, s[40:43], s53 offen offset:1024 lds\n\t"
      "v_mfma_f32_16x16x32_bf16 v[180:183], a[20:23], a[48:51], v[180:183]\n\t"
      "s_add_u32 s53, s53, 229376\n\t"
      "v_mfma_f32_16x16x32_bf16 v[184:187], a[24:27], a[48:51], v[184:187]\n\t"
      "v_mfma_f32_16x16x32_bf16 v[188:191], a[28:31], a[48:51], v[188:191]\n\t"
      "ds_read_b64_tr_b16 a[114:115], v3 offset:30720\n\t"
      "v_mfma_f32_16x16x32_bf16 a[152:155], a[172:175], a[48:51], a[152:155]\n\t"
      "v_mfma_f32_16x16x32_bf16 v[220:223], a[28:31], a[52:55], v[220:223]\n\t"
      "v_mfma_f32_16x16x32_bf16 v[216:219], a[24:27], a[52:55], v[216:219]\n\t"
      "ds_read_b128 a[116:119], v7 offset:20480\n\t"
      "v_mfma_f32_16x16x32_bf16 v[212:215], a[20:23], a[52:55], v[212:215]\n\t"
      "v_mfma_f32_16x16x32_bf16 v[208:211], a[16:19], a[52:55], v[208:211]\n\t"
      "v_mfma_f32_16x16x32_bf16 v[204:207], a[12:15], a[52:55], v[204:207]\n\t"
      "ds_read_b128 a[120:123], v7 offset:22528\n\t"
      "v_mfma_f32_16x16x32_bf16 v[200:203], a[8:11], a[52:55], v[200:203]\n\t"
      "v_mfma_f32_16x16x32_bf16 v[196:199], a[4:7], a[52:55], v[196:199]\n\t"
      "v_mfma_f32_16x16x32_bf16 v[192:195], a[0:3], a[52:55], v[192:195]\n\t"
      "ds_read_b128 a[124:127], v7 offset:24576\n\t"
      "v_mfma_f32_16x16x32_bf16 v[224:227], a[0:3], a[56:59], v[224:227]\n\t"
      "v_mfma_f32_16x16x32_bf16 v[228:231], a[4:7], a[56:59], v[228:231]\n\t"
      "v_mfma_f32_16x16x32_bf16 v[232:235], a[8:11], a[56:59], v[232:235]\n\t"
      "ds_read_b128 a[128:131], v7 offset:26624\n\t"
      "v_mfma_f32_16x16x32_bf16 v[236:239], a[12:15], a[56:59], v[236:239]\n\t"
      "v_mfma_f32_16x16x32_bf16 v[240:243], a[16:19], a[56:59], v[240:243]\n\t"
      "v_mfma_f32_16x16x32_bf16 v[244:247], a[20:23], a[56:59], v[244:247]\n\t"
      "ds_read_b128 a[132:135], v7 offset:28672\n\t"
      "v_mfma_f32_16x16x32_bf16 v[248:251], a[24:27], a[56:59], v[248:251]\n\t"
      "v_mfma_f32_16x16x32_bf16 v[252:255], a[28:31], a[56:59], v[252:255]\n\t"
      "v_mfma_f32_16x16x32_bf16 a[220:223], a[28:31], a[60:63], a[220:223]\n\t"
      "v_mfma_f32_16x16x32_bf16 a[216:219], a[24:27], a[60:63], a[216:219]\n\t"
      "v_mfma_f32_16x16x32_bf16 a[212:215], a[20:23], a[60:63], a[212:215]\n\t"
      "v_mfma_f32_16x16x32_bf16 a[208:211], a[16:19], a[60:63], a[208:211]\n\t"
      "v_mfma_f32_16x16x32_bf16 a[204:207], a[12:15], a[60:63], a[204:207]\n\t"
      "v_mfma_f32_16x16x32_bf16 a[200:203], a[8:11], a[60:63], a[200:203]\n\t"
      "v_mfma_f32_16x16x32_bf16 a[196:199], a[4:7], a[60:63], a[196:199]\n\t"
      "v_mfma_f32_16x16x32_bf16 a[192:195], a[0:3], a[60:63], a[192:195]\n\t"
      "v_mfma_f32_16x16x32_bf16 a[224:227], a[0:3], a[64:67], a[224:227]\n\t"
      "v_mfma_f32_16x16x32_bf16 a[228:231], a[4:7], a[64:67], a[228:231]\n\t"
      "v_mfma_f32_16x16x32_bf16 a[232:235], a[8:11], a[64:67], a[232:235]\n\t"
      "v_mfma_f32_16x16x32_bf16 a[236:239], a[12:15], a[64:67], a[236:239]\n\t"
      "v_mfma_f32_16x16x32_bf16 a[240:243], a[16:19], a[64:67], a[240:243]\n\t"
      "v_mfma_f32_16x16x32_bf16 a[244:247], a[20:23], a[64:67], a[244:247]\n\t"
      "v_mfma_f32_16x16x32_bf16 a[248:251], a[24:27], a[64:67], a[248:251]\n\t"
      "v_mfma_f32_16x16x32_bf16 a[252:255], a[28:31], a[64:67], a[252:255]\n\t"
      "s_waitcnt vmcnt(5) lgkmcnt(0)\n\t"
      "s_bitcmp1_b32 s81, s55\n\t"
      "s_cbranch_scc0 LA_nofl0_r1_%=\n\t"
      "s_not_b32 s74, s68\n\t"
      "s_not_b32 s75, s69\n\t"
      "s_cmp_eq_u32 s63, 1\n\t"
      "s_cselect_b32 s74, s68, s74\n\t"
      "s_cselect_b32 s75, s69, s75\n\t"
      "v_mov_b32 v22, s74\n\t"
      "v_mov_b32 v23, s75\n\t"
      "s_mov_b64 exec, 1\n\t"
      "buffer_store_dwordx4 v[20:23], v17, s[64:67], 0 offen sc1 ; alt\n\t"
      "s_mov_b64 exec, -1\n\t"
      "s_nop 1\n\t"
      "LA_nofl0_r1_%=:\n\t"
      "s_barrier\n\t"
      "v_mfma_f32_16x16x32_bf16 v[32:35], a[100:103], a[68:71], v[32:35]\n\t"
      "ds_read_b128 a[0:3], v0 offset:0\n\t"
      "buffer_load_dword v18, v16, s[56:59], s60 offen\n\t"
      "v_mfma_f32_16x16x32_bf16 v[48:51], a[100:103], a[72:75], v[48:51]\n\t"
      "s_add_u32 s60, s60, s61\n\t"
      "v_mfma_f32_16x16x32_bf16 v[64:67], a[100:103], a[76:79], v[64:67]\n\t"
      "ds_read_b128 a[4:7], v0 offset:2048\n\t"
      "s_add_u32 s62, s52, 65536\n\t"
      "s_add_u32 m0, s48, 32768\n\t"
      "v_mfma_f32_16x16x32_bf16 v[80:83], a[100:103], a[80:83], v[80:83]\n\t"
      "buffer_load_dwordx4 v8, s[36:39], s52 offen lds\n\t"
      "v_mfma_f32_16x16x32_bf16 v[96:99], a[100:103], a[84:87], v[96:99]\n\t"
      "v_mfma_f32_16x16x32_bf16 v[112:115], a[100:103], a[88:91], v[112:115]\n\t"
      "ds_read_b128 a[8:11], v0 offset:4096\n\t"
      "v_mfma_f32_16x16x32_bf16 v[128:131], a[100:103], a[92:95], v[128:131]\n\t"
      "buffer_load_dwordx4 v9, s[36:39], s52 offen offset:1024 lds\n\t"
      "v_mfma_f32_16x16x32_bf16 v[144:147], a[100:103], a[96:99], v[144:147]\n\t"
      "v_mfma_f32_16x16x32_bf16 a[136:139], a[100:103], a[172:175], a[136:139]\n\t"
      "ds_read_b128 a[12:15], v0 offset:6144\n\t"
      "v_mfma_f32_16x16x32_bf16 v[148:151], a[104:107], a[96:99], v[148:151]\n\t"
      "v_mfma_f32_16x16x32_bf16 v[132:135], a[104:107], a[92:95], v[132:135]\n\t"
      "buffer_load_dwordx4 v10, s[36:39], s52 offen offset:2048 lds\n\t"
      "v_mfma_f32_16x16x32_bf16 v[116:119], a[104:107], a[88:91], v[116:119]\n\t"
      "ds_read_b128 a[16:19], v0 offset:8192\n\t"
      "v_mfma_f32_16x16x32_bf16 v[100:103], a[104:107], a[84:87], v[100:103]\n\t"
      "v_mfma_f32_16x16x32_bf16 v[84:87], a[104:107], a[80:83], v[84:87]\n\t"
      "v_mfma_f32_16x16x32_bf16 v[68:71], a[104:107], a[76:79], v[68:71]\n\t"
      "ds_read_b128 a[20:23], v0 offset:10240\n\t"
      "buffer_load_dwordx4 v11, s[36:39], s52 offen offset:3072 lds\n\t"
      "v_mfma_f32_16x16x32_bf16 v[52:55], a[104:107], a[72:75], v[52:55]\n\t"
      "v_mfma_f32_16x16x32_bf16 v[36:39], a[104:107], a[68:71], v[36:39]\n\t"
      "v_mfma_f32_16x16x32_bf16 a[140:143], a[104:107], a[172:175], a[140:143]\n\t"
      "ds_read_b128 a[24:27], v0 offset:12288\n\t"
      "s_add_u32 m0, s48, 36864\n\t"
      "v_mfma_f32_16x16x32_bf16 v[40:43], a[108:111], a[68:71], v[40:43]\n\t"
      "buffer_load_dwordx4 v8, s[36:39], s62 offen lds\n\t"
      "v_mfma_f32_16x16x32_bf16 v[56:59], a[108:111], a[72:75], v[56:59]\n\t"
      "v_mfma_f32_16x16x32_bf16 v[72:75], a[108:111], a[76:79], v[72:75]\n\t"
      "ds_read_b128 a[28:31], v0 offset:14336\n\t"
      "v_mfma_f32_16x16x32_bf16 v[88:91], a[108:111], a[80:83], v[88:91]\n\t"
      "buffer_load_dwordx4 v9, s[36:39], s62 offen offset:1024 lds\n\t"
      "v_mfma_f32_16x16x32_bf16 v[104:107], a[108:111], a[84:87], v[104:107]\n\t"
      "v_mfma_f32_16x16x32_bf16 v[120:123], a[108:111], a[88:91], v[120:123]\n\t"
      "ds_read_b64_tr_b16 a[32:33], v2 offset:0\n\t"
      "v_mfma_f32_16x16x32_bf16 v[136:139], a[108:111], a[92:95], v[136:139]\n\t"
      "v_mfma_f32_16x16x32_bf16 v[152:155], a[108:111], a[96:99], v[152:155]\n\t"
      "buffer_load_dwordx4 v10, s[36:39], s62 offen offset:2048 lds\n\t"
      "v_mfma_f32_16x16x32_bf16 a[144:147], a[108:111], a[172:175], a[144:147]\n\t"
      "ds_read_b64_tr_b16 a[34:35], v2 offset:2048\n\t"
      "v_mfma_f32_16x16x32_bf16 v[156:159], a[112:115], a[96:99], v[156:159]\n\t"
      "v_mfma_f32_16x16x32_bf16 v[140:143], a[112:115], a[92:95], v[140:143]\n\t"
      "v_mfma_f32_16x16x32_bf16 v[124:127], a[112:115], a[88:91], v[124:127]\n\t"
      "ds_read_b64_tr_b16 a[36:37], v3 offset:0\n\t"
      "buffer_load_dwordx4 v11, s[36:39], s62 offen offset:3072 lds\n\t"
      "v_mfma_f32_16x16x32_bf16 v[108:111], a[112:115], a[84:87], v[108:111]\n\t"
      "s_add_u32 s52, s52, 128\n\t"
      "v_mfma_f32_16x16x32_bf16 v[92:95], a[112:115], a[80:83], v[92:95]\n\t"
      "ds_read_b64_tr_b16 a[38:39], v3 offset:2048\n\t"
      "v_mfma_f32_16x16x32_bf16 v[76:79], a[112:115], a[76:79], v[76:79]\n\t"
      "s_add_u32 m0, s50, 20480\n\t"
      "v_mfma_f32_16x16x32_bf16 v[60:63], a[112:115], a[72:75], v[60:63]\n\t"
      "buffer_load_dwordx4 v14, s[44:47], s54 offen lds\n\t"
      "v_mfma_f32_16x16x32_bf16 v[44:47], a[112:115], a[68:71], v[44:47]\n\t"
      "ds_read_b64_tr_b16 a[40:41], v2 offset:4096\n\t"
      "v_mfma_f32_16x16x32_bf16 a[148:151], a[112:115], a[172:175], a[148:151]\n\t"
      "v_mfma_f32_16x16x32_bf16 v[160:163], a[68:71], a[116:119], v[160:163]\n\t"
      "buffer_load_dwordx4 v15, s[44:47], s54 offen offset:1024 lds\n\t"
      "v_mfma_f32_16x16x32_bf16 v[164:167], a[72:75], a[116:119], v[164:167]\n\t"
      "ds_read_b64_tr_b16 a[42:43], v2 offset:6144\n\t"
      "v_mfma_f32_16x16x32_bf16 v[168:171], a[76:79], a[116:119], v[168:171]\n\t"
      "v_mfma_f32_16x16x32_bf16 v[172:175], a[80:83], a[116:119], v[172:175]\n\t"
      "v_mfma_f32_16x16x32_bf16 v[176:179], a[84:87], a[116:119], v[176:179]\n\t"
      "ds_read_b64_tr_b16 a[44:45], v3 offset:4096\n\t"
      "buffer_load_dwordx4 v19, s[44:47], s54 offen offset:2048 lds\n\t"
      "v_mfma_f32_16x16x32_bf16 v[180:183], a[88:91], a[116:119], v[180:183]\n\t"
      "v_mfma_f32_16x16x32_bf16 v[184:187], a[92:95], a[116:119], v[184:187]\n\t"
      "v_mfma_f32_16x16x32_bf16 v[188:191], a[96:99], a[116:119], v[188:191]\n\t"
      "ds_read_b64_tr_b16 a[46:47], v3 offset:6144\n\t"
      "v_mfma_f32_16x16x32_bf16 a[152:155], a[172:175], a[116:119], a[152:155]\n\t"
      "buffer_load_dwordx4 v4, s[44:47], s54 offen offset:3072 lds\n\t"
      "v_mfma_f32_16x16x32_bf16 v[220:223], a[96:99], a[120:123], v[220:223]\n\t"
      "v_mfma_f32_16x16x32_bf16 v[216:219], a[92:95], a[120:123], v[216:219]\n\t"
      "ds_read_b128 a[48:51], v6 offset:0\n\t"
      "v_mfma_f32_16x16x32_bf16 v[212:215], a[88:91], a[120:123], v[212:215]\n\t"
      "s_add_u32 m0, s50, 24576\n\t"
      "v_mfma_f32_16x16x32_bf16 v[208:211], a[84:87], a[120:123], v[208:211]\n\t"
      "buffer_load_dwordx4 v5, s[44:47], s54 offen lds\n\t"
      "v_mfma_f32_16x16x32_bf16 v[204:207], a[80:83], a[120:123], v[204:207]\n\t"
      "ds_read_b128 a[52:55], v6 offset:2048\n\t"
      "s_add_u32 s54, s54, 128\n\t"
      "v_mfma_f32_16x16x32_bf16 v[200:203], a[76:79], a[120:123], v[200:203]\n\t"
      "v_mfma_f32_16x16x32_bf16 v[196:199], a[72:75], a[120:123], v[196:199]\n\t"
      "v_mfma_f32_16x16x32_bf16 v[192:195], a[68:71], a[120:123], v[192:195]\n\t"
      "ds_read_b128 a[56:59], v6 offset:4096\n\t"
      "s_add_u32 m0, s49, 24576\n\t"
      "v_mfma_f32_16x16x32_bf16 v[224:227], a[68:71], a[124:127], v[224:227]\n\t"
      "buffer_load_dwordx4 v12, s[40:43], s53 offen lds\n\t"
      "v_mfma_f32_16x16x32_bf16 v[228:231], a[72:75], a[124:127], v[228:231]\n\t"
      "v_mfma_f32_16x16x32_bf16 v[232:235], a[76:79], a[124:127], v[232:235]\n\t"
      "ds_read_b128 a[60:63], v6 offset:6144\n\t"
      "v_mfma_f32_16x16x32_bf16 v[236:239], a[80:83], a[124:127], v[236:239]\n\t"
      "v_mfma_f32_16x16x32_bf16 v[240:243], a[84:87], a[124:127], v[240:243]\n\t"
      "v_mfma_f32_16x16x32_bf16 v[244:247], a[88:91], a[124:127], v[244:247]\n\t"
      "ds_read_b128 a[64:67], v6 offset:8192\n\t"
      "v_mfma_f32_16x16x32_bf16 v[248:251], a[92:95], a[124:127], v[248:251]\n\t"
      "v_mfma_f32_16x16x32_bf16 v[252:255], a[96:99], a[124:127], v[252:255]\n\t"
      "v_mfma_f32_16x16x32_bf16 a[220:223], a[96:99], a[128:131], a[220:223]\n\t"
      "v_mfma_f32_16x16x32_bf16 a[216:219], a[92:95], a[128:131], a[216:219]\n\t"
      "v_mfma_f32_16x16x32_bf16 a[212:215], a[88:91], a[128:131], a[212:215]\n\t"
      "v_mfma_f32_16x16x32_bf16 a[208:211], a[84:87], a[128:131], a[208:211]\n\t"
      "v_mfma_f32_16x16x32_bf16 a[204:207], a[80:83], a[128:131], a[204:207]\n\t"
      "v_mfma_f32_16x16x32_bf16 a[200:203], a[76:79], a[128:131], a[200:203]\n\t"
      "buffer_load_dwordx4 v13, s[40:43], s53 offen offset:1024 lds\n\t"
      "v_mfma_f32_16x16x32_bf16 a[196:199], a[72:75], a[128:131], a[196:199]\n\t"
      "s_add_u32 s53, s53, 229376\n\t"
      "v_mfma_f32_16x16x32_bf16 a[192:195], a[68:71], a[128:131], a[192:195]\n\t"
      "v_mfma_f32_16x16x32_bf16 a[224:227], a[68:71], a[132:135], a[224:227]\n\t"
      "v_mfma_f32_16x16x32_bf16 a[228:231], a[72:75], a[132:135], a[228:231]\n\t"
      "v_mfma_f32_16x16x32_bf16 a[232:235], a[76:79], a[132:135], a[232:235]\n\t"
      "v_mfma_f32_16x16x32_bf16 a[236:239], a[80:83], a[132:135], a[236:239]\n\t"
      "s_sub_u32 s55, s55, 1\n\t"
      "v_mfma_f32_16x16x32_bf16 a[240:243], a[84:87], a[132:135], a[240:243]\n\t"
      "v_mfma_f32_16x16x32_bf16 a[244:247], a[88:91], a[132:135], a[244:247]\n\t"
      "v_mfma_f32_16x16x32_bf16 a[248:251], a[92:95], a[132:135], a[248:251]\n\t"
      "v_mfma_f32_16x16x32_bf16 a[252:255], a[96:99], a[132:135], a[252:255]\n\t"
      "s_cmp_eq_u32 s55, 0\n\t"
      "s_cbranch_scc1 LA_nopub0_r1_%=\n\t"
      "s_nop 7\n\t"
      "s_mov_b32 exec_lo, 0x00010001\n\t"
      "s_mov_b32 exec_hi, 0x00010001\n\t"
      "buffer_store_dwordx4 a[136:139], v25, s[64:67], 0 offen offset:0 sc1\n\t"
      "buffer_store_dwordx4 a[140:143], v25, s[64:67], 0 offen offset:64 sc1\n\t"
      "buffer_store_dwordx4 a[144:147], v25, s[64:67], 0 offen offset:128 sc1\n\t"
      "buffer_store_dwordx4 a[148:151], v25, s[64:67], 0 offen offset:192 sc1\n\t"
      "s_mov_b64 exec, 0xffff\n\t"
      "buffer_store_dword a152, v26, s[64:67], 0 offen offset:256 sc1\n\t"
      "s_mov_b64 exec, -1\n\t"
      "s_mov_b32 s63, 1\n\t"
      "LA_nopub0_r1_%=:\n\t"
      "s_cmp_lg_u32 s55, 0\n\t"
      "s_cbranch_scc1 LA_loop_r1_%=\n\t"
      "s_branch LA_tail_r1_%=\n\t"
      "LA_cs1_r1_%=:\n\t"
      "s_waitcnt vmcnt(19) lgkmcnt(0)\n\t"
      "s_barrier\n\t"
      "v_mfma_f32_16x16x32_bf16 v[32:35], a[32:35], a[0:3], v[32:35]\n\t"
      "ds_read_b128 a[68:71], v1 offset:0\n\t"
      "buffer_load_dword v18, v16, s[56:59], s60 offen\n\t"
      "v_mfma_f32_16x16x32_bf16 v[48:51], a[32:35], a[4:7], v[48:51]\n\t"
      "s_add_u32 s60, s60, s61\n\t"
      "v_mfma_f32_16x16x32_bf16 v[64:67], a[32:35], a[8:11], v[64:67]\n\t"
      "ds_read_b128 a[72:75], v1 offset:2048\n\t"
      "v_mfma_f32_16x16x32_bf16 v[80:83], a[32:35], a[12:15], v[80:83]\n\t"
      "s_add_u32 m0, s49, 0\n\t"
      "v_mfma_f32_16x16x32_bf16 v[96:99], a[32:35], a[16:19], v[96:99]\n\t"
      "buffer_load_dwordx4 v12, s[40:43], s53 offen lds\n\t"
      "v_mfma_f32_16x16x32_bf16 v[112:115], a[32:35], a[20:23], v[112:115]\n\t"
      "ds_read_b128 a[76:79], v1 offset:4096\n\t"
      "v_mfma_f32_16x16x32_bf16 v[128:131], a[32:35], a[24:27], v[128:131]\n\t"
      "v_mfma_f32_16x16x32_bf16 v[144:147], a[32:35], a[28:31], v[144:147]\n\t"
      "v_mfma_f32_16x16x32_bf16 v[148:151], a[36:39], a[28:31], v[148:151]\n\t"
      "ds_read_b128 a[80:83], v1 offset:6144\n\t"
      "v_mfma_f32_16x16x32_bf16 v[132:135], a[36:39], a[24:27], v[132:135]\n\t"
      "v_mfma_f32_16x16x32_bf16 v[116:119], a[36:39], a[20:23], v[116:119]\n\t"
      "v_mfma_f32_16x16x32_bf16 v[100:103], a[36:39], a[16:19], v[100:103]\n\t"
      "ds_read_b128 a[84:87], v1 offset:8192\n\t"
      "v_mfma_f32_16x16x32_bf16 v[84:87], a[36:39], a[12:15], v[84:87]\n\t"
      "v_mfma_f32_16x16x32_bf16 v[68:71], a[36:39], a[8:11], v[68:71]\n\t"
      "v_mfma_f32_16x16x32_bf16 v[52:55], a[36:39], a[4:7], v[52:55]\n\t"
      "ds_read_b128 a[88:91], v1 offset:10240\n\t"
      "v_mfma_f32_16x16x32_bf16 v[36:39], a[36:39], a[0:3], v[36:39]\n\t"
      "v_mfma_f32_16x16x32_bf16 v[40:43], a[40:43], a[0:3], v[40:43]\n\t"
      "v_mfma_f32_16x16x32_bf16 v[56:59], a[40:43], a[4:7], v[56:59]\n\t"
      "ds_read_b128 a[92:95], v1 offset:12288\n\t"
      "v_mfma_f32_16x16x32_bf16 v[72:75], a[40:43], a[8:11], v[72:75]\n\t"
      "v_mfma_f32_16x16x32_bf16 v[88:91], a[40:43], a[12:15], v[88:91]\n\t"
      "v_mfma_f32_16x16x32_bf16 v[104:107], a[40:43], a[16:19], v[104:107]\n\t"
      "ds_read_b128 a[96:99], v1 offset:14336\n\t"
      "v_mfma_f32_16x16x32_bf16 v[120:123], a[40:43], a[20:23], v[120:123]\n\t"
      "v_mfma_f32_16x16x32_bf16 v[136:139], a[40:43], a[24:27], v[136:139]\n\t"
      "v_mfma_f32_16x16x32_bf16 v[152:155], a[40:43], a[28:31], v[152:155]\n\t"
      "ds_read_b64_tr_b16 a[100:101], v2 offset:8192\n\t"
      "v_mfma_f32_16x16x32_bf16 v[156:159], a[44:47], a[28:31], v[156:159]\n\t"
      "v_mfma_f32_16x16x32_bf16 v[140:143], a[44:47], a[24:27], v[140:143]\n\t"
      "v_mfma_f32_16x16x32_bf16 v[124:127], a[44:47], a[20:23], v[124:127]\n\t"
      "ds_read_b64_tr_b16 a[102:103], v2 offset:10240\n\t"
      "v_mfma_f32_16x16x32_bf16 v[108:111], a[44:47], a[16:19], v[108:111]\n\t"
      "v_mfma_f32_16x16x32_bf16 v[92:95], a[44:47], a[12:15], v[92:95]\n\t"
      "v_mfma_f32_16x16x32_bf16 v[76:79], a[44:47], a[8:11], v[76:79]\n\t"
      "ds_read_b64_tr_b16 a[104:105], v3 offset:8192\n\t"
      "v_mfma_f32_16x16x32_bf16 v[60:63], a[44:47], a[4:7], v[60:63]\n\t"
      "v_mfma_f32_16x16x32_bf16 v[44:47], a[44:47], a[0:3], v[44:47]\n\t"
      "ds_read_b64_tr_b16 a[106:107], v3 offset:10240\n\t"
      "v_mfma_f32_16x16x32_bf16 v[160:163], a[0:3], a[48:51], v[160:163]\n\t"
      "v_mfma_f32_16x16x32_bf16 v[164:167], a[4:7], a[48:51], v[164:167]\n\t"
      "v_mfma_f32_16x16x32_bf16 v[168:171], a[8:11], a[48:51], v[168:171]\n\t"
      "ds_read_b64_tr_b16 a[108:109], v2 offset:12288\n\t"
      "v_mfma_f32_16x16x32_bf16 v[172:175], a[12:15], a[48:51], v[172:175]\n\t"
      "v_mfma_f32_16x16x32_bf16 v[176:179], a[16:19], a[48:51], v[176:179]\n\t"
      "v_mfma_f32_16x16x32_bf16 v[180:183], a[20:23], a[48:51], v[180:183]\n\t"
      "ds_read_b64_tr_b16 a[110:111], v2 offset:14336\n\t"
      "v_mfma_f32_16x16x32_bf16 v[184:187], a[24:27], a[48:51], v[184:187]\n\t"
      "v_mfma_f32_16x16x32_bf16 v[188:191], a[28:31], a[48:51], v[188:191]\n\t"
      "v_mfma_f32_16x16x32_bf16 v[220:223], a[28:31], a[52:55], v[220:223]\n\t"
      "ds_read_b64_tr_b16 a[112:113], v3 offset:12288\n\t"
      "buffer_load_dwordx4 v13, s[40:43], s53 offen offset:1024 lds\n\t"
      "v_mfma_f32_16x16x32_bf16 v[216:219], a[24:27], a[52:55], v[216:219]\n\t"
      "s_add_u32 s53, s53, 229376\n\t"
      "v_mfma_f32_16x16x32_bf16 v[212:215], a[20:23], a[52:55], v[212:215]\n\t"
      "v_mfma_f32_16x16x32_bf16 v[208:211], a[16:19], a[52:55], v[208:211]\n\t"
      "ds_read_b64_tr_b16 a[114:115], v3 offset:14336\n\t"
      "v_mfma_f32_16x16x32_bf16 v[204:207], a[12:15], a[52:55], v[204:207]\n\t"
      "v_mfma_f32_16x16x32_bf16 v[200:203], a[8:11], a[52:55], v[200:203]\n\t"
      "v_mfma_f32_16x16x32_bf16 v[196:199], a[4:7], a[52:55], v[196:199]\n\t"
      "ds_read_b128 a[116:119], v7 offset:0\n\t"
      "v_mfma_f32_16x16x32_bf16 v[192:195], a[0:3], a[52:55], v[192:195]\n\t"
      "v_mfma_f32_16x16x32_bf16 a[156:159], a[172:175], a[52:55], 0\n\t"
      "v_mfma_f32_16x16x32_bf16 v[224:227], a[0:3], a[56:59], v[224:227]\n\t"
      "ds_read_b128 a[120:123], v7 offset:2048\n\t"
      "v_mfma_f32_16x16x32_bf16 v[228:231], a[4:7], a[56:59], v[228:231]\n\t"
      "v_mfma_f32_16x16x32_bf16 v[232:235], a[8:11], a[56:59], v[232:235]\n\t"
      "v_mfma_f32_16x16x32_bf16 v[236:239], a[12:15], a[56:59], v[236:239]\n\t"
      "ds_read_b128 a[124:127], v7 offset:4096\n\t"
      "v_mfma_f32_16x16x32_bf16 v[240:243], a[16:19], a[56:59], v[240:243]\n\t"
      "v_mfma_f32_16x16x32_bf16 v[244:247], a[20:23], a[56:59], v[244:247]\n\t"
      "v_mfma_f32_16x16x32_bf16 v[248:251], a[24:27], a[56:59], v[248:251]\n\t"
      "ds_read_b128 a[128:131], v7 offset:6144\n\t"
      "v_mfma_f32_16x16x32_bf16 v[252:255], a[28:31], a[56:59], v[252:255]\n\t"
      "v_mfma_f32_16x16x32_bf16 a[160:163], a[172:175], a[56:59], 0\n\t"
      "v_mfma_f32_16x16x32_bf16 a[220:223], a[28:31], a[60:63], a[220:223]\n\t"
      "ds_read_b128 a[132:135], v7 offset:8192\n\t"
      "v_mfma_f32_16x16x32_bf16 a[216:219], a[24:27], a[60:63], a[216:219]\n\t"
      "v_mfma_f32_16x16x32_bf16 a[212:215], a[20:23], a[60:63], a[212:215]\n\t"
      "v_mfma_f32_16x16x32_bf16 a[208:211], a[16:19], a[60:63], a[208:211]\n\t"
      "v_mfma_f32_16x16x32_bf16 a[204:207], a[12:15], a[60:63], a[204:207]\n\t"
      "v_mfma_f32_16x16x32_bf16 a[200:203], a[8:11], a[60:63], a[200:203]\n\t"
      "v_mfma_f32_16x16x32_bf16 a[196:199], a[4:7], a[60:63], a[196:199]\n\t"
      "v_mfma_f32_16x16x32_bf16 a[192:195], a[0:3], a[60:63], a[192:195]\n\t"
      "v_mfma_f32_16x16x32_bf16 a[164:167], a[172:175], a[60:63], 0\n\t"
      "v_mfma_f32_16x16x32_bf16 a[224:227], a[0:3], a[64:67], a[224:227]\n\t"
      "v_mfma_f32_16x16x32_bf16 a[228:231], a[4:7], a[64:67], a[228:231]\n\t"
      "v_mfma_f32_16x16x32_bf16 a[232:235], a[8:11], a[64:67], a[232:235]\n\t"
      "v_mfma_f32_16x16x32_bf16 a[236:239], a[12:15], a[64:67], a[236:239]\n\t"
      "v_mfma_f32_16x16x32_bf16 a[240:243], a[16:19], a[64:67], a[240:243]\n\t"
      "v_mfma_f32_16x16x32_bf16 a[244:247], a[20:23], a[64:67], a[244:247]\n\t"
      "v_mfma_f32_16x16x32_bf16 a[248:251], a[24:27], a[64:67], a[248:251]\n\t"
      "v_mfma_f32_16x16x32_bf16 a[252:255], a[28:31], a[64:67], a[252:255]\n\t"
      "v_mfma_f32_16x16x32_bf16 a[168:171], a[172:175], a[64:67], 0\n\t"
      "s_waitcnt vmcnt(5) lgkmcnt(0)\n\t"
      "s_barrier\n\t"
      "v_mfma_f32_16x16x32_bf16 v[32:35], a[100:103], a[68:71], v[32:35]\n\t"
      "ds_read_b128 a[0:3], v0 offset:32768\n\t"
      "buffer_load_dword v18, v16, s[56:59], s60 offen\n\t"
      "v_mfma_f32_16x16x32_bf16 v[48:51], a[100:103], a[72:75], v[48:51]\n\t"
      "s_add_u32 s60, s60, s61\n\t"
      "v_mfma_f32_16x16x32_bf16 v[64:67], a[100:103], a[76:79], v[64:67]\n\t"
      "ds_read_b128 a[4:7], v0 offset:34816\n\t"
      "s_add_u32 s62, s52, 65536\n\t"
      "s_add_u32 m0, s48, 0\n\t"
      "v_mfma_f32_16x16x32_bf16 v[80:83], a[100:103], a[80:83], v[80:83]\n\t"
      "buffer_load_dwordx4 v8, s[36:39], s52 offen lds\n\t"
      "v_mfma_f32_16x16x32_bf16 v[96:99], a[100:103], a[84:87], v[96:99]\n\t"
      "v_mfma_f32_16x16x32_bf16 v[112:115], a[100:103], a[88:91], v[112:115]\n\t"
      "ds_read_b128 a[8:11], v0 offset:36864\n\t"
      "v_mfma_f32_16x16x32_bf16 v[128:131], a[100:103], a[92:95], v[128:131]\n\t"
      "buffer_load_dwordx4 v9, s[36:39], s52 offen offset:1024 lds\n\t"
      "v_mfma_f32_16x16x32_bf16 v[144:147], a[100:103], a[96:99], v[144:147]\n\t"
      "v_mfma_f32_16x16x32_bf16 v[148:151], a[104:107], a[96:99], v[148:151]\n\t"
      "ds_read_b128 a[12:15], v0 offset:38912\n\t"
      "v_mfma_f32_16x16x32_bf16 v[132:135], a[104:107], a[92:95], v[132:135]\n\t"
      "v_mfma_f32_16x16x32_bf16 v[116:119], a[104:107], a[88:91], v[116:119]\n\t"
      "buffer_load_dwordx4 v10, s[36:39], s52 offen offset:2048 lds\n\t"
      "v_mfma_f32_16x16x32_bf16 v[100:103], a[104:107], a[84:87], v[100:103]\n\t"
      "ds_read_b128 a[16:19], v0 offset:40960\n\t"
      "v_mfma_f32_16x16x32_bf16 v[84:87], a[104:107], a[80:83], v[84:87]\n\t"
      "v_mfma_f32_16x16x32_bf16 v[68:71], a[104:107], a[76:79], v[68:71]\n\t"
      "v_mfma_f32_16x16x32_bf16 v[52:55], a[104:107], a[72:75], v[52:55]\n\t"
      "ds_read_b128 a[20:23], v0 offset:43008\n\t"
      "buffer_load_dwordx4 v11, s[36:39], s52 offen offset:3072 lds\n\t"
      "v_mfma_f32_16x16x32_bf16 v[36:39], a[104:107], a[68:71], v[36:39]\n\t"
      "v_mfma_f32_16x16x32_bf16 v[40:43], a[108:111], a[68:71], v[40:43]\n\t"
      "v_mfma_f32_16x16x32_bf16 v[56:59], a[108:111], a[72:75], v[56:59]\n\t"
      "ds_read_b128 a[24:27], v0 offset:45056\n\t"
      "s_add_u32 m0, s48, 4096\n\t"
      "v_mfma_f32_16x16x32_bf16 v[72:75], a[108:111], a[76:79], v[72:75]\n\t"
      "buffer_load_dwordx4 v8, s[36:39], s62 offen lds\n\t"
      "v_mfma_f32_16x16x32_bf16 v[88:91], a[108:111], a[80:83], v[88:91]\n\t"
      "v_mfma_f32_16x16x32_bf16 v[104:107], a[108:111], a[84:87], v[104:107]\n\t"
      "ds_read_b128 a[28:31], v0 offset:47104\n\t"
      "v_mfma_f32_16x16x32_bf16 v[120:123], a[108:111], a[88:91], v[120:123]\n\t"
      "buffer_load_dwordx4 v9, s[36:39], s62 offen offset:1024 lds\n\t"
      "v_mfma_f32_16x16x32_bf16 v[136:139], a[108:111], a[92:95], v[136:139]\n\t"
      "v_mfma_f32_16x16x32_bf16 v[152:155], a[108:111], a[96:99], v[152:155]\n\t"
      "ds_read_b64_tr_b16 a[32:33], v2 offset:16384\n\t"
      "v_mfma_f32_16x16x32_bf16 v[156:159], a[112:115], a[96:99], v[156:159]\n\t"
      "v_mfma_f32_16x16x32_bf16 v[140:143], a[112:115], a[92:95], v[140:143]\n\t"
      "buffer_load_dwordx4 v10, s[36:39], s62 offen offset:2048 lds\n\t"
      "v_mfma_f32_16x16x32_bf16 v[124:127], a[112:115], a[88:91], v[124:127]\n\t"
      "ds_read_b64_tr_b16 a[34:35], v2 offset:18432\n\t"
      "v_mfma_f32_16x16x32_bf16 v[108:111], a[112:115], a[84:87], v[108:111]\n\t"
      "v_mfma_f32_16x16x32_bf16 v[92:95], a[112:115], a[80:83], v[92:95]\n\t"
      "v_mfma_f32_16x16x32_bf16 v[76:79], a[112:115], a[76:79], v[76:79]\n\t"
      "ds_read_b64_tr_b16 a[36:37], v3 offset:16384\n\t"
      "buffer_load_dwordx4 v11, s[36:39], s62 offen offset:3072 lds\n\t"
      "v_mfma_f32_16x16x32_bf16 v[60:63], a[112:115], a[72:75], v[60:63]\n\t"
      "s_add_u32 s52, s52, 128\n\t"
      "v_mfma_f32_16x16x32_bf16 v[44:47], a[112:115], a[68:71], v[44:47]\n\t"
      "ds_read_b64_tr_b16 a[38:39], v3 offset:18432\n\t"
      "v_mfma_f32_16x16x32_bf16 v[160:163], a[68:71], a[116:119], v[160:163]\n\t"
      "s_add_u32 m0, s50, 0\n\t"
      "v_mfma_f32_16x16x32_bf16 v[164:167], a[72:75], a[116:119], v[164:167]\n\t"
      "buffer_load_dwordx4 v14, s[44:47], s54 offen lds\n\t"
      "v_mfma_f32_16x16x32_bf16 v[168:171], a[76:79], a[116:119], v[168:171]\n\t"
      "ds_read_b64_tr_b16 a[40:41], v2 offset:20480\n\t"
      "v_mfma_f32_16x16x32_bf16 v[172:175], a[80:83], a[116:119], v[172:175]\n\t"
      "v_mfma_f32_16x16x32_bf16 v[176:179], a[84:87], a[116:119], v[176:179]\n\t"
      "buffer_load_dwordx4 v15, s[44:47], s54 offen offset:1024 lds\n\t"
      "v_mfma_f32_16x16x32_bf16 v[180:183], a[88:91], a[116:119], v[180:183]\n\t"
      "ds_read_b64_tr_b16 a[42:43], v2 offset:22528\n\t"
      "v_mfma_f32_16x16x32_bf16 v[184:187], a[92:95], a[116:119], v[184:187]\n\t"
      "v_mfma_f32_16x16x32_bf16 v[188:191], a[96:99], a[116:119], v[188:191]\n\t"
      "v_mfma_f32_16x16x32_bf16 v[220:223], a[96:99], a[120:123], v[220:223]\n\t"
      "ds_read_b64_tr_b16 a[44:45], v3 offset:20480\n\t"
      "buffer_load_dwordx4 v19, s[44:47], s54 offen offset:2048 lds\n\t"
      "v_mfma_f32_16x16x32_bf16 v[216:219], a[92:95], a[120:123], v[216:219]\n\t"
      "v_mfma_f32_16x16x32_bf16 v[212:215], a[88:91], a[120:123], v[212:215]\n\t"
      "v_mfma_f32_16x16x32_bf16 v[208:211], a[84:87], a[120:123], v[208:211]\n\t"
      "ds_read_b64_tr_b16 a[46:47], v3 offset:22528\n\t"
      "v_mfma_f32_16x16x32_bf16 v[204:207], a[80:83], a[120:123], v[204:207]\n\t"
      "buffer_load_dwordx4 v4, s[44:47], s54 offen offset:3072 lds\n\t"
      "v_mfma_f32_16x16x32_bf16 v[200:203], a[76:79], a[120:123], v[200:203]\n\t"
      "v_mfma_f32_16x16x32_bf16 v[196:199], a[72:75], a[120:123], v[196:199]\n\t"
      "ds_read_b128 a[48:51], v6 offset:20480\n\t"
      "v_mfma_f32_16x16x32_bf16 v[192:195], a[68:71], a[120:123], v[192:195]\n\t"
      "s_add_u32 m0, s50, 4096\n\t"
      "v_mfma_f32_16x16x32_bf16 a[156:159], a[172:175], a[120:123], a[156:159]\n\t"
      "buffer_load_dwordx4 v5, s[44:47], s54 offen lds\n\t"
      "v_mfma_f32_16x16x32_bf16 v[224:227], a[68:71], a[124:127], v[224:227]\n\t"
      "ds_read_b128 a[52:55], v6 offset:22528\n\t"
      "s_add_u32 s54, s54, 128\n\t"
      "v_mfma_f32_16x16x32_bf16 v[228:231], a[72:75], a[124:127], v[228:231]\n\t"
      "v_mfma_f32_16x16x32_bf16 v[232:235], a[76:79], a[124:127], v[232:235]\n\t"
      "v_mfma_f32_16x16x32_bf16 v[236:239], a[80:83], a[124:127], v[236:239]\n\t"
      "ds_read_b128 a[56:59], v6 offset:24576\n\t"
      "s_add_u32 m0, s49, 8192\n\t"
      "v_mfma_f32_16x16x32_bf16 v[240:243], a[84:87], a[124:127], v[240:243]\n\t"
      "buffer_load_dwordx4 v12, s[40:43], s53 offen lds\n\t"
      "v_mfma_f32_16x16x32_bf16 v[244:247], a[88:91], a[124:127], v[244:247]\n\t"
      "v_mfma_f32_16x16x32_bf16 v[248:251], a[92:95], a[124:127], v[248:251]\n\t"
      "ds_read_b128 a[60:63], v6 offset:26624\n\t"
      "v_mfma_f32_16x16x32_bf16 v[252:255], a[96:99], a[124:127], v[252:255]\n\t"
      "v_mfma_f32_16x16x32_bf16 a[160:163], a[172:175], a[124:127], a[160:163]\n\t"
      "v_mfma_f32_16x16x32_bf16 a[220:223], a[96:99], a[128:131], a[220:223]\n\t"
      "ds_read_b128 a[64:67], v6 offset:28672\n\t"
      "v_mfma_f32_16x16x32_bf16 a[216:219], a[92:95], a[128:131], a[216:219]\n\t"
      "v_mfma_f32_16x16x32_bf16 a[212:215], a[88:91], a[128:131], a[212:215]\n\t"
      "v_mfma_f32_16x16x32_bf16 a[208:211], a[84:87], a[128:131], a[208:211]\n\t"
      "v_mfma_f32_16x16x32_bf16 a[204:207], a[80:83], a[128:131], a[204:207]\n\t"
      "v_mfma_f32_16x16x32_bf16 a[200:203], a[76:79], a[128:131], a[200:203]\n\t"
      "v_mfma_f32_16x16x32_bf16 a[196:199], a[72:75], a[128:131], a[196:199]\n\t"
      "v_mfma_f32_16x16x32_bf16 a[192:195], a[68:71], a[128:131], a[192:195]\n\t"
      "v_mfma_f32_16x16x32_bf16 a[164:167], a[172:175], a[128:131], a[164:167]\n\t"
      "buffer_load_dwordx4 v13, s[40:43], s53 offen offset:1024 lds\n\t"
      "v_mfma_f32_16x16x32_bf16 a[224:227], a[68:71], a[132:135], a[224:227]\n\t"
      "s_add_u32 s53, s53, 229376\n\t"
      "v_mfma_f32_16x16x32_bf16 a[228:231], a[72:75], a[132:135], a[228:231]\n\t"
      "v_mfma_f32_16x16x32_bf16 a[232:235], a[76:79], a[132:135], a[232:235]\n\t"
      "v_mfma_f32_16x16x32_bf16 a[236:239], a[80:83], a[132:135], a[236:239]\n\t"
      "v_mfma_f32_16x16x32_bf16 a[240:243], a[84:87], a[132:135], a[240:243]\n\t"
      "v_mfma_f32_16x16x32_bf16 a[244:247], a[88:91], a[132:135], a[244:247]\n\t"
      "v_mfma_f32_16x16x32_bf16 a[248:251], a[92:95], a[132:135], a[248:251]\n\t"
      "v_mfma_f32_16x16x32_bf16 a[252:255], a[96:99], a[132:135], a[252:255]\n\t"
      "v_mfma_f32_16x16x32_bf16 a[168:171], a[172:175], a[132:135], a[168:171]\n\t"
      "s_waitcnt vmcnt(19) lgkmcnt(0)\n\t"
      "s_barrier\n\t"
      "v_mfma_f32_16x16x32_bf16 v[32:35], a[32:35], a[0:3], v[32:35]\n\t"
      "ds_read_b128 a[68:71], v1 offset:32768\n\t"
      "buffer_load_dword v18, v16, s[56:59], s60 offen\n\t"
      "v_mfma_f32_16x16x32_bf16 v[48:51], a[32:35], a[4:7], v[48:51]\n\t"
      "s_add_u32 s60, s60, s61\n\t"
      "v_mfma_f32_16x16x32_bf16 v[64:67], a[32:35], a[8:11], v[64:67]\n\t"
      "ds_read_b128 a[72:75], v1 offset:34816\n\t"
      "v_mfma_f32_16x16x32_bf16 v[80:83], a[32:35], a[12:15], v[80:83]\n\t"
      "s_add_u32 m0, s49, 16384\n\t"
      "v_mfma_f32_16x16x32_bf16 v[96:99], a[32:35], a[16:19], v[96:99]\n\t"
      "buffer_load_dwordx4 v12, s[40:43], s53 offen lds\n\t"
      "v_mfma_f32_16x16x32_bf16 v[112:115], a[32:35], a[20:23], v[112:115]\n\t"
      "ds_read_b128 a[76:79], v1 offset:36864\n\t"
      "v_mfma_f32_16x16x32_bf16 v[128:131], a[32:35], a[24:27], v[128:131]\n\t"
      "v_mfma_f32_16x16x32_bf16 v[144:147], a[32:35], a[28:31], v[144:147]\n\t"
      "v_mfma_f32_16x16x32_bf16 v[148:151], a[36:39], a[28:31], v[148:151]\n\t"
      "ds_read_b128 a[80:83], v1 offset:38912\n\t"
      "v_mfma_f32_16x16x32_bf16 v[132:135], a[36:39], a[24:27], v[132:135]\n\t"
      "v_mfma_f32_16x16x32_bf16 v[116:119], a[36:39], a[20:23], v[116:119]\n\t"
      "v_mfma_f32_16x16x32_bf16 v[100:103], a[36:39], a[16:19], v[100:103]\n\t"
      "ds_read_b128 a[84:87], v1 offset:40960\n\t"
      "v_mfma_f32_16x16x32_bf16 v[84:87], a[36:39], a[12:15], v[84:87]\n\t"
      "v_mfma_f32_16x16x32_bf16 v[68:71], a[36:39], a[8:11], v[68:71]\n\t"
      "v_mfma_f32_16x16x32_bf16 v[52:55], a[36:39], a[4:7], v[52:55]\n\t"
      "ds_read_b128 a[88:91], v1 offset:43008\n\t"
      "v_mfma_f32_16x16x32_bf16 v[36:39], a[36:39], a[0:3], v[36:39]\n\t"
      "v_mfma_f32_16x16x32_bf16 v[40:43], a[40:43], a[0:3], v[40:43]\n\t"
      "v_mfma_f32_16x16x32_bf16 v[56:59], a[40:43], a[4:7], v[56:59]\n\t"
      "ds_read_b128 a[92:95], v1 offset:45056\n\t"
      "v_mfma_f32_16x16x32_bf16 v[72:75], a[40:43], a[8:11], v[72:75]\n\t"
      "v_mfma_f32_16x16x32_bf16 v[88:91], a[40:43], a[12:15], v[88:91]\n\t"
      "v_mfma_f32_16x16x32_bf16 v[104:107], a[40:43], a[16:19], v[104:107]\n\t"
      "ds_read_b128 a[96:99], v1 offset:47104\n\t"
      "v_mfma_f32_16x16x32_bf16 v[120:123], a[40:43], a[20:23], v[120:123]\n\t"
      "v_mfma_f32_16x16x32_bf16 v[136:139], a[40:43], a[24:27], v[136:139]\n\t"
      "v_mfma_f32_16x16x32_bf16 v[152:155], a[40:43], a[28:31], v[152:155]\n\t"
      "ds_read_b64_tr_b16 a[100:101], v2 offset:24576\n\t"
      "v_mfma_f32_16x16x32_bf16 v[156:159], a[44:47], a[28:31], v[156:159]\n\t"
      "v_mfma_f32_16x16x32_bf16 v[140:143], a[44:47], a[24:27], v[140:143]\n\t"
      "v_mfma_f32_16x16x32_bf16 v[124:127], a[44:47], a[20:23], v[124:127]\n\t"
      "ds_read_b64_tr_b16 a[102:103], v2 offset:26624\n\t"
      "v_mfma_f32_16x16x32_bf16 v[108:111], a[44:47], a[16:19], v[108:111]\n\t"
      "v_mfma_f32_16x16x32_bf16 v[92:95], a[44:47], a[12:15], v[92:95]\n\t"
      "v_mfma_f32_16x16x32_bf16 v[76:79], a[44:47], a[8:11], v[76:79]\n\t"
      "ds_read_b64_tr_b16 a[104:105], v3 offset:24576\n\t"
      "v_mfma_f32_16x16x32_bf16 v[60:63], a[44:47], a[4:7], v[60:63]\n\t"
      "v_mfma_f32_16x16x32_bf16 v[44:47], a[44:47], a[0:3], v[44:47]\n\t"
      "ds_read_b64_tr_b16 a[106:107], v3 offset:26624\n\t"
      "v_mfma_f32_16x16x32_bf16 v[160:163], a[0:3], a[48:51], v[160:163]\n\t"
      "v_mfma_f32_16x16x32_bf16 v[164:167], a[4:7], a[48:51], v[164:167]\n\t"
      "v_mfma_f32_16x16x32_bf16 v[168:171], a[8:11], a[48:51], v[168:171]\n\t"
      "ds_read_b64_tr_b16 a[108:109], v2 offset:28672\n\t"
      "v_mfma_f32_16x16x32_bf16 v[172:175], a[12:15], a[48:51], v[172:175]\n\t"
      "v_mfma_f32_16x16x32_bf16 v[176:179], a[16:19], a[48:51], v[176:179]\n\t"
      "v_mfma_f32_16x16x32_bf16 v[180:183], a[20:23], a[48:51], v[180:183]\n\t"
      "ds_read_b64_tr_b16 a[110:111], v2 offset:30720\n\t"
      "v_mfma_f32_16x16x32_bf16 v[184:187], a[24:27], a[48:51], v[184:187]\n\t"
      "v_mfma_f32_16x16x32_bf16 v[188:191], a[28:31], a[48:51], v[188:191]\n\t"
      "v_mfma_f32_16x16x32_bf16 v[220:223], a[28:31], a[52:55], v[220:223]\n\t"
      "ds_read_b64_tr_b16 a[112:113], v3 offset:28672\n\t"
      "buffer_load_dwordx4 v13, s[40:43], s53 offen offset:1024 lds\n\t"
      "v_mfma_f32_16x16x32_bf16 v[216:219], a[24:27], a[52:55], v[216:219]\n\t"
      "s_add_u32 s53, s53, 229376\n\t"
      "v_mfma_f32_16x16x32_bf16 v[212:215], a[20:23], a[52:55], v[212:215]\n\t"
      "v_mfma_f32_16x16x32_bf16 v[208:211], a[16:19], a[52:55], v[208:211]\n\t"
      "ds_read_b64_tr_b16 a[114:115], v3 offset:30720\n\t"
      "v_mfma_f32_16x16x32_bf16 v[204:207], a[12:15], a[52:55], v[204:207]\n\t"
      "v_mfma_f32_16x16x32_bf16 v[200:203], a[8:11], a[52:55], v[200:203]\n\t"
      "v_mfma_f32_16x16x32_bf16 v[196:199], a[4:7], a[52:55], v[196:199]\n\t"
      "ds_read_b128 a[116:119], v7 offset:20480\n\t"
      "v_mfma_f32_16x16x32_bf16 v[192:195], a[0:3], a[52:55], v[192:195]\n\t"
      "v_mfma_f32_16x16x32_bf16 a[156:159], a[172:175], a[52:55], a[156:159]\n\t"
      "v_mfma_f32_16x16x32_bf16 v[224:227], a[0:3], a[56:59], v[224:227]\n\t"
      "ds_read_b128 a[120:123], v7 offset:22528\n\t"
      "v_mfma_f32_16x16x32_bf16 v[228:231], a[4:7], a[56:59], v[228:231]\n\t"
      "v_mfma_f32_16x16x32_bf16 v[232:235], a[8:11], a[56:59], v[232:235]\n\t"
      "v_mfma_f32_16x16x32_bf16 v[236:239], a[12:15], a[56:59], v[236:239]\n\t"
      "ds_read_b128 a[124:127], v7 offset:24576\n\t"
      "v_mfma_f32_16x16x32_bf16 v[240:243], a[16:19], a[56:59], v[240:243]\n\t"
      "v_mfma_f32_16x16x32_bf16 v[244:247], a[20:23], a[56:59], v[244:247]\n\t"
      "v_mfma_f32_16x16x32_bf16 v[248:251], a[24:27], a[56:59], v[248:251]\n\t"
      "ds_read_b128 a[128:131], v7 offset:26624\n\t"
      "v_mfma_f32_16x16x32_bf16 v[252:255], a[28:31], a[56:59], v[252:255]\n\t"
      "v_mfma_f32_16x16x32_bf16 a[160:163], a[172:175], a[56:59], a[160:163]\n\t"
      "v_mfma_f32_16x16x32_bf16 a[220:223], a[28:31], a[60:63], a[220:223]\n\t"
      "ds_read_b128 a[132:135], v7 offset:28672\n\t"
      "v_mfma_f32_16x16x32_bf16 a[216:219], a[24:27], a[60:63], a[216:219]\n\t"
      "v_mfma_f32_16x16x32_bf16 a[212:215], a[20:23], a[60:63], a[212:215]\n\t"
      "v_mfma_f32_16x16x32_bf16 a[208:211], a[16:19], a[60:63], a[208:211]\n\t"
      "v_mfma_f32_16x16x32_bf16 a[204:207], a[12:15], a[60:63], a[204:207]\n\t"
      "v_mfma_f32_16x16x32_bf16 a[200:203], a[8:11], a[60:63], a[200:203]\n\t"
      "v_mfma_f32_16x16x32_bf16 a[196:199], a[4:7], a[60:63], a[196:199]\n\t"
      "v_mfma_f32_16x16x32_bf16 a[192:195], a[0:3], a[60:63], a[192:195]\n\t"
      "v_mfma_f32_16x16x32_bf16 a[164:167], a[172:175], a[60:63], a[164:167]\n\t"
      "v_mfma_f32_16x16x32_bf16 a[224:227], a[0:3], a[64:67], a[224:227]\n\t"
      "v_mfma_f32_16x16x32_bf16 a[228:231], a[4:7], a[64:67], a[228:231]\n\t"
      "v_mfma_f32_16x16x32_bf16 a[232:235], a[8:11], a[64:67], a[232:235]\n\t"
      "v_mfma_f32_16x16x32_bf16 a[236:239], a[12:15], a[64:67], a[236:239]\n\t"
      "v_mfma_f32_16x16x32_bf16 a[240:243], a[16:19], a[64:67], a[240:243]\n\t"
      "v_mfma_f32_16x16x32_bf16 a[244:247], a[20:23], a[64:67], a[244:247]\n\t"
      "v_mfma_f32_16x16x32_bf16 a[248:251], a[24:27], a[64:67], a[248:251]\n\t"
      "v_mfma_f32_16x16x32_bf16 a[252:255], a[28:31], a[64:67], a[252:255]\n\t"
      "v_mfma_f32_16x16x32_bf16 a[168:171], a[172:175], a[64:67], a[168:171]\n\t"
      "s_waitcnt vmcnt(5) lgkmcnt(0)\n\t"
      "s_bitcmp1_b32 s81, s55\n\t"
      "s_cbranch_scc0 LA_nofl1_r1_%=\n\t"
      "s_not_b32 s74, s68\n\t"
      "s_not_b32 s75, s69\n\t"
      "s_cmp_eq_u32 s63, 1\n\t"
      "s_cselect_b32 s74, s68, s74\n\t"
      "s_cselect_b32 s75, s69, s75\n\t"
      "v_mov_b32 v22, s74\n\t"
      "v_mov_b32 v23, s75\n\t"
      "s_mov_b64 exec, 1\n\t"
      "buffer_store_dwordx4 v[20:23], v17, s[64:67], 0 offen sc1 ; alt\n\t"
      "s_mov_b64 exec, -1\n\t"
      "s_nop 1\n\t"
      "LA_nofl1_r1_%=:\n\t"
      "s_barrier\n\t"
      "v_mfma_f32_16x16x32_bf16 v[32:35], a[100:103], a[68:71], v[32:35]\n\t"
      "ds_read_b128 a[0:3], v0 offset:0\n\t"
      "buffer_load_dword v18, v16, s[56:59], s60 offen\n\t"
      "v_mfma_f32_16x16x32_bf16 v[48:51], a[100:103], a[72:75], v[48:51]\n\t"
      "s_add_u32 s60, s60, s61\n\t"
      "v_mfma_f32_16x16x32_bf16 v[64:67], a[100:103], a[76:79], v[64:67]\n\t"
      "ds_read_b128 a[4:7], v0 offset:2048\n\t"
      "s_add_u32 s62, s52, 65536\n\t"
      "s_add_u32 m0, s48, 32768\n\t"
      "v_mfma_f32_16x16x32_bf16 v[80:83], a[100:103], a[80:83], v[80:83]\n\t"
      "buffer_load_dwordx4 v8, s[36:39], s52 offen lds\n\t"
      "v_mfma_f32_16x16x32_bf16 v[96:99], a[100:103], a[84:87], v[96:99]\n\t"
      "v_mfma_f32_16x16x32_bf16 v[112:115], a[100:103], a[88:91], v[112:115]\n\t"
      "ds_read_b128 a[8:11], v0 offset:4096\n\t"
      "v_mfma_f32_16x16x32_bf16 v[128:131], a[100:103], a[92:95], v[128:131]\n\t"
      "buffer_load_dwordx4 v9, s[36:39], s52 offen offset:1024 lds\n\t"
      "v_mfma_f32_16x16x32_bf16 v[144:147], a[100:103], a[96:99], v[144:147]\n\t"
      "v_mfma_f32_16x16x32_bf16 v[148:151], a[104:107], a[96:99], v[148:151]\n\t"
      "ds_read_b128 a[12:15], v0 offset:6144\n\t"
      "v_mfma_f32_16x16x32_bf16 v[132:135], a[104:107], a[92:95], v[132:135]\n\t"
      "v_mfma_f32_16x16x32_bf16 v[116:119], a[104:107], a[88:91], v[116:119]\n\t"
      "buffer_load_dwordx4 v10, s[36:39], s52 offen offset:2048 lds\n\t"
      "v_mfma_f32_16x16x32_bf16 v[100:103], a[104:107], a[84:87], v[100:103]\n\t"
      "ds_read_b128 a[16:19], v0 offset:8192\n\t"
      "v_mfma_f32_16x16x32_bf16 v[84:87], a[104:107], a[80:83], v[84:87]\n\t"
      "v_mfma_f32_16x16x32_bf16 v[68:71], a[104:107], a[76:79], v[68:71]\n\t"
      "v_mfma_f32_16x16x32_bf16 v[52:55], a[104:107], a[72:75], v[52:55]\n\t"
      "ds_read_b128 a[20:23], v0 offset:10240\n\t"
      "buffer_load_dwordx4 v11, s[36:39], s52 offen offset:3072 lds\n\t"
      "v_mfma_f32_16x16x32_bf16 v[36:39], a[104:107], a[68:71], v[36:39]\n\t"
      "v_mfma_f32_16x16x32_bf16 v[40:43], a[108:111], a[68:71], v[40:43]\n\t"
      "v_mfma_f32_16x16x32_bf16 v[56:59], a[108:111], a[72:75], v[56:59]\n\t"
      "ds_read_b128 a[24:27], v0 offset:12288\n\t"
      "s_add_u32 m0, s48, 36864\n\t"
      "v_mfma_f32_16x16x32_bf16 v[72:75], a[108:111], a[76:79], v[72:75]\n\t"
      "buffer_load_dwordx4 v8, s[36:39], s62 offen lds\n\t"
      "v_mfma_f32_16x16x32_bf16 v[88:91], a[108:111], a[80:83], v[88:91]\n\t"
      "v_mfma_f32_16x16x32_bf16 v[104:107], a[108:111], a[84:87], v[104:107]\n\t"
      "ds_read_b128 a[28:31], v0 offset:14336\n\t"
      "v_mfma_f32_16x16x32_bf16 v[120:123], a[108:111], a[88:91], v[120:123]\n\t"
      "buffer_load_dwordx4 v9, s[36:39], s62 offen offset:1024 lds\n\t"
      "v_mfma_f32_16x16x32_bf16 v[136:139], a[108:111], a[92:95], v[136:139]\n\t"
      "v_mfma_f32_16x16x32_bf16 v[152:155], a[108:111], a[96:99], v[152:155]\n\t"
      "ds_read_b64_tr_b16 a[32:33], v2 offset:0\n\t"
      "v_mfma_f32_16x16x32_bf16 v[156:159], a[112:115], a[96:99], v[156:159]\n\t"
      "v_mfma_f32_16x16x32_bf16 v[140:143], a[112:115], a[92:95], v[140:143]\n\t"
      "buffer_load_dwordx4 v10, s[36:39], s62 offen offset:2048 lds\n\t"
      "v_mfma_f32_16x16x32_bf16 v[124:127], a[112:115], a[88:91], v[124:127]\n\t"
      "ds_read_b64_tr_b16 a[34:35], v2 offset:2048\n\t"
      "v_mfma_f32_16x16x32_bf16 v[108:111], a[112:115], a[84:87], v[108:111]\n\t"
      "v_mfma_f32_16x16x32_bf16 v[92:95], a[112:115], a[80:83], v[92:95]\n\t"
      "v_mfma_f32_16x16x32_bf16 v[76:79], a[112:115], a[76:79], v[76:79]\n\t"
      "ds_read_b64_tr_b16 a[36:37], v3 offset:0\n\t"
      "buffer_load_dwordx4 v11, s[36:39], s62 offen offset:3072 lds\n\t"
      "v_mfma_f32_16x16x32_bf16 v[60:63], a[112:115], a[72:75], v[60:63]\n\t"
      "s_add_u32 s52, s52, 128\n\t"
      "v_mfma_f32_16x16x32_bf16 v[44:47], a[112:115], a[68:71], v[44:47]\n\t"
      "ds_read_b64_tr_b16 a[38:39], v3 offset:2048\n\t"
      "v_mfma_f32_16x16x32_bf16 v[160:163], a[68:71], a[116:119], v[160:163]\n\t"
      "s_add_u32 m0, s50, 20480\n\t"
      "v_mfma_f32_16x16x32_bf16 v[164:167], a[72:75], a[116:119], v[164:167]\n\t"
      "buffer_load_dwordx4 v14, s[44:47], s54 offen lds\n\t"
      "v_mfma_f32_16x16x32_bf16 v[168:171], a[76:79], a[116:119], v[168:171]\n\t"
      "ds_read_b64_tr_b16 a[40:41], v2 offset:4096\n\t"
      "v_mfma_f32_16x16x32_bf16 v[172:175], a[80:83], a[116:119], v[172:175]\n\t"
      "v_mfma_f32_16x16x32_bf16 v[176:179], a[84:87], a[116:119], v[176:179]\n\t"
      "buffer_load_dwordx4 v15, s[44:47], s54 offen offset:1024 lds\n\t"
      "v_mfma_f32_16x16x32_bf16 v[180:183], a[88:91], a[116:119], v[180:183]\n\t"
      "ds_read_b64_tr_b16 a[42:43], v2 offset:6144\n\t"
      "v_mfma_f32_16x16x32_bf16 v[184:187], a[92:95], a[116:119], v[184:187]\n\t"
      "v_mfma_f32_16x16x32_bf16 v[188:191], a[96:99], a[116:119], v[188:191]\n\t"
      "v_mfma_f32_16x16x32_bf16 v[220:223], a[96:99], a[120:123], v[220:223]\n\t"
      "ds_read_b64_tr_b16 a[44:45], v3 offset:4096\n\t"
      "buffer_load_dwordx4 v19, s[44:47], s54 offen offset:2048 lds\n\t"
      "v_mfma_f32_16x16x32_bf16 v[216:219], a[92:95], a[120:123], v[216:219]\n\t"
      "v_mfma_f32_16x16x32_bf16 v[212:215], a[88:91], a[120:123], v[212:215]\n\t"
      "v_mfma_f32_16x16x32_bf16 v[208:211], a[84:87], a[120:123], v[208:211]\n\t"
      "ds_read_b64_tr_b16 a[46:47], v3 offset:6144\n\t"
      "v_mfma_f32_16x16x32_bf16 v[204:207], a[80:83], a[120:123], v[204:207]\n\t"
      "buffer_load_dwordx4 v4, s[44:47], s54 offen offset:3072 lds\n\t"
      "v_mfma_f32_16x16x32_bf16 v[200:203], a[76:79], a[120:123], v[200:203]\n\t"
      "v_mfma_f32_16x16x32_bf16 v[196:199], a[72:75], a[120:123], v[196:199]\n\t"
      "ds_read_b128 a[48:51], v6 offset:0\n\t"
      "v_mfma_f32_16x16x32_bf16 v[192:195], a[68:71], a[120:123], v[192:195]\n\t"
      "s_add_u32 m0, s50, 24576\n\t"
      "v_mfma_f32_16x16x32_bf16 a[156:159], a[172:175], a[120:123], a[156:159]\n\t"
      "buffer_load_dwordx4 v5, s[44:47], s54 offen lds\n\t"
      "v_mfma_f32_16x16x32_bf16 v[224:227], a[68:71], a[124:127], v[224:227]\n\t"
      "ds_read_b128 a[52:55], v6 offset:2048\n\t"
      "s_add_u32 s54, s54, 128\n\t"
      "v_mfma_f32_16x16x32_bf16 v[228:231], a[72:75], a[124:127], v[228:231]\n\t"
      "v_mfma_f32_16x16x32_bf16 v[232:235], a[76:79], a[124:127], v[232:235]\n\t"
      "v_mfma_f32_16x16x32_bf16 v[236:239], a[80:83], a[124:127], v[236:239]\n\t"
      "ds_read_b128 a[56:59], v6 offset:4096\n\t"
      "s_add_u32 m0, s49, 24576\n\t"
      "v_mfma_f32_16x16x32_bf16 v[240:243], a[84:87], a[124:127], v[240:243]\n\t"
      "buffer_load_dwordx4 v12, s[40:43], s53 offen lds\n\t"
      "v_mfma_f32_16x16x32_bf16 v[244:247], a[88:91], a[124:127], v[244:247]\n\t"
      "v_mfma_f32_16x16x32_bf16 v[248:251], a[92:95], a[124:127], v[248:251]\n\t"
      "ds_read_b128 a[60:63], v6 offset:6144\n\t"
      "v_mfma_f32_16x16x32_bf16 v[252:255], a[96:99], a[124:127], v[252:255]\n\t"
      "v_mfma_f32_16x16x32_bf16 a[160:163], a[172:175], a[124:127], a[160:163]\n\t"
      "v_mfma_f32_16x16x32_bf16 a[220:223], a[96:99], a[128:131], a[220:223]\n\t"
      "ds_read_b128 a[64:67], v6 offset:8192\n\t"
      "v_mfma_f32_16x16x32_bf16 a[216:219], a[92:95], a[128:131], a[216:219]\n\t"
      "v_mfma_f32_16x16x32_bf16 a[212:215], a[88:91], a[128:131], a[212:215]\n\t"
      "v_mfma_f32_16x16x32_bf16 a[208:211], a[84:87], a[128:131], a[208:211]\n\t"
      "v_mfma_f32_16x16x32_bf16 a[204:207], a[80:83], a[128:131], a[204:207]\n\t"
      "v_mfma_f32_16x16x32_bf16 a[200:203], a[76:79], a[128:131], a[200:203]\n\t"
      "v_mfma_f32_16x16x32_bf16 a[196:199], a[72:75], a[128:131], a[196:199]\n\t"
      "v_mfma_f32_16x16x32_bf16 a[192:195], a[68:71], a[128:131], a[192:195]\n\t"
      "v_mfma_f32_16x16x32_bf16 a[164:167], a[172:175], a[128:131], a[164:167]\n\t"
      "buffer_load_dwordx4 v13, s[40:43], s53 offen offset:1024 lds\n\t"
      "v_mfma_f32_16x16x32_bf16 a[224:227], a[68:71], a[132:135], a[224:227]\n\t"
      "s_add_u32 s53, s53, 229376\n\t"
      "v_mfma_f32_16x16x32_bf16 a[228:231], a[72:75], a[132:135], a[228:231]\n\t"
      "v_mfma_f32_16x16x32_bf16 a[232:235], a[76:79], a[132:135], a[232:235]\n\t"
      "v_mfma_f32_16x16x32_bf16 a[236:239], a[80:83], a[132:135], a[236:239]\n\t"
      "v_mfma_f32_16x16x32_bf16 a[240:243], a[84:87], a[132:135], a[240:243]\n\t"
      "s_sub_u32 s55, s55, 1\n\t"
      "v_mfma_f32_16x16x32_bf16 a[244:247], a[88:91], a[132:135], a[244:247]\n\t"
      "v_mfma_f32_16x16x32_bf16 a[248:251], a[92:95], a[132:135], a[248:251]\n\t"
      "v_mfma_f32_16x16x32_bf16 a[252:255], a[96:99], a[132:135], a[252:255]\n\t"
      "v_mfma_f32_16x16x32_bf16 a[168:171], a[172:175], a[132:135], a[168:171]\n\t"
      "s_cmp_eq_u32 s55, 0\n\t"
      "s_cbranch_scc1 LA_nopub1_r1_%=\n\t"
      "s_nop 7\n\t"
      "s_mov_b32 exec_lo, 0x00010001\n\t"
      "s_mov_b32 exec_hi, 0x00010001\n\t"
      "s_mov_b64 exec, 0xffff\n\t"
      "buffer_store_dword a156, v26, s[64:67], 0 offen offset:320 sc1\n\t"
      "buffer_store_dword a160, v26, s[64:67], 0 offen offset:384 sc1\n\t"
      "buffer_store_dword a164, v26, s[64:67], 0 offen offset:448 sc1\n\t"
      "buffer_store_dword a168, v26, s[64:67], 0 offen offset:512 sc1\n\t"
      "s_mov_b64 exec, -1\n\t"
      "s_mov_b32 s63, 1\n\t"
      "LA_nopub1_r1_%=:\n\t"
      "s_cmp_lg_u32 s55, 0\n\t"
      "s_cbranch_scc1 LA_loop_r1_%=\n\t"
      "LA_tail_r1_%=:\n\t"
      "s_cmp_eq_u32 s73, 0\n\t"
      "s_cbranch_scc0 LA_tail1_r1_%=\n\t"
      "s_waitcnt vmcnt(19) lgkmcnt(0)\n\t"
      "s_barrier\n\t"
      "v_mfma_f32_16x16x32_bf16 v[32:35], a[32:35], a[0:3], v[32:35]\n\t"
      "ds_read_b128 a[68:71], v1 offset:0\n\t"
      "v_mfma_f32_16x16x32_bf16 v[48:51], a[32:35], a[4:7], v[48:51]\n\t"
      "v_mfma_f32_16x16x32_bf16 v[64:67], a[32:35], a[8:11], v[64:67]\n\t"
      "ds_read_b128 a[72:75], v1 offset:2048\n\t"
      "v_mfma_f32_16x16x32_bf16 v[80:83], a[32:35], a[12:15], v[80:83]\n\t"
      "v_mfma_f32_16x16x32_bf16 v[96:99], a[32:35], a[16:19], v[96:99]\n\t"
      "v_mfma_f32_16x16x32_bf16 v[112:115], a[32:35], a[20:23], v[112:115]\n\t"
      "ds_read_b128 a[76:79], v1 offset:4096\n\t"
      "v_mfma_f32_16x16x32_bf16 v[128:131], a[32:35], a[24:27], v[128:131]\n\t"
      "v_mfma_f32_16x16x32_bf16 v[144:147], a[32:35], a[28:31], v[144:147]\n\t"
      "v_mfma_f32_16x16x32_bf16 a[136:139], a[32:35], a[172:175], a[136:139]\n\t"
      "ds_read_b128 a[80:83], v1 offset:6144\n\t"
      "v_mfma_f32_16x16x32_bf16 v[148:151], a[36:39], a[28:31], v[148:151]\n\t"
      "v_mfma_f32_16x16x32_bf16 v[132:135], a[36:39], a[24:27], v[132:135]\n\t"
      "v_mfma_f32_16x16x32_bf16 v[116:119], a[36:39], a[20:23], v[116:119]\n\t"
      "ds_read_b128 a[84:87], v1 offset:8192\n\t"
      "v_mfma_f32_16x16x32_bf16 v[100:103], a[36:39], a[16:19], v[100:103]\n\t"
      "v_mfma_f32_16x16x32_bf16 v[84:87], a[36:39], a[12:15], v[84:87]\n\t"
      "v_mfma_f32_16x16x32_bf16 v[68:71], a[36:39], a[8:11], v[68:71]\n\t"
      "ds_read_b128 a[88:91], v1 offset:10240\n\t"
      "v_mfma_f32_16x16x32_bf16 v[52:55], a[36:39], a[4:7], v[52:55]\n\t"
      "v_mfma_f32_16x16x32_bf16 v[36:39], a[36:39], a[0:3], v[36:39]\n\t"
      "v_mfma_f32_16x16x32_bf16 a[140:143], a[36:39], a[172:175], a[140:143]\n\t"
      "ds_read_b128 a[92:95], v1 offset:12288\n\t"
      "v_mfma_f32_16x16x32_bf16 v[40:43], a[40:43], a[0:3], v[40:43]\n\t"
      "v_mfma_f32_16x16x32_bf16 v[56:59], a[40:43], a[4:7], v[56:59]\n\t"
      "v_mfma_f32_16x16x32_bf16 v[72:75], a[40:43], a[8:11], v[72:75]\n\t"
      "ds_read_b128 a[96:99], v1 offset:14336\n\t"
      "v_mfma_f32_16x16x32_bf16 v[88:91], a[40:43], a[12:15], v[88:91]\n\t"
      "v_mfma_f32_16x16x32_bf16 v[104:107], a[40:43], a[16:19], v[104:107]\n\t"
      "v_mfma_f32_16x16x32_bf16 v[120:123], a[40:43], a[20:23], v[120:123]\n\t"
      "ds_read_b64_tr_b16 a[100:101], v2 offset:8192\n\t"
      "v_mfma_f32_16x16x32_bf16 v[136:139], a[40:43], a[24:27], v[136:139]\n\t"
      "v_mfma_f32_16x16x32_bf16 v[152:155], a[40:43], a[28:31], v[152:155]\n\t"
      "v_mfma_f32_16x16x32_bf16 a[144:147], a[40:43], a[172:175], a[144:147]\n\t"
      "ds_read_b64_tr_b16 a[102:103], v2 offset:10240\n\t"
      "v_mfma_f32_16x16x32_bf16 v[156:159], a[44:47], a[28:31], v[156:159]\n\t"
      "v_mfma_f32_16x16x32_bf16 v[140:143], a[44:47], a[24:27], v[140:143]\n\t"
      "v_mfma_f32_16x16x32_bf16 v[124:127], a[44:47], a[20:23], v[124:127]\n\t"
      "ds_read_b64_tr_b16 a[104:105], v3 offset:8192\n\t"
      "v_mfma_f32_16x16x32_bf16 v[108:111], a[44:47], a[16:19], v[108:111]\n\t"
      "v_mfma_f32_16x16x32_bf16 v[92:95], a[44:47], a[12:15], v[92:95]\n\t"
      "ds_read_b64_tr_b16 a[106:107], v3 offset:10240\n\t"
      "v_mfma_f32_16x16x32_bf16 v[76:79], a[44:47], a[8:11], v[76:79]\n\t"
      "v_mfma_f32_16x16x32_bf16 v[60:63], a[44:47], a[4:7], v[60:63]\n\t"
      "v_mfma_f32_16x16x32_bf16 v[44:47], a[44:47], a[0:3], v[44:47]\n\t"
      "ds_read_b64_tr_b16 a[108:109], v2 offset:12288\n\t"
      "v_mfma_f32_16x16x32_bf16 a[148:151], a[44:47], a[172:175], a[148:151]\n\t"
      "v_mfma_f32_16x16x32_bf16 v[160:163], a[0:3], a[48:51], v[160:163]\n\t"
      "v_mfma_f32_16x16x32_bf16 v[164:167], a[4:7], a[48:51], v[164:167]\n\t"
      "ds_read_b64_tr_b16 a[110:111], v2 offset:14336\n\t"
      "v_mfma_f32_16x16x32_bf16 v[168:171], a[8:11], a[48:51], v[168:171]\n\t"
      "v_mfma_f32_16x16x32_bf16 v[172:175], a[12:15], a[48:51], v[172:175]\n\t"
      "v_mfma_f32_16x16x32_bf16 v[176:179], a[16:19], a[48:51], v[176:179]\n\t"
      "ds_read_b64_tr_b16 a[112:113], v3 offset:12288\n\t"
      "v_mfma_f32_16x16x32_bf16 v[180:183], a[20:23], a[48:51], v[180:183]\n\t"
      "v_mfma_f32_16x16x32_bf16 v[184:187], a[24:27], a[48:51], v[184:187]\n\t"
      "v_mfma_f32_16x16x32_bf16 v[188:191], a[28:31], a[48:51], v[188:191]\n\t"
      "ds_read_b64_tr_b16 a[114:115], v3 offset:14336\n\t"
      "v_mfma_f32_16x16x32_bf16 a[152:155], a[172:175], a[48:51], a[152:155]\n\t"
      "v_mfma_f32_16x16x32_bf16 v[220:223], a[28:31], a[52:55], v[220:223]\n\t"
      "v_mfma_f32_16x16x32_bf16 v[216:219], a[24:27], a[52:55], v[216:219]\n\t"
      "ds_read_b128 a[116:119], v7 offset:0\n\t"
      "v_mfma_f32_16x16x32_bf16 v[212:215], a[20:23], a[52:55], v[212:215]\n\t"
      "v_mfma_f32_16x16x32_bf16 v[208:211], a[16:19], a[52:55], v[208:211]\n\t"
      "v_mfma_f32_16x16x32_bf16 v[204:207], a[12:15], a[52:55], v[204:207]\n\t"
      "ds_read_b128 a[120:123], v7 offset:2048\n\t"
      "v_mfma_f32_16x16x32_bf16 v[200:203], a[8:11], a[52:55], v[200:203]\n\t"
      "v_mfma_f32_16x16x32_bf16 v[196:199], a[4:7], a[52:55], v[196:199]\n\t"
      "v_mfma_f32_16x16x32_bf16 v[192:195], a[0:3], a[52:55], v[192:195]\n\t"
      "ds_read_b128 a[124:127], v7 offset:4096\n\t"
      "v_mfma_f32_16x16x32_bf16 v[224:227], a[0:3], a[56:59], v[224:227]\n\t"
      "v_mfma_f32_16x16x32_bf16 v[228:231], a[4:7], a[56:59], v[228:231]\n\t"
      "v_mfma_f32_16x16x32_bf16 v[232:235], a[8:11], a[56:59], v[232:235]\n\t"
      "ds_read_b128 a[128:131], v7 offset:6144\n\t"
      "v_mfma_f32_16x16x32_bf16 v[236:239], a[12:15], a[56:59], v[236:239]\n\t"
      "v_mfma_f32_16x16x32_bf16 v[240:243], a[16:19], a[56:59], v[240:243]\n\t"
      "v_mfma_f32_16x16x32_bf16 v[244:247], a[20:23], a[56:59], v[244:247]\n\t"
      "ds_read_b128 a[132:135], v7 offset:8192\n\t"
      "v_mfma_f32_16x16x32_bf16 v[248:251], a[24:27], a[56:59], v[248:251]\n\t"
      "v_mfma_f32_16x16x32_bf16 v[252:255], a[28:31], a[56:59], v[252:255]\n\t"
      "v_mfma_f32_16x16x32_bf16 a[220:223], a[28:31], a[60:63], a[220:223]\n\t"
      "v_mfma_f32_16x16x32_bf16 a[216:219], a[24:27], a[60:63], a[216:219]\n\t"
      "v_mfma_f32_16x16x32_bf16 a[212:215], a[20:23], a[60:63], a[212:215]\n\t"
      "v_mfma_f32_16x16x32_bf16 a[208:211], a[16:19], a[60:63], a[208:211]\n\t"
      "v_mfma_f32_16x16x32_bf16 a[204:207], a[12:15], a[60:63], a[204:207]\n\t"
      "v_mfma_f32_16x16x32_bf16 a[200:203], a[8:11], a[60:63], a[200:203]\n\t"
      "v_mfma_f32_16x16x32_bf16 a[196:199], a[4:7], a[60:63], a[196:199]\n\t"
      "v_mfma_f32_16x16x32_bf16 a[192:195], a[0:3], a[60:63], a[192:195]\n\t"
      "v_mfma_f32_16x16x32_bf16 a[224:227], a[0:3], a[64:67], a[224:227]\n\t"
      "v_mfma_f32_16x16x32_bf16 a[228:231], a[4:7], a[64:67], a[228:231]\n\t"
      "v_mfma_f32_16x16x32_bf16 a[232:235], a[8:11], a[64:67], a[232:235]\n\t"
      "v_mfma_f32_16x16x32_bf16 a[236:239], a[12:15], a[64:67], a[236:239]\n\t"
      "v_mfma_f32_16x16x32_bf16 a[240:243], a[16:19], a[64:67], a[240:243]\n\t"
      "v_mfma_f32_16x16x32_bf16 a[244:247], a[20:23], a[64:67], a[244:247]\n\t"
      "v_mfma_f32_16x16x32_bf16 a[248:251], a[24:27], a[64:67], a[248:251]\n\t"
      "v_mfma_f32_16x16x32_bf16 a[252:255], a[28:31], a[64:67], a[252:255]\n\t"
      "s_waitcnt vmcnt(2) lgkmcnt(0)\n\t"
      "s_barrier\n\t"
      "v_mfma_f32_16x16x32_bf16 v[32:35], a[100:103], a[68:71], v[32:35]\n\t"
      "ds_read_b128 a[0:3], v0 offset:32768\n\t"
      "v_mfma_f32_16x16x32_bf16 v[48:51], a[100:103], a[72:75], v[48:51]\n\t"
      "v_mfma_f32_16x16x32_bf16 v[64:67], a[100:103], a[76:79], v[64:67]\n\t"
      "ds_read_b128 a[4:7], v0 offset:34816\n\t"
      "buffer_load_dwordx2 v[8:9], v27, s[64:67], 0 offen sc1\n\t"
      "buffer_load_dwordx2 v[10:11], v28, s[64:67], 0 offen sc1\n\t"
      "buffer_load_dwordx2 v[12:13], v29, s[64:67], 0 offen sc1\n\t"
      "v_mfma_f32_16x16x32_bf16 v[80:83], a[100:103], a[80:83], v[80:83]\n\t"
      "v_mfma_f32_16x16x32_bf16 v[96:99], a[100:103], a[84:87], v[96:99]\n\t"
      "v_mfma_f32_16x16x32_bf16 v[112:115], a[100:103], a[88:91], v[112:115]\n\t"
      "ds_read_b128 a[8:11], v0 offset:36864\n\t"
      "v_mfma_f32_16x16x32_bf16 v[128:131], a[100:103], a[92:95], v[128:131]\n\t"
      "v_mfma_f32_16x16x32_bf16 v[144:147], a[100:103], a[96:99], v[144:147]\n\t"
      "v_mfma_f32_16x16x32_bf16 a[136:139], a[100:103], a[172:175], a[136:139]\n\t"
      "ds_read_b128 a[12:15], v0 offset:38912\n\t"
      "v_mfma_f32_16x16x32_bf16 v[148:151], a[104:107], a[96:99], v[148:151]\n\t"
      "v_mfma_f32_16x16x32_bf16 v[132:135], a[104:107], a[92:95], v[132:135]\n\t"
      "v_mfma_f32_16x16x32_bf16 v[116:119], a[104:107], a[88:91], v[116:119]\n\t"
      "ds_read_b128 a[16:19], v0 offset:40960\n\t"
      "v_mfma_f32_16x16x32_bf16 v[100:103], a[104:107], a[84:87], v[100:103]\n\t"
      "v_mfma_f32_16x16x32_bf16 v[84:87], a[104:107], a[80:83], v[84:87]\n\t"
      "v_mfma_f32_16x16x32_bf16 v[68:71], a[104:107], a[76:79], v[68:71]\n\t"
      "ds_read_b128 a[20:23], v0 offset:43008\n\t"
      "v_mfma_f32_16x16x32_bf16 v[52:55], a[104:107], a[72:75], v[52:55]\n\t"
      "v_mfma_f32_16x16x32_bf16 v[36:39], a[104:107], a[68:71], v[36:39]\n\t"
      "v_mfma_f32_16x16x32_bf16 a[140:143], a[104:107], a[172:175], a[140:143]\n\t"
      "ds_read_b128 a[24:27], v0 offset:45056\n\t"
      "v_mfma_f32_16x16x32_bf16 v[40:43], a[108:111], a[68:71], v[40:43]\n\t"
      "v_mfma_f32_16x16x32_bf16 v[56:59], a[108:111], a[72:75], v[56:59]\n\t"
      "v_mfma_f32_16x16x32_bf16 v[72:75], a[108:111], a[76:79], v[72:75]\n\t"
      "ds_read_b128 a[28:31], v0 offset:47104\n\t"
      "v_mfma_f32_16x16x32_bf16 v[88:91], a[108:111], a[80:83], v[88:91]\n\t"
      "v_mfma_f32_16x16x32_bf16 v[104:107], a[108:111], a[84:87], v[104:107]\n\t"
      "v_mfma_f32_16x16x32_bf16 v[120:123], a[108:111], a[88:91], v[120:123]\n\t"
      "ds_read_b64_tr_b16 a[32:33], v2 offset:16384\n\t"
      "v_mfma_f32_16x16x32_bf16 v[136:139], a[108:111], a[92:95], v[136:139]\n\t"
      "v_mfma_f32_16x16x32_bf16 v[152:155], a[108:111], a[96:99], v[152:155]\n\t"
      "v_mfma_f32_16x16x32_bf16 a[144:147], a[108:111], a[172:175], a[144:147]\n\t"
      "ds_read_b64_tr_b16 a[34:35], v2 offset:18432\n\t"
      "v_mfma_f32_16x16x32_bf16 v[156:159], a[112:115], a[96:99], v[156:159]\n\t"
      "v_mfma_f32_16x16x32_bf16 v[140:143], a[112:115], a[92:95], v[140:143]\n\t"
      "v_mfma_f32_16x16x32_bf16 v[124:127], a[112:115], a[88:91], v[124:127]\n\t"
      "ds_read_b64_tr_b16 a[36:37], v3 offset:16384\n\t"
      "v_mfma_f32_16x16x32_bf16 v[108:111], a[112:115], a[84:87], v[108:111]\n\t"
      "v_mfma_f32_16x16x32_bf16 v[92:95], a[112:115], a[80:83], v[92:95]\n\t"
      "ds_read_b64_tr_b16 a[38:39], v3 offset:18432\n\t"
      "v_mfma_f32_16x16x32_bf16 v[76:79], a[112:115], a[76:79], v[76:79]\n\t"
      "v_mfma_f32_16x16x32_bf16 v[60:63], a[112:115], a[72:75], v[60:63]\n\t"
      "v_mfma_f32_16x16x32_bf16 v[44:47], a[112:115], a[68:71], v[44:47]\n\t"
      "ds_read_b64_tr_b16 a[40:41], v2 offset:20480\n\t"
      "v_mfma_f32_16x16x32_bf16 a[148:151], a[112:115], a[172:175], a[148:151]\n\t"
      "v_mfma_f32_16x16x32_bf16 v[160:163], a[68:71], a[116:119], v[160:163]\n\t"
      "v_mfma_f32_16x16x32_bf16 v[164:167], a[72:75], a[116:119], v[164:167]\n\t"
      "ds_read_b64_tr_b16 a[42:43], v2 offset:22528\n\t"
      "v_mfma_f32_16x16x32_bf16 v[168:171], a[76:79], a[116:119], v[168:171]\n\t"
      "v_mfma_f32_16x16x32_bf16 v[172:175], a[80:83], a[116:119], v[172:175]\n\t"
      "v_mfma_f32_16x16x32_bf16 v[176:179], a[84:87], a[116:119], v[176:179]\n\t"
      "ds_read_b64_tr_b16 a[44:45], v3 offset:20480\n\t"
      "v_mfma_f32_16x16x32_bf16 v[180:183], a[88:91], a[116:119], v[180:183]\n\t"
      "v_mfma_f32_16x16x32_bf16 v[184:187], a[92:95], a[116:119], v[184:187]\n\t"
      "v_mfma_f32_16x16x32_bf16 v[188:191], a[96:99], a[116:119], v[188:191]\n\t"
      "ds_read_b64_tr_b16 a[46:47], v3 offset:22528\n\t"
      "v_mfma_f32_16x16x32_bf16 a[152:155], a[172:175], a[116:119], a[152:155]\n\t"
      "v_mfma_f32_16x16x32_bf16 v[220:223], a[96:99], a[120:123], v[220:223]\n\t"
      "v_mfma_f32_16x16x32_bf16 v[216:219], a[92:95], a[120:123], v[216:219]\n\t"
      "ds_read_b128 a[48:51], v6 offset:20480\n\t"
      "v_mfma_f32_16x16x32_bf16 v[212:215], a[88:91], a[120:123], v[212:215]\n\t"
      "v_mfma_f32_16x16x32_bf16 v[208:211], a[84:87], a[120:123], v[208:211]\n\t"
      "v_mfma_f32_16x16x32_bf16 v[204:207], a[80:83], a[120:123], v[204:207]\n\t"
      "ds_read_b128 a[52:55], v6 offset:22528\n\t"
      "v_mfma_f32_16x16x32_bf16 v[200:203], a[76:79], a[120:123], v[200:203]\n\t"
      "v_mfma_f32_16x16x32_bf16 v[196:199], a[72:75], a[120:123], v[196:199]\n\t"
      "v_mfma_f32_16x16x32_bf16 v[192:195], a[68:71], a[120:123], v[192:195]\n\t"
      "ds_read_b128 a[56:59], v6 offset:24576\n\t"
      "v_mfma_f32_16x16x32_bf16 v[224:227], a[68:71], a[124:127], v[224:227]\n\t"
      "v_mfma_f32_16x16x32_bf16 v[228:231], a[72:75], a[124:127], v[228:231]\n\t"
      "v_mfma_f32_16x16x32_bf16 v[232:235], a[76:79], a[124:127], v[232:235]\n\t"
      "ds_read_b128 a[60:63], v6 offset:26624\n\t"
      "v_mfma_f32_16x16x32_bf16 v[236:239], a[80:83], a[124:127], v[236:239]\n\t"
      "v_mfma_f32_16x16x32_bf16 v[240:243], a[84:87], a[124:127], v[240:243]\n\t"
      "v_mfma_f32_16x16x32_bf16 v[244:247], a[88:91], a[124:127], v[244:247]\n\t"
      "ds_read_b128 a[64:67], v6 offset:28672\n\t"
      "v_mfma_f32_16x16x32_bf16 v[248:251], a[92:95], a[124:127], v[248:251]\n\t"
      "v_mfma_f32_16x16x32_bf16 v[252:255], a[96:99], a[124:127], v[252:255]\n\t"
      "v_mfma_f32_16x16x32_bf16 a[220:223], a[96:99], a[128:131], a[220:223]\n\t"
      "v_mfma_f32_16x16x32_bf16 a[216:219], a[92:95], a[128:131], a[216:219]\n\t"
      "v_mfma_f32_16x16x32_bf16 a[212:215], a[88:91], a[128:131], a[212:215]\n\t"
      "v_mfma_f32_16x16x32_bf16 a[208:211], a[84:87], a[128:131], a[208:211]\n\t"
      "v_mfma_f32_16x16x32_bf16 a[204:207], a[80:83], a[128:131], a[204:207]\n\t"
      "v_mfma_f32_16x16x32_bf16 a[200:203], a[76:79], a[128:131], a[200:203]\n\t"
      "v_mfma_f32_16x16x32_bf16 a[196:199], a[72:75], a[128:131], a[196:199]\n\t"
      "v_mfma_f32_16x16x32_bf16 a[192:195], a[68:71], a[128:131], a[192:195]\n\t"
      "v_mfma_f32_16x16x32_bf16 a[224:227], a[68:71], a[132:135], a[224:227]\n\t"
      "v_mfma_f32_16x16x32_bf16 a[228:231], a[72:75], a[132:135], a[228:231]\n\t"
      "v_mfma_f32_16x16x32_bf16 a[232:235], a[76:79], a[132:135], a[232:235]\n\t"
      "v_mfma_f32_16x16x32_bf16 a[236:239], a[80:83], a[132:135], a[236:239]\n\t"
      "v_mfma_f32_16x16x32_bf16 a[240:243], a[84:87], a[132:135], a[240:243]\n\t"
      "v_mfma_f32_16x16x32_bf16 a[244:247], a[88:91], a[132:135], a[244:247]\n\t"
      "v_mfma_f32_16x16x32_bf16 a[248:251], a[92:95], a[132:135], a[248:251]\n\t"
      "v_mfma_f32_16x16x32_bf16 a[252:255], a[96:99], a[132:135], a[252:255]\n\t"
      "s_waitcnt vmcnt(0) lgkmcnt(0)\n\t"
      "s_not_b32 s74, s68\n\t"
      "s_not_b32 s75, s69\n\t"
      "s_cmp_eq_u32 s63, 1\n\t"
      "s_cselect_b32 s74, s68, s74\n\t"
      "s_cselect_b32 s75, s69, s75\n\t"
      "v_mov_b32 v22, s74\n\t"
      "v_mov_b32 v23, s75\n\t"
      "s_mov_b64 exec, 1\n\t"
      "buffer_store_dwordx4 v[20:23], v17, s[64:67], 0 offen sc1\n\t"
      "s_mov_b64 exec, -1\n\t"
      "s_nop 1\n\t"
      "s_mov_b32 s72, 0x10000\n\t"
      "LPA0a_chk_r1_%=:\n\t"
      "v_cmp_ne_u64 vcc, v[8:9], s[68:69]\n\t"
      "s_nop 4\n\t"
      "s_cbranch_vccz LPA0a_ok_r1_%=\n\t"
      "s_sub_u32 s72, s72, 1\n\t"
      "s_cmp_eq_u32 s72, 0\n\t"
      "s_cbranch_scc1 LPA0a_ok_r1_%=\n\t"
      "s_sleep 1\n\t"
      "buffer_load_dwordx2 v[8:9], v27, s[64:67], 0 offen sc1 ; alt\n\t"
      "s_waitcnt vmcnt(0)\n\t"
      "s_branch LPA0a_chk_r1_%=\n\t"
      "LPA0a_ok_r1_%=:\n\t"
      "s_mov_b32 s72, 0x10000\n\t"
      "LPA1a_chk_r1_%=:\n\t"
      "v_cmp_ne_u64 vcc, v[10:11], s[68:69]\n\t"
      "s_nop 4\n\t"
      "s_cbranch_vccz LPA1a_ok_r1_%=\n\t"
      "s_sub_u32 s72, s72, 1\n\t"
      "s_cmp_eq_u32 s72, 0\n\t"
      "s_cbranch_scc1 LPA1a_ok_r1_%=\n\t"
      "s_sleep 1\n\t"
      "buffer_load_dwordx2 v[10:11], v28, s[64:67], 0 offen sc1 ; alt\n\t"
      "s_waitcnt vmcnt(0)\n\t"
      "s_branch LPA1a_chk_r1_%=\n\t"
      "LPA1a_ok_r1_%=:\n\t"
      "s_mov_b32 s72, 0x10000\n\t"
      "LPCa_chk_r1_%=:\n\t"
      "v_cmp_ne_u64 vcc, v[12:13], s[68:69]\n\t"
      "s_nop 4\n\t"
      "s_cbranch_vccz LPCa_ok_r1_%=\n\t"
      "s_sub_u32 s72, s72, 1\n\t"
      "s_cmp_eq_u32 s72, 0\n\t"
      "s_cbranch_scc1 LPCa_ok_r1_%=\n\t"
      "s_sleep 1\n\t"
      "buffer_load_dwordx2 v[12:13], v29, s[64:67], 0 offen sc1 ; alt\n\t"
      "s_waitcnt vmcnt(0)\n\t"
      "s_branch LPCa_chk_r1_%=\n\t"
      "LPCa_ok_r1_%=:\n\t"
      "s_barrier\n\t"
      "v_mfma_f32_16x16x32_bf16 v[32:35], a[32:35], a[0:3], v[32:35]\n\t"
      "ds_read_b128 a[68:71], v1 offset:32768\n\t"
      "v_mfma_f32_16x16x32_bf16 v[48:51], a[32:35], a[4:7], v[48:51]\n\t"
      "v_mfma_f32_16x16x32_bf16 v[64:67], a[32:35], a[8:11], v[64:67]\n\t"
      "ds_read_b128 a[72:75], v1 offset:34816\n\t"
      "s_mov_b32 m0, s70\n\t"
      "v_mfma_f32_16x16x32_bf16 v[80:83], a[32:35], a[12:15], v[80:83]\n\t"
      "buffer_load_dwordx4 v30, s[64:67], 0 offen sc1 lds\n\t"
      "v_mfma_f32_16x16x32_bf16 v[96:99], a[32:35], a[16:19], v[96:99]\n\t"
      "v_mfma_f32_16x16x32_bf16 v[112:115], a[32:35], a[20:23], v[112:115]\n\t"
      "ds_read_b128 a[76:79], v1 offset:36864\n\t"
      "v_mfma_f32_16x16x32_bf16 v[128:131], a[32:35], a[24:27], v[128:131]\n\t"
      "s_mov_b32 m0, s71\n\t"
      "v_mfma_f32_16x16x32_bf16 v[144:147], a[32:35], a[28:31], v[144:147]\n\t"
      "buffer_load_dwordx4 v31, s[64:67], 0 offen sc1 lds\n\t"
      "v_mfma_f32_16x16x32_bf16 a[136:139], a[32:35], a[172:175], a[136:139]\n\t"
      "ds_read_b128 a[80:83], v1 offset:38912\n\t"
      "v_mfma_f32_16x16x32_bf16 v[148:151], a[36:39], a[28:31], v[148:151]\n\t"
      "v_mfma_f32_16x16x32_bf16 v[132:135], a[36:39], a[24:27], v[132:135]\n\t"
      "v_mfma_f32_16x16x32_bf16 v[116:119], a[36:39], a[20:23], v[116:119]\n\t"
      "ds_read_b128 a[84:87], v1 offset:40960\n\t"
      "v_mfma_f32_16x16x32_bf16 v[100:103], a[36:39], a[16:19], v[100:103]\n\t"
      "buffer_load_dwordx4 v31, s[64:67], 0 offen offset:1024 sc1 lds\n\t"
      "v_mfma_f32_16x16x32_bf16 v[84:87], a[36:39], a[12:15], v[84:87]\n\t"
      "v_mfma_f32_16x16x32_bf16 v[68:71], a[36:39], a[8:11], v[68:71]\n\t"
      "ds_read_b128 a[88:91], v1 offset:43008\n\t"
      "v_mfma_f32_16x16x32_bf16 v[52:55], a[36:39], a[4:7], v[52:55]\n\t"
      "v_mfma_f32_16x16x32_bf16 v[36:39], a[36:39], a[0:3], v[36:39]\n\t"
      "v_mfma_f32_16x16x32_bf16 a[140:143], a[36:39], a[172:175], a[140:143]\n\t"
      "ds_read_b128 a[92:95], v1 offset:45056\n\t"
      "v_mfma_f32_16x16x32_bf16 v[40:43], a[40:43], a[0:3], v[40:43]\n\t"
      "v_mfma_f32_16x16x32_bf16 v[56:59], a[40:43], a[4:7], v[56:59]\n\t"
      "v_mfma_f32_16x16x32_bf16 v[72:75], a[40:43], a[8:11], v[72:75]\n\t"
      "ds_read_b128 a[96:99], v1 offset:47104\n\t"
      "v_mfma_f32_16x16x32_bf16 v[88:91], a[40:43], a[12:15], v[88:91]\n\t"
      "v_mfma_f32_16x16x32_bf16 v[104:107], a[40:43], a[16:19], v[104:107]\n\t"
      "v_mfma_f32_16x16x32_bf16 v[120:123], a[40:43], a[20:23], v[120:123]\n\t"
      "ds_read_b64_tr_b16 a[100:101], v2 offset:24576\n\t"
      "v_mfma_f32_16x16x32_bf16 v[136:139], a[40:43], a[24:27], v[136:139]\n\t"
      "v_mfma_f32_16x16x32_bf16 v[152:155], a[40:43], a[28:31], v[152:155]\n\t"
      "v_mfma_f32_16x16x32_bf16 a[144:147], a[40:43], a[172:175], a[144:147]\n\t"
      "ds_read_b64_tr_b16 a[102:103], v2 offset:26624\n\t"
      "v_mfma_f32_16x16x32_bf16 v[156:159], a[44:47], a[28:31], v[156:159]\n\t"
      "v_mfma_f32_16x16x32_bf16 v[140:143], a[44:47], a[24:27], v[140:143]\n\t"
      "v_mfma_f32_16x16x32_bf16 v[124:127], a[44:47], a[20:23], v[124:127]\n\t"
      "ds_read_b64_tr_b16 a[104:105], v3 offset:24576\n\t"
      "v_mfma_f32_16x16x32_bf16 v[108:111], a[44:47], a[16:19], v[108:111]\n\t"
      "v_mfma_f32_16x16x32_bf16 v[92:95], a[44:47], a[12:15], v[92:95]\n\t"
      "ds_read_b64_tr_b16 a[106:107], v3 offset:26624\n\t"
      "v_mfma_f32_16x16x32_bf16 v[76:79], a[44:47], a[8:11], v[76:79]\n\t"
      "v_mfma_f32_16x16x32_bf16 v[60:63], a[44:47], a[4:7], v[60:63]\n\t"
      "v_mfma_f32_16x16x32_bf16 v[44:47], a[44:47], a[0:3], v[44:47]\n\t"
      "ds_read_b64_tr_b16 a[108:109], v2 offset:28672\n\t"
      "v_mfma_f32_16x16x32_bf16 a[148:151], a[44:47], a[172:175], a[148:151]\n\t"
      "v_mfma_f32_16x16x32_bf16 v[160:163], a[0:3], a[48:51], v[160:163]\n\t"
      "v_mfma_f32_16x16x32_bf16 v[164:167], a[4:7], a[48:51], v[164:167]\n\t"
      "ds_read_b64_tr_b16 a[110:111], v2 offset:30720\n\t"
      "v_mfma_f32_16x16x32_bf16 v[168:171], a[8:11], a[48:51], v[168:171]\n\t"
      "v_mfma_f32_16x16x32_bf16 v[172:175], a[12:15], a[48:51], v[172:175]\n\t"
      "v_mfma_f32_16x16x32_bf16 v[176:179], a[16:19], a[48:51], v[176:179]\n\t"
      "ds_read_b64_tr_b16 a[112:113], v3 offset:28672\n\t"
      "v_mfma_f32_16x16x32_bf16 v[180:183], a[20:23], a[48:51], v[180:183]\n\t"
      "v_mfma_f32_16x16x32_bf16 v[184:187], a[24:27], a[48:51], v[184:187]\n\t"
      "v_mfma_f32_16x16x32_bf16 v[188:191], a[28:31], a[48:51], v[188:191]\n\t"
      "ds_read_b64_tr_b16 a[114:115], v3 offset:30720\n\t"
      "v_mfma_f32_16x16x32_bf16 a[152:155], a[172:175], a[48:51], a[152:155]\n\t"
      "v_mfma_f32_16x16x32_bf16 v[220:223], a[28:31], a[52:55], v[220:223]\n\t"
      "v_mfma_f32_16x16x32_bf16 v[216:219], a[24:27], a[52:55], v[216:219]\n\t"
      "ds_read_b128 a[116:119], v7 offset:20480\n\t"
      "v_mfma_f32_16x16x32_bf16 v[212:215], a[20:23], a[52:55], v[212:215]\n\t"
      "v_mfma_f32_16x16x32_bf16 v[208:211], a[16:19], a[52:55], v[208:211]\n\t"
      "v_mfma_f32_16x16x32_bf16 v[204:207], a[12:15], a[52:55], v[204:207]\n\t"
      "ds_read_b128 a[120:123], v7 offset:22528\n\t"
      "v_mfma_f32_16x16x32_bf16 v[200:203], a[8:11], a[52:55], v[200:203]\n\t"
      "v_mfma_f32_16x16x32_bf16 v[196:199], a[4:7], a[52:55], v[196:199]\n\t"
      "v_mfma_f32_16x16x32_bf16 v[192:195], a[0:3], a[52:55], v[192:195]\n\t"
      "ds_read_b128 a[124:127], v7 offset:24576\n\t"
      "v_mfma_f32_16x16x32_bf16 v[224:227], a[0:3], a[56:59], v[224:227]\n\t"
      "v_mfma_f32_16x16x32_bf16 v[228:231], a[4:7], a[56:59], v[228:231]\n\t"
      "v_mfma_f32_16x16x32_bf16 v[232:235], a[8:11], a[56:59], v[232:235]\n\t"
      "ds_read_b128 a[128:131], v7 offset:26624\n\t"
      "v_mfma_f32_16x16x32_bf16 v[236:239], a[12:15], a[56:59], v[236:239]\n\t"
      "v_mfma_f32_16x16x32_bf16 v[240:243], a[16:19], a[56:59], v[240:243]\n\t"
      "v_mfma_f32_16x16x32_bf16 v[244:247], a[20:23], a[56:59], v[244:247]\n\t"
      "ds_read_b128 a[132:135], v7 offset:28672\n\t"
      "v_mfma_f32_16x16x32_bf16 v[248:251], a[24:27], a[56:59], v[248:251]\n\t"
      "v_mfma_f32_16x16x32_bf16 v[252:255], a[28:31], a[56:59], v[252:255]\n\t"
      "v_mfma_f32_16x16x32_bf16 a[220:223], a[28:31], a[60:63], a[220:223]\n\t"
      "v_mfma_f32_16x16x32_bf16 a[216:219], a[24:27], a[60:63], a[216:219]\n\t"
      "v_mfma_f32_16x16x32_bf16 a[212:215], a[20:23], a[60:63], a[212:215]\n\t"
      "v_mfma_f32_16x16x32_bf16 a[208:211], a[16:19], a[60:63], a[208:211]\n\t"
      "v_mfma_f32_16x16x32_bf16 a[204:207], a[12:15], a[60:63], a[204:207]\n\t"
      "v_mfma_f32_16x16x32_bf16 a[200:203], a[8:11], a[60:63], a[200:203]\n\t"
      "v_mfma_f32_16x16x32_bf16 a[196:199], a[4:7], a[60:63], a[196:199]\n\t"
      "v_mfma_f32_16x16x32_bf16 a[192:195], a[0:3], a[60:63], a[192:195]\n\t"
      "v_mfma_f32_16x16x32_bf16 a[224:227], a[0:3], a[64:67], a[224:227]\n\t"
      "v_mfma_f32_16x16x32_bf16 a[228:231], a[4:7], a[64:67], a[228:231]\n\t"
      "v_mfma_f32_16x16x32_bf16 a[232:235], a[8:11], a[64:67], a[232:235]\n\t"
      "v_mfma_f32_16x16x32_bf16 a[236:239], a[12:15], a[64:67], a[236:239]\n\t"
      "v_mfma_f32_16x16x32_bf16 a[240:243], a[16:19], a[64:67], a[240:243]\n\t"
      "v_mfma_f32_16x16x32_bf16 a[244:247], a[20:23], a[64:67], a[244:247]\n\t"
      "v_mfma_f32_16x16x32_bf16 a[248:251], a[24:27], a[64:67], a[248:251]\n\t"
      "v_mfma_f32_16x16x32_bf16 a[252:255], a[28:31], a[64:67], a[252:255]\n\t"
      "s_waitcnt lgkmcnt(0)\n\t"
      "v_mfma_f32_16x16x32_bf16 v[32:35], a[100:103], a[68:71], v[32:35]\n\t"
      "v_mfma_f32_16x16x32_bf16 v[48:51], a[100:103], a[72:75], v[48:51]\n\t"
      "v_mfma_f32_16x16x32_bf16 v[64:67], a[100:103], a[76:79], v[64:67]\n\t"
      "v_mfma_f32_16x16x32_bf16 v[80:83], a[100:103], a[80:83], v[80:83]\n\t"
      "v_mfma_f32_16x16x32_bf16 v[96:99], a[100:103], a[84:87], v[96:99]\n\t"
      "v_mfma_f32_16x16x32_bf16 v[112:115], a[100:103], a[88:91], v[112:115]\n\t"
      "v_mfma_f32_16x16x32_bf16 v[128:131], a[100:103], a[92:95], v[128:131]\n\t"
      "v_mfma_f32_16x16x32_bf16 v[144:147], a[100:103], a[96:99], v[144:147]\n\t"
      "v_mfma_f32_16x16x32_bf16 a[136:139], a[100:103], a[172:175], a[136:139]\n\t"
      "v_mfma_f32_16x16x32_bf16 v[148:151], a[104:107], a[96:99], v[148:151]\n\t"
      "v_mfma_f32_16x16x32_bf16 v[132:135], a[104:107], a[92:95], v[132:135]\n\t"
      "v_mfma_f32_16x16x32_bf16 v[116:119], a[104:107], a[88:91], v[116:119]\n\t"
      "v_mfma_f32_16x16x32_bf16 v[100:103], a[104:107], a[84:87], v[100:103]\n\t"
      "v_mfma_f32_16x16x32_bf16 v[84:87], a[104:107], a[80:83], v[84:87]\n\t"
      "v_mfma_f32_16x16x32_bf16 v[68:71], a[104:107], a[76:79], v[68:71]\n\t"
      "v_mfma_f32_16x16x32_bf16 v[52:55], a[104:107], a[72:75], v[52:55]\n\t"
      "v_mfma_f32_16x16x32_bf16 v[36:39], a[104:107], a[68:71], v[36:39]\n\t"
      "v_mfma_f32_16x16x32_bf16 a[140:143], a[104:107], a[172:175], a[140:143]\n\t"
      "v_mfma_f32_16x16x32_bf16 v[40:43], a[108:111], a[68:71], v[40:43]\n\t"
      "v_mfma_f32_16x16x32_bf16 v[56:59], a[108:111], a[72:75], v[56:59]\n\t"
      "v_mfma_f32_16x16x32_bf16 v[72:75], a[108:111], a[76:79], v[72:75]\n\t"
      "v_mfma_f32_16x16x32_bf16 v[88:91], a[108:111], a[80:83], v[88:91]\n\t"
      "v_mfma_f32_16x16x32_bf16 v[104:107], a[108:111], a[84:87], v[104:107]\n\t"
      "v_mfma_f32_16x16x32_bf16 v[120:123], a[108:111], a[88:91], v[120:123]\n\t"
      "v_mfma_f32_16x16x32_bf16 v[136:139], a[108:111], a[92:95], v[136:139]\n\t"
      "v_mfma_f32_16x16x32_bf16 v[152:155], a[108:111], a[96:99], v[152:155]\n\t"
      "v_mfma_f32_16x16x32_bf16 a[144:147], a[108:111], a[172:175], a[144:147]\n\t"
      "v_mfma_f32_16x16x32_bf16 v[156:159], a[112:115], a[96:99], v[156:159]\n\t"
      "v_mfma_f32_16x16x32_bf16 v[140:143], a[112:115], a[92:95], v[140:143]\n\t"
      "v_mfma_f32_16x16x32_bf16 v[124:127], a[112:115], a[88:91], v[124:127]\n\t"
      "v_mfma_f32_16x16x32_bf16 v[108:111], a[112:115], a[84:87], v[108:111]\n\t"
      "v_mfma_f32_16x16x32_bf16 v[92:95], a[112:115], a[80:83], v[92:95]\n\t"
      "v_mfma_f32_16x16x32_bf16 v[76:79], a[112:115], a[76:79], v[76:79]\n\t"
      "v_mfma_f32_16x16x32_bf16 v[60:63], a[112:115], a[72:75], v[60:63]\n\t"
      "v_mfma_f32_16x16x32_bf16 v[44:47], a[112:115], a[68:71], v[44:47]\n\t"
      "v_mfma_f32_16x16x32_bf16 a[148:151], a[112:115], a[172:175], a[148:151]\n\t"
      "v_mfma_f32_16x16x32_bf16 v[160:163], a[68:71], a[116:119], v[160:163]\n\t"
      "v_mfma_f32_16x16x32_bf16 v[164:167], a[72:75], a[116:119], v[164:167]\n\t"
      "v_mfma_f32_16x16x32_bf16 v[168:171], a[76:79], a[116:119], v[168:171]\n\t"
      "v_mfma_f32_16x16x32_bf16 v[172:175], a[80:83], a[116:119], v[172:175]\n\t"
      "v_mfma_f32_16x16x32_bf16 v[176:179], a[84:87], a[116:119], v[176:179]\n\t"
      "v_mfma_f32_16x16x32_bf16 v[180:183], a[88:91], a[116:119], v[180:183]\n\t"
      "v_mfma_f32_16x16x32_bf16 v[184:187], a[92:95], a[116:119], v[184:187]\n\t"
      "v_mfma_f32_16x16x32_bf16 v[188:191], a[96:99], a[116:119], v[188:191]\n\t"
      "v_mfma_f32_16x16x32_bf16 a[152:155], a[172:175], a[116:119], a[152:155]\n\t"
      "v_mfma_f32_16x16x32_bf16 v[220:223], a[96:99], a[120:123], v[220:223]\n\t"
      "v_mfma_f32_16x16x32_bf16 v[216:219], a[92:95], a[120:123], v[216:219]\n\t"
      "v_mfma_f32_16x16x32_bf16 v[212:215], a[88:91], a[120:123], v[212:215]\n\t"
      "v_mfma_f32_16x16x32_bf16 v[208:211], a[84:87], a[120:123], v[208:211]\n\t"
      "v_mfma_f32_16x16x32_bf16 v[204:207], a[80:83], a[120:123], v[204:207]\n\t"
      "v_mfma_f32_16x16x32_bf16 v[200:203], a[76:79], a[120:123], v[200:203]\n\t"
      "v_mfma_f32_16x16x32_bf16 v[196:199], a[72:75], a[120:123], v[196:199]\n\t"
      "v_mfma_f32_16x16x32_bf16 v[192:195], a[68:71], a[120:123], v[192:195]\n\t"
      "v_mfma_f32_16x16x32_bf16 v[224:227], a[68:71], a[124:127], v[224:227]\n\t"
      "v_mfma_f32_16x16x32_bf16 v[228:231], a[72:75], a[124:127], v[228:231]\n\t"
      "v_mfma_f32_16x16x32_bf16 v[232:235], a[76:79], a[124:127], v[232:235]\n\t"
      "v_mfma_f32_16x16x32_bf16 v[236:239], a[80:83], a[124:127], v[236:239]\n\t"
      "v_mfma_f32_16x16x32_bf16 v[240:243], a[84:87], a[124:127], v[240:243]\n\t"
      "v_mfma_f32_16x16x32_bf16 v[244:247], a[88:91], a[124:127], v[244:247]\n\t"
      "v_mfma_f32_16x16x32_bf16 v[248:251], a[92:95], a[124:127], v[248:251]\n\t"
      "v_mfma_f32_16x16x32_bf16 v[252:255], a[96:99], a[124:127], v[252:255]\n\t"
      "v_mfma_f32_16x16x32_bf16 a[220:223], a[96:99], a[128:131], a[220:223]\n\t"
      "v_mfma_f32_16x16x32_bf16 a[216:219], a[92:95], a[128:131], a[216:219]\n\t"
      "v_mfma_f32_16x16x32_bf16 a[212:215], a[88:91], a[128:131], a[212:215]\n\t"
      "v_mfma_f32_16x16x32_bf16 a[208:211], a[84:87], a[128:131], a[208:211]\n\t"
      "v_mfma_f32_16x16x32_bf16 a[204:207], a[80:83], a[128:131], a[204:207]\n\t"
      "v_mfma_f32_16x16x32_bf16 a[200:203], a[76:79], a[128:131], a[200:203]\n\t"
      "v_mfma_f32_16x16x32_bf16 a[196:199], a[72:75], a[128:131], a[196:199]\n\t"
      "v_mfma_f32_16x16x32_bf16 a[192:195], a[68:71], a[128:131], a[192:195]\n\t"
      "v_mfma_f32_16x16x32_bf16 a[224:227], a[68:71], a[132:135], a[224:227]\n\t"
      "v_mfma_f32_16x16x32_bf16 a[228:231], a[72:75], a[132:135], a[228:231]\n\t"
      "v_mfma_f32_16x16x32_bf16 a[232:235], a[76:79], a[132:135], a[232:235]\n\t"
      "v_mfma_f32_16x16x32_bf16 a[236:239], a[80:83], a[132:135], a[236:239]\n\t"
      "v_mfma_f32_16x16x32_bf16 a[240:243], a[84:87], a[132:135], a[240:243]\n\t"
      "v_mfma_f32_16x16x32_bf16 a[244:247], a[88:91], a[132:135], a[244:247]\n\t"
      "v_mfma_f32_16x16x32_bf16 a[248:251], a[92:95], a[132:135], a[248:251]\n\t"
      "v_mfma_f32_16x16x32_bf16 a[252:255], a[96:99], a[132:135], a[252:255]\n\t"
      "s_nop 7\n\t"
      "v_mbcnt_lo_u32_b32 v22, -1, 0\n\t"
      "v_mbcnt_hi_u32_b32 v22, -1, v22\n\t"
      "v_and_b32 v23, 0x30, v22\n\t"
      "v_add_u32 v23, s82, v23\n\t"
      "v_lshlrev_b32 v22, 2, v22\n\t"
      "v_add_u32 v22, s82, v22\n\t"
      "s_mov_b32 exec_lo, 0x00010001\n\t"
      "s_mov_b32 exec_hi, 0x00010001\n\t"
      "ds_write_b128 v23, a[136:139] offset:0\n\t"
      "ds_write_b128 v23, a[140:143] offset:64\n\t"
      "ds_write_b128 v23, a[144:147] offset:128\n\t"
      "ds_write_b128 v23, a[148:151] offset:192\n\t"
      "s_mov_b64 exec, 0xffff\n\t"
      "ds_write_b32 v22, a152 offset:256\n\t"
      "s_mov_b64 exec, -1\n\t"
      "s_branch LA_end_r1_%=\n\t"
      "LA_tail1_r1_%=:\n\t"
      "s_waitcnt vmcnt(19) lgkmcnt(0)\n\t"
      "s_barrier\n\t"
      "v_mfma_f32_16x16x32_bf16 v[32:35], a[32:35], a[0:3], v[32:35]\n\t"
      "ds_read_b128 a[68:71], v1 offset:0\n\t"
      "v_mfma_f32_16x16x32_bf16 v[48:51], a[32:35], a[4:7], v[48:51]\n\t"
      "v_mfma_f32_16x16x32_bf16 v[64:67], a[32:35], a[8:11], v[64:67]\n\t"
      "ds_read_b128 a[72:75], v1 offset:2048\n\t"
      "v_mfma_f32_16x16x32_bf16 v[80:83], a[32:35], a[12:15], v[80:83]\n\t"
      "v_mfma_f32_16x16x32_bf16 v[96:99], a[32:35], a[16:19], v[96:99]\n\t"
      "v_mfma_f32_16x16x32_bf16 v[112:115], a[32:35], a[20:23], v[112:115]\n\t"
      "ds_read_b128 a[76:79], v1 offset:4096\n\t"
      "v_mfma_f32_16x16x32_bf16 v[128:131], a[32:35], a[24:27], v[128:131]\n\t"
      "v_mfma_f32_16x16x32_bf16 v[144:147], a[32:35], a[28:31], v[144:147]\n\t"
      "v_mfma_f32_16x16x32_bf16 v[148:151], a[36:39], a[28:31], v[148:151]\n\t"
      "ds_read_b128 a[80:83], v1 offset:6144\n\t"
      "v_mfma_f32_16x16x32_bf16 v[132:135], a[36:39], a[24:27], v[132:135]\n\t"
      "v_mfma_f32_16x16x32_bf16 v[116:119], a[36:39], a[20:23], v[116:119]\n\t"
      "v_mfma_f32_16x16x32_bf16 v[100:103], a[36:39], a[16:19], v[100:103]\n\t"
      "ds_read_b128 a[84:87], v1 offset:8192\n\t"
      "v_mfma_f32_16x16x32_bf16 v[84:87], a[36:39], a[12:15], v[84:87]\n\t"
      "v_mfma_f32_16x16x32_bf16 v[68:71], a[36:39], a[8:11], v[68:71]\n\t"
      "v_mfma_f32_16x16x32_bf16 v[52:55], a[36:39], a[4:7], v[52:55]\n\t"
      "ds_read_b128 a[88:91], v1 offset:10240\n\t"
      "v_mfma_f32_16x16x32_bf16 v[36:39], a[36:39], a[0:3], v[36:39]\n\t"
      "v_mfma_f32_16x16x32_bf16 v[40:43], a[40:43], a[0:3], v[40:43]\n\t"
      "v_mfma_f32_16x16x32_bf16 v[56:59], a[40:43], a[4:7], v[56:59]\n\t"
      "ds_read_b128 a[92:95], v1 offset:12288\n\t"
      "v_mfma_f32_16x16x32_bf16 v[72:75], a[40:43], a[8:11], v[72:75]\n\t"
      "v_mfma_f32_16x16x32_bf16 v[88:91], a[40:43], a[12:15], v[88:91]\n\t"
      "v_mfma_f32_16x16x32_bf16 v[104:107], a[40:43], a[16:19], v[104:107]\n\t"
      "ds_read_b128 a[96:99], v1 offset:14336\n\t"
      "v_mfma_f32_16x16x32_bf16 v[120:123], a[40:43], a[20:23], v[120:123]\n\t"
      "v_mfma_f32_16x16x32_bf16 v[136:139], a[40:43], a[24:27], v[136:139]\n\t"
      "v_mfma_f32_16x16x32_bf16 v[152:155], a[40:43], a[28:31], v[152:155]\n\t"
      "ds_read_b64_tr_b16 a[100:101], v2 offset:8192\n\t"
      "v_mfma_f32_16x16x32_bf16 v[156:159], a[44:47], a[28:31], v[156:159]\n\t"
      "v_mfma_f32_16x16x32_bf16 v[140:143], a[44:47], a[24:27], v[140:143]\n\t"
      "v_mfma_f32_16x16x32_bf16 v[124:127], a[44:47], a[20:23], v[124:127]\n\t"
      "ds_read_b64_tr_b16 a[102:103], v2 offset:10240\n\t"
      "v_mfma_f32_16x16x32_bf16 v[108:111], a[44:47], a[16:19], v[108:111]\n\t"
      "v_mfma_f32_16x16x32_bf16 v[92:95], a[44:47], a[12:15], v[92:95]\n\t"
      "v_mfma_f32_16x16x32_bf16 v[76:79], a[44:47], a[8:11], v[76:79]\n\t"
      "ds_read_b64_tr_b16 a[104:105], v3 offset:8192\n\t"
      "v_mfma_f32_16x16x32_bf16 v[60:63], a[44:47], a[4:7], v[60:63]\n\t"
      "v_mfma_f32_16x16x32_bf16 v[44:47], a[44:47], a[0:3], v[44:47]\n\t"
      "ds_read_b64_tr_b16 a[106:107], v3 offset:10240\n\t"
      "v_mfma_f32_16x16x32_bf16 v[160:163], a[0:3], a[48:51], v[160:163]\n\t"
      "v_mfma_f32_16x16x32_bf16 v[164:167], a[4:7], a[48:51], v[164:167]\n\t"
      "v_mfma_f32_16x16x32_bf16 v[168:171], a[8:11], a[48:51], v[168:171]\n\t"
      "ds_read_b64_tr_b16 a[108:109], v2 offset:12288\n\t"
      "v_mfma_f32_16x16x32_bf16 v[172:175], a[12:15], a[48:51], v[172:175]\n\t"
      "v_mfma_f32_16x16x32_bf16 v[176:179], a[16:19], a[48:51], v[176:179]\n\t"
      "v_mfma_f32_16x16x32_bf16 v[180:183], a[20:23], a[48:51], v[180:183]\n\t"
      "ds_read_b64_tr_b16 a[110:111], v2 offset:14336\n\t"
      "v_mfma_f32_16x16x32_bf16 v[184:187], a[24:27], a[48:51], v[184:187]\n\t"
      "v_mfma_f32_16x16x32_bf16 v[188:191], a[28:31], a[48:51], v[188:191]\n\t"
      "v_mfma_f32_16x16x32_bf16 v[220:223], a[28:31], a[52:55], v[220:223]\n\t"
      "ds_read_b64_tr_b16 a[112:113], v3 offset:12288\n\t"
      "v_mfma_f32_16x16x32_bf16 v[216:219], a[24:27], a[52:55], v[216:219]\n\t"
      "v_mfma_f32_16x16x32_bf16 v[212:215], a[20:23], a[52:55], v[212:215]\n\t"
      "v_mfma_f32_16x16x32_bf16 v[208:211], a[16:19], a[52:55], v[208:211]\n\t"
      "ds_read_b64_tr_b16 a[114:115], v3 offset:14336\n\t"
      "v_mfma_f32_16x16x32_bf16 v[204:207], a[12:15], a[52:55], v[204:207]\n\t"
      "v_mfma_f32_16x16x32_bf16 v[200:203], a[8:11], a[52:55], v[200:203]\n\t"
      "v_mfma_f32_16x16x32_bf16 v[196:199], a[4:7], a[52:55], v[196:199]\n\t"
      "ds_read_b128 a[116:119], v7 offset:0\n\t"
      "v_mfma_f32_16x16x32_bf16 v[192:195], a[0:3], a[52:55], v[192:195]\n\t"
      "v_mfma_f32_16x16x32_bf16 a[156:159], a[172:175], a[52:55], a[156:159]\n\t"
      "v_mfma_f32_16x16x32_bf16 v[224:227], a[0:3], a[56:59], v[224:227]\n\t"
      "ds_read_b128 a[120:123], v7 offset:2048\n\t"
      "v_mfma_f32_16x16x32_bf16 v[228:231], a[4:7], a[56:59], v[228:231]\n\t"
      "v_mfma_f32_16x16x32_bf16 v[232:235], a[8:11], a[56:59], v[232:235]\n\t"
      "v_mfma_f32_16x16x32_bf16 v[236:239], a[12:15], a[56:59], v[236:239]\n\t"
      "ds_read_b128 a[124:127], v7 offset:4096\n\t"
      "v_mfma_f32_16x16x32_bf16 v[240:243], a[16:19], a[56:59], v[240:243]\n\t"
      "v_mfma_f32_16x16x32_bf16 v[244:247], a[20:23], a[56:59], v[244:247]\n\t"
      "v_mfma_f32_16x16x32_bf16 v[248:251], a[24:27], a[56:59], v[248:251]\n\t"
      "ds_read_b128 a[128:131], v7 offset:6144\n\t"
      "v_mfma_f32_16x16x32_bf16 v[252:255], a[28:31], a[56:59], v[252:255]\n\t"
      "v_mfma_f32_16x16x32_bf16 a[160:163], a[172:175], a[56:59], a[160:163]\n\t"
      "v_mfma_f32_16x16x32_bf16 a[220:223], a[28:31], a[60:63], a[220:223]\n\t"
      "ds_read_b128 a[132:135], v7 offset:8192\n\t"
      "v_mfma_f32_16x16x32_bf16 a[216:219], a[24:27], a[60:63], a[216:219]\n\t"
      "v_mfma_f32_16x16x32_bf16 a[212:215], a[20:23], a[60:63], a[212:215]\n\t"
      "v_mfma_f32_16x16x32_bf16 a[208:211], a[16:19], a[60:63], a[208:211]\n\t"
      "v_mfma_f32_16x16x32_bf16 a[204:207], a[12:15], a[60:63], a[204:207]\n\t"
      "v_mfma_f32_16x16x32_bf16 a[200:203], a[8:11], a[60:63], a[200:203]\n\t"
      "v_mfma_f32_16x16x32_bf16 a[196:199], a[4:7], a[60:63], a[196:199]\n\t"
      "v_mfma_f32_16x16x32_bf16 a[192:195], a[0:3], a[60:63], a[192:195]\n\t"
      "v_mfma_f32_16x16x32_bf16 a[164:167], a[172:175], a[60:63], a[164:167]\n\t"
      "v_mfma_f32_16x16x32_bf16 a[224:227], a[0:3], a[64:67], a[224:227]\n\t"
      "v_mfma_f32_16x16x32_bf16 a[228:231], a[4:7], a[64:67], a[228:231]\n\t"
      "v_mfma_f32_16x16x32_bf16 a[232:235], a[8:11], a[64:67], a[232:235]\n\t"
      "v_mfma_f32_16x16x32_bf16 a[236:239], a[12:15], a[64:67], a[236:239]\n\t"
      "v_mfma_f32_16x16x32_bf16 a[240:243], a[16:19], a[64:67], a[240:243]\n\t"
      "v_mfma_f32_16x16x32_bf16 a[244:247], a[20:23], a[64:67], a[244:247]\n\t"
      "v_mfma_f32_16x16x32_bf16 a[248:251], a[24:27], a[64:67], a[248:251]\n\t"
      "v_mfma_f32_16x16x32_bf16 a[252:255], a[28:31], a[64:67], a[252:255]\n\t"
      "v_mfma_f32_16x16x32_bf16 a[168:171], a[172:175], a[64:67], a[168:171]\n\t"
      "s_waitcnt vmcnt(2) lgkmcnt(0)\n\t"
      "s_barrier\n\t"
      "v_mfma_f32_16x16x32_bf16 v[32:35], a[100:103], a[68:71], v[32:35]\n\t"
      "ds_read_b128 a[0:3], v0 offset:32768\n\t"
      "v_mfma_f32_16x16x32_bf16 v[48:51], a[100:103], a[72:75], v[48:51]\n\t"
      "v_mfma_f32_16x16x32_bf16 v[64:67], a[100:103], a[76:79], v[64:67]\n\t"
      "ds_read_b128 a[4:7], v0 offset:34816\n\t"
      "buffer_load_dwordx2 v[8:9], v27, s[64:67], 0 offen sc1\n\t"
      "buffer_load_dwordx2 v[10:11], v28, s[64:67], 0 offen sc1\n\t"
      "buffer_load_dwordx2 v[12:13], v29, s[64:67], 0 offen sc1\n\t"
      "v_mfma_f32_16x16x32_bf16 v[80:83], a[100:103], a[80:83], v[80:83]\n\t"
      "v_mfma_f32_16x16x32_bf16 v[96:99], a[100:103], a[84:87], v[96:99]\n\t"
      "v_mfma_f32_16x16x32_bf16 v[112:115], a[100:103], a[88:91], v[112:115]\n\t"
      "ds_read_b128 a[8:11], v0 offset:36864\n\t"
      "v_mfma_f32_16x16x32_bf16 v[128:131], a[100:103], a[92:95], v[128:131]\n\t"
      "v_mfma_f32_16x16x32_bf16 v[144:147], a[100:103], a[96:99], v[144:147]\n\t"
      "v_mfma_f32_16x16x32_bf16 v[148:151], a[104:107], a[96:99], v[148:151]\n\t"
      "ds_read_b128 a[12:15], v0 offset:38912\n\t"
      "v_mfma_f32_16x16x32_bf16 v[132:135], a[104:107], a[92:95], v[132:135]\n\t"
      "v_mfma_f32_16x16x32_bf16 v[116:119], a[104:107], a[88:91], v[116:119]\n\t"
      "v_mfma_f32_16x16x32_bf16 v[100:103], a[104:107], a[84:87], v[100:103]\n\t"
      "ds_read_b128 a[16:19], v0 offset:40960\n\t"
      "v_mfma_f32_16x16x32_bf16 v[84:87], a[104:107], a[80:83], v[84:87]\n\t"
      "v_mfma_f32_16x16x32_bf16 v[68:71], a[104:107], a[76:79], v[68:71]\n\t"
      "v_mfma_f32_16x16x32_bf16 v[52:55], a[104:107], a[72:75], v[52:55]\n\t"
      "ds_read_b128 a[20:23], v0 offset:43008\n\t"
      "v_mfma_f32_16x16x32_bf16 v[36:39], a[104:107], a[68:71], v[36:39]\n\t"
      "v_mfma_f32_16x16x32_bf16 v[40:43], a[108:111], a[68:71], v[40:43]\n\t"
      "v_mfma_f32_16x16x32_bf16 v[56:59], a[108:111], a[72:75], v[56:59]\n\t"
      "ds_read_b128 a[24:27], v0 offset:45056\n\t"
      "v_mfma_f32_16x16x32_bf16 v[72:75], a[108:111], a[76:79], v[72:75]\n\t"
      "v_mfma_f32_16x16x32_bf16 v[88:91], a[108:111], a[80:83], v[88:91]\n\t"
      "v_mfma_f32_16x16x32_bf16 v[104:107], a[108:111], a[84:87], v[104:107]\n\t"
      "ds_read_b128 a[28:31], v0 offset:47104\n\t"
      "v_mfma_f32_16x16x32_bf16 v[120:123], a[108:111], a[88:91], v[120:123]\n\t"
      "v_mfma_f32_16x16x32_bf16 v[136:139], a[108:111], a[92:95], v[136:139]\n\t"
      "v_mfma_f32_16x16x32_bf16 v[152:155], a[108:111], a[96:99], v[152:155]\n\t"
      "ds_read_b64_tr_b16 a[32:33], v2 offset:16384\n\t"
      "v_mfma_f32_16x16x32_bf16 v[156:159], a[112:115], a[96:99], v[156:159]\n\t"
      "v_mfma_f32_16x16x32_bf16 v[140:143], a[112:115], a[92:95], v[140:143]\n\t"
      "v_mfma_f32_16x16x32_bf16 v[124:127], a[112:115], a[88:91], v[124:127]\n\t"
      "ds_read_b64_tr_b16 a[34:35], v2 offset:18432\n\t"
      "v_mfma_f32_16x16x32_bf16 v[108:111], a[112:115], a[84:87], v[108:111]\n\t"
      "v_mfma_f32_16x16x32_bf16 v[92:95], a[112:115], a[80:83], v[92:95]\n\t"
      "v_mfma_f32_16x16x32_bf16 v[76:79], a[112:115], a[76:79], v[76:79]\n\t"
      "ds_read_b64_tr_b16 a[36:37], v3 offset:16384\n\t"
      "v_mfma_f32_16x16x32_bf16 v[60:63], a[112:115], a[72:75], v[60:63]\n\t"
      "v_mfma_f32_16x16x32_bf16 v[44:47], a[112:115], a[68:71], v[44:47]\n\t"
      "ds_read_b64_tr_b16 a[38:39], v3 offset:18432\n\t"
      "v_mfma_f32_16x16x32_bf16 v[160:163], a[68:71], a[116:119], v[160:163]\n\t"
      "v_mfma_f32_16x16x32_bf16 v[164:167], a[72:75], a[116:119], v[164:167]\n\t"
      "v_mfma_f32_16x16x32_bf16 v[168:171], a[76:79], a[116:119], v[168:171]\n\t"
      "ds_read_b64_tr_b16 a[40:41], v2 offset:20480\n\t"
      "v_mfma_f32_16x16x32_bf16 v[172:175], a[80:83], a[116:119], v[172:175]\n\t"
      "v_mfma_f32_16x16x32_bf16 v[176:179], a[84:87], a[116:119], v[176:179]\n\t"
      "v_mfma_f32_16x16x32_bf16 v[180:183], a[88:91], a[116:119], v[180:183]\n\t"
      "ds_read_b64_tr_b16 a[42:43], v2 offset:22528\n\t"
      "v_mfma_f32_16x16x32_bf16 v[184:187], a[92:95], a[116:119], v[184:187]\n\t"
      "v_mfma_f32_16x16x32_bf16 v[188:191], a[96:99], a[116:119], v[188:191]\n\t"
      "v_mfma_f32_16x16x32_bf16 v[220:223], a[96:99], a[120:123], v[220:223]\n\t"
      "ds_read_b64_tr_b16 a[44:45], v3 offset:20480\n\t"
      "v_mfma_f32_16x16x32_bf16 v[216:219], a[92:95], a[120:123], v[216:219]\n\t"
      "v_mfma_f32_16x16x32_bf16 v[212:215], a[88:91], a[120:123], v[212:215]\n\t"
      "v_mfma_f32_16x16x32_bf16 v[208:211], a[84:87], a[120:123], v[208:211]\n\t"
      "ds_read_b64_tr_b16 a[46:47], v3 offset:22528\n\t"
      "v_mfma_f32_16x16x32_bf16 v[204:207], a[80:83], a[120:123], v[204:207]\n\t"
      "v_mfma_f32_16x16x32_bf16 v[200:203], a[76:79], a[120:123], v[200:203]\n\t"
      "v_mfma_f32_16x16x32_bf16 v[196:199], a[72:75], a[120:123], v[196:199]\n\t"
      "ds_read_b128 a[48:51], v6 offset:20480\n\t"
      "v_mfma_f32_16x16x32_bf16 v[192:195], a[68:71], a[120:123], v[192:195]\n\t"
      "v_mfma_f32_16x16x32_bf16 a[156:159], a[172:175], a[120:123], a[156:159]\n\t"
      "v_mfma_f32_16x16x32_bf16 v[224:227], a[68:71], a[124:127], v[224:227]\n\t"
      "ds_read_b128 a[52:55], v6 offset:22528\n\t"
      "v_mfma_f32_16x16x32_bf16 v[228:231], a[72:75], a[124:127], v[228:231]\n\t"
      "v_mfma_f32_16x16x32_bf16 v[232:235], a[76:79], a[124:127], v[232:235]\n\t"
      "v_mfma_f32_16x16x32_bf16 v[236:239], a[80:83], a[124:127], v[236:239]\n\t"
      "ds_read_b128 a[56:59], v6 offset:24576\n\t"
      "v_mfma_f32_16x16x32_bf16 v[240:243], a[84:87], a[124:127], v[240:243]\n\t"
      "v_mfma_f32_16x16x32_bf16 v[244:247], a[88:91], a[124:127], v[244:247]\n\t"
      "v_mfma_f32_16x16x32_bf16 v[248:251], a[92:95], a[124:127], v[248:251]\n\t"
      "ds_read_b128 a[60:63], v6 offset:26624\n\t"
      "v_mfma_f32_16x16x32_bf16 v[252:255], a[96:99], a[124:127], v[252:255]\n\t"
      "v_mfma_f32_16x16x32_bf16 a[160:163], a[172:175], a[124:127], a[160:163]\n\t"
      "v_mfma_f32_16x16x32_bf16 a[220:223], a[96:99], a[128:131], a[220:223]\n\t"
      "ds_read_b128 a[64:67], v6 offset:28672\n\t"
      "v_mfma_f32_16x16x32_bf16 a[216:219], a[92:95], a[128:131], a[216:219]\n\t"
      "v_mfma_f32_16x16x32_bf16 a[212:215], a[88:91], a[128:131], a[212:215]\n\t"
      "v_mfma_f32_16x16x32_bf16 a[208:211], a[84:87], a[128:131], a[208:211]\n\t"
      "v_mfma_f32_16x16x32_bf16 a[204:207], a[80:83], a[128:131], a[204:207]\n\t"
      "v_mfma_f32_16x16x32_bf16 a[200:203], a[76:79], a[128:131], a[200:203]\n\t"
      "v_mfma_f32_16x16x32_bf16 a[196:199], a[72:75], a[128:131], a[196:199]\n\t"
      "v_mfma_f32_16x16x32_bf16 a[192:195], a[68:71], a[128:131], a[192:195]\n\t"
      "v_mfma_f32_16x16x32_bf16 a[164:167], a[172:175], a[128:131], a[164:167]\n\t"
      "v_mfma_f32_16x16x32_bf16 a[224:227], a[68:71], a[132:135], a[224:227]\n\t"
      "v_mfma_f32_16x16x32_bf16 a[228:231], a[72:75], a[132:135], a[228:231]\n\t"
      "v_mfma_f32_16x16x32_bf16 a[232:235], a[76:79], a[132:135], a[232:235]\n\t"
      "v_mfma_f32_16x16x32_bf16 a[236:239], a[80:83], a[132:135], a[236:239]\n\t"
      "v_mfma_f32_16x16x32_bf16 a[240:243], a[84:87], a[132:135], a[240:243]\n\t"
      "v_mfma_f32_16x16x32_bf16 a[244:247], a[88:91], a[132:135], a[244:247]\n\t"
      "v_mfma_f32_16x16x32_bf16 a[248:251], a[92:95], a[132:135], a[248:251]\n\t"
      "v_mfma_f32_16x16x32_bf16 a[252:255], a[96:99], a[132:135], a[252:255]\n\t"
      "v_mfma_f32_16x16x32_bf16 a[168:171], a[172:175], a[132:135], a[168:171]\n\t"
      "s_waitcnt vmcnt(0) lgkmcnt(0)\n\t"
      "s_not_b32 s74, s68\n\t"
      "s_not_b32 s75, s69\n\t"
      "s_cmp_eq_u32 s63, 1\n\t"
      "s_cselect_b32 s74, s68, s74\n\t"
      "s_cselect_b32 s75, s69, s75\n\t"
      "v_mov_b32 v22, s74\n\t"
      "v_mov_b32 v23, s75\n\t"
      "s_mov_b64 exec, 1\n\t"
      "buffer_store_dwordx4 v[20:23], v17, s[64:67], 0 offen sc1\n\t"
      "s_mov_b64 exec, -1\n\t"
      "s_nop 1\n\t"
      "s_mov_b32 s72, 0x10000\n\t"
      "LPA0b_chk_r1_%=:\n\t"
      "v_cmp_ne_u64 vcc, v[8:9], s[68:69]\n\t"
      "s_nop 4\n\t"
      "s_cbranch_vccz LPA0b_ok_r1_%=\n\t"
      "s_sub_u32 s72, s72, 1\n\t"
      "s_cmp_eq_u32 s72, 0\n\t"
      "s_cbranch_scc1 LPA0b_ok_r1_%=\n\t"
      "s_sleep 1\n\t"
      "buffer_load_dwordx2 v[8:9], v27, s[64:67], 0 offen sc1 ; alt\n\t"
      "s_waitcnt vmcnt(0)\n\t"
      "s_branch LPA0b_chk_r1_%=\n\t"
      "LPA0b_ok_r1_%=:\n\t"
      "s_mov_b32 s72, 0x10000\n\t"
      "LPA1b_chk_r1_%=:\n\t"
      "v_cmp_ne_u64 vcc, v[10:11], s[68:69]\n\t"
      "s_nop 4\n\t"
      "s_cbranch_vccz LPA1b_ok_r1_%=\n\t"
      "s_sub_u32 s72, s72, 1\n\t"
      "s_cmp_eq_u32 s72, 0\n\t"
      "s_cbranch_scc1 LPA1b_ok_r1_%=\n\t"
      "s_sleep 1\n\t"
      "buffer_load_dwordx2 v[10:11], v28, s[64:67], 0 offen sc1 ; alt\n\t"
      "s_waitcnt vmcnt(0)\n\t"
      "s_branch LPA1b_chk_r1_%=\n\t"
      "LPA1b_ok_r1_%=:\n\t"
      "s_mov_b32 s72, 0x10000\n\t"
      "LPCb_chk_r1_%=:\n\t"
      "v_cmp_ne_u64 vcc, v[12:13], s[68:69]\n\t"
      "s_nop 4\n\t"
      "s_cbranch_vccz LPCb_ok_r1_%=\n\t"
      "s_sub_u32 s72, s72, 1\n\t"
      "s_cmp_eq_u32 s72, 0\n\t"
      "s_cbranch_scc1 LPCb_ok_r1_%=\n\t"
      "s_sleep 1\n\t"
      "buffer_load_dwordx2 v[12:13], v29, s[64:67], 0 offen sc1 ; alt\n\t"
      "s_waitcnt vmcnt(0)\n\t"
      "s_branch LPCb_chk_r1_%=\n\t"
      "LPCb_ok_r1_%=:\n\t"
      "s_barrier\n\t"
      "v_mfma_f32_16x16x32_bf16 v[32:35], a[32:35], a[0:3], v[32:35]\n\t"
      "ds_read_b128 a[68:71], v1 offset:32768\n\t"
      "v_mfma_f32_16x16x32_bf16 v[48:51], a[32:35], a[4:7], v[48:51]\n\t"
      "v_mfma_f32_16x16x32_bf16 v[64:67], a[32:35], a[8:11], v[64:67]\n\t"
      "ds_read_b128 a[72:75], v1 offset:34816\n\t"
      "s_mov_b32 m0, s70\n\t"
      "v_mfma_f32_16x16x32_bf16 v[80:83], a[32:35], a[12:15], v[80:83]\n\t"
      "buffer_load_dwordx4 v30, s[64:67], 0 offen sc1 lds\n\t"
      "v_mfma_f32_16x16x32_bf16 v[96:99], a[32:35], a[16:19], v[96:99]\n\t"
      "v_mfma_f32_16x16x32_bf16 v[112:115], a[32:35], a[20:23], v[112:115]\n\t"
      "ds_read_b128 a[76:79], v1 offset:36864\n\t"
      "v_mfma_f32_16x16x32_bf16 v[128:131], a[32:35], a[24:27], v[128:131]\n\t"
      "s_mov_b32 m0, s71\n\t"
      "v_mfma_f32_16x16x32_bf16 v[144:147], a[32:35], a[28:31], v[144:147]\n\t"
      "buffer_load_dwordx4 v31, s[64:67], 0 offen sc1 lds\n\t"
      "v_mfma_f32_16x16x32_bf16 v[148:151], a[36:39], a[28:31], v[148:151]\n\t"
      "ds_read_b128 a[80:83], v1 offset:38912\n\t"
      "v_mfma_f32_16x16x32_bf16 v[132:135], a[36:39], a[24:27], v[132:135]\n\t"
      "v_mfma_f32_16x16x32_bf16 v[116:119], a[36:39], a[20:23], v[116:119]\n\t"
      "v_mfma_f32_16x16x32_bf16 v[100:103], a[36:39], a[16:19], v[100:103]\n\t"
      "ds_read_b128 a[84:87], v1 offset:40960\n\t"
      "v_mfma_f32_16x16x32_bf16 v[84:87], a[36:39], a[12:15], v[84:87]\n\t"
      "buffer_load_dwordx4 v31, s[64:67], 0 offen offset:1024 sc1 lds\n\t"
      "v_mfma_f32_16x16x32_bf16 v[68:71], a[36:39], a[8:11], v[68:71]\n\t"
      "v_mfma_f32_16x16x32_bf16 v[52:55], a[36:39], a[4:7], v[52:55]\n\t"
      "ds_read_b128 a[88:91], v1 offset:43008\n\t"
      "v_mfma_f32_16x16x32_bf16 v[36:39], a[36:39], a[0:3], v[36:39]\n\t"
      "v_mfma_f32_16x16x32_bf16 v[40:43], a[40:43], a[0:3], v[40:43]\n\t"
      "v_mfma_f32_16x16x32_bf16 v[56:59], a[40:43], a[4:7], v[56:59]\n\t"
      "ds_read_b128 a[92:95], v1 offset:45056\n\t"
      "v_mfma_f32_16x16x32_bf16 v[72:75], a[40:43], a[8:11], v[72:75]\n\t"
      "v_mfma_f32_16x16x32_bf16 v[88:91], a[40:43], a[12:15], v[88:91]\n\t"
      "v_mfma_f32_16x16x32_bf16 v[104:107], a[40:43], a[16:19], v[104:107]\n\t"
      "ds_read_b128 a[96:99], v1 offset:47104\n\t"
      "v_mfma_f32_16x16x32_bf16 v[120:123], a[40:43], a[20:23], v[120:123]\n\t"
      "v_mfma_f32_16x16x32_bf16 v[136:139], a[40:43], a[24:27], v[136:139]\n\t"
      "v_mfma_f32_16x16x32_bf16 v[152:155], a[40:43], a[28:31], v[152:155]\n\t"
      "ds_read_b64_tr_b16 a[100:101], v2 offset:24576\n\t"
      "v_mfma_f32_16x16x32_bf16 v[156:159], a[44:47], a[28:31], v[156:159]\n\t"
      "v_mfma_f32_16x16x32_bf16 v[140:143], a[44:47], a[24:27], v[140:143]\n\t"
      "v_mfma_f32_16x16x32_bf16 v[124:127], a[44:47], a[20:23], v[124:127]\n\t"
      "ds_read_b64_tr_b16 a[102:103], v2 offset:26624\n\t"
      "v_mfma_f32_16x16x32_bf16 v[108:111], a[44:47], a[16:19], v[108:111]\n\t"
      "v_mfma_f32_16x16x32_bf16 v[92:95], a[44:47], a[12:15], v[92:95]\n\t"
      "v_mfma_f32_16x16x32_bf16 v[76:79], a[44:47], a[8:11], v[76:79]\n\t"
      "ds_read_b64_tr_b16 a[104:105], v3 offset:24576\n\t"
      "v_mfma_f32_16x16x32_bf16 v[60:63], a[44:47], a[4:7], v[60:63]\n\t"
      "v_mfma_f32_16x16x32_bf16 v[44:47], a[44:47], a[0:3], v[44:47]\n\t"
      "ds_read_b64_tr_b16 a[106:107], v3 offset:26624\n\t"
      "v_mfma_f32_16x16x32_bf16 v[160:163], a[0:3], a[48:51], v[160:163]\n\t"
      "v_mfma_f32_16x16x32_bf16 v[164:167], a[4:7], a[48:51], v[164:167]\n\t"
      "v_mfma_f32_16x16x32_bf16 v[168:171], a[8:11], a[48:51], v[168:171]\n\t"
      "ds_read_b64_tr_b16 a[108:109], v2 offset:28672\n\t"
      "v_mfma_f32_16x16x32_bf16 v[172:175], a[12:15], a[48:51], v[172:175]\n\t"
      "v_mfma_f32_16x16x32_bf16 v[176:179], a[16:19], a[48:51], v[176:179]\n\t"
      "v_mfma_f32_16x16x32_bf16 v[180:183], a[20:23], a[48:51], v[180:183]\n\t"
      "ds_read_b64_tr_b16 a[110:111], v2 offset:30720\n\t"
      "v_mfma_f32_16x16x32_bf16 v[184:187], a[24:27], a[48:51], v[184:187]\n\t"
      "v_mfma_f32_16x16x32_bf16 v[188:191], a[28:31], a[48:51], v[188:191]\n\t"
      "v_mfma_f32_16x16x32_bf16 v[220:223], a[28:31], a[52:55], v[220:223]\n\t"
      "ds_read_b64_tr_b16 a[112:113], v3 offset:28672\n\t"
      "v_mfma_f32_16x16x32_bf16 v[216:219], a[24:27], a[52:55], v[216:219]\n\t"
      "v_mfma_f32_16x16x32_bf16 v[212:215], a[20:23], a[52:55], v[212:215]\n\t"
      "v_mfma_f32_16x16x32_bf16 v[208:211], a[16:19], a[52:55], v[208:211]\n\t"
      "ds_read_b64_tr_b16 a[114:115], v3 offset:30720\n\t"
      "v_mfma_f32_16x16x32_bf16 v[204:207], a[12:15], a[52:55], v[204:207]\n\t"
      "v_mfma_f32_16x16x32_bf16 v[200:203], a[8:11], a[52:55], v[200:203]\n\t"
      "v_mfma_f32_16x16x32_bf16 v[196:199], a[4:7], a[52:55], v[196:199]\n\t"
      "ds_read_b128 a[116:119], v7 offset:20480\n\t"
      "v_mfma_f32_16x16x32_bf16 v[192:195], a[0:3], a[52:55], v[192:195]\n\t"
      "v_mfma_f32_16x16x32_bf16 a[156:159], a[172:175], a[52:55], a[156:159]\n\t"
      "v_mfma_f32_16x16x32_bf16 v[224:227], a[0:3], a[56:59], v[224:227]\n\t"
      "ds_read_b128 a[120:123], v7 offset:22528\n\t"
      "v_mfma_f32_16x16x32_bf16 v[228:231], a[4:7], a[56:59], v[228:231]\n\t"
      "v_mfma_f32_16x16x32_bf16 v[232:235], a[8:11], a[56:59], v[232:235]\n\t"
      "v_mfma_f32_16x16x32_bf16 v[236:239], a[12:15], a[56:59], v[236:239]\n\t"
      "ds_read_b128 a[124:127], v7 offset:24576\n\t"
      "v_mfma_f32_16x16x32_bf16 v[240:243], a[16:19], a[56:59], v[240:243]\n\t"
      "v_mfma_f32_16x16x32_bf16 v[244:247], a[20:23], a[56:59], v[244:247]\n\t"
      "v_mfma_f32_16x16x32_bf16 v[248:251], a[24:27], a[56:59], v[248:251]\n\t"
      "ds_read_b128 a[128:131], v7 offset:26624\n\t"
      "v_mfma_f32_16x16x32_bf16 v[252:255], a[28:31], a[56:59], v[252:255]\n\t"
      "v_mfma_f32_16x16x32_bf16 a[160:163], a[172:175], a[56:59], a[160:163]\n\t"
      "v_mfma_f32_16x16x32_bf16 a[220:223], a[28:31], a[60:63], a[220:223]\n\t"
      "ds_read_b128 a[132:135], v7 offset:28672\n\t"
      "v_mfma_f32_16x16x32_bf16 a[216:219], a[24:27], a[60:63], a[216:219]\n\t"
      "v_mfma_f32_16x16x32_bf16 a[212:215], a[20:23], a[60:63], a[212:215]\n\t"
      "v_mfma_f32_16x16x32_bf16 a[208:211], a[16:19], a[60:63], a[208:211]\n\t"
      "v_mfma_f32_16x16x32_bf16 a[204:207], a[12:15], a[60:63], a[204:207]\n\t"
      "v_mfma_f32_16x16x32_bf16 a[200:203], a[8:11], a[60:63], a[200:203]\n\t"
      "v_mfma_f32_16x16x32_bf16 a[196:199], a[4:7], a[60:63], a[196:199]\n\t"
      "v_mfma_f32_16x16x32_bf16 a[192:195], a[0:3], a[60:63], a[192:195]\n\t"
      "v_mfma_f32_16x16x32_bf16 a[164:167], a[172:175], a[60:63], a[164:167]\n\t"
      "v_mfma_f32_16x16x32_bf16 a[224:227], a[0:3], a[64:67], a[224:227]\n\t"
      "v_mfma_f32_16x16x32_bf16 a[228:231], a[4:7], a[64:67], a[228:231]\n\t"
      "v_mfma_f32_16x16x32_bf16 a[232:235], a[8:11], a[64:67], a[232:235]\n\t"
      "v_mfma_f32_16x16x32_bf16 a[236:239], a[12:15], a[64:67], a[236:239]\n\t"
      "v_mfma_f32_16x16x32_bf16 a[240:243], a[16:19], a[64:67], a[240:243]\n\t"
      "v_mfma_f32_16x16x32_bf16 a[244:247], a[20:23], a[64:67], a[244:247]\n\t"
      "v_mfma_f32_16x16x32_bf16 a[248:251], a[24:27], a[64:67], a[248:251]\n\t"
      "v_mfma_f32_16x16x32_bf16 a[252:255], a[28:31], a[64:67], a[252:255]\n\t"
      "v_mfma_f32_16x16x32_bf16 a[168:171], a[172:175], a[64:67], a[168:171]\n\t"
      "s_waitcnt lgkmcnt(0)\n\t"
      "v_mfma_f32_16x16x32_bf16 v[32:35], a[100:103], a[68:71], v[32:35]\n\t"
      "v_mfma_f32_16x16x32_bf16 v[48:51], a[100:103], a[72:75], v[48:51]\n\t"
      "v_mfma_f32_16x16x32_bf16 v[64:67], a[100:103], a[76:79], v[64:67]\n\t"
      "v_mfma_f32_16x16x32_bf16 v[80:83], a[100:103], a[80:83], v[80:83]\n\t"
      "v_mfma_f32_16x16x32_bf16 v[96:99], a[100:103], a[84:87], v[96:99]\n\t"
      "v_mfma_f32_16x16x32_bf16 v[112:115], a[100:103], a[88:91], v[112:115]\n\t"
      "v_mfma_f32_16x16x32_bf16 v[128:131], a[100:103], a[92:95], v[128:131]\n\t"
      "v_mfma_f32_16x16x32_bf16 v[144:147], a[100:103], a[96:99], v[144:147]\n\t"
      "v_mfma_f32_16x16x32_bf16 v[148:151], a[104:107], a[96:99], v[148:151]\n\t"
      "v_mfma_f32_16x16x32_bf16 v[132:135], a[104:107], a[92:95], v[132:135]\n\t"
      "v_mfma_f32_16x16x32_bf16 v[116:119], a[104:107], a[88:91], v[116:119]\n\t"
      "v_mfma_f32_16x16x32_bf16 v[100:103], a[104:107], a[84:87], v[100:103]\n\t"
      "v_mfma_f32_16x16x32_bf16 v[84:87], a[104:107], a[80:83], v[84:87]\n\t"
      "v_mfma_f32_16x16x32_bf16 v[68:71], a[104:107], a[76:79], v[68:71]\n\t"
      "v_mfma_f32_16x16x32_bf16 v[52:55], a[104:107], a[72:75], v[52:55]\n\t"
      "v_mfma_f32_16x16x32_bf16 v[36:39], a[104:107], a[68:71], v[36:39]\n\t"
      "v_mfma_f32_16x16x32_bf16 v[40:43], a[108:111], a[68:71], v[40:43]\n\t"
      "v_mfma_f32_16x16x32_bf16 v[56:59], a[108:111], a[72:75], v[56:59]\n\t"
      "v_mfma_f32_16x16x32_bf16 v[72:75], a[108:111], a[76:79], v[72:75]\n\t"
      "v_mfma_f32_16x16x32_bf16 v[88:91], a[108:111], a[80:83], v[88:91]\n\t"
      "v_mfma_f32_16x16x32_bf16 v[104:107], a[108:111], a[84:87], v[104:107]\n\t"
      "v_mfma_f32_16x16x32_bf16 v[120:123], a[108:111], a[88:91], v[120:123]\n\t"
      "v_mfma_f32_16x16x32_bf16 v[136:139], a[108:111], a[92:95], v[136:139]\n\t"
      "v_mfma_f32_16x16x32_bf16 v[152:155], a[108:111], a[96:99], v[152:155]\n\t"
      "v_mfma_f32_16x16x32_bf16 v[156:159], a[112:115], a[96:99], v[156:159]\n\t"
      "v_mfma_f32_16x16x32_bf16 v[140:143], a[112:115], a[92:95], v[140:143]\n\t"
      "v_mfma_f32_16x16x32_bf16 v[124:127], a[112:115], a[88:91], v[124:127]\n\t"
      "v_mfma_f32_16x16x32_bf16 v[108:111], a[112:115], a[84:87], v[108:111]\n\t"
      "v_mfma_f32_16x16x32_bf16 v[92:95], a[112:115], a[80:83], v[92:95]\n\t"
      "v_mfma_f32_16x16x32_bf16 v[76:79], a[112:115], a[76:79], v[76:79]\n\t"
      "v_mfma_f32_16x16x32_bf16 v[60:63], a[112:115], a[72:75], v[60:63]\n\t"
      "v_mfma_f32_16x16x32_bf16 v[44:47], a[112:115], a[68:71], v[44:47]\n\t"
      "v_mfma_f32_16x16x32_bf16 v[160:163], a[68:71], a[116:119], v[160:163]\n\t"
      "v_mfma_f32_16x16x32_bf16 v[164:167], a[72:75], a[116:119], v[164:167]\n\t"
      "v_mfma_f32_16x16x32_bf16 v[168:171], a[76:79], a[116:119], v[168:171]\n\t"
      "v_mfma_f32_16x16x32_bf16 v[172:175], a[80:83], a[116:119], v[172:175]\n\t"
      "v_mfma_f32_16x16x32_bf16 v[176:179], a[84:87], a[116:119], v[176:179]\n\t"
      "v_mfma_f32_16x16x32_bf16 v[180:183], a[88:91], a[116:119], v[180:183]\n\t"
      "v_mfma_f32_16x16x32_bf16 v[184:187], a[92:95], a[116:119], v[184:187]\n\t"
      "v_mfma_f32_16x16x32_bf16 v[188:191], a[96:99], a[116:119], v[188:191]\n\t"
      "v_mfma_f32_16x16x32_bf16 v[220:223], a[96:99], a[120:123], v[220:223]\n\t"
      "v_mfma_f32_16x16x32_bf16 v[216:219], a[92:95], a[120:123], v[216:219]\n\t"
      "v_mfma_f32_16x16x32_bf16 v[212:215], a[88:91], a[120:123], v[212:215]\n\t"
      "v_mfma_f32_16x16x32_bf16 v[208:211], a[84:87], a[120:123], v[208:211]\n\t"
      "v_mfma_f32_16x16x32_bf16 v[204:207], a[80:83], a[120:123], v[204:207]\n\t"
      "v_mfma_f32_16x16x32_bf16 v[200:203], a[76:79], a[120:123], v[200:203]\n\t"
      "v_mfma_f32_16x16x32_bf16 v[196:199], a[72:75], a[120:123], v[196:199]\n\t"
      "v_mfma_f32_16x16x32_bf16 v[192:195], a[68:71], a[120:123], v[192:195]\n\t"
      "v_mfma_f32_16x16x32_bf16 a[156:159], a[172:175], a[120:123], a[156:159]\n\t"
      "v_mfma_f32_16x16x32_bf16 v[224:227], a[68:71], a[124:127], v[224:227]\n\t"
      "v_mfma_f32_16x16x32_bf16 v[228:231], a[72:75], a[124:127], v[228:231]\n\t"
      "v_mfma_f32_16x16x32_bf16 v[232:235], a[76:79], a[124:127], v[232:235]\n\t"
      "v_mfma_f32_16x16x32_bf16 v[236:239], a[80:83], a[124:127], v[236:239]\n\t"
      "v_mfma_f32_16x16x32_bf16 v[240:243], a[84:87], a[124:127], v[240:243]\n\t"
      "v_mfma_f32_16x16x32_bf16 v[244:247], a[88:91], a[124:127], v[244:247]\n\t"
      "v_mfma_f32_16x16x32_bf16 v[248:251], a[92:95], a[124:127], v[248:251]\n\t"
      "v_mfma_f32_16x16x32_bf16 v[252:255], a[96:99], a[124:127], v[252:255]\n\t"
      "v_mfma_f32_16x16x32_bf16 a[160:163], a[172:175], a[124:127], a[160:163]\n\t"
      "v_mfma_f32_16x16x32_bf16 a[220:223], a[96:99], a[128:131], a[220:223]\n\t"
      "v_mfma_f32_16x16x32_bf16 a[216:219], a[92:95], a[128:131], a[216:219]\n\t"
      "v_mfma_f32_16x16x32_bf16 a[212:215], a[88:91], a[128:131], a[212:215]\n\t"
      "v_mfma_f32_16x16x32_bf16 a[208:211], a[84:87], a[128:131], a[208:211]\n\t"
      "v_mfma_f32_16x16x32_bf16 a[204:207], a[80:83], a[128:131], a[204:207]\n\t"
      "v_mfma_f32_16x16x32_bf16 a[200:203], a[76:79], a[128:131], a[200:203]\n\t"
      "v_mfma_f32_16x16x32_bf16 a[196:199], a[72:75], a[128:131], a[196:199]\n\t"
      "v_mfma_f32_16x16x32_bf16 a[192:195], a[68:71], a[128:131], a[192:195]\n\t"
      "v_mfma_f32_16x16x32_bf16 a[164:167], a[172:175], a[128:131], a[164:167]\n\t"
      "v_mfma_f32_16x16x32_bf16 a[224:227], a[68:71], a[132:135], a[224:227]\n\t"
      "v_mfma_f32_16x16x32_bf16 a[228:231], a[72:75], a[132:135], a[228:231]\n\t"
      "v_mfma_f32_16x16x32_bf16 a[232:235], a[76:79], a[132:135], a[232:235]\n\t"
      "v_mfma_f32_16x16x32_bf16 a[236:239], a[80:83], a[132:135], a[236:239]\n\t"
      "v_mfma_f32_16x16x32_bf16 a[240:243], a[84:87], a[132:135], a[240:243]\n\t"
      "v_mfma_f32_16x16x32_bf16 a[244:247], a[88:91], a[132:135], a[244:247]\n\t"
      "v_mfma_f32_16x16x32_bf16 a[248:251], a[92:95], a[132:135], a[248:251]\n\t"
      "v_mfma_f32_16x16x32_bf16 a[252:255], a[96:99], a[132:135], a[252:255]\n\t"
      "v_mfma_f32_16x16x32_bf16 a[168:171], a[172:175], a[132:135], a[168:171]\n\t"
      "s_nop 7\n\t"
      "v_mbcnt_lo_u32_b32 v22, -1, 0\n\t"
      "v_mbcnt_hi_u32_b32 v22, -1, v22\n\t"
      "v_and_b32 v23, 0x30, v22\n\t"
      "v_add_u32 v23, s82, v23\n\t"
      "v_lshlrev_b32 v22, 2, v22\n\t"
      "v_add_u32 v22, s82, v22\n\t"
      "s_mov_b32 exec_lo, 0x00010001\n\t"
      "s_mov_b32 exec_hi, 0x00010001\n\t"
      "s_mov_b64 exec, 0xffff\n\t"
      "ds_write_b32 v22, a156 offset:320\n\t"
      "ds_write_b32 v22, a160 offset:384\n\t"
      "ds_write_b32 v22, a164 offset:448\n\t"
      "ds_write_b32 v22, a168 offset:512\n\t"
      "s_mov_b64 exec, -1\n\t"
      "LA_end_r1_%=:\n\t"
      "s_waitcnt vmcnt(0) lgkmcnt(0)\n\t"
      "s_barrier\n\t"
      "s_nop 15\n\t"
      "LAA_done_%=:\n\t"
      : "={v[32:63]}"(c0), "+{v[64:95]}"(c1), "+{v[96:127]}"(c2), "+{v[128:159]}"(c3), "+{v[160:191]}"(c4), "+{v[192:223]}"(c5), "+{v[224:255]}"(c6), "+{a[192:223]}"(d0), "+{a[224:255]}"(d1), "+{v[8:11]}"(va2), "+{v[12:15]}"(va3), "+{v18}"(vj)
      : "{v[0:3]}"(va0), "{v[4:7]}"(va1), "{v[16:17]}"(va4), "{v19}"(vw0), "{s82}"(slt), "{v[20:21]}"(vtag), "{v25}"(vcsa), "{v[26:31]}"(va5), "{s[36:51]}"(sa), "{s[56:59]}"(st2), "{s61}"(st2s), "{s[64:71]}"(sa2), "{s73}"(swm), "{s[80:81]}"(swn2)
      : "memory", "scc", "vcc", "a0", "a1", "a2", "a3", "a4", "a5", "a6", "a7", "a8", "a9", "a10", "a11", "a12", "a13", "a14", "a15", "a16", "a17", "a18", "a19", "a20", "a21", "a22", "a23", "a24", "a25", "a26", "a27", "a28", "a29", "a30", "a31", "a32", "a33", "a34", "a35", "a36", "a37", "a38", "a39", "a40", "a41", "a42", "a43", "a44", "a45", "a46", "a47", "a48", "a49", "a50", "a51", "a52", "a53", "a54", "a55", "a56", "a57", "a58", "a59", "a60", "a61", "a62", "a63", "a64", "a65", "a66", "a67", "a68", "a69", "a70", "a71", "a72", "a73", "a74", "a75", "a76", "a77", "a78", "a79", "a80", "a81", "a82", "a83", "a84", "a85", "a86", "a87", "a88", "a89", "a90", "a91", "a92", "a93", "a94", "a95", "a96", "a97", "a98", "a99", "a100", "a101", "a102", "a103", "a104", "a105", "a106", "a107", "a108", "a109", "a110", "a111", "a112", "a113", "a114", "a115", "a116", "a117", "a118", "a119", "a120", "a121", "a122", "a123", "a124", "a125", "a126", "a127", "a128", "a129", "a130", "a131", "a132", "a133", "a134", "a135", "a136", "a137", "a138", "a139", "a140", "a141", "a142", "a143", "a144", "a145", "a146", "a147", "a148", "a149", "a150", "a151", "a152", "a153", "a154", "a155", "a156", "a157", "a158", "a159", "a160", "a161", "a162", "a163", "a164", "a165", "a166", "a167", "a168", "a169", "a170", "a171", "a172", "a173", "a174", "a175", "v22", "v23", "s52", "s53", "s54", "s55", "s60", "s62", "s63", "s72", "s74", "s75");
    }
    {
      SECTION_VARS
      const unsigned ln = lane_id();
      const unsigned smT = rfl(wave * 8192), smE = rfl(32768 + wave * 4096);
      if (q < 2) {
        u32x4 ve, sb;
        const unsigned l3 = ln >> 3;
#pragma unroll
        for (unsigned i = 0; i < 4; ++i) {
          const unsigned row = 256 * tpn + 64 * wave + 16 * (i & 1) + 8 * (l3 >> 2) + 4 * (i >> 1) + (l3 & 3);
          const unsigned c = (ln & 7) ^ (4 * (i & 1) + (l3 >> 1));
          ve[i] = row * 4096 + c * 16 - 1024 * i;
        }
        put_srd(sb, 0, q == 0 ? kp->wsre : kp->wsim, (unsigned)(S_ * S_ * 2));
        asm volatile(
      "s_nop 4\n\t"
      "s_mov_b32 s48, 0\n\t"
      "s_mov_b32 s55, 131072\n\t"
      "s_add_u32 m0, s44, 0\n\t"
      "s_nop 0\n\t"
      "buffer_load_dwordx4 v16, s[36:39], s48 offen lds\n\t"
      "buffer_load_dwordx4 v17, s[36:39], s48 offen offset:1024 lds\n\t"
      "buffer_load_dwordx4 v18, s[36:39], s48 offen offset:2048 lds\n\t"
      "buffer_load_dwordx4 v19, s[36:39], s48 offen offset:3072 lds\n\t"
      "s_add_u32 m0, s44, 4096\n\t"
      "s_nop 0\n\t"
      "buffer_load_dwordx4 v16, s[36:39], s55 offen lds\n\t"
      "buffer_load_dwordx4 v17, s[36:39], s55 offen offset:1024 lds\n\t"
      "buffer_load_dwordx4 v18, s[36:39], s55 offen offset:2048 lds\n\t"
      "buffer_load_dwordx4 v19, s[36:39], s55 offen offset:3072 lds\n\t"
      "s_mov_b32 s48, 128\n\t"
      "s_mov_b32 s55, 131200\n\t"
      "s_add_u32 m0, s44, 49152\n\t"
      "s_nop 0\n\t"
      "buffer_load_dwordx4 v16, s[36:39], s48 offen lds\n\t"
      "buffer_load_dwordx4 v17, s[36:39], s48 offen offset:1024 lds\n\t"
      "buffer_load_dwordx4 v18, s[36:39], s48 offen offset:2048 lds\n\t"
      "buffer_load_dwordx4 v19, s[36:39], s48 offen offset:3072 lds\n\t"
      "s_add_u32 m0, s44, 53248\n\t"
      "s_nop 0\n\t"
      "buffer_load_dwordx4 v16, s[36:39], s55 offen lds\n\t"
      "buffer_load_dwordx4 v17, s[36:39], s55 offen offset:1024 lds\n\t"
      "buffer_load_dwordx4 v18, s[36:39], s55 offen offset:2048 lds\n\t"
      "buffer_load_dwordx4 v19, s[36:39], s55 offen offset:3072 lds\n\t"
        :: "{v[16:19]}"(ve), "{s[36:39]}"(sb), "{s44}"(smT) : "memory", "scc", "s48", "s55");
      } else {
        u32x4 ve, sb;
#pragma unroll
        for (unsigned i = 0; i < 4; ++i) {
          const unsigned rho = 16 * wave + 4 * i + (ln >> 4), c = (ln & 15) ^ (2 * (rho & 7));
          ve[i] = ((1792 * (q - 2) + rho) * D_ + 128 * ec + 8 * c) * 2 - 1024 * i;
        }
        put_srd(sb, 0, kp->wdn, (unsigned)(F_ * D_ * 2));
        asm volatile(
      "s_nop 4\n\t"
      "s_mov_b32 s49, 0\n\t"
      "s_add_u32 m0, s45, 0\n\t"
      "s_nop 0\n\t"
      "buffer_load_dwordx4 v20, s[40:43], s49 offen lds\n\t"
      "buffer_load_dwordx4 v21, s[40:43], s49 offen offset:1024 lds\n\t"
      "buffer_load_dwordx4 v22, s[40:43], s49 offen offset:2048 lds\n\t"
      "buffer_load_dwordx4 v23, s[40:43], s49 offen offset:3072 lds\n\t"
      "s_mov_b32 s49, 131072\n\t"
      "s_add_u32 m0, s45, 49152\n\t"
      "s_nop 0\n\t"
      "buffer_load_dwordx4 v20, s[40:43], s49 offen lds\n\t"
      "buffer_load_dwordx4 v21, s[40:43], s49 offen offset:1024 lds\n\t"
      "buffer_load_dwordx4 v22, s[40:43], s49 offen offset:2048 lds\n\t"
      "buffer_load_dwordx4 v23, s[40:43], s49 offen offset:3072 lds\n\t"
        :: "{v[20:23]}"(ve), "{s[40:43]}"(sb), "{s45}"(smE) : "memory", "scc", "s49");
      }
    }
    {
      SECTION_VARS
      const unsigned lane = lane_id(), fr = lane & 15, fq = lane >> 4;
      const unsigned csf = L_CSF + wave * 576;
      u32x7 ve;
      ve[0] = lane * 16;
      ve[1] = 0;
      ve[2] = L_RS + (128 * wm + 8 * (fr >> 2) + (fr & 3)) * 4;
      ve[3] = L_RS + (128 * wm + 8 * fq) * 4;
      ve[4] = csf + fq * 16;
      ve[5] = csf + fr * 4;
      ve[6] = L_CSP + wn * 4096 + lane * 16;
      u32x2 ve2 = {L_T + wn * 576 + lane * 16, csf + lane * 16};
      u32x16 se;
      put_srd(se, 0, ws + WS_AB, 8u * 1024 * 1024);
      put_srd(se, 4, ws + WS_HF, 112u * 128 * 1024);
      put_srd(se, 8, fl, FO_END);
      const unsigned sb0 = 16 * p + 8 * wm;
      const unsigned gi = 16 * xcd + (big ? 2 * t4 + wn : 6 + 5 * wn);
      const unsigned kb0 = 14 * xcd + (big ? 4 * t4 + 2 * wn : 12 + wn), kb1 = kb0 + 1;
      se[12] = rfl((((gi >> 6) * 64 + 8 * p + 4 * wm) * 64 + (gi & 63)) * 1024);
      se[13] = rfl((kb0 * 128 + sb0) * 1024); se[14] = rfl((kb1 * 128 + sb0) * 1024); se[15] = 0;
      const u32x2 stag = {(unsigned)tag, (unsigned)(tag >> 32)};
      const u32x2 se2 = {rfl(FO_A + (4 * b + wave) * 8), 0};
      u32x8 se3;
      { const unsigned pb = FO_A + 32 * (4 * q + (ec >> 1));
        se3[0] = rfl(q < 2 ? 1u : 0u); se3[1] = rfl(pb + ((ec & 1) ? 768u : 0u)); se3[2] = rfl(pb + ((ec & 1) ? 768u : 512u)); se3[3] = rfl((ec & 1) ? 0u : 256u);
        se3[4] = rfl(((q * 64 + (wave >> 1)) * 64 + 8 * ec + 4 * (wave & 1)) * 1024); se3[5] = rfl(32768 + wave * 4096); se3[6] = 0; se3[7] = 0; }
      const unsigned swn = rfl(big ? 0u : 1u);
      asm volatile(
      "s_cmp_eq_u32 s80, 0\n\t"
      "s_cbranch_scc0 LEE_role1_%=\n\t"
      "s_nop 4\n\t"
      "s_mov_b32 s52, 0xbfb8aa3b\n\t"
      "s_mov_b32 s53, 0xbfb8aa3b\n\t"
      "s_add_u32 s56, s49, 4096\n\t"
      "s_add_u32 s57, s50, 4096\n\t"
      "s_mov_b32 exec_lo, -1\n\t"
      "s_mov_b32 exec_hi, 15\n\t"
      "ds_read_b128 v[0:3], v22 offset:0\n\t"
      "ds_read_b128 v[4:7], v22 offset:576\n\t"
      "ds_read_b128 v[8:11], v22 offset:1152\n\t"
      "ds_read_b128 v[12:15], v22 offset:1728\n\t"
      "s_waitcnt lgkmcnt(0)\n\t"
      "v_pk_add_f32 v[0:1], v[0:1], v[4:5]\n\t"
      "v_pk_add_f32 v[2:3], v[2:3], v[6:7]\n\t"
      "v_pk_add_f32 v[0:1], v[0:1], v[8:9]\n\t"
      "v_pk_add_f32 v[2:3], v[2:3], v[10:11]\n\t"
      "v_pk_add_f32 v[0:1], v[0:1], v[12:13]\n\t"
      "v_pk_add_f32 v[2:3], v[2:3], v[14:15]\n\t"
      "ds_read_b128 v[4:7], v22 offset:2304\n\t"
      "ds_read_b128 v[8:11], v22 offset:2880\n\t"
      "s_waitcnt lgkmcnt(0)\n\t"
      "v_pk_add_f32 v[0:1], v[0:1], v[4:5]\n\t"
      "v_pk_add_f32 v[2:3], v[2:3], v[6:7]\n\t"
      "v_pk_add_f32 v[0:1], v[0:1], v[8:9]\n\t"
      "v_pk_add_f32 v[2:3], v[2:3], v[10:11]\n\t"
      "ds_read_b128 v[4:7], v24\n\t"
      "s_waitcnt lgkmcnt(0)\n\t"
      "v_pk_add_f32 v[0:1], v[0:1], v[4:5]\n\t"
      "v_pk_add_f32 v[2:3], v[2:3], v[6:7]\n\t"
      "ds_write_b128 v25, v[0:3]\n\t"
      "s_mov_b64 exec, -1\n\t"
      "s_waitcnt lgkmcnt(0)\n\t"
      "ds_read_b32 v23, v21 offset:512\n\t"
      "ds_read_b128 v[0:3], v19 offset:0\n\t"
      "ds_read_b128 v[4:7], v19 offset:1024\n\t"
      "ds_read_b128 v[8:11], v19 offset:16\n\t"
      "ds_read_b128 v[12:15], v19 offset:1040\n\t"
      "v_accvgpr_read_b32 v24, a224\n\t"
      "v_accvgpr_read_b32 v25, a225\n\t"
      "v_accvgpr_read_b32 v26, a226\n\t"
      "v_accvgpr_read_b32 v27, a227\n\t"
      "v_accvgpr_read_b32 v28, a228\n\t"
      "v_accvgpr_read_b32 v29, a229\n\t"
      "v_accvgpr_read_b32 v30, a230\n\t"
      "v_accvgpr_read_b32 v31, a231\n\t"
      "s_waitcnt lgkmcnt(0)\n\t"
      "v_pk_fma_f32 v[24:25], v[4:5], v[22:23], v[24:25] op_sel:[0,1,0] op_sel_hi:[1,1,1] neg_lo:[1,0,0] neg_hi:[1,0,0]\n\t"
      "v_pk_fma_f32 v[26:27], v[6:7], v[22:23], v[26:27] op_sel:[0,1,0] op_sel_hi:[1,1,1] neg_lo:[1,0,0] neg_hi:[1,0,0]\n\t"
      "v_pk_fma_f32 v[28:29], v[12:13], v[22:23], v[28:29] op_sel:[0,1,0] op_sel_hi:[1,1,1] neg_lo:[1,0,0] neg_hi:[1,0,0]\n\t"
      "v_pk_fma_f32 v[30:31], v[14:15], v[22:23], v[30:31] op_sel:[0,1,0] op_sel_hi:[1,1,1] neg_lo:[1,0,0] neg_hi:[1,0,0]\n\t"
      "v_pk_mul_f32 v[24:25], v[24:25], v[0:1]\n\t"
      "v_pk_mul_f32 v[26:27], v[26:27], v[2:3]\n\t"
      "v_pk_mul_f32 v[28:29], v[28:29], v[8:9]\n\t"
      "v_pk_mul_f32 v[30:31], v[30:31], v[10:11]\n\t"
      "v_cvt_pk_bf16_f32 v8, v24, v25\n\t"
      "v_cvt_pk_bf16_f32 v9, v26, v27\n\t"
      "v_cvt_pk_bf16_f32 v10, v28, v29\n\t"
      "v_cvt_pk_bf16_f32 v11, v30, v31\n\t"
      "s_add_u32 s61, s48, 0\n\t"
      "buffer_store_dwordx4 v[8:11], v16, s[36:39], s61 offen sc1\n\t"
      "s_nop 1\n\t"
      "ds_read_b128 v[0:3], v19 offset:128\n\t"
      "ds_read_b128 v[4:7], v19 offset:1152\n\t"
      "ds_read_b128 v[8:11], v19 offset:144\n\t"
      "ds_read_b128 v[12:15], v19 offset:1168\n\t"
      "v_accvgpr_read_b32 v24, a232\n\t"
      "v_accvgpr_read_b32 v25, a233\n\t"
      "v_accvgpr_read_b32 v26, a234\n\t"
      "v_accvgpr_read_b32 v27, a235\n\t"
      "v_accvgpr_read_b32 v28, a236\n\t"
      "v_accvgpr_read_b32 v29, a237\n\t"
      "v_accvgpr_read_b32 v30, a238\n\t"
      "v_accvgpr_read_b32 v31, a239\n\t"
      "s_waitcnt lgkmcnt(0)\n\t"
      "v_pk_fma_f32 v[24:25], v[4:5], v[22:23], v[24:25] op_sel:[0,1,0] op_sel_hi:[1,1,1] neg_lo:[1,0,0] neg_hi:[1,0,0]\n\t"
      "v_pk_fma_f32 v[26:27], v[6:7], v[22:23], v[26:27] op_sel:[0,1,0] op_sel_hi:[1,1,1] neg_lo:[1,0,0] neg_hi:[1,0,0]\n\t"
      "v_pk_fma_f32 v[28:29], v[12:13], v[22:23], v[28:29] op_sel:[0,1,0] op_sel_hi:[1,1,1] neg_lo:[1,0,0] neg_hi:[1,0,0]\n\t"
      "v_pk_fma_f32 v[30:31], v[14:15], v[22:23], v[30:31] op_sel:[0,1,0] op_sel_hi:[1,1,1] neg_lo:[1,0,0] neg_hi:[1,0,0]\n\t"
      "v_pk_mul_f32 v[24:25], v[24:25], v[0:1]\n\t"
      "v_pk_mul_f32 v[26:27], v[26:27], v[2:3]\n\t"
      "v_pk_mul_f32 v[28:29], v[28:29], v[8:9]\n\t"
      "v_pk_mul_f32 v[30:31], v[30:31], v[10:11]\n\t"
      "v_cvt_pk_bf16_f32 v8, v24, v25\n\t"
      "v_cvt_pk_bf16_f32 v9, v26, v27\n\t"
      "v_cvt_pk_bf16_f32 v10, v28, v29\n\t"
      "v_cvt_pk_bf16_f32 v11, v30, v31\n\t"
      "s_add_u32 s61, s48, 65536\n\t"
      "buffer_store_dwordx4 v[8:11], v16, s[36:39], s61 offen sc1\n\t"
      "s_nop 1\n\t"
      "ds_read_b128 v[0:3], v19 offset:256\n\t"
      "ds_read_b128 v[4:7], v19 offset:1280\n\t"
      "ds_read_b128 v[8:11], v19 offset:272\n\t"
      "ds_read_b128 v[12:15], v19 offset:1296\n\t"
      "v_accvgpr_read_b32 v24, a240\n\t"
      "v_accvgpr_read_b32 v25, a241\n\t"
      "v_accvgpr_read_b32 v26, a242\n\t"
      "v_accvgpr_read_b32 v27, a243\n\t"
      "v_accvgpr_read_b32 v28, a244\n\t"
      "v_accvgpr_read_b32 v29, a245\n\t"
      "v_accvgpr_read_b32 v30, a246\n\t"
      "v_accvgpr_read_b32 v31, a247\n\t"
      "s_waitcnt lgkmcnt(0)\n\t"
      "v_pk_fma_f32 v[24:25], v[4:5], v[22:23], v[24:25] op_sel:[0,1,0] op_sel_hi:[1,1,1] neg_lo:[1,0,0] neg_hi:[1,0,0]\n\t"
      "v_pk_fma_f32 v[26:27], v[6:7], v[22:23], v[26:27] op_sel:[0,1,0] op_sel_hi:[1,1,1] neg_lo:[1,0,0] neg_hi:[1,0,0]\n\t"
      "v_pk_fma_f32 v[28:29], v[12:13], v[22:23], v[28:29] op_sel:[0,1,0] op_sel_hi:[1,1,1] neg_lo:[1,0,0] neg_hi:[1,0,0]\n\t"
      "v_pk_fma_f32 v[30:31], v[14:15], v[22:23], v[30:31] op_sel:[0,1,0] op_sel_hi:[1,1,1] neg_lo:[1,0,0] neg_hi:[1,0,0]\n\t"
      "v_pk_mul_f32 v[24:25], v[24:25], v[0:1]\n\t"
      "v_pk_mul_f32 v[26:27], v[26:27], v[2:3]\n\t"
      "v_pk_mul_f32 v[28:29], v[28:29], v[8:9]\n\t"
      "v_pk_mul_f32 v[30:31], v[30:31], v[10:11]\n\t"
      "v_cvt_pk_bf16_f32 v8, v24, v25\n\t"
      "v_cvt_pk_bf16_f32 v9, v26, v27\n\t"
      "v_cvt_pk_bf16_f32 v10, v28, v29\n\t"
      "v_cvt_pk_bf16_f32 v11, v30, v31\n\t"
      "s_add_u32 s61, s48, 131072\n\t"
      "buffer_store_dwordx4 v[8:11], v16, s[36:39], s61 offen sc1\n\t"
      "s_nop 1\n\t"
      "ds_read_b128 v[0:3], v19 offset:384\n\t"
      "ds_read_b128 v[4:7], v19 offset:1408\n\t"
      "ds_read_b128 v[8:11], v19 offset:400\n\t"
      "ds_read_b128 v[12:15], v19 offset:1424\n\t"
      "v_accvgpr_read_b32 v24, a248\n\t"
      "v_accvgpr_read_b32 v25, a249\n\t"
      "v_accvgpr_read_b32 v26, a250\n\t"
      "v_accvgpr_read_b32 v27, a251\n\t"
      "v_accvgpr_read_b32 v28, a252\n\t"
      "v_accvgpr_read_b32 v29, a253\n\t"
      "v_accvgpr_read_b32 v30, a254\n\t"
      "v_accvgpr_read_b32 v31, a255\n\t"
      "s_waitcnt lgkmcnt(0)\n\t"
      "v_pk_fma_f32 v[24:25], v[4:5], v[22:23], v[24:25] op_sel:[0,1,0] op_sel_hi:[1,1,1] neg_lo:[1,0,0] neg_hi:[1,0,0]\n\t"
      "v_pk_fma_f32 v[26:27], v[6:7], v[22:23], v[26:27] op_sel:[0,1,0] op_sel_hi:[1,1,1] neg_lo:[1,0,0] neg_hi:[1,0,0]\n\t"
      "v_pk_fma_f32 v[28:29], v[12:13], v[22:23], v[28:29] op_sel:[0,1,0] op_sel_hi:[1,1,1] neg_lo:[1,0,0] neg_hi:[1,0,0]\n\t"
      "v_pk_fma_f32 v[30:31], v[14:15], v[22:23], v[30:31] op_sel:[0,1,0] op_sel_hi:[1,1,1] neg_lo:[1,0,0] neg_hi:[1,0,0]\n\t"
      "v_pk_mul_f32 v[24:25], v[24:25], v[0:1]\n\t"
      "v_pk_mul_f32 v[26:27], v[26:27], v[2:3]\n\t"
      "v_pk_mul_f32 v[28:29], v[28:29], v[8:9]\n\t"
      "v_pk_mul_f32 v[30:31], v[30:31], v[10:11]\n\t"
      "v_cvt_pk_bf16_f32 v8, v24, v25\n\t"
      "v_cvt_pk_bf16_f32 v9, v26, v27\n\t"
      "v_cvt_pk_bf16_f32 v10, v28, v29\n\t"
      "v_cvt_pk_bf16_f32 v11, v30, v31\n\t"
      "s_add_u32 s61, s48, 196608\n\t"
      "buffer_store_dwordx4 v[8:11], v16, s[36:39], s61 offen sc1\n\t"
      "s_nop 1\n\t"
      "ds_read_b32 v0, v18 offset:0\n\t"
      "ds_read_b32 v1, v18 offset:1024\n\t"
      "ds_read_b32 v2, v18 offset:16\n\t"
      "ds_read_b32 v3, v18 offset:1040\n\t"
      "ds_read_b32 v4, v18 offset:128\n\t"
      "ds_read_b32 v5, v18 offset:1152\n\t"
      "ds_read_b32 v6, v18 offset:144\n\t"
      "ds_read_b32 v7, v18 offset:1168\n\t"
      "ds_read_b32 v8, v18 offset:256\n\t"
      "ds_read_b32 v9, v18 offset:1280\n\t"
      "ds_read_b32 v10, v18 offset:272\n\t"
      "ds_read_b32 v11, v18 offset:1296\n\t"
      "ds_read_b32 v12, v18 offset:384\n\t"
      "ds_read_b32 v13, v18 offset:1408\n\t"
      "ds_read_b32 v14, v18 offset:400\n\t"
      "ds_read_b32 v15, v18 offset:1424\n\t"
      "s_waitcnt lgkmcnt(0)\n\t"
      "ds_read_b128 v[24:27], v20 offset:0\n\t"
      "ds_read_b128 v[28:31], v20 offset:64\n\t"
      "s_waitcnt lgkmcnt(0)\n\t"
      "v_pk_fma_f32 v[32:33], v[24:25], v[0:1], v[32:33] op_sel:[0,1,0] op_sel_hi:[1,1,1] neg_lo:[1,0,0] neg_hi:[1,0,0]\n\t"
      "v_pk_fma_f32 v[34:35], v[26:27], v[0:1], v[34:35] op_sel:[0,1,0] op_sel_hi:[1,1,1] neg_lo:[1,0,0] neg_hi:[1,0,0]\n\t"
      "v_pk_fma_f32 v[36:37], v[28:29], v[0:1], v[36:37] op_sel:[0,1,0] op_sel_hi:[1,1,1] neg_lo:[1,0,0] neg_hi:[1,0,0]\n\t"
      "v_pk_fma_f32 v[38:39], v[30:31], v[0:1], v[38:39] op_sel:[0,1,0] op_sel_hi:[1,1,1] neg_lo:[1,0,0] neg_hi:[1,0,0]\n\t"
      "ds_read_b128 v[24:27], v20 offset:256\n\t"
      "ds_read_b128 v[28:31], v20 offset:320\n\t"
      "s_waitcnt lgkmcnt(0)\n\t"
      "v_pk_fma_f32 v[48:49], v[24:25], v[0:1], v[48:49] op_sel:[0,1,0] op_sel_hi:[1,1,1] neg_lo:[1,0,0] neg_hi:[1,0,0]\n\t"
      "v_pk_fma_f32 v[50:51], v[26:27], v[0:1], v[50:51] op_sel:[0,1,0] op_sel_hi:[1,1,1] neg_lo:[1,0,0] neg_hi:[1,0,0]\n\t"
      "v_pk_fma_f32 v[52:53], v[28:29], v[0:1], v[52:53] op_sel:[0,1,0] op_sel_hi:[1,1,1] neg_lo:[1,0,0] neg_hi:[1,0,0]\n\t"
      "v_pk_fma_f32 v[54:55], v[30:31], v[0:1], v[54:55] op_sel:[0,1,0] op_sel_hi:[1,1,1] neg_lo:[1,0,0] neg_hi:[1,0,0]\n\t"
      "v_pk_mul_f32 v[32:33], v[32:33], v[0:1] op_sel:[0,0] op_sel_hi:[1,0]\n\t"
      "v_pk_mul_f32 v[34:35], v[34:35], v[0:1] op_sel:[0,0] op_sel_hi:[1,0]\n\t"
      "v_pk_mul_f32 v[36:37], v[36:37], v[0:1] op_sel:[0,0] op_sel_hi:[1,0]\n\t"
      "v_pk_mul_f32 v[38:39], v[38:39], v[0:1] op_sel:[0,0] op_sel_hi:[1,0]\n\t"
      "v_pk_mul_f32 v[48:49], v[48:49], v[0:1] op_sel:[0,0] op_sel_hi:[1,0]\n\t"
      "v_pk_mul_f32 v[50:51], v[50:51], v[0:1] op_sel:[0,0] op_sel_hi:[1,0]\n\t"
      "v_pk_mul_f32 v[52:53], v[52:53], v[0:1] op_sel:[0,0] op_sel_hi:[1,0]\n\t"
      "v_pk_mul_f32 v[54:55], v[54:55], v[0:1] op_sel:[0,0] op_sel_hi:[1,0]\n\t"
      "v_pk_mul_f32 v[24:25], v[32:33], s[52:53]\n\t"
      "v_pk_mul_f32 v[26:27], v[34:35], s[52:53]\n\t"
      "v_pk_mul_f32 v[28:29], v[36:37], s[52:53]\n\t"
      "v_pk_mul_f32 v[30:31], v[38:39], s[52:53]\n\t"
      "v_exp_f32 v24, v24\n\t"
      "v_exp_f32 v25, v25\n\t"
      "v_exp_f32 v26, v26\n\t"
      "v_exp_f32 v27, v27\n\t"
      "v_exp_f32 v28, v28\n\t"
      "v_exp_f32 v29, v29\n\t"
      "v_exp_f32 v30, v30\n\t"
      "v_exp_f32 v31, v31\n\t"
      "v_pk_mul_f32 v[32:33], v[32:33], v[48:49]\n\t"
      "v_pk_mul_f32 v[34:35], v[34:35], v[50:51]\n\t"
      "v_pk_mul_f32 v[36:37], v[36:37], v[52:53]\n\t"
      "v_pk_mul_f32 v[38:39], v[38:39], v[54:55]\n\t"
      "v_pk_add_f32 v[24:25], v[24:25], 1.0 op_sel_hi:[1,0]\n\t"
      "v_pk_add_f32 v[26:27], v[26:27], 1.0 op_sel_hi:[1,0]\n\t"
      "v_pk_add_f32 v[28:29], v[28:29], 1.0 op_sel_hi:[1,0]\n\t"
      "v_pk_add_f32 v[30:31], v[30:31], 1.0 op_sel_hi:[1,0]\n\t"
      "v_rcp_f32 v24, v24\n\t"
      "v_rcp_f32 v25, v25\n\t"
      "v_rcp_f32 v26, v26\n\t"
      "v_rcp_f32 v27, v27\n\t"
      "v_rcp_f32 v28, v28\n\t"
      "v_rcp_f32 v29, v29\n\t"
      "v_rcp_f32 v30, v30\n\t"
      "v_rcp_f32 v31, v31\n\t"
      "s_nop 0\n\t"
      "v_pk_mul_f32 v[32:33], v[32:33], v[24:25]\n\t"
      "v_pk_mul_f32 v[34:35], v[34:35], v[26:27]\n\t"
      "v_pk_mul_f32 v[36:37], v[36:37], v[28:29]\n\t"
      "v_pk_mul_f32 v[38:39], v[38:39], v[30:31]\n\t"
      "v_cvt_pk_bf16_f32 v48, v32, v33\n\t"
      "v_cvt_pk_bf16_f32 v49, v34, v35\n\t"
      "v_cvt_pk_bf16_f32 v50, v36, v37\n\t"
      "v_cvt_pk_bf16_f32 v51, v38, v39\n\t"
      "buffer_store_dwordx4 v[48:51], v16, s[40:43], s49 offen sc1\n\t"
      "ds_read_b128 v[24:27], v20 offset:128\n\t"
      "ds_read_b128 v[28:31], v20 offset:192\n\t"
      "s_waitcnt lgkmcnt(0)\n\t"
      "v_pk_fma_f32 v[40:41], v[24:25], v[0:1], v[40:41] op_sel:[0,1,0] op_sel_hi:[1,1,1] neg_lo:[1,0,0] neg_hi:[1,0,0]\n\t"
      "v_pk_fma_f32 v[42:43], v[26:27], v[0:1], v[42:43] op_sel:[0,1,0] op_sel_hi:[1,1,1] neg_lo:[1,0,0] neg_hi:[1,0,0]\n\t"
      "v_pk_fma_f32 v[44:45], v[28:29], v[0:1], v[44:45] op_sel:[0,1,0] op_sel_hi:[1,1,1] neg_lo:[1,0,0] neg_hi:[1,0,0]\n\t"
      "v_pk_fma_f32 v[46:47], v[30:31], v[0:1], v[46:47] op_sel:[0,1,0] op_sel_hi:[1,1,1] neg_lo:[1,0,0] neg_hi:[1,0,0]\n\t"
      "ds_read_b128 v[24:27], v20 offset:384\n\t"
      "ds_read_b128 v[28:31], v20 offset:448\n\t"
      "s_waitcnt lgkmcnt(0)\n\t"
      "v_pk_fma_f32 v[56:57], v[24:25], v[0:1], v[56:57] op_sel:[0,1,0] op_sel_hi:[1,1,1] neg_lo:[1,0,0] neg_hi:[1,0,0]\n\t"
      "v_pk_fma_f32 v[58:59], v[26:27], v[0:1], v[58:59] op_sel:[0,1,0] op_sel_hi:[1,1,1] neg_lo:[1,0,0] neg_hi:[1,0,0]\n\t"
      "v_pk_fma_f32 v[60:61], v[28:29], v[0:1], v[60:61] op_sel:[0,1,0] op_sel_hi:[1,1,1] neg_lo:[1,0,0] neg_hi:[1,0,0]\n\t"
      "v_pk_fma_f32 v[62:63], v[30:31], v[0:1], v[62:63] op_sel:[0,1,0] op_sel_hi:[1,1,1] neg_lo:[1,0,0] neg_hi:[1,0,0]\n\t"
      "v_pk_mul_f32 v[40:41], v[40:41], v[0:1] op_sel:[0,0] op_sel_hi:[1,0]\n\t"
      "v_pk_mul_f32 v[42:43], v[42:43], v[0:1] op_sel:[0,0] op_sel_hi:[1,0]\n\t"
      "v_pk_mul_f32 v[44:45], v[44:45], v[0:1] op_sel:[0,0] op_sel_hi:[1,0]\n\t"
      "v_pk_mul_f32 v[46:47], v[46:47], v[0:1] op_sel:[0,0] op_sel_hi:[1,0]\n\t"
      "v_pk_mul_f32 v[56:57], v[56:57], v[0:1] op_sel:[0,0] op_sel_hi:[1,0]\n\t"
      "v_pk_mul_f32 v[58:59], v[58:59], v[0:1] op_sel:[0,0] op_sel_hi:[1,0]\n\t"
      "v_pk_mul_f32 v[60:61], v[60:61], v[0:1] op_sel:[0,0] op_sel_hi:[1,0]\n\t"
      "v_pk_mul_f32 v[62:63], v[62:63], v[0:1] op_sel:[0,0] op_sel_hi:[1,0]\n\t"
      "v_pk_mul_f32 v[24:25], v[40:41], s[52:53]\n\t"
      "v_pk_mul_f32 v[26:27], v[42:43], s[52:53]\n\t"
      "v_pk_mul_f32 v[28:29], v[44:45], s[52:53]\n\t"
      "v_pk_mul_f32 v[30:31], v[46:47], s[52:53]\n\t"
      "v_exp_f32 v24, v24\n\t"
      "v_exp_f32 v25, v25\n\t"
      "v_exp_f32 v26, v26\n\t"
      "v_exp_f32 v27, v27\n\t"
      "v_exp_f32 v28, v28\n\t"
      "v_exp_f32 v29, v29\n\t"
      "v_exp_f32 v30, v30\n\t"
      "v_exp_f32 v31, v31\n\t"
      "v_pk_mul_f32 v[40:41], v[40:41], v[56:57]\n\t"
      "v_pk_mul_f32 v[42:43], v[42:43], v[58:59]\n\t"
      "v_pk_mul_f32 v[44:45], v[44:45], v[60:61]\n\t"
      "v_pk_mul_f32 v[46:47], v[46:47], v[62:63]\n\t"
      "v_pk_add_f32 v[24:25], v[24:25], 1.0 op_sel_hi:[1,0]\n\t"
      "v_pk_add_f32 v[26:27], v[26:27], 1.0 op_sel_hi:[1,0]\n\t"
      "v_pk_add_f32 v[28:29], v[28:29], 1.0 op_sel_hi:[1,0]\n\t"
      "v_pk_add_f32 v[30:31], v[30:31], 1.0 op_sel_hi:[1,0]\n\t"
      "v_rcp_f32 v24, v24\n\t"
      "v_rcp_f32 v25, v25\n\t"
      "v_rcp_f32 v26, v26\n\t"
      "v_rcp_f32 v27, v27\n\t"
      "v_rcp_f32 v28, v28\n\t"
      "v_rcp_f32 v29, v29\n\t"
      "v_rcp_f32 v30, v30\n\t"
      "v_rcp_f32 v31, v31\n\t"
      "s_nop 0\n\t"
      "v_pk_mul_f32 v[40:41], v[40:41], v[24:25]\n\t"
      "v_pk_mul_f32 v[42:43], v[42:43], v[26:27]\n\t"
      "v_pk_mul_f32 v[44:45], v[44:45], v[28:29]\n\t"
      "v_pk_mul_f32 v[46:47], v[46:47], v[30:31]\n\t"
      "v_cvt_pk_bf16_f32 v56, v40, v41\n\t"
      "v_cvt_pk_bf16_f32 v57, v42, v43\n\t"
      "v_cvt_pk_bf16_f32 v58, v44, v45\n\t"
      "v_cvt_pk_bf16_f32 v59, v46, v47\n\t"
      "buffer_store_dwordx4 v[56:59], v16, s[40:43], s50 offen sc1\n\t"
      "s_nop 1\n\t"
      "ds_read_b128 v[32:35], v20 offset:0\n\t"
      "ds_read_b128 v[36:39], v20 offset:64\n\t"
      "ds_read_b128 v[40:43], v20 offset:128\n\t"
      "ds_read_b128 v[44:47], v20 offset:192\n\t"
      "ds_read_b128 v[48:51], v20 offset:256\n\t"
      "ds_read_b128 v[52:55], v20 offset:320\n\t"
      "ds_read_b128 v[56:59], v20 offset:384\n\t"
      "ds_read_b128 v[60:63], v20 offset:448\n\t"
      "s_waitcnt lgkmcnt(0)\n\t"
      "v_pk_fma_f32 v[64:65], v[32:33], v[2:3], v[64:65] op_sel:[0,1,0] op_sel_hi:[1,1,1] neg_lo:[1,0,0] neg_hi:[1,0,0]\n\t"
      "v_pk_fma_f32 v[66:67], v[34:35], v[2:3], v[66:67] op_sel:[0,1,0] op_sel_hi:[1,1,1] neg_lo:[1,0,0] neg_hi:[1,0,0]\n\t"
      "v_pk_fma_f32 v[68:69], v[36:37], v[2:3], v[68:69] op_sel:[0,1,0] op_sel_hi:[1,1,1] neg_lo:[1,0,0] neg_hi:[1,0,0]\n\t"
      "v_pk_fma_f32 v[70:71], v[38:39], v[2:3], v[70:71] op_sel:[0,1,0] op_sel_hi:[1,1,1] neg_lo:[1,0,0] neg_hi:[1,0,0]\n\t"
      "v_pk_fma_f32 v[80:81], v[48:49], v[2:3], v[80:81] op_sel:[0,1,0] op_sel_hi:[1,1,1] neg_lo:[1,0,0] neg_hi:[1,0,0]\n\t"
      "v_pk_fma_f32 v[82:83], v[50:51], v[2:3], v[82:83] op_sel:[0,1,0] op_sel_hi:[1,1,1] neg_lo:[1,0,0] neg_hi:[1,0,0]\n\t"
      "v_pk_fma_f32 v[84:85], v[52:53], v[2:3], v[84:85] op_sel:[0,1,0] op_sel_hi:[1,1,1] neg_lo:[1,0,0] neg_hi:[1,0,0]\n\t"
      "v_pk_fma_f32 v[86:87], v[54:55], v[2:3], v[86:87] op_sel:[0,1,0] op_sel_hi:[1,1,1] neg_lo:[1,0,0] neg_hi:[1,0,0]\n\t"
      "v_pk_mul_f32 v[64:65], v[64:65], v[2:3] op_sel:[0,0] op_sel_hi:[1,0]\n\t"
      "v_pk_mul_f32 v[66:67], v[66:67], v[2:3] op_sel:[0,0] op_sel_hi:[1,0]\n\t"
      "v_pk_mul_f32 v[68:69], v[68:69], v[2:3] op_sel:[0,0] op_sel_hi:[1,0]\n\t"
      "v_pk_mul_f32 v[70:71], v[70:71], v[2:3] op_sel:[0,0] op_sel_hi:[1,0]\n\t"
      "v_pk_mul_f32 v[80:81], v[80:81], v[2:3] op_sel:[0,0] op_sel_hi:[1,0]\n\t"
      "v_pk_mul_f32 v[82:83], v[82:83], v[2:3] op_sel:[0,0] op_sel_hi:[1,0]\n\t"
      "v_pk_mul_f32 v[84:85], v[84:85], v[2:3] op_sel:[0,0] op_sel_hi:[1,0]\n\t"
      "v_pk_mul_f32 v[86:87], v[86:87], v[2:3] op_sel:[0,0] op_sel_hi:[1,0]\n\t"
      "v_pk_mul_f32 v[24:25], v[64:65], s[52:53]\n\t"
      "v_pk_mul_f32 v[26:27], v[66:67], s[52:53]\n\t"
      "v_pk_mul_f32 v[28:29], v[68:69], s[52:53]\n\t"
      "v_pk_mul_f32 v[30:31], v[70:71], s[52:53]\n\t"
      "v_exp_f32 v24, v24\n\t"
      "v_exp_f32 v25, v25\n\t"
      "v_exp_f32 v26, v26\n\t"
      "v_exp_f32 v27, v27\n\t"
      "v_exp_f32 v28, v28\n\t"
      "v_exp_f32 v29, v29\n\t"
      "v_exp_f32 v30, v30\n\t"
      "v_exp_f32 v31, v31\n\t"
      "v_pk_mul_f32 v[64:65], v[64:65], v[80:81]\n\t"
      "v_pk_mul_f32 v[66:67], v[66:67], v[82:83]\n\t"
      "v_pk_mul_f32 v[68:69], v[68:69], v[84:85]\n\t"
      "v_pk_mul_f32 v[70:71], v[70:71], v[86:87]\n\t"
      "v_pk_add_f32 v[24:25], v[24:25], 1.0 op_sel_hi:[1,0]\n\t"
      "v_pk_add_f32 v[26:27], v[26:27], 1.0 op_sel_hi:[1,0]\n\t"
      "v_pk_add_f32 v[28:29], v[28:29], 1.0 op_sel_hi:[1,0]\n\t"
      "v_pk_add_f32 v[30:31], v[30:31], 1.0 op_sel_hi:[1,0]\n\t"
      "v_rcp_f32 v24, v24\n\t"
      "v_rcp_f32 v25, v25\n\t"
      "v_rcp_f32 v26, v26\n\t"
      "v_rcp_f32 v27, v27\n\t"
      "v_rcp_f32 v28, v28\n\t"
      "v_rcp_f32 v29, v29\n\t"
      "v_rcp_f32 v30, v30\n\t"
      "v_rcp_f32 v31, v31\n\t"
      "s_nop 0\n\t"
      "v_pk_mul_f32 v[64:65], v[64:65], v[24:25]\n\t"
      "v_pk_mul_f32 v[66:67], v[66:67], v[26:27]\n\t"
      "v_pk_mul_f32 v[68:69], v[68:69], v[28:29]\n\t"
      "v_pk_mul_f32 v[70:71], v[70:71], v[30:31]\n\t"
      "v_cvt_pk_bf16_f32 v80, v64, v65\n\t"
      "v_cvt_pk_bf16_f32 v81, v66, v67\n\t"
      "v_cvt_pk_bf16_f32 v82, v68, v69\n\t"
      "v_cvt_pk_bf16_f32 v83, v70, v71\n\t"
      "buffer_store_dwordx4 v[80:83], v16, s[40:43], s49 offen offset:1024 sc1\n\t"
      "v_pk_fma_f32 v[72:73], v[40:41], v[2:3], v[72:73] op_sel:[0,1,0] op_sel_hi:[1,1,1] neg_lo:[1,0,0] neg_hi:[1,0,0]\n\t"
      "v_pk_fma_f32 v[74:75], v[42:43], v[2:3], v[74:75] op_sel:[0,1,0] op_sel_hi:[1,1,1] neg_lo:[1,0,0] neg_hi:[1,0,0]\n\t"
      "v_pk_fma_f32 v[76:77], v[44:45], v[2:3], v[76:77] op_sel:[0,1,0] op_sel_hi:[1,1,1] neg_lo:[1,0,0] neg_hi:[1,0,0]\n\t"
      "v_pk_fma_f32 v[78:79], v[46:47], v[2:3], v[78:79] op_sel:[0,1,0] op_sel_hi:[1,1,1] neg_lo:[1,0,0] neg_hi:[1,0,0]\n\t"
      "v_pk_fma_f32 v[88:89], v[56:57], v[2:3], v[88:89] op_sel:[0,1,0] op_sel_hi:[1,1,1] neg_lo:[1,0,0] neg_hi:[1,0,0]\n\t"
      "v_pk_fma_f32 v[90:91], v[58:59], v[2:3], v[90:91] op_sel:[0,1,0] op_sel_hi:[1,1,1] neg_lo:[1,0,0] neg_hi:[1,0,0]\n\t"
      "v_pk_fma_f32 v[92:93], v[60:61], v[2:3], v[92:93] op_sel:[0,1,0] op_sel_hi:[1,1,1] neg_lo:[1,0,0] neg_hi:[1,0,0]\n\t"
      "v_pk_fma_f32 v[94:95], v[62:63], v[2:3], v[94:95] op_sel:[0,1,0] op_sel_hi:[1,1,1] neg_lo:[1,0,0] neg_hi:[1,0,0]\n\t"
      "v_pk_mul_f32 v[72:73], v[72:73], v[2:3] op_sel:[0,0] op_sel_hi:[1,0]\n\t"
      "v_pk_mul_f32 v[74:75], v[74:75], v[2:3] op_sel:[0,0] op_sel_hi:[1,0]\n\t"
      "v_pk_mul_f32 v[76:77], v[76:77], v[2:3] op_sel:[0,0] op_sel_hi:[1,0]\n\t"
      "v_pk_mul_f32 v[78:79], v[78:79], v[2:3] op_sel:[0,0] op_sel_hi:[1,0]\n\t"
      "v_pk_mul_f32 v[88:89], v[88:89], v[2:3] op_sel:[0,0] op_sel_hi:[1,0]\n\t"
      "v_pk_mul_f32 v[90:91], v[90:91], v[2:3] op_sel:[0,0] op_sel_hi:[1,0]\n\t"
      "v_pk_mul_f32 v[92:93], v[92:93], v[2:3] op_sel:[0,0] op_sel_hi:[1,0]\n\t"
      "v_pk_mul_f32 v[94:95], v[94:95], v[2:3] op_sel:[0,0] op_sel_hi:[1,0]\n\t"
      "v_pk_mul_f32 v[24:25], v[72:73], s[52:53]\n\t"
      "v_pk_mul_f32 v[26:27], v[74:75], s[52:53]\n\t"
      "v_pk_mul_f32 v[28:29], v[76:77], s[52:53]\n\t"
      "v_pk_mul_f32 v[30:31], v[78:79], s[52:53]\n\t"
      "v_exp_f32 v24, v24\n\t"
      "v_exp_f32 v25, v25\n\t"
      "v_exp_f32 v26, v26\n\t"
      "v_exp_f32 v27, v27\n\t"
      "v_exp_f32 v28, v28\n\t"
      "v_exp_f32 v29, v29\n\t"
      "v_exp_f32 v30, v30\n\t"
      "v_exp_f32 v31, v31\n\t"
      "v_pk_mul_f32 v[72:73], v[72:73], v[88:89]\n\t"
      "v_pk_mul_f32 v[74:75], v[74:75], v[90:91]\n\t"
      "v_pk_mul_f32 v[76:77], v[76:77], v[92:93]\n\t"
      "v_pk_mul_f32 v[78:79], v[78:79], v[94:95]\n\t"
      "v_pk_add_f32 v[24:25], v[24:25], 1.0 op_sel_hi:[1,0]\n\t"
      "v_pk_add_f32 v[26:27], v[26:27], 1.0 op_sel_hi:[1,0]\n\t"
      "v_pk_add_f32 v[28:29], v[28:29], 1.0 op_sel_hi:[1,0]\n\t"
      "v_pk_add_f32 v[30:31], v[30:31], 1.0 op_sel_hi:[1,0]\n\t"
      "v_rcp_f32 v24, v24\n\t"
      "v_rcp_f32 v25, v25\n\t"
      "v_rcp_f32 v26, v26\n\t"
      "v_rcp_f32 v27, v27\n\t"
      "v_rcp_f32 v28, v28\n\t"
      "v_rcp_f32 v29, v29\n\t"
      "v_rcp_f32 v30, v30\n\t"
      "v_rcp_f32 v31, v31\n\t"
      "s_nop 0\n\t"
      "v_pk_mul_f32 v[72:73], v[72:73], v[24:25]\n\t"
      "v_pk_mul_f32 v[74:75], v[74:75], v[26:27]\n\t"
      "v_pk_mul_f32 v[76:77], v[76:77], v[28:29]\n\t"
      "v_pk_mul_f32 v[78:79], v[78:79], v[30:31]\n\t"
      "v_cvt_pk_bf16_f32 v88, v72, v73\n\t"
      "v_cvt_pk_bf16_f32 v89, v74, v75\n\t"
      "v_cvt_pk_bf16_f32 v90, v76, v77\n\t"
      "v_cvt_pk_bf16_f32 v91, v78, v79\n\t"
      "buffer_store_dwordx4 v[88:91], v16, s[40:43], s50 offen offset:1024 sc1\n\t"
      "v_pk_fma_f32 v[96:97], v[32:33], v[4:5], v[96:97] op_sel:[0,1,0] op_sel_hi:[1,1,1] neg_lo:[1,0,0] neg_hi:[1,0,0]\n\t"
      "v_pk_fma_f32 v[98:99], v[34:35], v[4:5], v[98:99] op_sel:[0,1,0] op_sel_hi:[1,1,1] neg_lo:[1,0,0] neg_hi:[1,0,0]\n\t"
      "v_pk_fma_f32 v[100:101], v[36:37], v[4:5], v[100:101] op_sel:[0,1,0] op_sel_hi:[1,1,1] neg_lo:[1,0,0] neg_hi:[1,0,0]\n\t"
      "v_pk_fma_f32 v[102:103], v[38:39], v[4:5], v[102:103] op_sel:[0,1,0] op_sel_hi:[1,1,1] neg_lo:[1,0,0] neg_hi:[1,0,0]\n\t"
      "v_pk_fma_f32 v[112:113], v[48:49], v[4:5], v[112:113] op_sel:[0,1,0] op_sel_hi:[1,1,1] neg_lo:[1,0,0] neg_hi:[1,0,0]\n\t"
      "v_pk_fma_f32 v[114:115], v[50:51], v[4:5], v[114:115] op_sel:[0,1,0] op_sel_hi:[1,1,1] neg_lo:[1,0,0] neg_hi:[1,0,0]\n\t"
      "v_pk_fma_f32 v[116:117], v[52:53], v[4:5], v[116:117] op_sel:[0,1,0] op_sel_hi:[1,1,1] neg_lo:[1,0,0] neg_hi:[1,0,0]\n\t"
      "v_pk_fma_f32 v[118:119], v[54:55], v[4:5], v[118:119] op_sel:[0,1,0] op_sel_hi:[1,1,1] neg_lo:[1,0,0] neg_hi:[1,0,0]\n\t"
      "v_pk_mul_f32 v[96:97], v[96:97], v[4:5] op_sel:[0,0] op_sel_hi:[1,0]\n\t"
      "v_pk_mul_f32 v[98:99], v[98:99], v[4:5] op_sel:[0,0] op_sel_hi:[1,0]\n\t"
      "v_pk_mul_f32 v[100:101], v[100:101], v[4:5] op_sel:[0,0] op_sel_hi:[1,0]\n\t"
      "v_pk_mul_f32 v[102:103], v[102:103], v[4:5] op_sel:[0,0] op_sel_hi:[1,0]\n\t"
      "v_pk_mul_f32 v[112:113], v[112:113], v[4:5] op_sel:[0,0] op_sel_hi:[1,0]\n\t"
      "v_pk_mul_f32 v[114:115], v[114:115], v[4:5] op_sel:[0,0] op_sel_hi:[1,0]\n\t"
      "v_pk_mul_f32 v[116:117], v[116:117], v[4:5] op_sel:[0,0] op_sel_hi:[1,0]\n\t"
      "v_pk_mul_f32 v[118:119], v[118:119], v[4:5] op_sel:[0,0] op_sel_hi:[1,0]\n\t"
      "v_pk_mul_f32 v[24:25], v[96:97], s[52:53]\n\t"
      "v_pk_mul_f32 v[26:27], v[98:99], s[52:53]\n\t"
      "v_pk_mul_f32 v[28:29], v[100:101], s[52:53]\n\t"
      "v_pk_mul_f32 v[30:31], v[102:103], s[52:53]\n\t"
      "v_exp_f32 v24, v24\n\t"
      "v_exp_f32 v25, v25\n\t"
      "v_exp_f32 v26, v26\n\t"
      "v_exp_f32 v27, v27\n\t"
      "v_exp_f32 v28, v28\n\t"
      "v_exp_f32 v29, v29\n\t"
      "v_exp_f32 v30, v30\n\t"
      "v_exp_f32 v31, v31\n\t"
      "v_pk_mul_f32 v[96:97], v[96:97], v[112:113]\n\t"
      "v_pk_mul_f32 v[98:99], v[98:99], v[114:115]\n\t"
      "v_pk_mul_f32 v[100:101], v[100:101], v[116:117]\n\t"
      "v_pk_mul_f32 v[102:103], v[102:103], v[118:119]\n\t"
      "v_pk_add_f32 v[24:25], v[24:25], 1.0 op_sel_hi:[1,0]\n\t"
      "v_pk_add_f32 v[26:27], v[26:27], 1.0 op_sel_hi:[1,0]\n\t"
      "v_pk_add_f32 v[28:29], v[28:29], 1.0 op_sel_hi:[1,0]\n\t"
      "v_pk_add_f32 v[30:31], v[30:31], 1.0 op_sel_hi:[1,0]\n\t"
      "v_rcp_f32 v24, v24\n\t"
      "v_rcp_f32 v25, v25\n\t"
      "v_rcp_f32 v26, v26\n\t"
      "v_rcp_f32 v27, v27\n\t"
      "v_rcp_f32 v28, v28\n\t"
      "v_rcp_f32 v29, v29\n\t"
      "v_rcp_f32 v30, v30\n\t"
      "v_rcp_f32 v31, v31\n\t"
      "s_nop 0\n\t"
      "v_pk_mul_f32 v[96:97], v[96:97], v[24:25]\n\t"
      "v_pk_mul_f32 v[98:99], v[98:99], v[26:27]\n\t"
      "v_pk_mul_f32 v[100:101], v[100:101], v[28:29]\n\t"
      "v_pk_mul_f32 v[102:103], v[102:103], v[30:31]\n\t"
      "v_cvt_pk_bf16_f32 v112, v96, v97\n\t"
      "v_cvt_pk_bf16_f32 v113, v98, v99\n\t"
      "v_cvt_pk_bf16_f32 v114, v100, v101\n\t"
      "v_cvt_pk_bf16_f32 v115, v102, v103\n\t"
      "buffer_store_dwordx4 v[112:115], v16, s[40:43], s49 offen offset:2048 sc1\n\t"
      "v_pk_fma_f32 v[104:105], v[40:41], v[4:5], v[104:105] op_sel:[0,1,0] op_sel_hi:[1,1,1] neg_lo:[1,0,0] neg_hi:[1,0,0]\n\t"
      "v_pk_fma_f32 v[106:107], v[42:43], v[4:5], v[106:107] op_sel:[0,1,0] op_sel_hi:[1,1,1] neg_lo:[1,0,0] neg_hi:[1,0,0]\n\t"
      "v_pk_fma_f32 v[108:109], v[44:45], v[4:5], v[108:109] op_sel:[0,1,0] op_sel_hi:[1,1,1] neg_lo:[1,0,0] neg_hi:[1,0,0]\n\t"
      "v_pk_fma_f32 v[110:111], v[46:47], v[4:5], v[110:111] op_sel:[0,1,0] op_sel_hi:[1,1,1] neg_lo:[1,0,0] neg_hi:[1,0,0]\n\t"
      "v_pk_fma_f32 v[120:121], v[56:57], v[4:5], v[120:121] op_sel:[0,1,0] op_sel_hi:[1,1,1] neg_lo:[1,0,0] neg_hi:[1,0,0]\n\t"
      "v_pk_fma_f32 v[122:123], v[58:59], v[4:5], v[122:123] op_sel:[0,1,0] op_sel_hi:[1,1,1] neg_lo:[1,0,0] neg_hi:[1,0,0]\n\t"
      "v_pk_fma_f32 v[124:125], v[60:61], v[4:5], v[124:125] op_sel:[0,1,0] op_sel_hi:[1,1,1] neg_lo:[1,0,0] neg_hi:[1,0,0]\n\t"
      "v_pk_fma_f32 v[126:127], v[62:63], v[4:5], v[126:127] op_sel:[0,1,0] op_sel_hi:[1,1,1] neg_lo:[1,0,0] neg_hi:[1,0,0]\n\t"
      "v_pk_mul_f32 v[104:105], v[104:105], v[4:5] op_sel:[0,0] op_sel_hi:[1,0]\n\t"
      "v_pk_mul_f32 v[106:107], v[106:107], v[4:5] op_sel:[0,0] op_sel_hi:[1,0]\n\t"
      "v_pk_mul_f32 v[108:109], v[108:109], v[4:5] op_sel:[0,0] op_sel_hi:[1,0]\n\t"
      "v_pk_mul_f32 v[110:111], v[110:111], v[4:5] op_sel:[0,0] op_sel_hi:[1,0]\n\t"
      "v_pk_mul_f32 v[120:121], v[120:121], v[4:5] op_sel:[0,0] op_sel_hi:[1,0]\n\t"
      "v_pk_mul_f32 v[122:123], v[122:123], v[4:5] op_sel:[0,0] op_sel_hi:[1,0]\n\t"
      "v_pk_mul_f32 v[124:125], v[124:125], v[4:5] op_sel:[0,0] op_sel_hi:[1,0]\n\t"
      "v_pk_mul_f32 v[126:127], v[126:127], v[4:5] op_sel:[0,0] op_sel_hi:[1,0]\n\t"
      "v_pk_mul_f32 v[24:25], v[104:105], s[52:53]\n\t"
      "v_pk_mul_f32 v[26:27], v[106:107], s[52:53]\n\t"
      "v_pk_mul_f32 v[28:29], v[108:109], s[52:53]\n\t"
      "v_pk_mul_f32 v[30:31], v[110:111], s[52:53]\n\t"
      "v_exp_f32 v24, v24\n\t"
      "v_exp_f32 v25, v25\n\t"
      "v_exp_f32 v26, v26\n\t"
      "v_exp_f32 v27, v27\n\t"
      "v_exp_f32 v28, v28\n\t"
      "v_exp_f32 v29, v29\n\t"
      "v_exp_f32 v30, v30\n\t"
      "v_exp_f32 v31, v31\n\t"
      "v_pk_mul_f32 v[104:105], v[104:105], v[120:121]\n\t"
      "v_pk_mul_f32 v[106:107], v[106:107], v[122:123]\n\t"
      "v_pk_mul_f32 v[108:109], v[108:109], v[124:125]\n\t"
      "v_pk_mul_f32 v[110:111], v[110:111], v[126:127]\n\t"
      "v_pk_add_f32 v[24:25], v[24:25], 1.0 op_sel_hi:[1,0]\n\t"
      "v_pk_add_f32 v[26:27], v[26:27], 1.0 op_sel_hi:[1,0]\n\t"
      "v_pk_add_f32 v[28:29], v[28:29], 1.0 op_sel_hi:[1,0]\n\t"
      "v_pk_add_f32 v[30:31], v[30:31], 1.0 op_sel_hi:[1,0]\n\t"
      "v_rcp_f32 v24, v24\n\t"
      "v_rcp_f32 v25, v25\n\t"
      "v_rcp_f32 v26, v26\n\t"
      "v_rcp_f32 v27, v27\n\t"
      "v_rcp_f32 v28, v28\n\t"
      "v_rcp_f32 v29, v29\n\t"
      "v_rcp_f32 v30, v30\n\t"
      "v_rcp_f32 v31, v31\n\t"
      "s_nop 0\n\t"
      "v_pk_mul_f32 v[104:105], v[104:105], v[24:25]\n\t"
      "v_pk_mul_f32 v[106:107], v[106:107], v[26:27]\n\t"
      "v_pk_mul_f32 v[108:109], v[108:109], v[28:29]\n\t"
      "v_pk_mul_f32 v[110:111], v[110:111], v[30:31]\n\t"
      "v_cvt_pk_bf16_f32 v120, v104, v105\n\t"
      "v_cvt_pk_bf16_f32 v121, v106, v107\n\t"
      "v_cvt_pk_bf16_f32 v122, v108, v109\n\t"
      "v_cvt_pk_bf16_f32 v123, v110, v111\n\t"
      "buffer_store_dwordx4 v[120:123], v16, s[40:43], s50 offen offset:2048 sc1\n\t"
      "s_waitcnt vmcnt(6)\n\t"
      "v_mov_b32 v29, 0\n\t"
      "v_mov_b32 v30, s54\n\t"
      "v_mov_b32 v31, s55\n\t"
      "s_mov_b64 exec, 1\n\t"
      "buffer_store_dwordx2 v[30:31], v29, s[44:47], s58 offen sc1\n\t"
      "s_mov_b64 exec, -1\n\t"
      "s_nop 1\n\t"
      "v_pk_fma_f32 v[128:129], v[32:33], v[6:7], v[128:129] op_sel:[0,1,0] op_sel_hi:[1,1,1] neg_lo:[1,0,0] neg_hi:[1,0,0]\n\t"
      "v_pk_fma_f32 v[130:131], v[34:35], v[6:7], v[130:131] op_sel:[0,1,0] op_sel_hi:[1,1,1] neg_lo:[1,0,0] neg_hi:[1,0,0]\n\t"
      "v_pk_fma_f32 v[132:133], v[36:37], v[6:7], v[132:133] op_sel:[0,1,0] op_sel_hi:[1,1,1] neg_lo:[1,0,0] neg_hi:[1,0,0]\n\t"
      "v_pk_fma_f32 v[134:135], v[38:39], v[6:7], v[134:135] op_sel:[0,1,0] op_sel_hi:[1,1,1] neg_lo:[1,0,0] neg_hi:[1,0,0]\n\t"
      "v_pk_fma_f32 v[144:145], v[48:49], v[6:7], v[144:145] op_sel:[0,1,0] op_sel_hi:[1,1,1] neg_lo:[1,0,0] neg_hi:[1,0,0]\n\t"
      "v_pk_fma_f32 v[146:147], v[50:51], v[6:7], v[146:147] op_sel:[0,1,0] op_sel_hi:[1,1,1] neg_lo:[1,0,0] neg_hi:[1,0,0]\n\t"
      "v_pk_fma_f32 v[148:149], v[52:53], v[6:7], v[148:149] op_sel:[0,1,0] op_sel_hi:[1,1,1] neg_lo:[1,0,0] neg_hi:[1,0,0]\n\t"
      "v_pk_fma_f32 v[150:151], v[54:55], v[6:7], v[150:151] op_sel:[0,1,0] op_sel_hi:[1,1,1] neg_lo:[1,0,0] neg_hi:[1,0,0]\n\t"
      "v_pk_mul_f32 v[128:129], v[128:129], v[6:7] op_sel:[0,0] op_sel_hi:[1,0]\n\t"
      "v_pk_mul_f32 v[130:131], v[130:131], v[6:7] op_sel:[0,0] op_sel_hi:[1,0]\n\t"
      "v_pk_mul_f32 v[132:133], v[132:133], v[6:7] op_sel:[0,0] op_sel_hi:[1,0]\n\t"
      "v_pk_mul_f32 v[134:135], v[134:135], v[6:7] op_sel:[0,0] op_sel_hi:[1,0]\n\t"
      "v_pk_mul_f32 v[144:145], v[144:145], v[6:7] op_sel:[0,0] op_sel_hi:[1,0]\n\t"
      "v_pk_mul_f32 v[146:147], v[146:147], v[6:7] op_sel:[0,0] op_sel_hi:[1,0]\n\t"
      "v_pk_mul_f32 v[148:149], v[148:149], v[6:7] op_sel:[0,0] op_sel_hi:[1,0]\n\t"
      "v_pk_mul_f32 v[150:151], v[150:151], v[6:7] op_sel:[0,0] op_sel_hi:[1,0]\n\t"
      "v_pk_mul_f32 v[24:25], v[128:129], s[52:53]\n\t"
      "v_pk_mul_f32 v[26:27], v[130:131], s[52:53]\n\t"
      "v_pk_mul_f32 v[28:29], v[132:133], s[52:53]\n\t"
      "v_pk_mul_f32 v[30:31], v[134:135], s[52:53]\n\t"
      "v_exp_f32 v24, v24\n\t"
      "v_exp_f32 v25, v25\n\t"
      "v_exp_f32 v26, v26\n\t"
      "v_exp_f32 v27, v27\n\t"
      "v_exp_f32 v28, v28\n\t"
      "v_exp_f32 v29, v29\n\t"
      "v_exp_f32 v30, v30\n\t"
      "v_exp_f32 v31, v31\n\t"
      "v_pk_mul_f32 v[128:129], v[128:129], v[144:145]\n\t"
      "v_pk_mul_f32 v[130:131], v[130:131], v[146:147]\n\t"
      "v_pk_mul_f32 v[132:133], v[132:133], v[148:149]\n\t"
      "v_pk_mul_f32 v[134:135], v[134:135], v[150:151]\n\t"
      "v_pk_add_f32 v[24:25], v[24:25], 1.0 op_sel_hi:[1,0]\n\t"
      "v_pk_add_f32 v[26:27], v[26:27], 1.0 op_sel_hi:[1,0]\n\t"
      "v_pk_add_f32 v[28:29], v[28:29], 1.0 op_sel_hi:[1,0]\n\t"
      "v_pk_add_f32 v[30:31], v[30:31], 1.0 op_sel_hi:[1,0]\n\t"
      "v_rcp_f32 v24, v24\n\t"
      "v_rcp_f32 v25, v25\n\t"
      "v_rcp_f32 v26, v26\n\t"
      "v_rcp_f32 v27, v27\n\t"
      "v_rcp_f32 v28, v28\n\t"
      "v_rcp_f32 v29, v29\n\t"
      "v_rcp_f32 v30, v30\n\t"
      "v_rcp_f32 v31, v31\n\t"
      "s_nop 0\n\t"
      "v_pk_mul_f32 v[128:129], v[128:129], v[24:25]\n\t"
      "v_pk_mul_f32 v[130:131], v[130:131], v[26:27]\n\t"
      "v_pk_mul_f32 v[132:133], v[132:133], v[28:29]\n\t"
      "v_pk_mul_f32 v[134:135], v[134:135], v[30:31]\n\t"
      "v_cvt_pk_bf16_f32 v144, v128, v129\n\t"
      "v_cvt_pk_bf16_f32 v145, v130, v131\n\t"
      "v_cvt_pk_bf16_f32 v146, v132, v133\n\t"
      "v_cvt_pk_bf16_f32 v147, v134, v135\n\t"
      "buffer_store_dwordx4 v[144:147], v16, s[40:43], s49 offen offset:3072 sc1\n\t"
      "v_pk_fma_f32 v[136:137], v[40:41], v[6:7], v[136:137] op_sel:[0,1,0] op_sel_hi:[1,1,1] neg_lo:[1,0,0] neg_hi:[1,0,0]\n\t"
      "v_pk_fma_f32 v[138:139], v[42:43], v[6:7], v[138:139] op_sel:[0,1,0] op_sel_hi:[1,1,1] neg_lo:[1,0,0] neg_hi:[1,0,0]\n\t"
      "v_pk_fma_f32 v[140:141], v[44:45], v[6:7], v[140:141] op_sel:[0,1,0] op_sel_hi:[1,1,1] neg_lo:[1,0,0] neg_hi:[1,0,0]\n\t"
      "v_pk_fma_f32 v[142:143], v[46:47], v[6:7], v[142:143] op_sel:[0,1,0] op_sel_hi:[1,1,1] neg_lo:[1,0,0] neg_hi:[1,0,0]\n\t"
      "v_pk_fma_f32 v[152:153], v[56:57], v[6:7], v[152:153] op_sel:[0,1,0] op_sel_hi:[1,1,1] neg_lo:[1,0,0] neg_hi:[1,0,0]\n\t"
      "v_pk_fma_f32 v[154:155], v[58:59], v[6:7], v[154:155] op_sel:[0,1,0] op_sel_hi:[1,1,1] neg_lo:[1,0,0] neg_hi:[1,0,0]\n\t"
      "v_pk_fma_f32 v[156:157], v[60:61], v[6:7], v[156:157] op_sel:[0,1,0] op_sel_hi:[1,1,1] neg_lo:[1,0,0] neg_hi:[1,0,0]\n\t"
      "v_pk_fma_f32 v[158:159], v[62:63], v[6:7], v[158:159] op_sel:[0,1,0] op_sel_hi:[1,1,1] neg_lo:[1,0,0] neg_hi:[1,0,0]\n\t"
      "v_pk_mul_f32 v[136:137], v[136:137], v[6:7] op_sel:[0,0] op_sel_hi:[1,0]\n\t"
      "v_pk_mul_f32 v[138:139], v[138:139], v[6:7] op_sel:[0,0] op_sel_hi:[1,0]\n\t"
      "v_pk_mul_f32 v[140:141], v[140:141], v[6:7] op_sel:[0,0] op_sel_hi:[1,0]\n\t"
      "v_pk_mul_f32 v[142:143], v[142:143], v[6:7] op_sel:[0,0] op_sel_hi:[1,0]\n\t"
      "v_pk_mul_f32 v[152:153], v[152:153], v[6:7] op_sel:[0,0] op_sel_hi:[1,0]\n\t"
      "v_pk_mul_f32 v[154:155], v[154:155], v[6:7] op_sel:[0,0] op_sel_hi:[1,0]\n\t"
      "v_pk_mul_f32 v[156:157], v[156:157], v[6:7] op_sel:[0,0] op_sel_hi:[1,0]\n\t"
      "v_pk_mul_f32 v[158:159], v[158:159], v[6:7] op_sel:[0,0] op_sel_hi:[1,0]\n\t"
      "v_pk_mul_f32 v[24:25], v[136:137], s[52:53]\n\t"
      "v_pk_mul_f32 v[26:27], v[138:139], s[52:53]\n\t"
      "v_pk_mul_f32 v[28:29], v[140:141], s[52:53]\n\t"
      "v_pk_mul_f32 v[30:31], v[142:143], s[52:53]\n\t"
      "v_exp_f32 v24, v24\n\t"
      "v_exp_f32 v25, v25\n\t"
      "v_exp_f32 v26, v26\n\t"
      "v_exp_f32 v27, v27\n\t"
      "v_exp_f32 v28, v28\n\t"
      "v_exp_f32 v29, v29\n\t"
      "v_exp_f32 v30, v30\n\t"
      "v_exp_f32 v31, v31\n\t"
      "v_pk_mul_f32 v[136:137], v[136:137], v[152:153]\n\t"
      "v_pk_mul_f32 v[138:139], v[138:139], v[154:155]\n\t"
      "v_pk_mul_f32 v[140:141], v[140:141], v[156:157]\n\t"
      "v_pk_mul_f32 v[142:143], v[142:143], v[158:159]\n\t"
      "v_pk_add_f32 v[24:25], v[24:25], 1.0 op_sel_hi:[1,0]\n\t"
      "v_pk_add_f32 v[26:27], v[26:27], 1.0 op_sel_hi:[1,0]\n\t"
      "v_pk_add_f32 v[28:29], v[28:29], 1.0 op_sel_hi:[1,0]\n\t"
      "v_pk_add_f32 v[30:31], v[30:31], 1.0 op_sel_hi:[1,0]\n\t"
      "v_rcp_f32 v24, v24\n\t"
      "v_rcp_f32 v25, v25\n\t"
      "v_rcp_f32 v26, v26\n\t"
      "v_rcp_f32 v27, v27\n\t"
      "v_rcp_f32 v28, v28\n\t"
      "v_rcp_f32 v29, v29\n\t"
      "v_rcp_f32 v30, v30\n\t"
      "v_rcp_f32 v31, v31\n\t"
      "s_nop 0\n\t"
      "v_pk_mul_f32 v[136:137], v[136:137], v[24:25]\n\t"
      "v_pk_mul_f32 v[138:139], v[138:139], v[26:27]\n\t"
      "v_pk_mul_f32 v[140:141], v[140:141], v[28:29]\n\t"
      "v_pk_mul_f32 v[142:143], v[142:143], v[30:31]\n\t"
      "v_cvt_pk_bf16_f32 v152, v136, v137\n\t"
      "v_cvt_pk_bf16_f32 v153, v138, v139\n\t"
      "v_cvt_pk_bf16_f32 v154, v140, v141\n\t"
      "v_cvt_pk_bf16_f32 v155, v142, v143\n\t"
      "buffer_store_dwordx4 v[152:155], v16, s[40:43], s50 offen offset:3072 sc1\n\t"
      "v_pk_fma_f32 v[160:161], v[32:33], v[8:9], v[160:161] op_sel:[0,1,0] op_sel_hi:[1,1,1] neg_lo:[1,0,0] neg_hi:[1,0,0]\n\t"
      "v_pk_fma_f32 v[162:163], v[34:35], v[8:9], v[162:163] op_sel:[0,1,0] op_sel_hi:[1,1,1] neg_lo:[1,0,0] neg_hi:[1,0,0]\n\t"
      "v_pk_fma_f32 v[164:165], v[36:37], v[8:9], v[164:165] op_sel:[0,1,0] op_sel_hi:[1,1,1] neg_lo:[1,0,0] neg_hi:[1,0,0]\n\t"
      "v_pk_fma_f32 v[166:167], v[38:39], v[8:9], v[166:167] op_sel:[0,1,0] op_sel_hi:[1,1,1] neg_lo:[1,0,0] neg_hi:[1,0,0]\n\t"
      "v_pk_fma_f32 v[176:177], v[48:49], v[8:9], v[176:177] op_sel:[0,1,0] op_sel_hi:[1,1,1] neg_lo:[1,0,0] neg_hi:[1,0,0]\n\t"
      "v_pk_fma_f32 v[178:179], v[50:51], v[8:9], v[178:179] op_sel:[0,1,0] op_sel_hi:[1,1,1] neg_lo:[1,0,0] neg_hi:[1,0,0]\n\t"
      "v_pk_fma_f32 v[180:181], v[52:53], v[8:9], v[180:181] op_sel:[0,1,0] op_sel_hi:[1,1,1] neg_lo:[1,0,0] neg_hi:[1,0,0]\n\t"
      "v_pk_fma_f32 v[182:183], v[54:55], v[8:9], v[182:183] op_sel:[0,1,0] op_sel_hi:[1,1,1] neg_lo:[1,0,0] neg_hi:[1,0,0]\n\t"
      "v_pk_mul_f32 v[160:161], v[160:161], v[8:9] op_sel:[0,0] op_sel_hi:[1,0]\n\t"
      "v_pk_mul_f32 v[162:163], v[162:163], v[8:9] op_sel:[0,0] op_sel_hi:[1,0]\n\t"
      "v_pk_mul_f32 v[164:165], v[164:165], v[8:9] op_sel:[0,0] op_sel_hi:[1,0]\n\t"
      "v_pk_mul_f32 v[166:167], v[166:167], v[8:9] op_sel:[0,0] op_sel_hi:[1,0]\n\t"
      "v_pk_mul_f32 v[176:177], v[176:177], v[8:9] op_sel:[0,0] op_sel_hi:[1,0]\n\t"
      "v_pk_mul_f32 v[178:179], v[178:179], v[8:9] op_sel:[0,0] op_sel_hi:[1,0]\n\t"
      "v_pk_mul_f32 v[180:181], v[180:181], v[8:9] op_sel:[0,0] op_sel_hi:[1,0]\n\t"
      "v_pk_mul_f32 v[182:183], v[182:183], v[8:9] op_sel:[0,0] op_sel_hi:[1,0]\n\t"
      "v_pk_mul_f32 v[24:25], v[160:161], s[52:53]\n\t"
      "v_pk_mul_f32 v[26:27], v[162:163], s[52:53]\n\t"
      "v_pk_mul_f32 v[28:29], v[164:165], s[52:53]\n\t"
      "v_pk_mul_f32 v[30:31], v[166:167], s[52:53]\n\t"
      "v_exp_f32 v24, v24\n\t"
      "v_exp_f32 v25, v25\n\t"
      "v_exp_f32 v26, v26\n\t"
      "v_exp_f32 v27, v27\n\t"
      "v_exp_f32 v28, v28\n\t"
      "v_exp_f32 v29, v29\n\t"
      "v_exp_f32 v30, v30\n\t"
      "v_exp_f32 v31, v31\n\t"
      "v_pk_mul_f32 v[160:161], v[160:161], v[176:177]\n\t"
      "v_pk_mul_f32 v[162:163], v[162:163], v[178:179]\n\t"
      "v_pk_mul_f32 v[164:165], v[164:165], v[180:181]\n\t"
      "v_pk_mul_f32 v[166:167], v[166:167], v[182:183]\n\t"
      "v_pk_add_f32 v[24:25], v[24:25], 1.0 op_sel_hi:[1,0]\n\t"
      "v_pk_add_f32 v[26:27], v[26:27], 1.0 op_sel_hi:[1,0]\n\t"
      "v_pk_add_f32 v[28:29], v[28:29], 1.0 op_sel_hi:[1,0]\n\t"
      "v_pk_add_f32 v[30:31], v[30:31], 1.0 op_sel_hi:[1,0]\n\t"
      "v_rcp_f32 v24, v24\n\t"
      "v_rcp_f32 v25, v25\n\t"
      "v_rcp_f32 v26, v26\n\t"
      "v_rcp_f32 v27, v27\n\t"
      "v_rcp_f32 v28, v28\n\t"
      "v_rcp_f32 v29, v29\n\t"
      "v_rcp_f32 v30, v30\n\t"
      "v_rcp_f32 v31, v31\n\t"
      "s_nop 0\n\t"
      "v_pk_mul_f32 v[160:161], v[160:161], v[24:25]\n\t"
      "v_pk_mul_f32 v[162:163], v[162:163], v[26:27]\n\t"
      "v_pk_mul_f32 v[164:165], v[164:165], v[28:29]\n\t"
      "v_pk_mul_f32 v[166:167], v[166:167], v[30:31]\n\t"
      "v_cvt_pk_bf16_f32 v176, v160, v161\n\t"
      "v_cvt_pk_bf16_f32 v177, v162, v163\n\t"
      "v_cvt_pk_bf16_f32 v178, v164, v165\n\t"
      "v_cvt_pk_bf16_f32 v179, v166, v167\n\t"
      "buffer_store_dwordx4 v[176:179], v16, s[40:43], s56 offen sc1\n\t"
      "v_pk_fma_f32 v[168:169], v[40:41], v[8:9], v[168:169] op_sel:[0,1,0] op_sel_hi:[1,1,1] neg_lo:[1,0,0] neg_hi:[1,0,0]\n\t"
      "v_pk_fma_f32 v[170:171], v[42:43], v[8:9], v[170:171] op_sel:[0,1,0] op_sel_hi:[1,1,1] neg_lo:[1,0,0] neg_hi:[1,0,0]\n\t"
      "v_pk_fma_f32 v[172:173], v[44:45], v[8:9], v[172:173] op_sel:[0,1,0] op_sel_hi:[1,1,1] neg_lo:[1,0,0] neg_hi:[1,0,0]\n\t"
      "v_pk_fma_f32 v[174:175], v[46:47], v[8:9], v[174:175] op_sel:[0,1,0] op_sel_hi:[1,1,1] neg_lo:[1,0,0] neg_hi:[1,0,0]\n\t"
      "v_pk_fma_f32 v[184:185], v[56:57], v[8:9], v[184:185] op_sel:[0,1,0] op_sel_hi:[1,1,1] neg_lo:[1,0,0] neg_hi:[1,0,0]\n\t"
      "v_pk_fma_f32 v[186:187], v[58:59], v[8:9], v[186:187] op_sel:[0,1,0] op_sel_hi:[1,1,1] neg_lo:[1,0,0] neg_hi:[1,0,0]\n\t"
      "v_pk_fma_f32 v[188:189], v[60:61], v[8:9], v[188:189] op_sel:[0,1,0] op_sel_hi:[1,1,1] neg_lo:[1,0,0] neg_hi:[1,0,0]\n\t"
      "v_pk_fma_f32 v[190:191], v[62:63], v[8:9], v[190:191] op_sel:[0,1,0] op_sel_hi:[1,1,1] neg_lo:[1,0,0] neg_hi:[1,0,0]\n\t"
      "v_pk_mul_f32 v[168:169], v[168:169], v[8:9] op_sel:[0,0] op_sel_hi:[1,0]\n\t"
      "v_pk_mul_f32 v[170:171], v[170:171], v[8:9] op_sel:[0,0] op_sel_hi:[1,0]\n\t"
      "v_pk_mul_f32 v[172:173], v[172:173], v[8:9] op_sel:[0,0] op_sel_hi:[1,0]\n\t"
      "v_pk_mul_f32 v[174:175], v[174:175], v[8:9] op_sel:[0,0] op_sel_hi:[1,0]\n\t"
      "v_pk_mul_f32 v[184:185], v[184:185], v[8:9] op_sel:[0,0] op_sel_hi:[1,0]\n\t"
      "v_pk_mul_f32 v[186:187], v[186:187], v[8:9] op_sel:[0,0] op_sel_hi:[1,0]\n\t"
      "v_pk_mul_f32 v[188:189], v[188:189], v[8:9] op_sel:[0,0] op_sel_hi:[1,0]\n\t"
      "v_pk_mul_f32 v[190:191], v[190:191], v[8:9] op_sel:[0,0] op_sel_hi:[1,0]\n\t"
      "v_pk_mul_f32 v[24:25], v[168:169], s[52:53]\n\t"
      "v_pk_mul_f32 v[26:27], v[170:171], s[52:53]\n\t"
      "v_pk_mul_f32 v[28:29], v[172:173], s[52:53]\n\t"
      "v_pk_mul_f32 v[30:31], v[174:175], s[52:53]\n\t"
      "v_exp_f32 v24, v24\n\t"
      "v_exp_f32 v25, v25\n\t"
      "v_exp_f32 v26, v26\n\t"
      "v_exp_f32 v27, v27\n\t"
      "v_exp_f32 v28, v28\n\t"
      "v_exp_f32 v29, v29\n\t"
      "v_exp_f32 v30, v30\n\t"
      "v_exp_f32 v31, v31\n\t"
      "v_pk_mul_f32 v[168:169], v[168:169], v[184:185]\n\t"
      "v_pk_mul_f32 v[170:171], v[170:171], v[186:187]\n\t"
      "v_pk_mul_f32 v[172:173], v[172:173], v[188:189]\n\t"
      "v_pk_mul_f32 v[174:175], v[174:175], v[190:191]\n\t"
      "v_pk_add_f32 v[24:25], v[24:25], 1.0 op_sel_hi:[1,0]\n\t"
      "v_pk_add_f32 v[26:27], v[26:27], 1.0 op_sel_hi:[1,0]\n\t"
      "v_pk_add_f32 v[28:29], v[28:29], 1.0 op_sel_hi:[1,0]\n\t"
      "v_pk_add_f32 v[30:31], v[30:31], 1.0 op_sel_hi:[1,0]\n\t"
      "v_rcp_f32 v24, v24\n\t"
      "v_rcp_f32 v25, v25\n\t"
      "v_rcp_f32 v26, v26\n\t"
      "v_rcp_f32 v27, v27\n\t"
      "v_rcp_f32 v28, v28\n\t"
      "v_rcp_f32 v29, v29\n\t"
      "v_rcp_f32 v30, v30\n\t"
      "v_rcp_f32 v31, v31\n\t"
      "s_nop 0\n\t"
      "v_pk_mul_f32 v[168:169], v[168:169], v[24:25]\n\t"
      "v_pk_mul_f32 v[170:171], v[170:171], v[26:27]\n\t"
      "v_pk_mul_f32 v[172:173], v[172:173], v[28:29]\n\t"
      "v_pk_mul_f32 v[174:175], v[174:175], v[30:31]\n\t"
      "v_cvt_pk_bf16_f32 v184, v168, v169\n\t"
      "v_cvt_pk_bf16_f32 v185, v170, v171\n\t"
      "v_cvt_pk_bf16_f32 v186, v172, v173\n\t"
      "v_cvt_pk_bf16_f32 v187, v174, v175\n\t"
      "buffer_store_dwordx4 v[184:187], v16, s[40:43], s57 offen sc1\n\t"
      "v_pk_fma_f32 v[192:193], v[32:33], v[10:11], v[192:193] op_sel:[0,1,0] op_sel_hi:[1,1,1] neg_lo:[1,0,0] neg_hi:[1,0,0]\n\t"
      "v_pk_fma_f32 v[194:195], v[34:35], v[10:11], v[194:195] op_sel:[0,1,0] op_sel_hi:[1,1,1] neg_lo:[1,0,0] neg_hi:[1,0,0]\n\t"
      "v_pk_fma_f32 v[196:197], v[36:37], v[10:11], v[196:197] op_sel:[0,1,0] op_sel_hi:[1,1,1] neg_lo:[1,0,0] neg_hi:[1,0,0]\n\t"
      "v_pk_fma_f32 v[198:199], v[38:39], v[10:11], v[198:199] op_sel:[0,1,0] op_sel_hi:[1,1,1] neg_lo:[1,0,0] neg_hi:[1,0,0]\n\t"
      "v_pk_fma_f32 v[208:209], v[48:49], v[10:11], v[208:209] op_sel:[0,1,0] op_sel_hi:[1,1,1] neg_lo:[1,0,0] neg_hi:[1,0,0]\n\t"
      "v_pk_fma_f32 v[210:211], v[50:51], v[10:11], v[210:211] op_sel:[0,1,0] op_sel_hi:[1,1,1] neg_lo:[1,0,0] neg_hi:[1,0,0]\n\t"
      "v_pk_fma_f32 v[212:213], v[52:53], v[10:11], v[212:213] op_sel:[0,1,0] op_sel_hi:[1,1,1] neg_lo:[1,0,0] neg_hi:[1,0,0]\n\t"
      "v_pk_fma_f32 v[214:215], v[54:55], v[10:11], v[214:215] op_sel:[0,1,0] op_sel_hi:[1,1,1] neg_lo:[1,0,0] neg_hi:[1,0,0]\n\t"
      "v_pk_mul_f32 v[192:193], v[192:193], v[10:11] op_sel:[0,0] op_sel_hi:[1,0]\n\t"
      "v_pk_mul_f32 v[194:195], v[194:195], v[10:11] op_sel:[0,0] op_sel_hi:[1,0]\n\t"
      "v_pk_mul_f32 v[196:197], v[196:197], v[10:11] op_sel:[0,0] op_sel_hi:[1,0]\n\t"
      "v_pk_mul_f32 v[198:199], v[198:199], v[10:11] op_sel:[0,0] op_sel_hi:[1,0]\n\t"
      "v_pk_mul_f32 v[208:209], v[208:209], v[10:11] op_sel:[0,0] op_sel_hi:[1,0]\n\t"
      "v_pk_mul_f32 v[210:211], v[210:211], v[10:11] op_sel:[0,0] op_sel_hi:[1,0]\n\t"
      "v_pk_mul_f32 v[212:213], v[212:213], v[10:11] op_sel:[0,0] op_sel_hi:[1,0]\n\t"
      "v_pk_mul_f32 v[214:215], v[214:215], v[10:11] op_sel:[0,0] op_sel_hi:[1,0]\n\t"
      "v_pk_mul_f32 v[24:25], v[192:193], s[52:53]\n\t"
      "v_pk_mul_f32 v[26:27], v[194:195], s[52:53]\n\t"
      "v_pk_mul_f32 v[28:29], v[196:197], s[52:53]\n\t"
      "v_pk_mul_f32 v[30:31], v[198:199], s[52:53]\n\t"
      "v_exp_f32 v24, v24\n\t"
      "v_exp_f32 v25, v25\n\t"
      "v_exp_f32 v26, v26\n\t"
      "v_exp_f32 v27, v27\n\t"
      "v_exp_f32 v28, v28\n\t"
      "v_exp_f32 v29, v29\n\t"
      "v_exp_f32 v30, v30\n\t"
      "v_exp_f32 v31, v31\n\t"
      "v_pk_mul_f32 v[192:193], v[192:193], v[208:209]\n\t"
      "v_pk_mul_f32 v[194:195], v[194:195], v[210:211]\n\t"
      "v_pk_mul_f32 v[196:197], v[196:197], v[212:213]\n\t"
      "v_pk_mul_f32 v[198:199], v[198:199], v[214:215]\n\t"
      "v_pk_add_f32 v[24:25], v[24:25], 1.0 op_sel_hi:[1,0]\n\t"
      "v_pk_add_f32 v[26:27], v[26:27], 1.0 op_sel_hi:[1,0]\n\t"
      "v_pk_add_f32 v[28:29], v[28:29], 1.0 op_sel_hi:[1,0]\n\t"
      "v_pk_add_f32 v[30:31], v[30:31], 1.0 op_sel_hi:[1,0]\n\t"
      "v_rcp_f32 v24, v24\n\t"
      "v_rcp_f32 v25, v25\n\t"
      "v_rcp_f32 v26, v26\n\t"
      "v_rcp_f32 v27, v27\n\t"
      "v_rcp_f32 v28, v28\n\t"
      "v_rcp_f32 v29, v29\n\t"
      "v_rcp_f32 v30, v30\n\t"
      "v_rcp_f32 v31, v31\n\t"
      "s_nop 0\n\t"
      "v_pk_mul_f32 v[192:193], v[192:193], v[24:25]\n\t"
      "v_pk_mul_f32 v[194:195], v[194:195], v[26:27]\n\t"
      "v_pk_mul_f32 v[196:197], v[196:197], v[28:29]\n\t"
      "v_pk_mul_f32 v[198:199], v[198:199], v[30:31]\n\t"
      "v_cvt_pk_bf16_f32 v208, v192, v193\n\t"
      "v_cvt_pk_bf16_f32 v209, v194, v195\n\t"
      "v_cvt_pk_bf16_f32 v210, v196, v197\n\t"
      "v_cvt_pk_bf16_f32 v211, v198, v199\n\t"
      "buffer_store_dwordx4 v[208:211], v16, s[40:43], s56 offen offset:1024 sc1\n\t"
      "v_pk_fma_f32 v[200:201], v[40:41], v[10:11], v[200:201] op_sel:[0,1,0] op_sel_hi:[1,1,1] neg_lo:[1,0,0] neg_hi:[1,0,0]\n\t"
      "v_pk_fma_f32 v[202:203], v[42:43], v[10:11], v[202:203] op_sel:[0,1,0] op_sel_hi:[1,1,1] neg_lo:[1,0,0] neg_hi:[1,0,0]\n\t"
      "v_pk_fma_f32 v[204:205], v[44:45], v[10:11], v[204:205] op_sel:[0,1,0] op_sel_hi:[1,1,1] neg_lo:[1,0,0] neg_hi:[1,0,0]\n\t"
      "v_pk_fma_f32 v[206:207], v[46:47], v[10:11], v[206:207] op_sel:[0,1,0] op_sel_hi:[1,1,1] neg_lo:[1,0,0] neg_hi:[1,0,0]\n\t"
      "v_pk_fma_f32 v[216:217], v[56:57], v[10:11], v[216:217] op_sel:[0,1,0] op_sel_hi:[1,1,1] neg_lo:[1,0,0] neg_hi:[1,0,0]\n\t"
      "v_pk_fma_f32 v[218:219], v[58:59], v[10:11], v[218:219] op_sel:[0,1,0] op_sel_hi:[1,1,1] neg_lo:[1,0,0] neg_hi:[1,0,0]\n\t"
      "v_pk_fma_f32 v[220:221], v[60:61], v[10:11], v[220:221] op_sel:[0,1,0] op_sel_hi:[1,1,1] neg_lo:[1,0,0] neg_hi:[1,0,0]\n\t"
      "v_pk_fma_f32 v[222:223], v[62:63], v[10:11], v[222:223] op_sel:[0,1,0] op_sel_hi:[1,1,1] neg_lo:[1,0,0] neg_hi:[1,0,0]\n\t"
      "v_pk_mul_f32 v[200:201], v[200:201], v[10:11] op_sel:[0,0] op_sel_hi:[1,0]\n\t"
      "v_pk_mul_f32 v[202:203], v[202:203], v[10:11] op_sel:[0,0] op_sel_hi:[1,0]\n\t"
      "v_pk_mul_f32 v[204:205], v[204:205], v[10:11] op_sel:[0,0] op_sel_hi:[1,0]\n\t"
      "v_pk_mul_f32 v[206:207], v[206:207], v[10:11] op_sel:[0,0] op_sel_hi:[1,0]\n\t"
      "v_pk_mul_f32 v[216:217], v[216:217], v[10:11] op_sel:[0,0] op_sel_hi:[1,0]\n\t"
      "v_pk_mul_f32 v[218:219], v[218:219], v[10:11] op_sel:[0,0] op_sel_hi:[1,0]\n\t"
      "v_pk_mul_f32 v[220:221], v[220:221], v[10:11] op_sel:[0,0] op_sel_hi:[1,0]\n\t"
      "v_pk_mul_f32 v[222:223], v[222:223], v[10:11] op_sel:[0,0] op_sel_hi:[1,0]\n\t"
      "v_pk_mul_f32 v[24:25], v[200:201], s[52:53]\n\t"
      "v_pk_mul_f32 v[26:27], v[202:203], s[52:53]\n\t"
      "v_pk_mul_f32 v[28:29], v[204:205], s[52:53]\n\t"
      "v_pk_mul_f32 v[30:31], v[206:207], s[52:53]\n\t"
      "v_exp_f32 v24, v24\n\t"
      "v_exp_f32 v25, v25\n\t"
      "v_exp_f32 v26, v26\n\t"
      "v_exp_f32 v27, v27\n\t"
      "v_exp_f32 v28, v28\n\t"
      "v_exp_f32 v29, v29\n\t"
      "v_exp_f32 v30, v30\n\t"
      "v_exp_f32 v31, v31\n\t"
      "v_pk_mul_f32 v[200:201], v[200:201], v[216:217]\n\t"
      "v_pk_mul_f32 v[202:203], v[202:203], v[218:219]\n\t"
      "v_pk_mul_f32 v[204:205], v[204:205], v[220:221]\n\t"
      "v_pk_mul_f32 v[206:207], v[206:207], v[222:223]\n\t"
      "v_pk_add_f32 v[24:25], v[24:25], 1.0 op_sel_hi:[1,0]\n\t"
      "v_pk_add_f32 v[26:27], v[26:27], 1.0 op_sel_hi:[1,0]\n\t"
      "v_pk_add_f32 v[28:29], v[28:29], 1.0 op_sel_hi:[1,0]\n\t"
      "v_pk_add_f32 v[30:31], v[30:31], 1.0 op_sel_hi:[1,0]\n\t"
      "v_rcp_f32 v24, v24\n\t"
      "v_rcp_f32 v25, v25\n\t"
      "v_rcp_f32 v26, v26\n\t"
      "v_rcp_f32 v27, v27\n\t"
      "v_rcp_f32 v28, v28\n\t"
      "v_rcp_f32 v29, v29\n\t"
      "v_rcp_f32 v30, v30\n\t"
      "v_rcp_f32 v31, v31\n\t"
      "s_nop 0\n\t"
      "v_pk_mul_f32 v[200:201], v[200:201], v[24:25]\n\t"
      "v_pk_mul_f32 v[202:203], v[202:203], v[26:27]\n\t"
      "v_pk_mul_f32 v[204:205], v[204:205], v[28:29]\n\t"
      "v_pk_mul_f32 v[206:207], v[206:207], v[30:31]\n\t"
      "v_cvt_pk_bf16_f32 v216, v200, v201\n\t"
      "v_cvt_pk_bf16_f32 v217, v202, v203\n\t"
      "v_cvt_pk_bf16_f32 v218, v204, v205\n\t"
      "v_cvt_pk_bf16_f32 v219, v206, v207\n\t"
      "buffer_store_dwordx4 v[216:219], v16, s[40:43], s57 offen offset:1024 sc1\n\t"
      "v_pk_fma_f32 v[224:225], v[32:33], v[12:13], v[224:225] op_sel:[0,1,0] op_sel_hi:[1,1,1] neg_lo:[1,0,0] neg_hi:[1,0,0]\n\t"
      "v_pk_fma_f32 v[226:227], v[34:35], v[12:13], v[226:227] op_sel:[0,1,0] op_sel_hi:[1,1,1] neg_lo:[1,0,0] neg_hi:[1,0,0]\n\t"
      "v_pk_fma_f32 v[228:229], v[36:37], v[12:13], v[228:229] op_sel:[0,1,0] op_sel_hi:[1,1,1] neg_lo:[1,0,0] neg_hi:[1,0,0]\n\t"
      "v_pk_fma_f32 v[230:231], v[38:39], v[12:13], v[230:231] op_sel:[0,1,0] op_sel_hi:[1,1,1] neg_lo:[1,0,0] neg_hi:[1,0,0]\n\t"
      "v_pk_fma_f32 v[240:241], v[48:49], v[12:13], v[240:241] op_sel:[0,1,0] op_sel_hi:[1,1,1] neg_lo:[1,0,0] neg_hi:[1,0,0]\n\t"
      "v_pk_fma_f32 v[242:243], v[50:51], v[12:13], v[242:243] op_sel:[0,1,0] op_sel_hi:[1,1,1] neg_lo:[1,0,0] neg_hi:[1,0,0]\n\t"
      "v_pk_fma_f32 v[244:245], v[52:53], v[12:13], v[244:245] op_sel:[0,1,0] op_sel_hi:[1,1,1] neg_lo:[1,0,0] neg_hi:[1,0,0]\n\t"
      "v_pk_fma_f32 v[246:247], v[54:55], v[12:13], v[246:247] op_sel:[0,1,0] op_sel_hi:[1,1,1] neg_lo:[1,0,0] neg_hi:[1,0,0]\n\t"
      "v_pk_mul_f32 v[224:225], v[224:225], v[12:13] op_sel:[0,0] op_sel_hi:[1,0]\n\t"
      "v_pk_mul_f32 v[226:227], v[226:227], v[12:13] op_sel:[0,0] op_sel_hi:[1,0]\n\t"
      "v_pk_mul_f32 v[228:229], v[228:229], v[12:13] op_sel:[0,0] op_sel_hi:[1,0]\n\t"
      "v_pk_mul_f32 v[230:231], v[230:231], v[12:13] op_sel:[0,0] op_sel_hi:[1,0]\n\t"
      "v_pk_mul_f32 v[240:241], v[240:241], v[12:13] op_sel:[0,0] op_sel_hi:[1,0]\n\t"
      "v_pk_mul_f32 v[242:243], v[242:243], v[12:13] op_sel:[0,0] op_sel_hi:[1,0]\n\t"
      "v_pk_mul_f32 v[244:245], v[244:245], v[12:13] op_sel:[0,0] op_sel_hi:[1,0]\n\t"
      "v_pk_mul_f32 v[246:247], v[246:247], v[12:13] op_sel:[0,0] op_sel_hi:[1,0]\n\t"
      "v_pk_mul_f32 v[24:25], v[224:225], s[52:53]\n\t"
      "v_pk_mul_f32 v[26:27], v[226:227], s[52:53]\n\t"
      "v_pk_mul_f32 v[28:29], v[228:229], s[52:53]\n\t"
      "v_pk_mul_f32 v[30:31], v[230:231], s[52:53]\n\t"
      "v_exp_f32 v24, v24\n\t"
      "v_exp_f32 v25, v25\n\t"
      "v_exp_f32 v26, v26\n\t"
      "v_exp_f32 v27, v27\n\t"
      "v_exp_f32 v28, v28\n\t"
      "v_exp_f32 v29, v29\n\t"
      "v_exp_f32 v30, v30\n\t"
      "v_exp_f32 v31, v31\n\t"
      "v_pk_mul_f32 v[224:225], v[224:225], v[240:241]\n\t"
      "v_pk_mul_f32 v[226:227], v[226:227], v[242:243]\n\t"
      "v_pk_mul_f32 v[228:229], v[228:229], v[244:245]\n\t"
      "v_pk_mul_f32 v[230:231], v[230:231], v[246:247]\n\t"
      "v_pk_add_f32 v[24:25], v[24:25], 1.0 op_sel_hi:[1,0]\n\t"
      "v_pk_add_f32 v[26:27], v[26:27], 1.0 op_sel_hi:[1,0]\n\t"
      "v_pk_add_f32 v[28:29], v[28:29], 1.0 op_sel_hi:[1,0]\n\t"
      "v_pk_add_f32 v[30:31], v[30:31], 1.0 op_sel_hi:[1,0]\n\t"
      "v_rcp_f32 v24, v24\n\t"
      "v_rcp_f32 v25, v25\n\t"
      "v_rcp_f32 v26, v26\n\t"
      "v_rcp_f32 v27, v27\n\t"
      "v_rcp_f32 v28, v28\n\t"
      "v_rcp_f32 v29, v29\n\t"
      "v_rcp_f32 v30, v30\n\t"
      "v_rcp_f32 v31, v31\n\t"
      "s_nop 0\n\t"
      "v_pk_mul_f32 v[224:225], v[224:225], v[24:25]\n\t"
      "v_pk_mul_f32 v[226:227], v[226:227], v[26:27]\n\t"
      "v_pk_mul_f32 v[228:229], v[228:229], v[28:29]\n\t"
      "v_pk_mul_f32 v[230:231], v[230:231], v[30:31]\n\t"
      "v_cvt_pk_bf16_f32 v240, v224, v225\n\t"
      "v_cvt_pk_bf16_f32 v241, v226, v227\n\t"
      "v_cvt_pk_bf16_f32 v242, v228, v229\n\t"
      "v_cvt_pk_bf16_f32 v243, v230, v231\n\t"
      "buffer_store_dwordx4 v[240:243], v16, s[40:43], s56 offen offset:2048 sc1\n\t"
      "v_pk_fma_f32 v[232:233], v[40:41], v[12:13], v[232:233] op_sel:[0,1,0] op_sel_hi:[1,1,1] neg_lo:[1,0,0] neg_hi:[1,0,0]\n\t"
      "v_pk_fma_f32 v[234:235], v[42:43], v[12:13], v[234:235] op_sel:[0,1,0] op_sel_hi:[1,1,1] neg_lo:[1,0,0] neg_hi:[1,0,0]\n\t"
      "v_pk_fma_f32 v[236:237], v[44:45], v[12:13], v[236:237] op_sel:[0,1,0] op_sel_hi:[1,1,1] neg_lo:[1,0,0] neg_hi:[1,0,0]\n\t"
      "v_pk_fma_f32 v[238:239], v[46:47], v[12:13], v[238:239] op_sel:[0,1,0] op_sel_hi:[1,1,1] neg_lo:[1,0,0] neg_hi:[1,0,0]\n\t"
      "v_pk_fma_f32 v[248:249], v[56:57], v[12:13], v[248:249] op_sel:[0,1,0] op_sel_hi:[1,1,1] neg_lo:[1,0,0] neg_hi:[1,0,0]\n\t"
      "v_pk_fma_f32 v[250:251], v[58:59], v[12:13], v[250:251] op_sel:[0,1,0] op_sel_hi:[1,1,1] neg_lo:[1,0,0] neg_hi:[1,0,0]\n\t"
      "v_pk_fma_f32 v[252:253], v[60:61], v[12:13], v[252:253] op_sel:[0,1,0] op_sel_hi:[1,1,1] neg_lo:[1,0,0] neg_hi:[1,0,0]\n\t"
      "v_pk_fma_f32 v[254:255], v[62:63], v[12:13], v[254:255] op_sel:[0,1,0] op_sel_hi:[1,1,1] neg_lo:[1,0,0] neg_hi:[1,0,0]\n\t"
      "v_pk_mul_f32 v[232:233], v[232:233], v[12:13] op_sel:[0,0] op_sel_hi:[1,0]\n\t"
      "v_pk_mul_f32 v[234:235], v[234:235], v[12:13] op_sel:[0,0] op_sel_hi:[1,0]\n\t"
      "v_pk_mul_f32 v[236:237], v[236:237], v[12:13] op_sel:[0,0] op_sel_hi:[1,0]\n\t"
      "v_pk_mul_f32 v[238:239], v[238:239], v[12:13] op_sel:[0,0] op_sel_hi:[1,0]\n\t"
      "v_pk_mul_f32 v[248:249], v[248:249], v[12:13] op_sel:[0,0] op_sel_hi:[1,0]\n\t"
      "v_pk_mul_f32 v[250:251], v[250:251], v[12:13] op_sel:[0,0] op_sel_hi:[1,0]\n\t"
      "v_pk_mul_f32 v[252:253], v[252:253], v[12:13] op_sel:[0,0] op_sel_hi:[1,0]\n\t"
      "v_pk_mul_f32 v[254:255], v[254:255], v[12:13] op_sel:[0,0] op_sel_hi:[1,0]\n\t"
      "v_pk_mul_f32 v[24:25], v[232:233], s[52:53]\n\t"
      "v_pk_mul_f32 v[26:27], v[234:235], s[52:53]\n\t"
      "v_pk_mul_f32 v[28:29], v[236:237], s[52:53]\n\t"
      "v_pk_mul_f32 v[30:31], v[238:239], s[52:53]\n\t"
      "v_exp_f32 v24, v24\n\t"
      "v_exp_f32 v25, v25\n\t"
      "v_exp_f32 v26, v26\n\t"
      "v_exp_f32 v27, v27\n\t"
      "v_exp_f32 v28, v28\n\t"
      "v_exp_f32 v29, v29\n\t"
      "v_exp_f32 v30, v30\n\t"
      "v_exp_f32 v31, v31\n\t"
      "v_pk_mul_f32 v[232:233], v[232:233], v[248:249]\n\t"
      "v_pk_mul_f32 v[234:235], v[234:235], v[250:251]\n\t"
      "v_pk_mul_f32 v[236:237], v[236:237], v[252:253]\n\t"
      "v_pk_mul_f32 v[238:239], v[238:239], v[254:255]\n\t"
      "v_pk_add_f32 v[24:25], v[24:25], 1.0 op_sel_hi:[1,0]\n\t"
      "v_pk_add_f32 v[26:27], v[26:27], 1.0 op_sel_hi:[1,0]\n\t"
      "v_pk_add_f32 v[28:29], v[28:29], 1.0 op_sel_hi:[1,0]\n\t"
      "v_pk_add_f32 v[30:31], v[30:31], 1.0 op_sel_hi:[1,0]\n\t"
      "v_rcp_f32 v24, v24\n\t"
      "v_rcp_f32 v25, v25\n\t"
      "v_rcp_f32 v26, v26\n\t"
      "v_rcp_f32 v27, v27\n\t"
      "v_rcp_f32 v28, v28\n\t"
      "v_rcp_f32 v29, v29\n\t"
      "v_rcp_f32 v30, v30\n\t"
      "v_rcp_f32 v31, v31\n\t"
      "s_nop 0\n\t"
      "v_pk_mul_f32 v[232:233], v[232:233], v[24:25]\n\t"
      "v_pk_mul_f32 v[234:235], v[234:235], v[26:27]\n\t"
      "v_pk_mul_f32 v[236:237], v[236:237], v[28:29]\n\t"
      "v_pk_mul_f32 v[238:239], v[238:239], v[30:31]\n\t"
      "v_cvt_pk_bf16_f32 v248, v232, v233\n\t"
      "v_cvt_pk_bf16_f32 v249, v234, v235\n\t"
      "v_cvt_pk_bf16_f32 v250, v236, v237\n\t"
      "v_cvt_pk_bf16_f32 v251, v238, v239\n\t"
      "buffer_store_dwordx4 v[248:251], v16, s[40:43], s57 offen offset:2048 sc1\n\t"
      "v_accvgpr_read_b32 v64, a192\n\t"
      "v_accvgpr_read_b32 v65, a193\n\t"
      "v_accvgpr_read_b32 v66, a194\n\t"
      "v_accvgpr_read_b32 v67, a195\n\t"
      "v_accvgpr_read_b32 v68, a196\n\t"
      "v_accvgpr_read_b32 v69, a197\n\t"
      "v_accvgpr_read_b32 v70, a198\n\t"
      "v_accvgpr_read_b32 v71, a199\n\t"
      "v_accvgpr_read_b32 v72, a200\n\t"
      "v_accvgpr_read_b32 v73, a201\n\t"
      "v_accvgpr_read_b32 v74, a202\n\t"
      "v_accvgpr_read_b32 v75, a203\n\t"
      "v_accvgpr_read_b32 v76, a204\n\t"
      "v_accvgpr_read_b32 v77, a205\n\t"
      "v_accvgpr_read_b32 v78, a206\n\t"
      "v_accvgpr_read_b32 v79, a207\n\t"
      "v_accvgpr_read_b32 v80, a208\n\t"
      "v_accvgpr_read_b32 v81, a209\n\t"
      "v_accvgpr_read_b32 v82, a210\n\t"
      "v_accvgpr_read_b32 v83, a211\n\t"
      "v_accvgpr_read_b32 v84, a212\n\t"
      "v_accvgpr_read_b32 v85, a213\n\t"
      "v_accvgpr_read_b32 v86, a214\n\t"
      "v_accvgpr_read_b32 v87, a215\n\t"
      "v_accvgpr_read_b32 v88, a216\n\t"
      "v_accvgpr_read_b32 v89, a217\n\t"
      "v_accvgpr_read_b32 v90, a218\n\t"
      "v_accvgpr_read_b32 v91, a219\n\t"
      "v_accvgpr_read_b32 v92, a220\n\t"
      "v_accvgpr_read_b32 v93, a221\n\t"
      "v_accvgpr_read_b32 v94, a222\n\t"
      "v_accvgpr_read_b32 v95, a223\n\t"
      "v_pk_fma_f32 v[64:65], v[32:33], v[14:15], v[64:65] op_sel:[0,1,0] op_sel_hi:[1,1,1] neg_lo:[1,0,0] neg_hi:[1,0,0]\n\t"
      "v_pk_fma_f32 v[66:67], v[34:35], v[14:15], v[66:67] op_sel:[0,1,0] op_sel_hi:[1,1,1] neg_lo:[1,0,0] neg_hi:[1,0,0]\n\t"
      "v_pk_fma_f32 v[68:69], v[36:37], v[14:15], v[68:69] op_sel:[0,1,0] op_sel_hi:[1,1,1] neg_lo:[1,0,0] neg_hi:[1,0,0]\n\t"
      "v_pk_fma_f32 v[70:71], v[38:39], v[14:15], v[70:71] op_sel:[0,1,0] op_sel_hi:[1,1,1] neg_lo:[1,0,0] neg_hi:[1,0,0]\n\t"
      "v_pk_fma_f32 v[80:81], v[48:49], v[14:15], v[80:81] op_sel:[0,1,0] op_sel_hi:[1,1,1] neg_lo:[1,0,0] neg_hi:[1,0,0]\n\t"
      "v_pk_fma_f32 v[82:83], v[50:51], v[14:15], v[82:83] op_sel:[0,1,0] op_sel_hi:[1,1,1] neg_lo:[1,0,0] neg_hi:[1,0,0]\n\t"
      "v_pk_fma_f32 v[84:85], v[52:53], v[14:15], v[84:85] op_sel:[0,1,0] op_sel_hi:[1,1,1] neg_lo:[1,0,0] neg_hi:[1,0,0]\n\t"
      "v_pk_fma_f32 v[86:87], v[54:55], v[14:15], v[86:87] op_sel:[0,1,0] op_sel_hi:[1,1,1] neg_lo:[1,0,0] neg_hi:[1,0,0]\n\t"
      "v_pk_mul_f32 v[64:65], v[64:65], v[14:15] op_sel:[0,0] op_sel_hi:[1,0]\n\t"
      "v_pk_mul_f32 v[66:67], v[66:67], v[14:15] op_sel:[0,0] op_sel_hi:[1,0]\n\t"
      "v_pk_mul_f32 v[68:69], v[68:69], v[14:15] op_sel:[0,0] op_sel_hi:[1,0]\n\t"
      "v_pk_mul_f32 v[70:71], v[70:71], v[14:15] op_sel:[0,0] op_sel_hi:[1,0]\n\t"
      "v_pk_mul_f32 v[80:81], v[80:81], v[14:15] op_sel:[0,0] op_sel_hi:[1,0]\n\t"
      "v_pk_mul_f32 v[82:83], v[82:83], v[14:15] op_sel:[0,0] op_sel_hi:[1,0]\n\t"
      "v_pk_mul_f32 v[84:85], v[84:85], v[14:15] op_sel:[0,0] op_sel_hi:[1,0]\n\t"
      "v_pk_mul_f32 v[86:87], v[86:87], v[14:15] op_sel:[0,0] op_sel_hi:[1,0]\n\t"
      "v_pk_mul_f32 v[24:25], v[64:65], s[52:53]\n\t"
      "v_pk_mul_f32 v[26:27], v[66:67], s[52:53]\n\t"
      "v_pk_mul_f32 v[28:29], v[68:69], s[52:53]\n\t"
      "v_pk_mul_f32 v[30:31], v[70:71], s[52:53]\n\t"
      "v_exp_f32 v24, v24\n\t"
      "v_exp_f32 v25, v25\n\t"
      "v_exp_f32 v26, v26\n\t"
      "v_exp_f32 v27, v27\n\t"
      "v_exp_f32 v28, v28\n\t"
      "v_exp_f32 v29, v29\n\t"
      "v_exp_f32 v30, v30\n\t"
      "v_exp_f32 v31, v31\n\t"
      "v_pk_mul_f32 v[64:65], v[64:65], v[80:81]\n\t"
      "v_pk_mul_f32 v[66:67], v[66:67], v[82:83]\n\t"
      "v_pk_mul_f32 v[68:69], v[68:69], v[84:85]\n\t"
      "v_pk_mul_f32 v[70:71], v[70:71], v[86:87]\n\t"
      "v_pk_add_f32 v[24:25], v[24:25], 1.0 op_sel_hi:[1,0]\n\t"
      "v_pk_add_f32 v[26:27], v[26:27], 1.0 op_sel_hi:[1,0]\n\t"
      "v_pk_add_f32 v[28:29], v[28:29], 1.0 op_sel_hi:[1,0]\n\t"
      "v_pk_add_f32 v[30:31], v[30:31], 1.0 op_sel_hi:[1,0]\n\t"
      "v_rcp_f32 v24, v24\n\t"
      "v_rcp_f32 v25, v25\n\t"
      "v_rcp_f32 v26, v26\n\t"
      "v_rcp_f32 v27, v27\n\t"
      "v_rcp_f32 v28, v28\n\t"
      "v_rcp_f32 v29, v29\n\t"
      "v_rcp_f32 v30, v30\n\t"
      "v_rcp_f32 v31, v31\n\t"
      "s_nop 0\n\t"
      "v_pk_mul_f32 v[64:65], v[64:65], v[24:25]\n\t"
      "v_pk_mul_f32 v[66:67], v[66:67], v[26:27]\n\t"
      "v_pk_mul_f32 v[68:69], v[68:69], v[28:29]\n\t"
      "v_pk_mul_f32 v[70:71], v[70:71], v[30:31]\n\t"
      "v_cvt_pk_bf16_f32 v80, v64, v65\n\t"
      "v_cvt_pk_bf16_f32 v81, v66, v67\n\t"
      "v_cvt_pk_bf16_f32 v82, v68, v69\n\t"
      "v_cvt_pk_bf16_f32 v83, v70, v71\n\t"
      "buffer_store_dwordx4 v[80:83], v16, s[40:43], s56 offen offset:3072 sc1\n\t"
      "v_pk_fma_f32 v[72:73], v[40:41], v[14:15], v[72:73] op_sel:[0,1,0] op_sel_hi:[1,1,1] neg_lo:[1,0,0] neg_hi:[1,0,0]\n\t"
      "v_pk_fma_f32 v[74:75], v[42:43], v[14:15], v[74:75] op_sel:[0,1,0] op_sel_hi:[1,1,1] neg_lo:[1,0,0] neg_hi:[1,0,0]\n\t"
      "v_pk_fma_f32 v[76:77], v[44:45], v[14:15], v[76:77] op_sel:[0,1,0] op_sel_hi:[1,1,1] neg_lo:[1,0,0] neg_hi:[1,0,0]\n\t"
      "v_pk_fma_f32 v[78:79], v[46:47], v[14:15], v[78:79] op_sel:[0,1,0] op_sel_hi:[1,1,1] neg_lo:[1,0,0] neg_hi:[1,0,0]\n\t"
      "v_pk_fma_f32 v[88:89], v[56:57], v[14:15], v[88:89] op_sel:[0,1,0] op_sel_hi:[1,1,1] neg_lo:[1,0,0] neg_hi:[1,0,0]\n\t"
      "v_pk_fma_f32 v[90:91], v[58:59], v[14:15], v[90:91] op_sel:[0,1,0] op_sel_hi:[1,1,1] neg_lo:[1,0,0] neg_hi:[1,0,0]\n\t"
      "v_pk_fma_f32 v[92:93], v[60:61], v[14:15], v[92:93] op_sel:[0,1,0] op_sel_hi:[1,1,1] neg_lo:[1,0,0] neg_hi:[1,0,0]\n\t"
      "v_pk_fma_f32 v[94:95], v[62:63], v[14:15], v[94:95] op_sel:[0,1,0] op_sel_hi:[1,1,1] neg_lo:[1,0,0] neg_hi:[1,0,0]\n\t"
      "v_pk_mul_f32 v[72:73], v[72:73], v[14:15] op_sel:[0,0] op_sel_hi:[1,0]\n\t"
      "v_pk_mul_f32 v[74:75], v[74:75], v[14:15] op_sel:[0,0] op_sel_hi:[1,0]\n\t"
      "v_pk_mul_f32 v[76:77], v[76:77], v[14:15] op_sel:[0,0] op_sel_hi:[1,0]\n\t"
      "v_pk_mul_f32 v[78:79], v[78:79], v[14:15] op_sel:[0,0] op_sel_hi:[1,0]\n\t"
      "v_pk_mul_f32 v[88:89], v[88:89], v[14:15] op_sel:[0,0] op_sel_hi:[1,0]\n\t"
      "v_pk_mul_f32 v[90:91], v[90:91], v[14:15] op_sel:[0,0] op_sel_hi:[1,0]\n\t"
      "v_pk_mul_f32 v[92:93], v[92:93], v[14:15] op_sel:[0,0] op_sel_hi:[1,0]\n\t"
      "v_pk_mul_f32 v[94:95], v[94:95], v[14:15] op_sel:[0,0] op_sel_hi:[1,0]\n\t"
      "v_pk_mul_f32 v[24:25], v[72:73], s[52:53]\n\t"
      "v_pk_mul_f32 v[26:27], v[74:75], s[52:53]\n\t"
      "v_pk_mul_f32 v[28:29], v[76:77], s[52:53]\n\t"
      "v_pk_mul_f32 v[30:31], v[78:79], s[52:53]\n\t"
      "v_exp_f32 v24, v24\n\t"
      "v_exp_f32 v25, v25\n\t"
      "v_exp_f32 v26, v26\n\t"
      "v_exp_f32 v27, v27\n\t"
      "v_exp_f32 v28, v28\n\t"
      "v_exp_f32 v29, v29\n\t"
      "v_exp_f32 v30, v30\n\t"
      "v_exp_f32 v31, v31\n\t"
      "v_pk_mul_f32 v[72:73], v[72:73], v[88:89]\n\t"
      "v_pk_mul_f32 v[74:75], v[74:75], v[90:91]\n\t"
      "v_pk_mul_f32 v[76:77], v[76:77], v[92:93]\n\t"
      "v_pk_mul_f32 v[78:79], v[78:79], v[94:95]\n\t"
      "v_pk_add_f32 v[24:25], v[24:25], 1.0 op_sel_hi:[1,0]\n\t"
      "v_pk_add_f32 v[26:27], v[26:27], 1.0 op_sel_hi:[1,0]\n\t"
      "v_pk_add_f32 v[28:29], v[28:29], 1.0 op_sel_hi:[1,0]\n\t"
      "v_pk_add_f32 v[30:31], v[30:31], 1.0 op_sel_hi:[1,0]\n\t"
      "v_rcp_f32 v24, v24\n\t"
      "v_rcp_f32 v25, v25\n\t"
      "v_rcp_f32 v26, v26\n\t"
      "v_rcp_f32 v27, v27\n\t"
      "v_rcp_f32 v28, v28\n\t"
      "v_rcp_f32 v29, v29\n\t"
      "v_rcp_f32 v30, v30\n\t"
      "v_rcp_f32 v31, v31\n\t"
      "s_nop 0\n\t"
      "v_pk_mul_f32 v[72:73], v[72:73], v[24:25]\n\t"
      "v_pk_mul_f32 v[74:75], v[74:75], v[26:27]\n\t"
      "v_pk_mul_f32 v[76:77], v[76:77], v[28:29]\n\t"
      "v_pk_mul_f32 v[78:79], v[78:79], v[30:31]\n\t"
      "v_cvt_pk_bf16_f32 v88, v72, v73\n\t"
      "v_cvt_pk_bf16_f32 v89, v74, v75\n\t"
      "v_cvt_pk_bf16_f32 v90, v76, v77\n\t"
      "v_cvt_pk_bf16_f32 v91, v78, v79\n\t"
      "buffer_store_dwordx4 v[88:91], v16, s[40:43], s57 offen offset:3072 sc1\n\t"
      "s_branch LEE_done_%=\n\t"
      "LEE_role1_%=:\n\t"
      "s_nop 4\n\t"
      "s_mov_b32 s52, 0xbfb8aa3b\n\t"
      "s_mov_b32 s53, 0xbfb8aa3b\n\t"
      "s_add_u32 s56, s49, 4096\n\t"
      "s_add_u32 s57, s50, 4096\n\t"
      "s_mov_b32 exec_lo, -1\n\t"
      "s_mov_b32 exec_hi, 15\n\t"
      "ds_read_b128 v[0:3], v22 offset:0\n\t"
      "ds_read_b128 v[4:7], v22 offset:576\n\t"
      "ds_read_b128 v[8:11], v22 offset:1152\n\t"
      "ds_read_b128 v[12:15], v22 offset:1728\n\t"
      "s_waitcnt lgkmcnt(0)\n\t"
      "v_pk_add_f32 v[0:1], v[0:1], v[4:5]\n\t"
      "v_pk_add_f32 v[2:3], v[2:3], v[6:7]\n\t"
      "v_pk_add_f32 v[0:1], v[0:1], v[8:9]\n\t"
      "v_pk_add_f32 v[2:3], v[2:3], v[10:11]\n\t"
      "v_pk_add_f32 v[0:1], v[0:1], v[12:13]\n\t"
      "v_pk_add_f32 v[2:3], v[2:3], v[14:15]\n\t"
      "ds_read_b128 v[4:7], v22 offset:2304\n\t"
      "ds_read_b128 v[8:11], v22 offset:2880\n\t"
      "s_waitcnt lgkmcnt(0)\n\t"
      "v_pk_add_f32 v[0:1], v[0:1], v[4:5]\n\t"
      "v_pk_add_f32 v[2:3], v[2:3], v[6:7]\n\t"
      "v_pk_add_f32 v[0:1], v[0:1], v[8:9]\n\t"
      "v_pk_add_f32 v[2:3], v[2:3], v[10:11]\n\t"
      "ds_read_b128 v[4:7], v24\n\t"
      "s_waitcnt lgkmcnt(0)\n\t"
      "v_pk_add_f32 v[0:1], v[0:1], v[4:5]\n\t"
      "v_pk_add_f32 v[2:3], v[2:3], v[6:7]\n\t"
      "ds_write_b128 v25, v[0:3]\n\t"
      "s_mov_b64 exec, -1\n\t"
      "s_waitcnt lgkmcnt(0)\n\t"
      "ds_read_b32 v23, v21 offset:256\n\t"
      "ds_read_b128 v[0:3], v19 offset:0\n\t"
      "ds_read_b128 v[4:7], v19 offset:1024\n\t"
      "ds_read_b128 v[8:11], v19 offset:16\n\t"
      "ds_read_b128 v[12:15], v19 offset:1040\n\t"
      "v_mov_b32 v24, v160\n\t"
      "v_mov_b32 v25, v161\n\t"
      "v_mov_b32 v26, v162\n\t"
      "v_mov_b32 v27, v163\n\t"
      "v_mov_b32 v28, v164\n\t"
      "v_mov_b32 v29, v165\n\t"
      "v_mov_b32 v30, v166\n\t"
      "v_mov_b32 v31, v167\n\t"
      "s_waitcnt lgkmcnt(0)\n\t"
      "v_pk_fma_f32 v[24:25], v[4:5], v[22:23], v[24:25] op_sel:[0,1,0] op_sel_hi:[1,1,1] neg_lo:[1,0,0] neg_hi:[1,0,0]\n\t"
      "v_pk_fma_f32 v[26:27], v[6:7], v[22:23], v[26:27] op_sel:[0,1,0] op_sel_hi:[1,1,1] neg_lo:[1,0,0] neg_hi:[1,0,0]\n\t"
      "v_pk_fma_f32 v[28:29], v[12:13], v[22:23], v[28:29] op_sel:[0,1,0] op_sel_hi:[1,1,1] neg_lo:[1,0,0] neg_hi:[1,0,0]\n\t"
      "v_pk_fma_f32 v[30:31], v[14:15], v[22:23], v[30:31] op_sel:[0,1,0] op_sel_hi:[1,1,1] neg_lo:[1,0,0] neg_hi:[1,0,0]\n\t"
      "v_pk_mul_f32 v[24:25], v[24:25], v[0:1]\n\t"
      "v_pk_mul_f32 v[26:27], v[26:27], v[2:3]\n\t"
      "v_pk_mul_f32 v[28:29], v[28:29], v[8:9]\n\t"
      "v_pk_mul_f32 v[30:31], v[30:31], v[10:11]\n\t"
      "v_cvt_pk_bf16_f32 v8, v24, v25\n\t"
      "v_cvt_pk_bf16_f32 v9, v26, v27\n\t"
      "v_cvt_pk_bf16_f32 v10, v28, v29\n\t"
      "v_cvt_pk_bf16_f32 v11, v30, v31\n\t"
      "s_add_u32 s61, s48, 0\n\t"
      "buffer_store_dwordx4 v[8:11], v16, s[36:39], s61 offen sc1\n\t"
      "s_nop 1\n\t"
      "ds_read_b128 v[0:3], v19 offset:128\n\t"
      "ds_read_b128 v[4:7], v19 offset:1152\n\t"
      "ds_read_b128 v[8:11], v19 offset:144\n\t"
      "ds_read_b128 v[12:15], v19 offset:1168\n\t"
      "v_mov_b32 v24, v168\n\t"
      "v_mov_b32 v25, v169\n\t"
      "v_mov_b32 v26, v170\n\t"
      "v_mov_b32 v27, v171\n\t"
      "v_mov_b32 v28, v172\n\t"
      "v_mov_b32 v29, v173\n\t"
      "v_mov_b32 v30, v174\n\t"
      "v_mov_b32 v31, v175\n\t"
      "s_waitcnt lgkmcnt(0)\n\t"
      "v_pk_fma_f32 v[24:25], v[4:5], v[22:23], v[24:25] op_sel:[0,1,0] op_sel_hi:[1,1,1] neg_lo:[1,0,0] neg_hi:[1,0,0]\n\t"
      "v_pk_fma_f32 v[26:27], v[6:7], v[22:23], v[26:27] op_sel:[0,1,0] op_sel_hi:[1,1,1] neg_lo:[1,0,0] neg_hi:[1,0,0]\n\t"
      "v_pk_fma_f32 v[28:29], v[12:13], v[22:23], v[28:29] op_sel:[0,1,0] op_sel_hi:[1,1,1] neg_lo:[1,0,0] neg_hi:[1,0,0]\n\t"
      "v_pk_fma_f32 v[30:31], v[14:15], v[22:23], v[30:31] op_sel:[0,1,0] op_sel_hi:[1,1,1] neg_lo:[1,0,0] neg_hi:[1,0,0]\n\t"
      "v_pk_mul_f32 v[24:25], v[24:25], v[0:1]\n\t"
      "v_pk_mul_f32 v[26:27], v[26:27], v[2:3]\n\t"
      "v_pk_mul_f32 v[28:29], v[28:29], v[8:9]\n\t"
      "v_pk_mul_f32 v[30:31], v[30:31], v[10:11]\n\t"
      "v_cvt_pk_bf16_f32 v8, v24, v25\n\t"
      "v_cvt_pk_bf16_f32 v9, v26, v27\n\t"
      "v_cvt_pk_bf16_f32 v10, v28, v29\n\t"
      "v_cvt_pk_bf16_f32 v11, v30, v31\n\t"
      "s_add_u32 s61, s48, 65536\n\t"
      "buffer_store_dwordx4 v[8:11], v16, s[36:39], s61 offen sc1\n\t"
      "s_nop 1\n\t"
      "ds_read_b128 v[0:3], v19 offset:256\n\t"
      "ds_read_b128 v[4:7], v19 offset:1280\n\t"
      "ds_read_b128 v[8:11], v19 offset:272\n\t"
      "ds_read_b128 v[12:15], v19 offset:1296\n\t"
      "v_mov_b32 v24, v176\n\t"
      "v_mov_b32 v25, v177\n\t"
      "v_mov_b32 v26, v178\n\t"
      "v_mov_b32 v27, v179\n\t"
      "v_mov_b32 v28, v180\n\t"
      "v_mov_b32 v29, v181\n\t"
      "v_mov_b32 v30, v182\n\t"
      "v_mov_b32 v31, v183\n\t"
      "s_waitcnt lgkmcnt(0)\n\t"
      "v_pk_fma_f32 v[24:25], v[4:5], v[22:23], v[24:25] op_sel:[0,1,0] op_sel_hi:[1,1,1] neg_lo:[1,0,0] neg_hi:[1,0,0]\n\t"
      "v_pk_fma_f32 v[26:27], v[6:7], v[22:23], v[26:27] op_sel:[0,1,0] op_sel_hi:[1,1,1] neg_lo:[1,0,0] neg_hi:[1,0,0]\n\t"
      "v_pk_fma_f32 v[28:29], v[12:13], v[22:23], v[28:29] op_sel:[0,1,0] op_sel_hi:[1,1,1] neg_lo:[1,0,0] neg_hi:[1,0,0]\n\t"
      "v_pk_fma_f32 v[30:31], v[14:15], v[22:23], v[30:31] op_sel:[0,1,0] op_sel_hi:[1,1,1] neg_lo:[1,0,0] neg_hi:[1,0,0]\n\t"
      "v_pk_mul_f32 v[24:25], v[24:25], v[0:1]\n\t"
      "v_pk_mul_f32 v[26:27], v[26:27], v[2:3]\n\t"
      "v_pk_mul_f32 v[28:29], v[28:29], v[8:9]\n\t"
      "v_pk_mul_f32 v[30:31], v[30:31], v[10:11]\n\t"
      "v_cvt_pk_bf16_f32 v8, v24, v25\n\t"
      "v_cvt_pk_bf16_f32 v9, v26, v27\n\t"
      "v_cvt_pk_bf16_f32 v10, v28, v29\n\t"
      "v_cvt_pk_bf16_f32 v11, v30, v31\n\t"
      "s_add_u32 s61, s48, 131072\n\t"
      "buffer_store_dwordx4 v[8:11], v16, s[36:39], s61 offen sc1\n\t"
      "s_nop 1\n\t"
      "ds_read_b128 v[0:3], v19 offset:384\n\t"
      "ds_read_b128 v[4:7], v19 offset:1408\n\t"
      "ds_read_b128 v[8:11], v19 offset:400\n\t"
      "ds_read_b128 v[12:15], v19 offset:1424\n\t"
      "v_mov_b32 v24, v184\n\t"
      "v_mov_b32 v25, v185\n\t"
      "v_mov_b32 v26, v186\n\t"
      "v_mov_b32 v27, v187\n\t"
      "v_mov_b32 v28, v188\n\t"
      "v_mov_b32 v29, v189\n\t"
      "v_mov_b32 v30, v190\n\t"
      "v_mov_b32 v31, v191\n\t"
      "s_waitcnt lgkmcnt(0)\n\t"
      "v_pk_fma_f32 v[24:25], v[4:5], v[22:23], v[24:25] op_sel:[0,1,0] op_sel_hi:[1,1,1] neg_lo:[1,0,0] neg_hi:[1,0,0]\n\t"
      "v_pk_fma_f32 v[26:27], v[6:7], v[22:23], v[26:27] op_sel:[0,1,0] op_sel_hi:[1,1,1] neg_lo:[1,0,0] neg_hi:[1,0,0]\n\t"
      "v_pk_fma_f32 v[28:29], v[12:13], v[22:23], v[28:29] op_sel:[0,1,0] op_sel_hi:[1,1,1] neg_lo:[1,0,0] neg_hi:[1,0,0]\n\t"
      "v_pk_fma_f32 v[30:31], v[14:15], v[22:23], v[30:31] op_sel:[0,1,0] op_sel_hi:[1,1,1] neg_lo:[1,0,0] neg_hi:[1,0,0]\n\t"
      "v_pk_mul_f32 v[24:25], v[24:25], v[0:1]\n\t"
      "v_pk_mul_f32 v[26:27], v[26:27], v[2:3]\n\t"
      "v_pk_mul_f32 v[28:29], v[28:29], v[8:9]\n\t"
      "v_pk_mul_f32 v[30:31], v[30:31], v[10:11]\n\t"
      "v_cvt_pk_bf16_f32 v8, v24, v25\n\t"
      "v_cvt_pk_bf16_f32 v9, v26, v27\n\t"
      "v_cvt_pk_bf16_f32 v10, v28, v29\n\t"
      "v_cvt_pk_bf16_f32 v11, v30, v31\n\t"
      "s_add_u32 s61, s48, 196608\n\t"
      "buffer_store_dwordx4 v[8:11], v16, s[36:39], s61 offen sc1\n\t"
      "s_nop 1\n\t"
      "ds_read_b32 v23, v21 offset:320\n\t"
      "ds_read_b128 v[0:3], v19 offset:0\n\t"
      "ds_read_b128 v[4:7], v19 offset:1024\n\t"
      "ds_read_b128 v[8:11], v19 offset:16\n\t"
      "ds_read_b128 v[12:15], v19 offset:1040\n\t"
      "v_mov_b32 v24, v192\n\t"
      "v_mov_b32 v25, v193\n\t"
      "v_mov_b32 v26, v194\n\t"
      "v_mov_b32 v27, v195\n\t"
      "v_mov_b32 v28, v196\n\t"
      "v_mov_b32 v29, v197\n\t"
      "v_mov_b32 v30, v198\n\t"
      "v_mov_b32 v31, v199\n\t"
      "s_waitcnt lgkmcnt(0)\n\t"
      "v_pk_fma_f32 v[24:25], v[4:5], v[22:23], v[24:25] op_sel:[0,1,0] op_sel_hi:[1,1,1] neg_lo:[1,0,0] neg_hi:[1,0,0]\n\t"
      "v_pk_fma_f32 v[26:27], v[6:7], v[22:23], v[26:27] op_sel:[0,1,0] op_sel_hi:[1,1,1] neg_lo:[1,0,0] neg_hi:[1,0,0]\n\t"
      "v_pk_fma_f32 v[28:29], v[12:13], v[22:23], v[28:29] op_sel:[0,1,0] op_sel_hi:[1,1,1] neg_lo:[1,0,0] neg_hi:[1,0,0]\n\t"
      "v_pk_fma_f32 v[30:31], v[14:15], v[22:23], v[30:31] op_sel:[0,1,0] op_sel_hi:[1,1,1] neg_lo:[1,0,0] neg_hi:[1,0,0]\n\t"
      "v_pk_mul_f32 v[24:25], v[24:25], v[0:1]\n\t"
      "v_pk_mul_f32 v[26:27], v[26:27], v[2:3]\n\t"
      "v_pk_mul_f32 v[28:29], v[28:29], v[8:9]\n\t"
      "v_pk_mul_f32 v[30:31], v[30:31], v[10:11]\n\t"
      "v_cvt_pk_bf16_f32 v8, v24, v25\n\t"
      "v_cvt_pk_bf16_f32 v9, v26, v27\n\t"
      "v_cvt_pk_bf16_f32 v10, v28, v29\n\t"
      "v_cvt_pk_bf16_f32 v11, v30, v31\n\t"
      "s_add_u32 s61, s48, 1024\n\t"
      "buffer_store_dwordx4 v[8:11], v16, s[36:39], s61 offen sc1\n\t"
      "s_nop 1\n\t"
      "ds_read_b128 v[0:3], v19 offset:128\n\t"
      "ds_read_b128 v[4:7], v19 offset:1152\n\t"
      "ds_read_b128 v[8:11], v19 offset:144\n\t"
      "ds_read_b128 v[12:15], v19 offset:1168\n\t"
      "v_mov_b32 v24, v200\n\t"
      "v_mov_b32 v25, v201\n\t"
      "v_mov_b32 v26, v202\n\t"
      "v_mov_b32 v27, v203\n\t"
      "v_mov_b32 v28, v204\n\t"
      "v_mov_b32 v29, v205\n\t"
      "v_mov_b32 v30, v206\n\t"
      "v_mov_b32 v31, v207\n\t"
      "s_waitcnt lgkmcnt(0)\n\t"
      "v_pk_fma_f32 v[24:25], v[4:5], v[22:23], v[24:25] op_sel:[0,1,0] op_sel_hi:[1,1,1] neg_lo:[1,0,0] neg_hi:[1,0,0]\n\t"
      "v_pk_fma_f32 v[26:27], v[6:7], v[22:23], v[26:27] op_sel:[0,1,0] op_sel_hi:[1,1,1] neg_lo:[1,0,0] neg_hi:[1,0,0]\n\t"
      "v_pk_fma_f32 v[28:29], v[12:13], v[22:23], v[28:29] op_sel:[0,1,0] op_sel_hi:[1,1,1] neg_lo:[1,0,0] neg_hi:[1,0,0]\n\t"
      "v_pk_fma_f32 v[30:31], v[14:15], v[22:23], v[30:31] op_sel:[0,1,0] op_sel_hi:[1,1,1] neg_lo:[1,0,0] neg_hi:[1,0,0]\n\t"
      "v_pk_mul_f32 v[24:25], v[24:25], v[0:1]\n\t"
      "v_pk_mul_f32 v[26:27], v[26:27], v[2:3]\n\t"
      "v_pk_mul_f32 v[28:29], v[28:29], v[8:9]\n\t"
      "v_pk_mul_f32 v[30:31], v[30:31], v[10:11]\n\t"
      "v_cvt_pk_bf16_f32 v8, v24, v25\n\t"
      "v_cvt_pk_bf16_f32 v9, v26, v27\n\t"
      "v_cvt_pk_bf16_f32 v10, v28, v29\n\t"
      "v_cvt_pk_bf16_f32 v11, v30, v31\n\t"
      "s_add_u32 s61, s48, 66560\n\t"
      "buffer_store_dwordx4 v[8:11], v16, s[36:39], s61 offen sc1\n\t"
      "s_nop 1\n\t"
      "ds_read_b128 v[0:3], v19 offset:256\n\t"
      "ds_read_b128 v[4:7], v19 offset:1280\n\t"
      "ds_read_b128 v[8:11], v19 offset:272\n\t"
      "ds_read_b128 v[12:15], v19 offset:1296\n\t"
      "v_mov_b32 v24, v208\n\t"
      "v_mov_b32 v25, v209\n\t"
      "v_mov_b32 v26, v210\n\t"
      "v_mov_b32 v27, v211\n\t"
      "v_mov_b32 v28, v212\n\t"
      "v_mov_b32 v29, v213\n\t"
      "v_mov_b32 v30, v214\n\t"
      "v_mov_b32 v31, v215\n\t"
      "s_waitcnt lgkmcnt(0)\n\t"
      "v_pk_fma_f32 v[24:25], v[4:5], v[22:23], v[24:25] op_sel:[0,1,0] op_sel_hi:[1,1,1] neg_lo:[1,0,0] neg_hi:[1,0,0]\n\t"
      "v_pk_fma_f32 v[26:27], v[6:7], v[22:23], v[26:27] op_sel:[0,1,0] op_sel_hi:[1,1,1] neg_lo:[1,0,0] neg_hi:[1,0,0]\n\t"
      "v_pk_fma_f32 v[28:29], v[12:13], v[22:23], v[28:29] op_sel:[0,1,0] op_sel_hi:[1,1,1] neg_lo:[1,0,0] neg_hi:[1,0,0]\n\t"
      "v_pk_fma_f32 v[30:31], v[14:15], v[22:23], v[30:31] op_sel:[0,1,0] op_sel_hi:[1,1,1] neg_lo:[1,0,0] neg_hi:[1,0,0]\n\t"
      "v_pk_mul_f32 v[24:25], v[24:25], v[0:1]\n\t"
      "v_pk_mul_f32 v[26:27], v[26:27], v[2:3]\n\t"
      "v_pk_mul_f32 v[28:29], v[28:29], v[8:9]\n\t"
      "v_pk_mul_f32 v[30:31], v[30:31], v[10:11]\n\t"
      "v_cvt_pk_bf16_f32 v8, v24, v25\n\t"
      "v_cvt_pk_bf16_f32 v9, v26, v27\n\t"
      "v_cvt_pk_bf16_f32 v10, v28, v29\n\t"
      "v_cvt_pk_bf16_f32 v11, v30, v31\n\t"
      "s_add_u32 s61, s48, 132096\n\t"
      "buffer_store_dwordx4 v[8:11], v16, s[36:39], s61 offen sc1\n\t"
      "s_nop 1\n\t"
      "ds_read_b128 v[0:3], v19 offset:384\n\t"
      "ds_read_b128 v[4:7], v19 offset:1408\n\t"
      "ds_read_b128 v[8:11], v19 offset:400\n\t"
      "ds_read_b128 v[12:15], v19 offset:1424\n\t"
      "v_mov_b32 v24, v216\n\t"
      "v_mov_b32 v25, v217\n\t"
      "v_mov_b32 v26, v218\n\t"
      "v_mov_b32 v27, v219\n\t"
      "v_mov_b32 v28, v220\n\t"
      "v_mov_b32 v29, v221\n\t"
      "v_mov_b32 v30, v222\n\t"
      "v_mov_b32 v31, v223\n\t"
      "s_waitcnt lgkmcnt(0)\n\t"
      "v_pk_fma_f32 v[24:25], v[4:5], v[22:23], v[24:25] op_sel:[0,1,0] op_sel_hi:[1,1,1] neg_lo:[1,0,0] neg_hi:[1,0,0]\n\t"
      "v_pk_fma_f32 v[26:27], v[6:7], v[22:23], v[26:27] op_sel:[0,1,0] op_sel_hi:[1,1,1] neg_lo:[1,0,0] neg_hi:[1,0,0]\n\t"
      "v_pk_fma_f32 v[28:29], v[12:13], v[22:23], v[28:29] op_sel:[0,1,0] op_sel_hi:[1,1,1] neg_lo:[1,0,0] neg_hi:[1,0,0]\n\t"
      "v_pk_fma_f32 v[30:31], v[14:15], v[22:23], v[30:31] op_sel:[0,1,0] op_sel_hi:[1,1,1] neg_lo:[1,0,0] neg_hi:[1,0,0]\n\t"
      "v_pk_mul_f32 v[24:25], v[24:25], v[0:1]\n\t"
      "v_pk_mul_f32 v[26:27], v[26:27], v[2:3]\n\t"
      "v_pk_mul_f32 v[28:29], v[28:29], v[8:9]\n\t"
      "v_pk_mul_f32 v[30:31], v[30:31], v[10:11]\n\t"
      "v_cvt_pk_bf16_f32 v8, v24, v25\n\t"
      "v_cvt_pk_bf16_f32 v9, v26, v27\n\t"
      "v_cvt_pk_bf16_f32 v10, v28, v29\n\t"
      "v_cvt_pk_bf16_f32 v11, v30, v31\n\t"
      "s_add_u32 s61, s48, 197632\n\t"
      "buffer_store_dwordx4 v[8:11], v16, s[36:39], s61 offen sc1\n\t"
      "s_nop 1\n\t"
      "ds_read_b32 v23, v21 offset:384\n\t"
      "ds_read_b128 v[0:3], v19 offset:0\n\t"
      "ds_read_b128 v[4:7], v19 offset:1024\n\t"
      "ds_read_b128 v[8:11], v19 offset:16\n\t"
      "ds_read_b128 v[12:15], v19 offset:1040\n\t"
      "v_mov_b32 v24, v224\n\t"
      "v_mov_b32 v25, v225\n\t"
      "v_mov_b32 v26, v226\n\t"
      "v_mov_b32 v27, v227\n\t"
      "v_mov_b32 v28, v228\n\t"
      "v_mov_b32 v29, v229\n\t"
      "v_mov_b32 v30, v230\n\t"
      "v_mov_b32 v31, v231\n\t"
      "s_waitcnt lgkmcnt(0)\n\t"
      "v_pk_fma_f32 v[24:25], v[4:5], v[22:23], v[24:25] op_sel:[0,1,0] op_sel_hi:[1,1,1] neg_lo:[1,0,0] neg_hi:[1,0,0]\n\t"
      "v_pk_fma_f32 v[26:27], v[6:7], v[22:23], v[26:27] op_sel:[0,1,0] op_sel_hi:[1,1,1] neg_lo:[1,0,0] neg_hi:[1,0,0]\n\t"
      "v_pk_fma_f32 v[28:29], v[12:13], v[22:23], v[28:29] op_sel:[0,1,0] op_sel_hi:[1,1,1] neg_lo:[1,0,0] neg_hi:[1,0,0]\n\t"
      "v_pk_fma_f32 v[30:31], v[14:15], v[22:23], v[30:31] op_sel:[0,1,0] op_sel_hi:[1,1,1] neg_lo:[1,0,0] neg_hi:[1,0,0]\n\t"
      "v_pk_mul_f32 v[24:25], v[24:25], v[0:1]\n\t"
      "v_pk_mul_f32 v[26:27], v[26:27], v[2:3]\n\t"
      "v_pk_mul_f32 v[28:29], v[28:29], v[8:9]\n\t"
      "v_pk_mul_f32 v[30:31], v[30:31], v[10:11]\n\t"
      "v_cvt_pk_bf16_f32 v8, v24, v25\n\t"
      "v_cvt_pk_bf16_f32 v9, v26, v27\n\t"
      "v_cvt_pk_bf16_f32 v10, v28, v29\n\t"
      "v_cvt_pk_bf16_f32 v11, v30, v31\n\t"
      "s_add_u32 s61, s48, 2048\n\t"
      "buffer_store_dwordx4 v[8:11], v16, s[36:39], s61 offen sc1\n\t"
      "s_nop 1\n\t"
      "ds_read_b128 v[0:3], v19 offset:128\n\t"
      "ds_read_b128 v[4:7], v19 offset:1152\n\t"
      "ds_read_b128 v[8:11], v19 offset:144\n\t"
      "ds_read_b128 v[12:15], v19 offset:1168\n\t"
      "v_mov_b32 v24, v232\n\t"
      "v_mov_b32 v25, v233\n\t"
      "v_mov_b32 v26, v234\n\t"
      "v_mov_b32 v27, v235\n\t"
      "v_mov_b32 v28, v236\n\t"
      "v_mov_b32 v29, v237\n\t"
      "v_mov_b32 v30, v238\n\t"
      "v_mov_b32 v31, v239\n\t"
      "s_waitcnt lgkmcnt(0)\n\t"
      "v_pk_fma_f32 v[24:25], v[4:5], v[22:23], v[24:25] op_sel:[0,1,0] op_sel_hi:[1,1,1] neg_lo:[1,0,0] neg_hi:[1,0,0]\n\t"
      "v_pk_fma_f32 v[26:27], v[6:7], v[22:23], v[26:27] op_sel:[0,1,0] op_sel_hi:[1,1,1] neg_lo:[1,0,0] neg_hi:[1,0,0]\n\t"
      "v_pk_fma_f32 v[28:29], v[12:13], v[22:23], v[28:29] op_sel:[0,1,0] op_sel_hi:[1,1,1] neg_lo:[1,0,0] neg_hi:[1,0,0]\n\t"
      "v_pk_fma_f32 v[30:31], v[14:15], v[22:23], v[30:31] op_sel:[0,1,0] op_sel_hi:[1,1,1] neg_lo:[1,0,0] neg_hi:[1,0,0]\n\t"
      "v_pk_mul_f32 v[24:25], v[24:25], v[0:1]\n\t"
      "v_pk_mul_f32 v[26:27], v[26:27], v[2:3]\n\t"
      "v_pk_mul_f32 v[28:29], v[28:29], v[8:9]\n\t"
      "v_pk_mul_f32 v[30:31], v[30:31], v[10:11]\n\t"
      "v_cvt_pk_bf16_f32 v8, v24, v25\n\t"
      "v_cvt_pk_bf16_f32 v9, v26, v27\n\t"
      "v_cvt_pk_bf16_f32 v10, v28, v29\n\t"
      "v_cvt_pk_bf16_f32 v11, v30, v31\n\t"
      "s_add_u32 s61, s48, 67584\n\t"
      "buffer_store_dwordx4 v[8:11], v16, s[36:39], s61 offen sc1\n\t"
      "s_nop 1\n\t"
      "ds_read_b128 v[0:3], v19 offset:256\n\t"
      "ds_read_b128 v[4:7], v19 offset:1280\n\t"
      "ds_read_b128 v[8:11], v19 offset:272\n\t"
      "ds_read_b128 v[12:15], v19 offset:1296\n\t"
      "v_mov_b32 v24, v240\n\t"
      "v_mov_b32 v25, v241\n\t"
      "v_mov_b32 v26, v242\n\t"
      "v_mov_b32 v27, v243\n\t"
      "v_mov_b32 v28, v244\n\t"
      "v_mov_b32 v29, v245\n\t"
      "v_mov_b32 v30, v246\n\t"
      "v_mov_b32 v31, v247\n\t"
      "s_waitcnt lgkmcnt(0)\n\t"
      "v_pk_fma_f32 v[24:25], v[4:5], v[22:23], v[24:25] op_sel:[0,1,0] op_sel_hi:[1,1,1] neg_lo:[1,0,0] neg_hi:[1,0,0]\n\t"
      "v_pk_fma_f32 v[26:27], v[6:7], v[22:23], v[26:27] op_sel:[0,1,0] op_sel_hi:[1,1,1] neg_lo:[1,0,0] neg_hi:[1,0,0]\n\t"
      "v_pk_fma_f32 v[28:29], v[12:13], v[22:23], v[28:29] op_sel:[0,1,0] op_sel_hi:[1,1,1] neg_lo:[1,0,0] neg_hi:[1,0,0]\n\t"
      "v_pk_fma_f32 v[30:31], v[14:15], v[22:23], v[30:31] op_sel:[0,1,0] op_sel_hi:[1,1,1] neg_lo:[1,0,0] neg_hi:[1,0,0]\n\t"
      "v_pk_mul_f32 v[24:25], v[24:25], v[0:1]\n\t"
      "v_pk_mul_f32 v[26:27], v[26:27], v[2:3]\n\t"
      "v_pk_mul_f32 v[28:29], v[28:29], v[8:9]\n\t"
      "v_pk_mul_f32 v[30:31], v[30:31], v[10:11]\n\t"
      "v_cvt_pk_bf16_f32 v8, v24, v25\n\t"
      "v_cvt_pk_bf16_f32 v9, v26, v27\n\t"
      "v_cvt_pk_bf16_f32 v10, v28, v29\n\t"
      "v_cvt_pk_bf16_f32 v11, v30, v31\n\t"
      "s_add_u32 s61, s48, 133120\n\t"
      "buffer_store_dwordx4 v[8:11], v16, s[36:39], s61 offen sc1\n\t"
      "s_nop 1\n\t"
      "ds_read_b128 v[0:3], v19 offset:384\n\t"
      "ds_read_b128 v[4:7], v19 offset:1408\n\t"
      "ds_read_b128 v[8:11], v19 offset:400\n\t"
      "ds_read_b128 v[12:15], v19 offset:1424\n\t"
      "v_mov_b32 v24, v248\n\t"
      "v_mov_b32 v25, v249\n\t"
      "v_mov_b32 v26, v250\n\t"
      "v_mov_b32 v27, v251\n\t"
      "v_mov_b32 v28, v252\n\t"
      "v_mov_b32 v29, v253\n\t"
      "v_mov_b32 v30, v254\n\t"
      "v_mov_b32 v31, v255\n\t"
      "s_waitcnt lgkmcnt(0)\n\t"
      "v_pk_fma_f32 v[24:25], v[4:5], v[22:23], v[24:25] op_sel:[0,1,0] op_sel_hi:[1,1,1] neg_lo:[1,0,0] neg_hi:[1,0,0]\n\t"
      "v_pk_fma_f32 v[26:27], v[6:7], v[22:23], v[26:27] op_sel:[0,1,0] op_sel_hi:[1,1,1] neg_lo:[1,0,0] neg_hi:[1,0,0]\n\t"
      "v_pk_fma_f32 v[28:29], v[12:13], v[22:23], v[28:29] op_sel:[0,1,0] op_sel_hi:[1,1,1] neg_lo:[1,0,0] neg_hi:[1,0,0]\n\t"
      "v_pk_fma_f32 v[30:31], v[14:15], v[22:23], v[30:31] op_sel:[0,1,0] op_sel_hi:[1,1,1] neg_lo:[1,0,0] neg_hi:[1,0,0]\n\t"
      "v_pk_mul_f32 v[24:25], v[24:25], v[0:1]\n\t"
      "v_pk_mul_f32 v[26:27], v[26:27], v[2:3]\n\t"
      "v_pk_mul_f32 v[28:29], v[28:29], v[8:9]\n\t"
      "v_pk_mul_f32 v[30:31], v[30:31], v[10:11]\n\t"
      "v_cvt_pk_bf16_f32 v8, v24, v25\n\t"
      "v_cvt_pk_bf16_f32 v9, v26, v27\n\t"
      "v_cvt_pk_bf16_f32 v10, v28, v29\n\t"
      "v_cvt_pk_bf16_f32 v11, v30, v31\n\t"
      "s_add_u32 s61, s48, 198656\n\t"
      "buffer_store_dwordx4 v[8:11], v16, s[36:39], s61 offen sc1\n\t"
      "s_nop 1\n\t"
      "ds_read_b32 v23, v21 offset:448\n\t"
      "ds_read_b128 v[0:3], v19 offset:0\n\t"
      "ds_read_b128 v[4:7], v19 offset:1024\n\t"
      "ds_read_b128 v[8:11], v19 offset:16\n\t"
      "ds_read_b128 v[12:15], v19 offset:1040\n\t"
      "v_accvgpr_read_b32 v24, a192\n\t"
      "v_accvgpr_read_b32 v25, a193\n\t"
      "v_accvgpr_read_b32 v26, a194\n\t"
      "v_accvgpr_read_b32 v27, a195\n\t"
      "v_accvgpr_read_b32 v28, a196\n\t"
      "v_accvgpr_read_b32 v29, a197\n\t"
      "v_accvgpr_read_b32 v30, a198\n\t"
      "v_accvgpr_read_b32 v31, a199\n\t"
      "s_waitcnt lgkmcnt(0)\n\t"
      "v_pk_fma_f32 v[24:25], v[4:5], v[22:23], v[24:25] op_sel:[0,1,0] op_sel_hi:[1,1,1] neg_lo:[1,0,0] neg_hi:[1,0,0]\n\t"
      "v_pk_fma_f32 v[26:27], v[6:7], v[22:23], v[26:27] op_sel:[0,1,0] op_sel_hi:[1,1,1] neg_lo:[1,0,0] neg_hi:[1,0,0]\n\t"
      "v_pk_fma_f32 v[28:29], v[12:13], v[22:23], v[28:29] op_sel:[0,1,0] op_sel_hi:[1,1,1] neg_lo:[1,0,0] neg_hi:[1,0,0]\n\t"
      "v_pk_fma_f32 v[30:31], v[14:15], v[22:23], v[30:31] op_sel:[0,1,0] op_sel_hi:[1,1,1] neg_lo:[1,0,0] neg_hi:[1,0,0]\n\t"
      "v_pk_mul_f32 v[24:25], v[24:25], v[0:1]\n\t"
      "v_pk_mul_f32 v[26:27], v[26:27], v[2:3]\n\t"
      "v_pk_mul_f32 v[28:29], v[28:29], v[8:9]\n\t"
      "v_pk_mul_f32 v[30:31], v[30:31], v[10:11]\n\t"
      "v_cvt_pk_bf16_f32 v8, v24, v25\n\t"
      "v_cvt_pk_bf16_f32 v9, v26, v27\n\t"
      "v_cvt_pk_bf16_f32 v10, v28, v29\n\t"
      "v_cvt_pk_bf16_f32 v11, v30, v31\n\t"
      "s_add_u32 s61, s48, 3072\n\t"
      "buffer_store_dwordx4 v[8:11], v16, s[36:39], s61 offen sc1\n\t"
      "s_nop 1\n\t"
      "ds_read_b128 v[0:3], v19 offset:128\n\t"
      "ds_read_b128 v[4:7], v19 offset:1152\n\t"
      "ds_read_b128 v[8:11], v19 offset:144\n\t"
      "ds_read_b128 v[12:15], v19 offset:1168\n\t"
      "v_accvgpr_read_b32 v24, a200\n\t"
      "v_accvgpr_read_b32 v25, a201\n\t"
      "v_accvgpr_read_b32 v26, a202\n\t"
      "v_accvgpr_read_b32 v27, a203\n\t"
      "v_accvgpr_read_b32 v28, a204\n\t"
      "v_accvgpr_read_b32 v29, a205\n\t"
      "v_accvgpr_read_b32 v30, a206\n\t"
      "v_accvgpr_read_b32 v31, a207\n\t"
      "s_waitcnt lgkmcnt(0)\n\t"
      "v_pk_fma_f32 v[24:25], v[4:5], v[22:23], v[24:25] op_sel:[0,1,0] op_sel_hi:[1,1,1] neg_lo:[1,0,0] neg_hi:[1,0,0]\n\t"
      "v_pk_fma_f32 v[26:27], v[6:7], v[22:23], v[26:27] op_sel:[0,1,0] op_sel_hi:[1,1,1] neg_lo:[1,0,0] neg_hi:[1,0,0]\n\t"
      "v_pk_fma_f32 v[28:29], v[12:13], v[22:23], v[28:29] op_sel:[0,1,0] op_sel_hi:[1,1,1] neg_lo:[1,0,0] neg_hi:[1,0,0]\n\t"
      "v_pk_fma_f32 v[30:31], v[14:15], v[22:23], v[30:31] op_sel:[0,1,0] op_sel_hi:[1,1,1] neg_lo:[1,0,0] neg_hi:[1,0,0]\n\t"
      "v_pk_mul_f32 v[24:25], v[24:25], v[0:1]\n\t"
      "v_pk_mul_f32 v[26:27], v[26:27], v[2:3]\n\t"
      "v_pk_mul_f32 v[28:29], v[28:29], v[8:9]\n\t"
      "v_pk_mul_f32 v[30:31], v[30:31], v[10:11]\n\t"
      "v_cvt_pk_bf16_f32 v8, v24, v25\n\t"
      "v_cvt_pk_bf16_f32 v9, v26, v27\n\t"
      "v_cvt_pk_bf16_f32 v10, v28, v29\n\t"
      "v_cvt_pk_bf16_f32 v11, v30, v31\n\t"
      "s_add_u32 s61, s48, 68608\n\t"
      "buffer_store_dwordx4 v[8:11], v16, s[36:39], s61 offen sc1\n\t"
      "s_nop 1\n\t"
      "ds_read_b128 v[0:3], v19 offset:256\n\t"
      "ds_read_b128 v[4:7], v19 offset:1280\n\t"
      "ds_read_b128 v[8:11], v19 offset:272\n\t"
      "ds_read_b128 v[12:15], v19 offset:1296\n\t"
      "v_accvgpr_read_b32 v24, a208\n\t"
      "v_accvgpr_read_b32 v25, a209\n\t"
      "v_accvgpr_read_b32 v26, a210\n\t"
      "v_accvgpr_read_b32 v27, a211\n\t"
      "v_accvgpr_read_b32 v28, a212\n\t"
      "v_accvgpr_read_b32 v29, a213\n\t"
      "v_accvgpr_read_b32 v30, a214\n\t"
      "v_accvgpr_read_b32 v31, a215\n\t"
      "s_waitcnt lgkmcnt(0)\n\t"
      "v_pk_fma_f32 v[24:25], v[4:5], v[22:23], v[24:25] op_sel:[0,1,0] op_sel_hi:[1,1,1] neg_lo:[1,0,0] neg_hi:[1,0,0]\n\t"
      "v_pk_fma_f32 v[26:27], v[6:7], v[22:23], v[26:27] op_sel:[0,1,0] op_sel_hi:[1,1,1] neg_lo:[1,0,0] neg_hi:[1,0,0]\n\t"
      "v_pk_fma_f32 v[28:29], v[12:13], v[22:23], v[28:29] op_sel:[0,1,0] op_sel_hi:[1,1,1] neg_lo:[1,0,0] neg_hi:[1,0,0]\n\t"
      "v_pk_fma_f32 v[30:31], v[14:15], v[22:23], v[30:31] op_sel:[0,1,0] op_sel_hi:[1,1,1] neg_lo:[1,0,0] neg_hi:[1,0,0]\n\t"
      "v_pk_mul_f32 v[24:25], v[24:25], v[0:1]\n\t"
      "v_pk_mul_f32 v[26:27], v[26:27], v[2:3]\n\t"
      "v_pk_mul_f32 v[28:29], v[28:29], v[8:9]\n\t"
      "v_pk_mul_f32 v[30:31], v[30:31], v[10:11]\n\t"
      "v_cvt_pk_bf16_f32 v8, v24, v25\n\t"
      "v_cvt_pk_bf16_f32 v9, v26, v27\n\t"
      "v_cvt_pk_bf16_f32 v10, v28, v29\n\t"
      "v_cvt_pk_bf16_f32 v11, v30, v31\n\t"
      "s_add_u32 s61, s48, 134144\n\t"
      "buffer_store_dwordx4 v[8:11], v16, s[36:39], s61 offen sc1\n\t"
      "s_nop 1\n\t"
      "ds_read_b128 v[0:3], v19 offset:384\n\t"
      "ds_read_b128 v[4:7], v19 offset:1408\n\t"
      "ds_read_b128 v[8:11], v19 offset:400\n\t"
      "ds_read_b128 v[12:15], v19 offset:1424\n\t"
      "v_accvgpr_read_b32 v24, a216\n\t"
      "v_accvgpr_read_b32 v25, a217\n\t"
      "v_accvgpr_read_b32 v26, a218\n\t"
      "v_accvgpr_read_b32 v27, a219\n\t"
      "v_accvgpr_read_b32 v28, a220\n\t"
      "v_accvgpr_read_b32 v29, a221\n\t"
      "v_accvgpr_read_b32 v30, a222\n\t"
      "v_accvgpr_read_b32 v31, a223\n\t"
      "s_waitcnt lgkmcnt(0)\n\t"
      "v_pk_fma_f32 v[24:25], v[4:5], v[22:23], v[24:25] op_sel:[0,1,0] op_sel_hi:[1,1,1] neg_lo:[1,0,0] neg_hi:[1,0,0]\n\t"
      "v_pk_fma_f32 v[26:27], v[6:7], v[22:23], v[26:27] op_sel:[0,1,0] op_sel_hi:[1,1,1] neg_lo:[1,0,0] neg_hi:[1,0,0]\n\t"
      "v_pk_fma_f32 v[28:29], v[12:13], v[22:23], v[28:29] op_sel:[0,1,0] op_sel_hi:[1,1,1] neg_lo:[1,0,0] neg_hi:[1,0,0]\n\t"
      "v_pk_fma_f32 v[30:31], v[14:15], v[22:23], v[30:31] op_sel:[0,1,0] op_sel_hi:[1,1,1] neg_lo:[1,0,0] neg_hi:[1,0,0]\n\t"
      "v_pk_mul_f32 v[24:25], v[24:25], v[0:1]\n\t"
      "v_pk_mul_f32 v[26:27], v[26:27], v[2:3]\n\t"
      "v_pk_mul_f32 v[28:29], v[28:29], v[8:9]\n\t"
      "v_pk_mul_f32 v[30:31], v[30:31], v[10:11]\n\t"
      "v_cvt_pk_bf16_f32 v8, v24, v25\n\t"
      "v_cvt_pk_bf16_f32 v9, v26, v27\n\t"
      "v_cvt_pk_bf16_f32 v10, v28, v29\n\t"
      "v_cvt_pk_bf16_f32 v11, v30, v31\n\t"
      "s_add_u32 s61, s48, 199680\n\t"
      "buffer_store_dwordx4 v[8:11], v16, s[36:39], s61 offen sc1\n\t"
      "s_nop 1\n\t"
      "ds_read_b32 v23, v21 offset:512\n\t"
      "ds_read_b128 v[0:3], v19 offset:0\n\t"
      "ds_read_b128 v[4:7], v19 offset:1024\n\t"
      "ds_read_b128 v[8:11], v19 offset:16\n\t"
      "ds_read_b128 v[12:15], v19 offset:1040\n\t"
      "v_accvgpr_read_b32 v24, a224\n\t"
      "v_accvgpr_read_b32 v25, a225\n\t"
      "v_accvgpr_read_b32 v26, a226\n\t"
      "v_accvgpr_read_b32 v27, a227\n\t"
      "v_accvgpr_read_b32 v28, a228\n\t"
      "v_accvgpr_read_b32 v29, a229\n\t"
      "v_accvgpr_read_b32 v30, a230\n\t"
      "v_accvgpr_read_b32 v31, a231\n\t"
      "s_waitcnt lgkmcnt(0)\n\t"
      "v_pk_fma_f32 v[24:25], v[4:5], v[22:23], v[24:25] op_sel:[0,1,0] op_sel_hi:[1,1,1] neg_lo:[1,0,0] neg_hi:[1,0,0]\n\t"
      "v_pk_fma_f32 v[26:27], v[6:7], v[22:23], v[26:27] op_sel:[0,1,0] op_sel_hi:[1,1,1] neg_lo:[1,0,0] neg_hi:[1,0,0]\n\t"
      "v_pk_fma_f32 v[28:29], v[12:13], v[22:23], v[28:29] op_sel:[0,1,0] op_sel_hi:[1,1,1] neg_lo:[1,0,0] neg_hi:[1,0,0]\n\t"
      "v_pk_fma_f32 v[30:31], v[14:15], v[22:23], v[30:31] op_sel:[0,1,0] op_sel_hi:[1,1,1] neg_lo:[1,0,0] neg_hi:[1,0,0]\n\t"
      "v_pk_mul_f32 v[24:25], v[24:25], v[0:1]\n\t"
      "v_pk_mul_f32 v[26:27], v[26:27], v[2:3]\n\t"
      "v_pk_mul_f32 v[28:29], v[28:29], v[8:9]\n\t"
      "v_pk_mul_f32 v[30:31], v[30:31], v[10:11]\n\t"
      "v_cvt_pk_bf16_f32 v8, v24, v25\n\t"
      "v_cvt_pk_bf16_f32 v9, v26, v27\n\t"
      "v_cvt_pk_bf16_f32 v10, v28, v29\n\t"
      "v_cvt_pk_bf16_f32 v11, v30, v31\n\t"
      "s_add_u32 s61, s48, 4096\n\t"
      "buffer_store_dwordx4 v[8:11], v16, s[36:39], s61 offen sc1\n\t"
      "s_nop 1\n\t"
      "ds_read_b128 v[0:3], v19 offset:128\n\t"
      "ds_read_b128 v[4:7], v19 offset:1152\n\t"
      "ds_read_b128 v[8:11], v19 offset:144\n\t"
      "ds_read_b128 v[12:15], v19 offset:1168\n\t"
      "v_accvgpr_read_b32 v24, a232\n\t"
      "v_accvgpr_read_b32 v25, a233\n\t"
      "v_accvgpr_read_b32 v26, a234\n\t"
      "v_accvgpr_read_b32 v27, a235\n\t"
      "v_accvgpr_read_b32 v28, a236\n\t"
      "v_accvgpr_read_b32 v29, a237\n\t"
      "v_accvgpr_read_b32 v30, a238\n\t"
      "v_accvgpr_read_b32 v31, a239\n\t"
      "s_waitcnt lgkmcnt(0)\n\t"
      "v_pk_fma_f32 v[24:25], v[4:5], v[22:23], v[24:25] op_sel:[0,1,0] op_sel_hi:[1,1,1] neg_lo:[1,0,0] neg_hi:[1,0,0]\n\t"
      "v_pk_fma_f32 v[26:27], v[6:7], v[22:23], v[26:27] op_sel:[0,1,0] op_sel_hi:[1,1,1] neg_lo:[1,0,0] neg_hi:[1,0,0]\n\t"
      "v_pk_fma_f32 v[28:29], v[12:13], v[22:23], v[28:29] op_sel:[0,1,0] op_sel_hi:[1,1,1] neg_lo:[1,0,0] neg_hi:[1,0,0]\n\t"
      "v_pk_fma_f32 v[30:31], v[14:15], v[22:23], v[30:31] op_sel:[0,1,0] op_sel_hi:[1,1,1] neg_lo:[1,0,0] neg_hi:[1,0,0]\n\t"
      "v_pk_mul_f32 v[24:25], v[24:25], v[0:1]\n\t"
      "v_pk_mul_f32 v[26:27], v[26:27], v[2:3]\n\t"
      "v_pk_mul_f32 v[28:29], v[28:29], v[8:9]\n\t"
      "v_pk_mul_f32 v[30:31], v[30:31], v[10:11]\n\t"
      "v_cvt_pk_bf16_f32 v8, v24, v25\n\t"
      "v_cvt_pk_bf16_f32 v9, v26, v27\n\t"
      "v_cvt_pk_bf16_f32 v10, v28, v29\n\t"
      "v_cvt_pk_bf16_f32 v11, v30, v31\n\t"
      "s_add_u32 s61, s48, 69632\n\t"
      "buffer_store_dwordx4 v[8:11], v16, s[36:39], s61 offen sc1\n\t"
      "s_nop 1\n\t"
      "ds_read_b128 v[0:3], v19 offset:256\n\t"
      "ds_read_b128 v[4:7], v19 offset:1280\n\t"
      "ds_read_b128 v[8:11], v19 offset:272\n\t"
      "ds_read_b128 v[12:15], v19 offset:1296\n\t"
      "v_accvgpr_read_b32 v24, a240\n\t"
      "v_accvgpr_read_b32 v25, a241\n\t"
      "v_accvgpr_read_b32 v26, a242\n\t"
      "v_accvgpr_read_b32 v27, a243\n\t"
      "v_accvgpr_read_b32 v28, a244\n\t"
      "v_accvgpr_read_b32 v29, a245\n\t"
      "v_accvgpr_read_b32 v30, a246\n\t"
      "v_accvgpr_read_b32 v31, a247\n\t"
      "s_waitcnt lgkmcnt(0)\n\t"
      "v_pk_fma_f32 v[24:25], v[4:5], v[22:23], v[24:25] op_sel:[0,1,0] op_sel_hi:[1,1,1] neg_lo:[1,0,0] neg_hi:[1,0,0]\n\t"
      "v_pk_fma_f32 v[26:27], v[6:7], v[22:23], v[26:27] op_sel:[0,1,0] op_sel_hi:[1,1,1] neg_lo:[1,0,0] neg_hi:[1,0,0]\n\t"
      "v_pk_fma_f32 v[28:29], v[12:13], v[22:23], v[28:29] op_sel:[0,1,0] op_sel_hi:[1,1,1] neg_lo:[1,0,0] neg_hi:[1,0,0]\n\t"
      "v_pk_fma_f32 v[30:31], v[14:15], v[22:23], v[30:31] op_sel:[0,1,0] op_sel_hi:[1,1,1] neg_lo:[1,0,0] neg_hi:[1,0,0]\n\t"
      "v_pk_mul_f32 v[24:25], v[24:25], v[0:1]\n\t"
      "v_pk_mul_f32 v[26:27], v[26:27], v[2:3]\n\t"
      "v_pk_mul_f32 v[28:29], v[28:29], v[8:9]\n\t"
      "v_pk_mul_f32 v[30:31], v[30:31], v[10:11]\n\t"
      "v_cvt_pk_bf16_f32 v8, v24, v25\n\t"
      "v_cvt_pk_bf16_f32 v9, v26, v27\n\t"
      "v_cvt_pk_bf16_f32 v10, v28, v29\n\t"
      "v_cvt_pk_bf16_f32 v11, v30, v31\n\t"
      "s_add_u32 s61, s48, 135168\n\t"
      "buffer_store_dwordx4 v[8:11], v16, s[36:39], s61 offen sc1\n\t"
      "s_nop 1\n\t"
      "ds_read_b128 v[0:3], v19 offset:384\n\t"
      "ds_read_b128 v[4:7], v19 offset:1408\n\t"
      "ds_read_b128 v[8:11], v19 offset:400\n\t"
      "ds_read_b128 v[12:15], v19 offset:1424\n\t"
      "v_accvgpr_read_b32 v24, a248\n\t"
      "v_accvgpr_read_b32 v25, a249\n\t"
      "v_accvgpr_read_b32 v26, a250\n\t"
      "v_accvgpr_read_b32 v27, a251\n\t"
      "v_accvgpr_read_b32 v28, a252\n\t"
      "v_accvgpr_read_b32 v29, a253\n\t"
      "v_accvgpr_read_b32 v30, a254\n\t"
      "v_accvgpr_read_b32 v31, a255\n\t"
      "s_waitcnt lgkmcnt(0)\n\t"
      "v_pk_fma_f32 v[24:25], v[4:5], v[22:23], v[24:25] op_sel:[0,1,0] op_sel_hi:[1,1,1] neg_lo:[1,0,0] neg_hi:[1,0,0]\n\t"
      "v_pk_fma_f32 v[26:27], v[6:7], v[22:23], v[26:27] op_sel:[0,1,0] op_sel_hi:[1,1,1] neg_lo:[1,0,0] neg_hi:[1,0,0]\n\t"
      "v_pk_fma_f32 v[28:29], v[12:13], v[22:23], v[28:29] op_sel:[0,1,0] op_sel_hi:[1,1,1] neg_lo:[1,0,0] neg_hi:[1,0,0]\n\t"
      "v_pk_fma_f32 v[30:31], v[14:15], v[22:23], v[30:31] op_sel:[0,1,0] op_sel_hi:[1,1,1] neg_lo:[1,0,0] neg_hi:[1,0,0]\n\t"
      "v_pk_mul_f32 v[24:25], v[24:25], v[0:1]\n\t"
      "v_pk_mul_f32 v[26:27], v[26:27], v[2:3]\n\t"
      "v_pk_mul_f32 v[28:29], v[28:29], v[8:9]\n\t"
      "v_pk_mul_f32 v[30:31], v[30:31], v[10:11]\n\t"
      "v_cvt_pk_bf16_f32 v8, v24, v25\n\t"
      "v_cvt_pk_bf16_f32 v9, v26, v27\n\t"
      "v_cvt_pk_bf16_f32 v10, v28, v29\n\t"
      "v_cvt_pk_bf16_f32 v11, v30, v31\n\t"
      "s_add_u32 s61, s48, 200704\n\t"
      "buffer_store_dwordx4 v[8:11], v16, s[36:39], s61 offen sc1\n\t"
      "s_nop 1\n\t"
      "ds_read_b32 v0, v18 offset:0\n\t"
      "ds_read_b32 v1, v18 offset:1024\n\t"
      "ds_read_b32 v2, v18 offset:16\n\t"
      "ds_read_b32 v3, v18 offset:1040\n\t"
      "ds_read_b32 v4, v18 offset:128\n\t"
      "ds_read_b32 v5, v18 offset:1152\n\t"
      "ds_read_b32 v6, v18 offset:144\n\t"
      "ds_read_b32 v7, v18 offset:1168\n\t"
      "ds_read_b32 v8, v18 offset:256\n\t"
      "ds_read_b32 v9, v18 offset:1280\n\t"
      "ds_read_b32 v10, v18 offset:272\n\t"
      "ds_read_b32 v11, v18 offset:1296\n\t"
      "ds_read_b32 v12, v18 offset:384\n\t"
      "ds_read_b32 v13, v18 offset:1408\n\t"
      "ds_read_b32 v14, v18 offset:400\n\t"
      "ds_read_b32 v15, v18 offset:1424\n\t"
      "s_waitcnt lgkmcnt(0)\n\t"
      "ds_read_b128 v[24:27], v20 offset:0\n\t"
      "ds_read_b128 v[28:31], v20 offset:64\n\t"
      "s_waitcnt lgkmcnt(0)\n\t"
      "v_pk_fma_f32 v[32:33], v[24:25], v[0:1], v[32:33] op_sel:[0,1,0] op_sel_hi:[1,1,1] neg_lo:[1,0,0] neg_hi:[1,0,0]\n\t"
      "v_pk_fma_f32 v[34:35], v[26:27], v[0:1], v[34:35] op_sel:[0,1,0] op_sel_hi:[1,1,1] neg_lo:[1,0,0] neg_hi:[1,0,0]\n\t"
      "v_pk_fma_f32 v[36:37], v[28:29], v[0:1], v[36:37] op_sel:[0,1,0] op_sel_hi:[1,1,1] neg_lo:[1,0,0] neg_hi:[1,0,0]\n\t"
      "v_pk_fma_f32 v[38:39], v[30:31], v[0:1], v[38:39] op_sel:[0,1,0] op_sel_hi:[1,1,1] neg_lo:[1,0,0] neg_hi:[1,0,0]\n\t"
      "ds_read_b128 v[24:27], v20 offset:128\n\t"
      "ds_read_b128 v[28:31], v20 offset:192\n\t"
      "s_waitcnt lgkmcnt(0)\n\t"
      "v_pk_fma_f32 v[40:41], v[24:25], v[0:1], v[40:41] op_sel:[0,1,0] op_sel_hi:[1,1,1] neg_lo:[1,0,0] neg_hi:[1,0,0]\n\t"
      "v_pk_fma_f32 v[42:43], v[26:27], v[0:1], v[42:43] op_sel:[0,1,0] op_sel_hi:[1,1,1] neg_lo:[1,0,0] neg_hi:[1,0,0]\n\t"
      "v_pk_fma_f32 v[44:45], v[28:29], v[0:1], v[44:45] op_sel:[0,1,0] op_sel_hi:[1,1,1] neg_lo:[1,0,0] neg_hi:[1,0,0]\n\t"
      "v_pk_fma_f32 v[46:47], v[30:31], v[0:1], v[46:47] op_sel:[0,1,0] op_sel_hi:[1,1,1] neg_lo:[1,0,0] neg_hi:[1,0,0]\n\t"
      "v_pk_mul_f32 v[32:33], v[32:33], v[0:1] op_sel:[0,0] op_sel_hi:[1,0]\n\t"
      "v_pk_mul_f32 v[34:35], v[34:35], v[0:1] op_sel:[0,0] op_sel_hi:[1,0]\n\t"
      "v_pk_mul_f32 v[36:37], v[36:37], v[0:1] op_sel:[0,0] op_sel_hi:[1,0]\n\t"
      "v_pk_mul_f32 v[38:39], v[38:39], v[0:1] op_sel:[0,0] op_sel_hi:[1,0]\n\t"
      "v_pk_mul_f32 v[40:41], v[40:41], v[0:1] op_sel:[0,0] op_sel_hi:[1,0]\n\t"
      "v_pk_mul_f32 v[42:43], v[42:43], v[0:1] op_sel:[0,0] op_sel_hi:[1,0]\n\t"
      "v_pk_mul_f32 v[44:45], v[44:45], v[0:1] op_sel:[0,0] op_sel_hi:[1,0]\n\t"
      "v_pk_mul_f32 v[46:47], v[46:47], v[0:1] op_sel:[0,0] op_sel_hi:[1,0]\n\t"
      "v_pk_mul_f32 v[24:25], v[32:33], s[52:53]\n\t"
      "v_pk_mul_f32 v[26:27], v[34:35], s[52:53]\n\t"
      "v_pk_mul_f32 v[28:29], v[36:37], s[52:53]\n\t"
      "v_pk_mul_f32 v[30:31], v[38:39], s[52:53]\n\t"
      "v_exp_f32 v24, v24\n\t"
      "v_exp_f32 v25, v25\n\t"
      "v_exp_f32 v26, v26\n\t"
      "v_exp_f32 v27, v27\n\t"
      "v_exp_f32 v28, v28\n\t"
      "v_exp_f32 v29, v29\n\t"
      "v_exp_f32 v30, v30\n\t"
      "v_exp_f32 v31, v31\n\t"
      "v_pk_mul_f32 v[32:33], v[32:33], v[40:41]\n\t"
      "v_pk_mul_f32 v[34:35], v[34:35], v[42:43]\n\t"
      "v_pk_mul_f32 v[36:37], v[36:37], v[44:45]\n\t"
      "v_pk_mul_f32 v[38:39], v[38:39], v[46:47]\n\t"
      "v_pk_add_f32 v[24:25], v[24:25], 1.0 op_sel_hi:[1,0]\n\t"
      "v_pk_add_f32 v[26:27], v[26:27], 1.0 op_sel_hi:[1,0]\n\t"
      "v_pk_add_f32 v[28:29], v[28:29], 1.0 op_sel_hi:[1,0]\n\t"
      "v_pk_add_f32 v[30:31], v[30:31], 1.0 op_sel_hi:[1,0]\n\t"
      "v_rcp_f32 v24, v24\n\t"
      "v_rcp_f32 v25, v25\n\t"
      "v_rcp_f32 v26, v26\n\t"
      "v_rcp_f32 v27, v27\n\t"
      "v_rcp_f32 v28, v28\n\t"
      "v_rcp_f32 v29, v29\n\t"
      "v_rcp_f32 v30, v30\n\t"
      "v_rcp_f32 v31, v31\n\t"
      "s_nop 0\n\t"
      "v_pk_mul_f32 v[32:33], v[32:33], v[24:25]\n\t"
      "v_pk_mul_f32 v[34:35], v[34:35], v[26:27]\n\t"
      "v_pk_mul_f32 v[36:37], v[36:37], v[28:29]\n\t"
      "v_pk_mul_f32 v[38:39], v[38:39], v[30:31]\n\t"
      "v_cvt_pk_bf16_f32 v40, v32, v33\n\t"
      "v_cvt_pk_bf16_f32 v41, v34, v35\n\t"
      "v_cvt_pk_bf16_f32 v42, v36, v37\n\t"
      "v_cvt_pk_bf16_f32 v43, v38, v39\n\t"
      "buffer_store_dwordx4 v[40:43], v16, s[40:43], s49 offen sc1\n\t"
      "s_nop 1\n\t"
      "ds_read_b128 v[32:35], v20 offset:0\n\t"
      "ds_read_b128 v[36:39], v20 offset:64\n\t"
      "ds_read_b128 v[40:43], v20 offset:128\n\t"
      "ds_read_b128 v[44:47], v20 offset:192\n\t"
      "s_waitcnt lgkmcnt(0)\n\t"
      "v_pk_fma_f32 v[48:49], v[32:33], v[2:3], v[48:49] op_sel:[0,1,0] op_sel_hi:[1,1,1] neg_lo:[1,0,0] neg_hi:[1,0,0]\n\t"
      "v_pk_fma_f32 v[50:51], v[34:35], v[2:3], v[50:51] op_sel:[0,1,0] op_sel_hi:[1,1,1] neg_lo:[1,0,0] neg_hi:[1,0,0]\n\t"
      "v_pk_fma_f32 v[52:53], v[36:37], v[2:3], v[52:53] op_sel:[0,1,0] op_sel_hi:[1,1,1] neg_lo:[1,0,0] neg_hi:[1,0,0]\n\t"
      "v_pk_fma_f32 v[54:55], v[38:39], v[2:3], v[54:55] op_sel:[0,1,0] op_sel_hi:[1,1,1] neg_lo:[1,0,0] neg_hi:[1,0,0]\n\t"
      "v_pk_fma_f32 v[56:57], v[40:41], v[2:3], v[56:57] op_sel:[0,1,0] op_sel_hi:[1,1,1] neg_lo:[1,0,0] neg_hi:[1,0,0]\n\t"
      "v_pk_fma_f32 v[58:59], v[42:43], v[2:3], v[58:59] op_sel:[0,1,0] op_sel_hi:[1,1,1] neg_lo:[1,0,0] neg_hi:[1,0,0]\n\t"
      "v_pk_fma_f32 v[60:61], v[44:45], v[2:3], v[60:61] op_sel:[0,1,0] op_sel_hi:[1,1,1] neg_lo:[1,0,0] neg_hi:[1,0,0]\n\t"
      "v_pk_fma_f32 v[62:63], v[46:47], v[2:3], v[62:63] op_sel:[0,1,0] op_sel_hi:[1,1,1] neg_lo:[1,0,0] neg_hi:[1,0,0]\n\t"
      "v_pk_mul_f32 v[48:49], v[48:49], v[2:3] op_sel:[0,0] op_sel_hi:[1,0]\n\t"
      "v_pk_mul_f32 v[50:51], v[50:51], v[2:3] op_sel:[0,0] op_sel_hi:[1,0]\n\t"
      "v_pk_mul_f32 v[52:53], v[52:53], v[2:3] op_sel:[0,0] op_sel_hi:[1,0]\n\t"
      "v_pk_mul_f32 v[54:55], v[54:55], v[2:3] op_sel:[0,0] op_sel_hi:[1,0]\n\t"
      "v_pk_mul_f32 v[56:57], v[56:57], v[2:3] op_sel:[0,0] op_sel_hi:[1,0]\n\t"
      "v_pk_mul_f32 v[58:59], v[58:59], v[2:3] op_sel:[0,0] op_sel_hi:[1,0]\n\t"
      "v_pk_mul_f32 v[60:61], v[60:61], v[2:3] op_sel:[0,0] op_sel_hi:[1,0]\n\t"
      "v_pk_mul_f32 v[62:63], v[62:63], v[2:3] op_sel:[0,0] op_sel_hi:[1,0]\n\t"
      "v_pk_mul_f32 v[24:25], v[48:49], s[52:53]\n\t"
      "v_pk_mul_f32 v[26:27], v[50:51], s[52:53]\n\t"
      "v_pk_mul_f32 v[28:29], v[52:53], s[52:53]\n\t"
      "v_pk_mul_f32 v[30:31], v[54:55], s[52:53]\n\t"
      "v_exp_f32 v24, v24\n\t"
      "v_exp_f32 v25, v25\n\t"
      "v_exp_f32 v26, v26\n\t"
      "v_exp_f32 v27, v27\n\t"
      "v_exp_f32 v28, v28\n\t"
      "v_exp_f32 v29, v29\n\t"
      "v_exp_f32 v30, v30\n\t"
      "v_exp_f32 v31, v31\n\t"
      "v_pk_mul_f32 v[48:49], v[48:49], v[56:57]\n\t"
      "v_pk_mul_f32 v[50:51], v[50:51], v[58:59]\n\t"
      "v_pk_mul_f32 v[52:53], v[52:53], v[60:61]\n\t"
      "v_pk_mul_f32 v[54:55], v[54:55], v[62:63]\n\t"
      "v_pk_add_f32 v[24:25], v[24:25], 1.0 op_sel_hi:[1,0]\n\t"
      "v_pk_add_f32 v[26:27], v[26:27], 1.0 op_sel_hi:[1,0]\n\t"
      "v_pk_add_f32 v[28:29], v[28:29], 1.0 op_sel_hi:[1,0]\n\t"
      "v_pk_add_f32 v[30:31], v[30:31], 1.0 op_sel_hi:[1,0]\n\t"
      "v_rcp_f32 v24, v24\n\t"
      "v_rcp_f32 v25, v25\n\t"
      "v_rcp_f32 v26, v26\n\t"
      "v_rcp_f32 v27, v27\n\t"
      "v_rcp_f32 v28, v28\n\t"
      "v_rcp_f32 v29, v29\n\t"
      "v_rcp_f32 v30, v30\n\t"
      "v_rcp_f32 v31, v31\n\t"
      "s_nop 0\n\t"
      "v_pk_mul_f32 v[48:49], v[48:49], v[24:25]\n\t"
      "v_pk_mul_f32 v[50:51], v[50:51], v[26:27]\n\t"
      "v_pk_mul_f32 v[52:53], v[52:53], v[28:29]\n\t"
      "v_pk_mul_f32 v[54:55], v[54:55], v[30:31]\n\t"
      "v_cvt_pk_bf16_f32 v56, v48, v49\n\t"
      "v_cvt_pk_bf16_f32 v57, v50, v51\n\t"
      "v_cvt_pk_bf16_f32 v58, v52, v53\n\t"
      "v_cvt_pk_bf16_f32 v59, v54, v55\n\t"
      "buffer_store_dwordx4 v[56:59], v16, s[40:43], s49 offen offset:1024 sc1\n\t"
      "v_pk_fma_f32 v[64:65], v[32:33], v[4:5], v[64:65] op_sel:[0,1,0] op_sel_hi:[1,1,1] neg_lo:[1,0,0] neg_hi:[1,0,0]\n\t"
      "v_pk_fma_f32 v[66:67], v[34:35], v[4:5], v[66:67] op_sel:[0,1,0] op_sel_hi:[1,1,1] neg_lo:[1,0,0] neg_hi:[1,0,0]\n\t"
      "v_pk_fma_f32 v[68:69], v[36:37], v[4:5], v[68:69] op_sel:[0,1,0] op_sel_hi:[1,1,1] neg_lo:[1,0,0] neg_hi:[1,0,0]\n\t"
      "v_pk_fma_f32 v[70:71], v[38:39], v[4:5], v[70:71] op_sel:[0,1,0] op_sel_hi:[1,1,1] neg_lo:[1,0,0] neg_hi:[1,0,0]\n\t"
      "v_pk_fma_f32 v[72:73], v[40:41], v[4:5], v[72:73] op_sel:[0,1,0] op_sel_hi:[1,1,1] neg_lo:[1,0,0] neg_hi:[1,0,0]\n\t"
      "v_pk_fma_f32 v[74:75], v[42:43], v[4:5], v[74:75] op_sel:[0,1,0] op_sel_hi:[1,1,1] neg_lo:[1,0,0] neg_hi:[1,0,0]\n\t"
      "v_pk_fma_f32 v[76:77], v[44:45], v[4:5], v[76:77] op_sel:[0,1,0] op_sel_hi:[1,1,1] neg_lo:[1,0,0] neg_hi:[1,0,0]\n\t"
      "v_pk_fma_f32 v[78:79], v[46:47], v[4:5], v[78:79] op_sel:[0,1,0] op_sel_hi:[1,1,1] neg_lo:[1,0,0] neg_hi:[1,0,0]\n\t"
      "v_pk_mul_f32 v[64:65], v[64:65], v[4:5] op_sel:[0,0] op_sel_hi:[1,0]\n\t"
      "v_pk_mul_f32 v[66:67], v[66:67], v[4:5] op_sel:[0,0] op_sel_hi:[1,0]\n\t"
      "v_pk_mul_f32 v[68:69], v[68:69], v[4:5] op_sel:[0,0] op_sel_hi:[1,0]\n\t"
      "v_pk_mul_f32 v[70:71], v[70:71], v[4:5] op_sel:[0,0] op_sel_hi:[1,0]\n\t"
      "v_pk_mul_f32 v[72:73], v[72:73], v[4:5] op_sel:[0,0] op_sel_hi:[1,0]\n\t"
      "v_pk_mul_f32 v[74:75], v[74:75], v[4:5] op_sel:[0,0] op_sel_hi:[1,0]\n\t"
      "v_pk_mul_f32 v[76:77], v[76:77], v[4:5] op_sel:[0,0] op_sel_hi:[1,0]\n\t"
      "v_pk_mul_f32 v[78:79], v[78:79], v[4:5] op_sel:[0,0] op_sel_hi:[1,0]\n\t"
      "v_pk_mul_f32 v[24:25], v[64:65], s[52:53]\n\t"
      "v_pk_mul_f32 v[26:27], v[66:67], s[52:53]\n\t"
      "v_pk_mul_f32 v[28:29], v[68:69], s[52:53]\n\t"
      "v_pk_mul_f32 v[30:31], v[70:71], s[52:53]\n\t"
      "v_exp_f32 v24, v24\n\t"
      "v_exp_f32 v25, v25\n\t"
      "v_exp_f32 v26, v26\n\t"
      "v_exp_f32 v27, v27\n\t"
      "v_exp_f32 v28, v28\n\t"
      "v_exp_f32 v29, v29\n\t"
      "v_exp_f32 v30, v30\n\t"
      "v_exp_f32 v31, v31\n\t"
      "v_pk_mul_f32 v[64:65], v[64:65], v[72:73]\n\t"
      "v_pk_mul_f32 v[66:67], v[66:67], v[74:75]\n\t"
      "v_pk_mul_f32 v[68:69], v[68:69], v[76:77]\n\t"
      "v_pk_mul_f32 v[70:71], v[70:71], v[78:79]\n\t"
      "v_pk_add_f32 v[24:25], v[24:25], 1.0 op_sel_hi:[1,0]\n\t"
      "v_pk_add_f32 v[26:27], v[26:27], 1.0 op_sel_hi:[1,0]\n\t"
      "v_pk_add_f32 v[28:29], v[28:29], 1.0 op_sel_hi:[1,0]\n\t"
      "v_pk_add_f32 v[30:31], v[30:31], 1.0 op_sel_hi:[1,0]\n\t"
      "v_rcp_f32 v24, v24\n\t"
      "v_rcp_f32 v25, v25\n\t"
      "v_rcp_f32 v26, v26\n\t"
      "v_rcp_f32 v27, v27\n\t"
      "v_rcp_f32 v28, v28\n\t"
      "v_rcp_f32 v29, v29\n\t"
      "v_rcp_f32 v30, v30\n\t"
      "v_rcp_f32 v31, v31\n\t"
      "s_nop 0\n\t"
      "v_pk_mul_f32 v[64:65], v[64:65], v[24:25]\n\t"
      "v_pk_mul_f32 v[66:67], v[66:67], v[26:27]\n\t"
      "v_pk_mul_f32 v[68:69], v[68:69], v[28:29]\n\t"
      "v_pk_mul_f32 v[70:71], v[70:71], v[30:31]\n\t"
      "v_cvt_pk_bf16_f32 v72, v64, v65\n\t"
      "v_cvt_pk_bf16_f32 v73, v66, v67\n\t"
      "v_cvt_pk_bf16_f32 v74, v68, v69\n\t"
      "v_cvt_pk_bf16_f32 v75, v70, v71\n\t"
      "buffer_store_dwordx4 v[72:75], v16, s[40:43], s49 offen offset:2048 sc1\n\t"
      "s_waitcnt vmcnt(3)\n\t"
      "v_mov_b32 v29, 0\n\t"
      "v_mov_b32 v30, s54\n\t"
      "v_mov_b32 v31, s55\n\t"
      "s_mov_b64 exec, 1\n\t"
      "buffer_store_dwordx2 v[30:31], v29, s[44:47], s58 offen sc1\n\t"
      "s_mov_b64 exec, -1\n\t"
      "s_nop 1\n\t"
      "v_pk_fma_f32 v[80:81], v[32:33], v[6:7], v[80:81] op_sel:[0,1,0] op_sel_hi:[1,1,1] neg_lo:[1,0,0] neg_hi:[1,0,0]\n\t"
      "v_pk_fma_f32 v[82:83], v[34:35], v[6:7], v[82:83] op_sel:[0,1,0] op_sel_hi:[1,1,1] neg_lo:[1,0,0] neg_hi:[1,0,0]\n\t"
      "v_pk_fma_f32 v[84:85], v[36:37], v[6:7], v[84:85] op_sel:[0,1,0] op_sel_hi:[1,1,1] neg_lo:[1,0,0] neg_hi:[1,0,0]\n\t"
      "v_pk_fma_f32 v[86:87], v[38:39], v[6:7], v[86:87] op_sel:[0,1,0] op_sel_hi:[1,1,1] neg_lo:[1,0,0] neg_hi:[1,0,0]\n\t"
      "v_pk_fma_f32 v[88:89], v[40:41], v[6:7], v[88:89] op_sel:[0,1,0] op_sel_hi:[1,1,1] neg_lo:[1,0,0] neg_hi:[1,0,0]\n\t"
      "v_pk_fma_f32 v[90:91], v[42:43], v[6:7], v[90:91] op_sel:[0,1,0] op_sel_hi:[1,1,1] neg_lo:[1,0,0] neg_hi:[1,0,0]\n\t"
      "v_pk_fma_f32 v[92:93], v[44:45], v[6:7], v[92:93] op_sel:[0,1,0] op_sel_hi:[1,1,1] neg_lo:[1,0,0] neg_hi:[1,0,0]\n\t"
      "v_pk_fma_f32 v[94:95], v[46:47], v[6:7], v[94:95] op_sel:[0,1,0] op_sel_hi:[1,1,1] neg_lo:[1,0,0] neg_hi:[1,0,0]\n\t"
      "v_pk_mul_f32 v[80:81], v[80:81], v[6:7] op_sel:[0,0] op_sel_hi:[1,0]\n\t"
      "v_pk_mul_f32 v[82:83], v[82:83], v[6:7] op_sel:[0,0] op_sel_hi:[1,0]\n\t"
      "v_pk_mul_f32 v[84:85], v[84:85], v[6:7] op_sel:[0,0] op_sel_hi:[1,0]\n\t"
      "v_pk_mul_f32 v[86:87], v[86:87], v[6:7] op_sel:[0,0] op_sel_hi:[1,0]\n\t"
      "v_pk_mul_f32 v[88:89], v[88:89], v[6:7] op_sel:[0,0] op_sel_hi:[1,0]\n\t"
      "v_pk_mul_f32 v[90:91], v[90:91], v[6:7] op_sel:[0,0] op_sel_hi:[1,0]\n\t"
      "v_pk_mul_f32 v[92:93], v[92:93], v[6:7] op_sel:[0,0] op_sel_hi:[1,0]\n\t"
      "v_pk_mul_f32 v[94:95], v[94:95], v[6:7] op_sel:[0,0] op_sel_hi:[1,0]\n\t"
      "v_pk_mul_f32 v[24:25], v[80:81], s[52:53]\n\t"
      "v_pk_mul_f32 v[26:27], v[82:83], s[52:53]\n\t"
      "v_pk_mul_f32 v[28:29], v[84:85], s[52:53]\n\t"
      "v_pk_mul_f32 v[30:31], v[86:87], s[52:53]\n\t"
      "v_exp_f32 v24, v24\n\t"
      "v_exp_f32 v25, v25\n\t"
      "v_exp_f32 v26, v26\n\t"
      "v_exp_f32 v27, v27\n\t"
      "v_exp_f32 v28, v28\n\t"
      "v_exp_f32 v29, v29\n\t"
      "v_exp_f32 v30, v30\n\t"
      "v_exp_f32 v31, v31\n\t"
      "v_pk_mul_f32 v[80:81], v[80:81], v[88:89]\n\t"
      "v_pk_mul_f32 v[82:83], v[82:83], v[90:91]\n\t"
      "v_pk_mul_f32 v[84:85], v[84:85], v[92:93]\n\t"
      "v_pk_mul_f32 v[86:87], v[86:87], v[94:95]\n\t"
      "v_pk_add_f32 v[24:25], v[24:25], 1.0 op_sel_hi:[1,0]\n\t"
      "v_pk_add_f32 v[26:27], v[26:27], 1.0 op_sel_hi:[1,0]\n\t"
      "v_pk_add_f32 v[28:29], v[28:29], 1.0 op_sel_hi:[1,0]\n\t"
      "v_pk_add_f32 v[30:31], v[30:31], 1.0 op_sel_hi:[1,0]\n\t"
      "v_rcp_f32 v24, v24\n\t"
      "v_rcp_f32 v25, v25\n\t"
      "v_rcp_f32 v26, v26\n\t"
      "v_rcp_f32 v27, v27\n\t"
      "v_rcp_f32 v28, v28\n\t"
      "v_rcp_f32 v29, v29\n\t"
      "v_rcp_f32 v30, v30\n\t"
      "v_rcp_f32 v31, v31\n\t"
      "s_nop 0\n\t"
      "v_pk_mul_f32 v[80:81], v[80:81], v[24:25]\n\t"
      "v_pk_mul_f32 v[82:83], v[82:83], v[26:27]\n\t"
      "v_pk_mul_f32 v[84:85], v[84:85], v[28:29]\n\t"
      "v_pk_mul_f32 v[86:87], v[86:87], v[30:31]\n\t"
      "v_cvt_pk_bf16_f32 v88, v80, v81\n\t"
      "v_cvt_pk_bf16_f32 v89, v82, v83\n\t"
      "v_cvt_pk_bf16_f32 v90, v84, v85\n\t"
      "v_cvt_pk_bf16_f32 v91, v86, v87\n\t"
      "buffer_store_dwordx4 v[88:91], v16, s[40:43], s49 offen offset:3072 sc1\n\t"
      "v_pk_fma_f32 v[96:97], v[32:33], v[8:9], v[96:97] op_sel:[0,1,0] op_sel_hi:[1,1,1] neg_lo:[1,0,0] neg_hi:[1,0,0]\n\t"
      "v_pk_fma_f32 v[98:99], v[34:35], v[8:9], v[98:99] op_sel:[0,1,0] op_sel_hi:[1,1,1] neg_lo:[1,0,0] neg_hi:[1,0,0]\n\t"
      "v_pk_fma_f32 v[100:101], v[36:37], v[8:9], v[100:101] op_sel:[0,1,0] op_sel_hi:[1,1,1] neg_lo:[1,0,0] neg_hi:[1,0,0]\n\t"
      "v_pk_fma_f32 v[102:103], v[38:39], v[8:9], v[102:103] op_sel:[0,1,0] op_sel_hi:[1,1,1] neg_lo:[1,0,0] neg_hi:[1,0,0]\n\t"
      "v_pk_fma_f32 v[104:105], v[40:41], v[8:9], v[104:105] op_sel:[0,1,0] op_sel_hi:[1,1,1] neg_lo:[1,0,0] neg_hi:[1,0,0]\n\t"
      "v_pk_fma_f32 v[106:107], v[42:43], v[8:9], v[106:107] op_sel:[0,1,0] op_sel_hi:[1,1,1] neg_lo:[1,0,0] neg_hi:[1,0,0]\n\t"
      "v_pk_fma_f32 v[108:109], v[44:45], v[8:9], v[108:109] op_sel:[0,1,0] op_sel_hi:[1,1,1] neg_lo:[1,0,0] neg_hi:[1,0,0]\n\t"
      "v_pk_fma_f32 v[110:111], v[46:47], v[8:9], v[110:111] op_sel:[0,1,0] op_sel_hi:[1,1,1] neg_lo:[1,0,0] neg_hi:[1,0,0]\n\t"
      "v_pk_mul_f32 v[96:97], v[96:97], v[8:9] op_sel:[0,0] op_sel_hi:[1,0]\n\t"
      "v_pk_mul_f32 v[98:99], v[98:99], v[8:9] op_sel:[0,0] op_sel_hi:[1,0]\n\t"
      "v_pk_mul_f32 v[100:101], v[100:101], v[8:9] op_sel:[0,0] op_sel_hi:[1,0]\n\t"
      "v_pk_mul_f32 v[102:103], v[102:103], v[8:9] op_sel:[0,0] op_sel_hi:[1,0]\n\t"
      "v_pk_mul_f32 v[104:105], v[104:105], v[8:9] op_sel:[0,0] op_sel_hi:[1,0]\n\t"
      "v_pk_mul_f32 v[106:107], v[106:107], v[8:9] op_sel:[0,0] op_sel_hi:[1,0]\n\t"
      "v_pk_mul_f32 v[108:109], v[108:109], v[8:9] op_sel:[0,0] op_sel_hi:[1,0]\n\t"
      "v_pk_mul_f32 v[110:111], v[110:111], v[8:9] op_sel:[0,0] op_sel_hi:[1,0]\n\t"
      "v_pk_mul_f32 v[24:25], v[96:97], s[52:53]\n\t"
      "v_pk_mul_f32 v[26:27], v[98:99], s[52:53]\n\t"
      "v_pk_mul_f32 v[28:29], v[100:101], s[52:53]\n\t"
      "v_pk_mul_f32 v[30:31], v[102:103], s[52:53]\n\t"
      "v_exp_f32 v24, v24\n\t"
      "v_exp_f32 v25, v25\n\t"
      "v_exp_f32 v26, v26\n\t"
      "v_exp_f32 v27, v27\n\t"
      "v_exp_f32 v28, v28\n\t"
      "v_exp_f32 v29, v29\n\t"
      "v_exp_f32 v30, v30\n\t"
      "v_exp_f32 v31, v31\n\t"
      "v_pk_mul_f32 v[96:97], v[96:97], v[104:105]\n\t"
      "v_pk_mul_f32 v[98:99], v[98:99], v[106:107]\n\t"
      "v_pk_mul_f32 v[100:101], v[100:101], v[108:109]\n\t"
      "v_pk_mul_f32 v[102:103], v[102:103], v[110:111]\n\t"
      "v_pk_add_f32 v[24:25], v[24:25], 1.0 op_sel_hi:[1,0]\n\t"
      "v_pk_add_f32 v[26:27], v[26:27], 1.0 op_sel_hi:[1,0]\n\t"
      "v_pk_add_f32 v[28:29], v[28:29], 1.0 op_sel_hi:[1,0]\n\t"
      "v_pk_add_f32 v[30:31], v[30:31], 1.0 op_sel_hi:[1,0]\n\t"
      "v_rcp_f32 v24, v24\n\t"
      "v_rcp_f32 v25, v25\n\t"
      "v_rcp_f32 v26, v26\n\t"
      "v_rcp_f32 v27, v27\n\t"
      "v_rcp_f32 v28, v28\n\t"
      "v_rcp_f32 v29, v29\n\t"
      "v_rcp_f32 v30, v30\n\t"
      "v_rcp_f32 v31, v31\n\t"
      "s_nop 0\n\t"
      "v_pk_mul_f32 v[96:97], v[96:97], v[24:25]\n\t"
      "v_pk_mul_f32 v[98:99], v[98:99], v[26:27]\n\t"
      "v_pk_mul_f32 v[100:101], v[100:101], v[28:29]\n\t"
      "v_pk_mul_f32 v[102:103], v[102:103], v[30:31]\n\t"
      "v_cvt_pk_bf16_f32 v104, v96, v97\n\t"
      "v_cvt_pk_bf16_f32 v105, v98, v99\n\t"
      "v_cvt_pk_bf16_f32 v106, v100, v101\n\t"
      "v_cvt_pk_bf16_f32 v107, v102, v103\n\t"
      "buffer_store_dwordx4 v[104:107], v16, s[40:43], s56 offen sc1\n\t"
      "v_pk_fma_f32 v[112:113], v[32:33], v[10:11], v[112:113] op_sel:[0,1,0] op_sel_hi:[1,1,1] neg_lo:[1,0,0] neg_hi:[1,0,0]\n\t"
      "v_pk_fma_f32 v[114:115], v[34:35], v[10:11], v[114:115] op_sel:[0,1,0] op_sel_hi:[1,1,1] neg_lo:[1,0,0] neg_hi:[1,0,0]\n\t"
      "v_pk_fma_f32 v[116:117], v[36:37], v[10:11], v[116:117] op_sel:[0,1,0] op_sel_hi:[1,1,1] neg_lo:[1,0,0] neg_hi:[1,0,0]\n\t"
      "v_pk_fma_f32 v[118:119], v[38:39], v[10:11], v[118:119] op_sel:[0,1,0] op_sel_hi:[1,1,1] neg_lo:[1,0,0] neg_hi:[1,0,0]\n\t"
      "v_pk_fma_f32 v[120:121], v[40:41], v[10:11], v[120:121] op_sel:[0,1,0] op_sel_hi:[1,1,1] neg_lo:[1,0,0] neg_hi:[1,0,0]\n\t"
      "v_pk_fma_f32 v[122:123], v[42:43], v[10:11], v[122:123] op_sel:[0,1,0] op_sel_hi:[1,1,1] neg_lo:[1,0,0] neg_hi:[1,0,0]\n\t"
      "v_pk_fma_f32 v[124:125], v[44:45], v[10:11], v[124:125] op_sel:[0,1,0] op_sel_hi:[1,1,1] neg_lo:[1,0,0] neg_hi:[1,0,0]\n\t"
      "v_pk_fma_f32 v[126:127], v[46:47], v[10:11], v[126:127] op_sel:[0,1,0] op_sel_hi:[1,1,1] neg_lo:[1,0,0] neg_hi:[1,0,0]\n\t"
      "v_pk_mul_f32 v[112:113], v[112:113], v[10:11] op_sel:[0,0] op_sel_hi:[1,0]\n\t"
      "v_pk_mul_f32 v[114:115], v[114:115], v[10:11] op_sel:[0,0] op_sel_hi:[1,0]\n\t"
      "v_pk_mul_f32 v[116:117], v[116:117], v[10:11] op_sel:[0,0] op_sel_hi:[1,0]\n\t"
      "v_pk_mul_f32 v[118:119], v[118:119], v[10:11] op_sel:[0,0] op_sel_hi:[1,0]\n\t"
      "v_pk_mul_f32 v[120:121], v[120:121], v[10:11] op_sel:[0,0] op_sel_hi:[1,0]\n\t"
      "v_pk_mul_f32 v[122:123], v[122:123], v[10:11] op_sel:[0,0] op_sel_hi:[1,0]\n\t"
      "v_pk_mul_f32 v[124:125], v[124:125], v[10:11] op_sel:[0,0] op_sel_hi:[1,0]\n\t"
      "v_pk_mul_f32 v[126:127], v[126:127], v[10:11] op_sel:[0,0] op_sel_hi:[1,0]\n\t"
      "v_pk_mul_f32 v[24:25], v[112:113], s[52:53]\n\t"
      "v_pk_mul_f32 v[26:27], v[114:115], s[52:53]\n\t"
      "v_pk_mul_f32 v[28:29], v[116:117], s[52:53]\n\t"
      "v_pk_mul_f32 v[30:31], v[118:119], s[52:53]\n\t"
      "v_exp_f32 v24, v24\n\t"
      "v_exp_f32 v25, v25\n\t"
      "v_exp_f32 v26, v26\n\t"
      "v_exp_f32 v27, v27\n\t"
      "v_exp_f32 v28, v28\n\t"
      "v_exp_f32 v29, v29\n\t"
      "v_exp_f32 v30, v30\n\t"
      "v_exp_f32 v31, v31\n\t"
      "v_pk_mul_f32 v[112:113], v[112:113], v[120:121]\n\t"
      "v_pk_mul_f32 v[114:115], v[114:115], v[122:123]\n\t"
      "v_pk_mul_f32 v[116:117], v[116:117], v[124:125]\n\t"
      "v_pk_mul_f32 v[118:119], v[118:119], v[126:127]\n\t"
      "v_pk_add_f32 v[24:25], v[24:25], 1.0 op_sel_hi:[1,0]\n\t"
      "v_pk_add_f32 v[26:27], v[26:27], 1.0 op_sel_hi:[1,0]\n\t"
      "v_pk_add_f32 v[28:29], v[28:29], 1.0 op_sel_hi:[1,0]\n\t"
      "v_pk_add_f32 v[30:31], v[30:31], 1.0 op_sel_hi:[1,0]\n\t"
      "v_rcp_f32 v24, v24\n\t"
      "v_rcp_f32 v25, v25\n\t"
      "v_rcp_f32 v26, v26\n\t"
      "v_rcp_f32 v27, v27\n\t"
      "v_rcp_f32 v28, v28\n\t"
      "v_rcp_f32 v29, v29\n\t"
      "v_rcp_f32 v30, v30\n\t"
      "v_rcp_f32 v31, v31\n\t"
      "s_nop 0\n\t"
      "v_pk_mul_f32 v[112:113], v[112:113], v[24:25]\n\t"
      "v_pk_mul_f32 v[114:115], v[114:115], v[26:27]\n\t"
      "v_pk_mul_f32 v[116:117], v[116:117], v[28:29]\n\t"
      "v_pk_mul_f32 v[118:119], v[118:119], v[30:31]\n\t"
      "v_cvt_pk_bf16_f32 v120, v112, v113\n\t"
      "v_cvt_pk_bf16_f32 v121, v114, v115\n\t"
      "v_cvt_pk_bf16_f32 v122, v116, v117\n\t"
      "v_cvt_pk_bf16_f32 v123, v118, v119\n\t"
      "buffer_store_dwordx4 v[120:123], v16, s[40:43], s56 offen offset:1024 sc1\n\t"
      "v_pk_fma_f32 v[128:129], v[32:33], v[12:13], v[128:129] op_sel:[0,1,0] op_sel_hi:[1,1,1] neg_lo:[1,0,0] neg_hi:[1,0,0]\n\t"
      "v_pk_fma_f32 v[130:131], v[34:35], v[12:13], v[130:131] op_sel:[0,1,0] op_sel_hi:[1,1,1] neg_lo:[1,0,0] neg_hi:[1,0,0]\n\t"
      "v_pk_fma_f32 v[132:133], v[36:37], v[12:13], v[132:133] op_sel:[0,1,0] op_sel_hi:[1,1,1] neg_lo:[1,0,0] neg_hi:[1,0,0]\n\t"
      "v_pk_fma_f32 v[134:135], v[38:39], v[12:13], v[134:135] op_sel:[0,1,0] op_sel_hi:[1,1,1] neg_lo:[1,0,0] neg_hi:[1,0,0]\n\t"
      "v_pk_fma_f32 v[136:137], v[40:41], v[12:13], v[136:137] op_sel:[0,1,0] op_sel_hi:[1,1,1] neg_lo:[1,0,0] neg_hi:[1,0,0]\n\t"
      "v_pk_fma_f32 v[138:139], v[42:43], v[12:13], v[138:139] op_sel:[0,1,0] op_sel_hi:[1,1,1] neg_lo:[1,0,0] neg_hi:[1,0,0]\n\t"
      "v_pk_fma_f32 v[140:141], v[44:45], v[12:13], v[140:141] op_sel:[0,1,0] op_sel_hi:[1,1,1] neg_lo:[1,0,0] neg_hi:[1,0,0]\n\t"
      "v_pk_fma_f32 v[142:143], v[46:47], v[12:13], v[142:143] op_sel:[0,1,0] op_sel_hi:[1,1,1] neg_lo:[1,0,0] neg_hi:[1,0,0]\n\t"
      "v_pk_mul_f32 v[128:129], v[128:129], v[12:13] op_sel:[0,0] op_sel_hi:[1,0]\n\t"
      "v_pk_mul_f32 v[130:131], v[130:131], v[12:13] op_sel:[0,0] op_sel_hi:[1,0]\n\t"
      "v_pk_mul_f32 v[132:133], v[132:133], v[12:13] op_sel:[0,0] op_sel_hi:[1,0]\n\t"
      "v_pk_mul_f32 v[134:135], v[134:135], v[12:13] op_sel:[0,0] op_sel_hi:[1,0]\n\t"
      "v_pk_mul_f32 v[136:137], v[136:137], v[12:13] op_sel:[0,0] op_sel_hi:[1,0]\n\t"
      "v_pk_mul_f32 v[138:139], v[138:139], v[12:13] op_sel:[0,0] op_sel_hi:[1,0]\n\t"
      "v_pk_mul_f32 v[140:141], v[140:141], v[12:13] op_sel:[0,0] op_sel_hi:[1,0]\n\t"
      "v_pk_mul_f32 v[142:143], v[142:143], v[12:13] op_sel:[0,0] op_sel_hi:[1,0]\n\t"
      "v_pk_mul_f32 v[24:25], v[128:129], s[52:53]\n\t"
      "v_pk_mul_f32 v[26:27], v[130:131], s[52:53]\n\t"
      "v_pk_mul_f32 v[28:29], v[132:133], s[52:53]\n\t"
      "v_pk_mul_f32 v[30:31], v[134:135], s[52:53]\n\t"
      "v_exp_f32 v24, v24\n\t"
      "v_exp_f32 v25, v25\n\t"
      "v_exp_f32 v26, v26\n\t"
      "v_exp_f32 v27, v27\n\t"
      "v_exp_f32 v28, v28\n\t"
      "v_exp_f32 v29, v29\n\t"
      "v_exp_f32 v30, v30\n\t"
      "v_exp_f32 v31, v31\n\t"
      "v_pk_mul_f32 v[128:129], v[128:129], v[136:137]\n\t"
      "v_pk_mul_f32 v[130:131], v[130:131], v[138:139]\n\t"
      "v_pk_mul_f32 v[132:133], v[132:133], v[140:141]\n\t"
      "v_pk_mul_f32 v[134:135], v[134:135], v[142:143]\n\t"
      "v_pk_add_f32 v[24:25], v[24:25], 1.0 op_sel_hi:[1,0]\n\t"
      "v_pk_add_f32 v[26:27], v[26:27], 1.0 op_sel_hi:[1,0]\n\t"
      "v_pk_add_f32 v[28:29], v[28:29], 1.0 op_sel_hi:[1,0]\n\t"
      "v_pk_add_f32 v[30:31], v[30:31], 1.0 op_sel_hi:[1,0]\n\t"
      "v_rcp_f32 v24, v24\n\t"
      "v_rcp_f32 v25, v25\n\t"
      "v_rcp_f32 v26, v26\n\t"
      "v_rcp_f32 v27, v27\n\t"
      "v_rcp_f32 v28, v28\n\t"
      "v_rcp_f32 v29, v29\n\t"
      "v_rcp_f32 v30, v30\n\t"
      "v_rcp_f32 v31, v31\n\t"
      "s_nop 0\n\t"
      "v_pk_mul_f32 v[128:129], v[128:129], v[24:25]\n\t"
      "v_pk_mul_f32 v[130:131], v[130:131], v[26:27]\n\t"
      "v_pk_mul_f32 v[132:133], v[132:133], v[28:29]\n\t"
      "v_pk_mul_f32 v[134:135], v[134:135], v[30:31]\n\t"
      "v_cvt_pk_bf16_f32 v136, v128, v129\n\t"
      "v_cvt_pk_bf16_f32 v137, v130, v131\n\t"
      "v_cvt_pk_bf16_f32 v138, v132, v133\n\t"
      "v_cvt_pk_bf16_f32 v139, v134, v135\n\t"
      "buffer_store_dwordx4 v[136:139], v16, s[40:43], s56 offen offset:2048 sc1\n\t"
      "v_pk_fma_f32 v[144:145], v[32:33], v[14:15], v[144:145] op_sel:[0,1,0] op_sel_hi:[1,1,1] neg_lo:[1,0,0] neg_hi:[1,0,0]\n\t"
      "v_pk_fma_f32 v[146:147], v[34:35], v[14:15], v[146:147] op_sel:[0,1,0] op_sel_hi:[1,1,1] neg_lo:[1,0,0] neg_hi:[1,0,0]\n\t"
      "v_pk_fma_f32 v[148:149], v[36:37], v[14:15], v[148:149] op_sel:[0,1,0] op_sel_hi:[1,1,1] neg_lo:[1,0,0] neg_hi:[1,0,0]\n\t"
      "v_pk_fma_f32 v[150:151], v[38:39], v[14:15], v[150:151] op_sel:[0,1,0] op_sel_hi:[1,1,1] neg_lo:[1,0,0] neg_hi:[1,0,0]\n\t"
      "v_pk_fma_f32 v[152:153], v[40:41], v[14:15], v[152:153] op_sel:[0,1,0] op_sel_hi:[1,1,1] neg_lo:[1,0,0] neg_hi:[1,0,0]\n\t"
      "v_pk_fma_f32 v[154:155], v[42:43], v[14:15], v[154:155] op_sel:[0,1,0] op_sel_hi:[1,1,1] neg_lo:[1,0,0] neg_hi:[1,0,0]\n\t"
      "v_pk_fma_f32 v[156:157], v[44:45], v[14:15], v[156:157] op_sel:[0,1,0] op_sel_hi:[1,1,1] neg_lo:[1,0,0] neg_hi:[1,0,0]\n\t"
      "v_pk_fma_f32 v[158:159], v[46:47], v[14:15], v[158:159] op_sel:[0,1,0] op_sel_hi:[1,1,1] neg_lo:[1,0,0] neg_hi:[1,0,0]\n\t"
      "v_pk_mul_f32 v[144:145], v[144:145], v[14:15] op_sel:[0,0] op_sel_hi:[1,0]\n\t"
      "v_pk_mul_f32 v[146:147], v[146:147], v[14:15] op_sel:[0,0] op_sel_hi:[1,0]\n\t"
      "v_pk_mul_f32 v[148:149], v[148:149], v[14:15] op_sel:[0,0] op_sel_hi:[1,0]\n\t"
      "v_pk_mul_f32 v[150:151], v[150:151], v[14:15] op_sel:[0,0] op_sel_hi:[1,0]\n\t"
      "v_pk_mul_f32 v[152:153], v[152:153], v[14:15] op_sel:[0,0] op_sel_hi:[1,0]\n\t"
      "v_pk_mul_f32 v[154:155], v[154:155], v[14:15] op_sel:[0,0] op_sel_hi:[1,0]\n\t"
      "v_pk_mul_f32 v[156:157], v[156:157], v[14:15] op_sel:[0,0] op_sel_hi:[1,0]\n\t"
      "v_pk_mul_f32 v[158:159], v[158:159], v[14:15] op_sel:[0,0] op_sel_hi:[1,0]\n\t"
      "v_pk_mul_f32 v[24:25], v[144:145], s[52:53]\n\t"
      "v_pk_mul_f32 v[26:27], v[146:147], s[52:53]\n\t"
      "v_pk_mul_f32 v[28:29], v[148:149], s[52:53]\n\t"
      "v_pk_mul_f32 v[30:31], v[150:151], s[52:53]\n\t"
      "v_exp_f32 v24, v24\n\t"
      "v_exp_f32 v25, v25\n\t"
      "v_exp_f32 v26, v26\n\t"
      "v_exp_f32 v27, v27\n\t"
      "v_exp_f32 v28, v28\n\t"
      "v_exp_f32 v29, v29\n\t"
      "v_exp_f32 v30, v30\n\t"
      "v_exp_f32 v31, v31\n\t"
      "v_pk_mul_f32 v[144:145], v[144:145], v[152:153]\n\t"
      "v_pk_mul_f32 v[146:147], v[146:147], v[154:155]\n\t"
      "v_pk_mul_f32 v[148:149], v[148:149], v[156:157]\n\t"
      "v_pk_mul_f32 v[150:151], v[150:151], v[158:159]\n\t"
      "v_pk_add_f32 v[24:25], v[24:25], 1.0 op_sel_hi:[1,0]\n\t"
      "v_pk_add_f32 v[26:27], v[26:27], 1.0 op_sel_hi:[1,0]\n\t"
      "v_pk_add_f32 v[28:29], v[28:29], 1.0 op_sel_hi:[1,0]\n\t"
      "v_pk_add_f32 v[30:31], v[30:31], 1.0 op_sel_hi:[1,0]\n\t"
      "v_rcp_f32 v24, v24\n\t"
      "v_rcp_f32 v25, v25\n\t"
      "v_rcp_f32 v26, v26\n\t"
      "v_rcp_f32 v27, v27\n\t"
      "v_rcp_f32 v28, v28\n\t"
      "v_rcp_f32 v29, v29\n\t"
      "v_rcp_f32 v30, v30\n\t"
      "v_rcp_f32 v31, v31\n\t"
      "s_nop 0\n\t"
      "v_pk_mul_f32 v[144:145], v[144:145], v[24:25]\n\t"
      "v_pk_mul_f32 v[146:147], v[146:147], v[26:27]\n\t"
      "v_pk_mul_f32 v[148:149], v[148:149], v[28:29]\n\t"
      "v_pk_mul_f32 v[150:151], v[150:151], v[30:31]\n\t"
      "v_cvt_pk_bf16_f32 v152, v144, v145\n\t"
      "v_cvt_pk_bf16_f32 v153, v146, v147\n\t"
      "v_cvt_pk_bf16_f32 v154, v148, v149\n\t"
      "v_cvt_pk_bf16_f32 v155, v150, v151\n\t"
      "buffer_store_dwordx4 v[152:155], v16, s[40:43], s56 offen offset:3072 sc1\n\t"
      "LEE_done_%=:\n\t"
      : "+{v[32:63]}"(c0), "+{v[64:95]}"(c1), "+{v[96:127]}"(c2), "+{v[128:159]}"(c3), "+{v[160:191]}"(c4), "+{v[192:223]}"(c5), "+{v[224:255]}"(c6), "+{a[192:223]}"(d0), "+{a[224:255]}"(d1), "+{v[24:25]}"(ve2)
      : "{v[16:22]}"(ve), "{s[36:51]}"(se), "{s[54:55]}"(stag), "{s[58:59]}"(se2), "{s[64:71]}"(se3), "{s80}"(swn)
      : "memory", "scc", "vcc", "v0", "v1", "v2", "v3", "v4", "v5", "v6", "v7", "v8", "v9", "v10", "v11", "v12", "v13", "v14", "v15", "v23", "v26", "v27", "v28", "v29", "v30", "v31", "s52", "s53", "s56", "s57", "s61");
    }
  }
  {
    SECTION_VARS
    if (b == 0 && wave == 1) {
      const unsigned lane = lane_id();
      for (int i = 0; i < 4; ++i) wait_flags(fl, FO_REC + (4 * (64 * i + lane)) * 16, tag);
      put_flag(fl, FO_E, tag);
    }
  }
  f32x32 c0; u32x16 xin, in0, in1, in2;
  {
    SECTION_VARS
    const unsigned lane = lane_id(), fr = lane & 15, fq = lane >> 4, tq = (lane >> 2) & 3, tp = lane & 3;
    const unsigned tile = tpn * 8 + ec;
#define B_COMMON \
      u32x16 vb0, sb; u32x12 vb1; u32x6 vb2; u32x4 sb2; u32x8 sb3, sb4; u32x3 ssend, srecv; \
      _Pragma("unroll") for (int i = 0; i < 16; ++i) { vb0[i] = 0; sb[i] = 0; } \
      _Pragma("unroll") for (int i = 0; i < 12; ++i) vb1[i] = 0; \
      sb[8] = wave * 8192; sb[9] = 32768 + wave * 4096; \
      put_srd(sb3, 0, fl, FO_END); sb3[4] = (unsigned)tag; sb3[5] = (unsigned)(tag >> 32); sb3[6] = 0; sb3[7] = 0; \
      put_srd(sb4, 0, kp->x, (unsigned)(S_ * D_ * 2)); put_srd(sb4, 4, ws + WS_XB, 16u * 1024 * 1024); \
      vb1[8] = (unsigned)tag; vb1[9] = (unsigned)(tag >> 32); vb1[10] = FO_H + (4 * b + wave) * 8; vb1[11] = FO_X + (4 * b + wave) * 8; \
      {   \
        const unsigned pk_ = lane & 3, pq = (q + (pk_ == 0 ? 1 : pk_)) & 3; \
        vb2[2] = FO_X + (4 * (idx * 8 + 2 * pq + (xcd & 1)) + wave) * 8; } \
      _Pragma("unroll") for (unsigned mu = 0; mu < 2; ++mu) { \
        const unsigned sbl = 8 * wm + 2 * q + mu; \
        const unsigned row = 256 * tpn + 32 * (sbl >> 1) + 8 * (fr >> 2) + 4 * (sbl & 1) + (fr & 3); \
        vb2[3 + mu] = (row * D_ + 128 * ec + 64 * wn + 4 * fq) * 2; } \
      vb2[5] = lane * 16; \
      _Pragma("unroll") for (unsigned k = 1; k < 4; ++k) { \
        ssend[k - 1] = rfl((((tile * 4 + ((q + k) & 3)) * 4 + q) * 4 + wave) * 4096); \
        srecv[k - 1] = rfl((((tile * 4 + q) * 4 + ((q + k) & 3)) * 4 + wave) * 4096); }
    if (q < 2) {
      u32x4 vp;
      const unsigned pp = lane >> 3, jb = 4 * (4 * q + (ec >> 1));
      const unsigned j0 = jb + ((ec & 1) ? 3u : ((lane >> 2) & 1)), j1 = jb + ((ec & 1) ? 3u : 2 + ((lane >> 2) & 1));
      const u32x2 ve2b = {FO_A + (4 * A_BLOCK(pp, j0) + (lane & 3)) * 8, FO_A + (4 * A_BLOCK(pp, j1) + (lane & 3)) * 8};
      {
        u32x4 se2f; put_srd(se2f, 0, fl, FO_END);
        const u32x4 ve2a = {(unsigned)tag, (unsigned)(tag >> 32), FO_H + (4 * b + wave) * 8, 0};
        asm volatile(
      "s_nop 4\n\t"
      "buffer_load_dwordx2 a[0:1], v32, s[56:59], 0 offen sc1\n\t"
      "buffer_load_dwordx2 a[2:3], v33, s[56:59], 0 offen sc1\n\t"
        : "=&{a[0:3]}"(vp) : "{v[24:27]}"(ve2a), "{v[32:33]}"(ve2b), "{s[56:59]}"(se2f) : "memory");
      }
      B_COMMON
#pragma unroll
      for (unsigned pr = 0; pr < 4; ++pr) {
        const unsigned m0 = 8 * wm + 2 * ((pr + q) & 3);
        const unsigned a = (16 * m0 + fr) * 128 + ((fq ^ ((fr >> 1) & 7)) * 16);
        vb0[2 * pr] = a; vb0[2 * pr + 1] = a ^ 64;
      }
      vb0[8] = 32768 + wn * 4096 + lane * 16;
      const unsigned l3 = lane >> 3;
#pragma unroll
      for (unsigned i = 0; i < 4; ++i) {
        const unsigned row = 256 * tpn + 64 * wave + 16 * (i & 1) + 8 * (l3 >> 2) + 4 * (i >> 1) + (l3 & 3);
        const unsigned c = (lane & 7) ^ (4 * (i & 1) + (l3 >> 1));
        vb1[i] = row * 4096 + c * 16 - 1024 * i;
      }
      vb1[4] = lane * 16 + ((q * 64 + (wave >> 1)) * 64 + 8 * ec + 4 * (wave & 1)) * 1024;
      vb2[0] = ve2b[0]; vb2[1] = ve2b[1];
      put_srd(sb, 0, q == 0 ? kp->wsre : kp->wsim, (unsigned)(S_ * S_ * 2));
      put_srd(sb, 4, ws + WS_AB, 8u * 1024 * 1024);
#pragma unroll
      for (int i = 0; i < 16; ++i) sb[i] = rfl(sb[i]);
      sb2[0] = 0; sb2[1] = 14; sb2[2] = 0; sb2[3] = 0;
      asm volatile(
      "s_nop 4\n\t"
      "s_waitcnt vmcnt(0)\n\t"
      "v_accvgpr_read_b32 v28, a0\n\t"
      "v_accvgpr_read_b32 v29, a1\n\t"
      "v_accvgpr_read_b32 v30, a2\n\t"
      "v_accvgpr_read_b32 v31, a3\n\t"
      "s_mov_b64 exec, 1\n\t"
      "buffer_store_dwordx2 v[24:25], v26, s[56:59], 0 offen sc1\n\t"
      "s_mov_b64 exec, -1\n\t"
      "s_nop 1\n\t"
      "s_mov_b32 s75, 0x10000\n\t"
      "LBa_chk_%=:\n\t"
      "v_cmp_ne_u64 vcc, v[28:29], s[60:61]\n\t"
      "s_nop 4\n\t"
      "s_cbranch_vccz LBa_ok_%=\n\t"
      "s_sub_u32 s75, s75, 1\n\t"
      "s_cmp_eq_u32 s75, 0\n\t"
      "s_cbranch_scc1 LBa_ok_%=\n\t"
      "s_sleep 1\n\t"
      "buffer_load_dwordx2 v[28:29], v32, s[56:59], 0 offen sc1 ; alt\n\t"
      "s_waitcnt vmcnt(0)\n\t"
      "s_branch LBa_chk_%=\n\t"
      "LBa_ok_%=:\n\t"
      "s_mov_b32 s75, 0x10000\n\t"
      "LBb_chk_%=:\n\t"
      "v_cmp_ne_u64 vcc, v[30:31], s[60:61]\n\t"
      "s_nop 4\n\t"
      "s_cbranch_vccz LBb_ok_%=\n\t"
      "s_sub_u32 s75, s75, 1\n\t"
      "s_cmp_eq_u32 s75, 0\n\t"
      "s_cbranch_scc1 LBb_ok_%=\n\t"
      "s_sleep 1\n\t"
      "buffer_load_dwordx2 v[30:31], v33, s[56:59], 0 offen sc1 ; alt\n\t"
      "s_waitcnt vmcnt(0)\n\t"
      "s_branch LBb_chk_%=\n\t"
      "LBb_ok_%=:\n\t"
      "s_mov_b32 s49, 0\n\t"
      "s_add_u32 m0, s45, 0\n\t"
      "s_nop 0\n\t"
      "buffer_load_dwordx4 v20, s[40:43], s49 offen sc1 lds\n\t"
      "v_mov_b32 v128, 0\n\t"
      "v_mov_b32 v129, 0\n\t"
      "v_mov_b32 v130, 0\n\t"
      "v_mov_b32 v131, 0\n\t"
      "v_mov_b32 v132, 0\n\t"
      "v_mov_b32 v133, 0\n\t"
      "v_mov_b32 v134, 0\n\t"
      "buffer_load_dwordx4 v20, s[40:43], s49 offen offset:1024 sc1 lds\n\t"
      "v_mov_b32 v135, 0\n\t"
      "v_mov_b32 v136, 0\n\t"
      "v_mov_b32 v137, 0\n\t"
      "v_mov_b32 v138, 0\n\t"
      "v_mov_b32 v139, 0\n\t"
      "v_mov_b32 v140, 0\n\t"
      "v_mov_b32 v141, 0\n\t"
      "buffer_load_dwordx4 v20, s[40:43], s49 offen offset:2048 sc1 lds\n\t"
      "v_mov_b32 v142, 0\n\t"
      "v_mov_b32 v143, 0\n\t"
      "v_mov_b32 v144, 0\n\t"
      "v_mov_b32 v145, 0\n\t"
      "v_mov_b32 v146, 0\n\t"
      "v_mov_b32 v147, 0\n\t"
      "v_mov_b32 v148, 0\n\t"
      "buffer_load_dwordx4 v20, s[40:43], s49 offen offset:3072 sc1 lds\n\t"
      "v_mov_b32 v149, 0\n\t"
      "v_mov_b32 v150, 0\n\t"
      "v_mov_b32 v151, 0\n\t"
      "v_mov_b32 v152, 0\n\t"
      "v_mov_b32 v153, 0\n\t"
      "v_mov_b32 v154, 0\n\t"
      "v_mov_b32 v155, 0\n\t"
      "s_mov_b32 s49, 131072\n\t"
      "s_add_u32 m0, s45, 49152\n\t"
      "s_nop 0\n\t"
      "buffer_load_dwordx4 v20, s[40:43], s49 offen sc1 lds\n\t"
      "v_mov_b32 v156, 0\n\t"
      "v_mov_b32 v157, 0\n\t"
      "v_mov_b32 v158, 0\n\t"
      "v_mov_b32 v159, 0\n\t"
      "v_mov_b32 v160, 0\n\t"
      "v_mov_b32 v161, 0\n\t"
      "v_mov_b32 v162, 0\n\t"
      "buffer_load_dwordx4 v20, s[40:43], s49 offen offset:1024 sc1 lds\n\t"
      "v_mov_b32 v163, 0\n\t"
      "v_mov_b32 v164, 0\n\t"
      "v_mov_b32 v165, 0\n\t"
      "v_mov_b32 v166, 0\n\t"
      "v_mov_b32 v167, 0\n\t"
      "v_mov_b32 v168, 0\n\t"
      "v_mov_b32 v169, 0\n\t"
      "buffer_load_dwordx4 v20, s[40:43], s49 offen offset:2048 sc1 lds\n\t"
      "v_mov_b32 v170, 0\n\t"
      "v_mov_b32 v171, 0\n\t"
      "v_mov_b32 v172, 0\n\t"
      "v_mov_b32 v173, 0\n\t"
      "v_mov_b32 v174, 0\n\t"
      "v_mov_b32 v175, 0\n\t"
      "v_mov_b32 v176, 0\n\t"
      "buffer_load_dwordx4 v20, s[40:43], s49 offen offset:3072 sc1 lds\n\t"
      "v_mov_b32 v177, 0\n\t"
      "v_mov_b32 v178, 0\n\t"
      "v_mov_b32 v179, 0\n\t"
      "v_mov_b32 v180, 0\n\t"
      "v_mov_b32 v181, 0\n\t"
      "v_mov_b32 v182, 0\n\t"
      "v_mov_b32 v183, 0\n\t"
      "s_barrier\n\t"
      "s_mov_b32 s48, 256\n\t"
      "s_mov_b32 s55, 131328\n\t"
      "s_mov_b32 s49, 262144\n\t"
      "s_add_u32 m0, s44, 98304\n\t"
      "s_nop 0\n\t"
      "buffer_load_dwordx4 v16, s[36:39], s48 offen lds\n\t"
      "v_mov_b32 v184, 0\n\t"
      "v_mov_b32 v185, 0\n\t"
      "v_mov_b32 v186, 0\n\t"
      "v_mov_b32 v187, 0\n\t"
      "v_mov_b32 v188, 0\n\t"
      "v_mov_b32 v189, 0\n\t"
      "v_mov_b32 v190, 0\n\t"
      "buffer_load_dwordx4 v17, s[36:39], s48 offen offset:1024 lds\n\t"
      "v_mov_b32 v191, 0\n\t"
      "v_mov_b32 v192, 0\n\t"
      "v_mov_b32 v193, 0\n\t"
      "v_mov_b32 v194, 0\n\t"
      "v_mov_b32 v195, 0\n\t"
      "v_mov_b32 v196, 0\n\t"
      "v_mov_b32 v197, 0\n\t"
      "buffer_load_dwordx4 v18, s[36:39], s48 offen offset:2048 lds\n\t"
      "v_mov_b32 v198, 0\n\t"
      "v_mov_b32 v199, 0\n\t"
      "v_mov_b32 v200, 0\n\t"
      "v_mov_b32 v201, 0\n\t"
      "v_mov_b32 v202, 0\n\t"
      "v_mov_b32 v203, 0\n\t"
      "v_mov_b32 v204, 0\n\t"
      "buffer_load_dwordx4 v19, s[36:39], s48 offen offset:3072 lds\n\t"
      "v_mov_b32 v205, 0\n\t"
      "v_mov_b32 v206, 0\n\t"
      "v_mov_b32 v207, 0\n\t"
      "v_mov_b32 v208, 0\n\t"
      "v_mov_b32 v209, 0\n\t"
      "v_mov_b32 v210, 0\n\t"
      "v_mov_b32 v211, 0\n\t"
      "s_add_u32 m0, s44, 102400\n\t"
      "s_nop 0\n\t"
      "buffer_load_dwordx4 v16, s[36:39], s55 offen lds\n\t"
      "v_mov_b32 v212, 0\n\t"
      "v_mov_b32 v213, 0\n\t"
      "v_mov_b32 v214, 0\n\t"
      "v_mov_b32 v215, 0\n\t"
      "v_mov_b32 v216, 0\n\t"
      "v_mov_b32 v217, 0\n\t"
      "v_mov_b32 v218, 0\n\t"
      "buffer_load_dwordx4 v17, s[36:39], s55 offen offset:1024 lds\n\t"
      "v_mov_b32 v219, 0\n\t"
      "v_mov_b32 v220, 0\n\t"
      "v_mov_b32 v221, 0\n\t"
      "v_mov_b32 v222, 0\n\t"
      "v_mov_b32 v223, 0\n\t"
      "v_mov_b32 v224, 0\n\t"
      "v_mov_b32 v225, 0\n\t"
      "buffer_load_dwordx4 v18, s[36:39], s55 offen offset:2048 lds\n\t"
      "v_mov_b32 v226, 0\n\t"
      "v_mov_b32 v227, 0\n\t"
      "v_mov_b32 v228, 0\n\t"
      "v_mov_b32 v229, 0\n\t"
      "v_mov_b32 v230, 0\n\t"
      "v_mov_b32 v231, 0\n\t"
      "v_mov_b32 v232, 0\n\t"
      "buffer_load_dwordx4 v19, s[36:39], s55 offen offset:3072 lds\n\t"
      "v_mov_b32 v233, 0\n\t"
      "v_mov_b32 v234, 0\n\t"
      "v_mov_b32 v235, 0\n\t"
      "v_mov_b32 v236, 0\n\t"
      "v_mov_b32 v237, 0\n\t"
      "v_mov_b32 v238, 0\n\t"
      "v_mov_b32 v239, 0\n\t"
      "s_add_u32 m0, s45, 98304\n\t"
      "s_nop 0\n\t"
      "buffer_load_dwordx4 v20, s[40:43], s49 offen sc1 lds\n\t"
      "v_mov_b32 v240, 0\n\t"
      "v_mov_b32 v241, 0\n\t"
      "v_mov_b32 v242, 0\n\t"
      "v_mov_b32 v243, 0\n\t"
      "v_mov_b32 v244, 0\n\t"
      "v_mov_b32 v245, 0\n\t"
      "v_mov_b32 v246, 0\n\t"
      "buffer_load_dwordx4 v20, s[40:43], s49 offen offset:1024 sc1 lds\n\t"
      "v_mov_b32 v247, 0\n\t"
      "v_mov_b32 v248, 0\n\t"
      "v_mov_b32 v249, 0\n\t"
      "v_mov_b32 v250, 0\n\t"
      "v_mov_b32 v251, 0\n\t"
      "v_mov_b32 v252, 0\n\t"
      "v_mov_b32 v253, 0\n\t"
      "buffer_load_dwordx4 v20, s[40:43], s49 offen offset:2048 sc1 lds\n\t"
      "v_mov_b32 v254, 0\n\t"
      "v_mov_b32 v255, 0\n\t"
      "buffer_load_dwordx4 v20, s[40:43], s49 offen offset:3072 sc1 lds\n\t"
      "s_mov_b32 s48, 384\n\t"
      "s_mov_b32 s49, 393216\n\t"
      "s_mov_b32 s50, 0\n\t"
      "s_waitcnt vmcnt(16)\n\t"
      "s_barrier\n\t"
      "ds_read_b128 a[0:3], v0 offset:0\n\t"
      "ds_read_b128 a[4:7], v0 offset:2048\n\t"
      "ds_read_b128 a[8:11], v2 offset:0\n\t"
      "ds_read_b128 a[12:15], v2 offset:2048\n\t"
      "ds_read_b128 a[16:19], v4 offset:0\n\t"
      "ds_read_b128 a[20:23], v4 offset:2048\n\t"
      "ds_read_b128 a[24:27], v6 offset:0\n\t"
      "ds_read_b128 a[28:31], v6 offset:2048\n\t"
      "ds_read_b128 a[32:35], v1 offset:0\n\t"
      "ds_read_b128 a[36:39], v1 offset:2048\n\t"
      "ds_read_b128 a[40:43], v3 offset:0\n\t"
      "ds_read_b128 a[44:47], v3 offset:2048\n\t"
      "ds_read_b128 a[48:51], v5 offset:0\n\t"
      "ds_read_b128 a[52:55], v5 offset:2048\n\t"
      "ds_read_b128 a[56:59], v7 offset:0\n\t"
      "ds_read_b128 a[60:63], v7 offset:2048\n\t"
      "ds_read_b128 a[64:67], v8 offset:0\n\t"
      "ds_read_b128 a[68:71], v8 offset:1024\n\t"
      "ds_read_b128 a[72:75], v8 offset:2048\n\t"
      "ds_read_b128 a[76:79], v8 offset:3072\n\t"
      "ds_read_b128 a[80:83], v8 offset:8192\n\t"
      "ds_read_b128 a[84:87], v8 offset:9216\n\t"
      "ds_read_b128 a[88:91], v8 offset:10240\n\t"
      "ds_read_b128 a[92:95], v8 offset:11264\n\t"
      "LB_loop_%=:\n\t"
      "s_waitcnt vmcnt(12) lgkmcnt(0)\n\t"
      "s_barrier\n\t"
      "v_mfma_f32_16x16x32_bf16 v[128:131], a[64:67], a[0:3], v[128:131]\n\t"
      "s_mov_b32 s51, s50\n\t"
      "s_add_u32 s50, s50, 49152\n\t"
      "s_cmp_eq_u32 s50, 147456\n\t"
      "v_mfma_f32_16x16x32_bf16 v[128:131], a[80:83], a[32:35], v[128:131]\n\t"
      "s_cselect_b32 s50, 0, s50\n\t"
      "s_sub_u32 s52, s50, s51\n\t"
      "v_mfma_f32_16x16x32_bf16 v[144:147], a[64:67], a[4:7], v[144:147]\n\t"
      "v_add_u32 v0, s52, v0\n\t"
      "v_add_u32 v1, s52, v1\n\t"
      "s_add_u32 s55, s48, 131072\n\t"
      "s_add_u32 s54, s44, s51\n\t"
      "v_mfma_f32_16x16x32_bf16 v[144:147], a[80:83], a[36:39], v[144:147]\n\t"
      "v_add_u32 v2, s52, v2\n\t"
      "v_add_u32 v3, s52, v3\n\t"
      "s_mov_b32 m0, s54\n\t"
      "v_mfma_f32_16x16x32_bf16 v[160:163], a[64:67], a[8:11], v[160:163]\n\t"
      "v_add_u32 v4, s52, v4\n\t"
      "v_add_u32 v5, s52, v5\n\t"
      "buffer_load_dwordx4 v16, s[36:39], s48 offen lds\n\t"
      "v_mfma_f32_16x16x32_bf16 v[160:163], a[80:83], a[40:43], v[160:163]\n\t"
      "v_add_u32 v6, s52, v6\n\t"
      "v_add_u32 v7, s52, v7\n\t"
      "v_mfma_f32_16x16x32_bf16 v[176:179], a[64:67], a[12:15], v[176:179]\n\t"
      "v_add_u32 v8, s52, v8\n\t"
      "v_mfma_f32_16x16x32_bf16 v[176:179], a[80:83], a[44:47], v[176:179]\n\t"
      "ds_read_b128 a[96:99], v0 offset:0\n\t"
      "buffer_load_dwordx4 v17, s[36:39], s48 offen offset:1024 lds\n\t"
      "v_mfma_f32_16x16x32_bf16 v[192:195], a[64:67], a[16:19], v[192:195]\n\t"
      "v_mfma_f32_16x16x32_bf16 v[192:195], a[80:83], a[48:51], v[192:195]\n\t"
      "ds_read_b128 a[100:103], v0 offset:2048\n\t"
      "v_mfma_f32_16x16x32_bf16 v[208:211], a[64:67], a[20:23], v[208:211]\n\t"
      "v_mfma_f32_16x16x32_bf16 v[208:211], a[80:83], a[52:55], v[208:211]\n\t"
      "ds_read_b128 a[104:107], v2 offset:0\n\t"
      "buffer_load_dwordx4 v18, s[36:39], s48 offen offset:2048 lds\n\t"
      "v_mfma_f32_16x16x32_bf16 v[224:227], a[64:67], a[24:27], v[224:227]\n\t"
      "v_mfma_f32_16x16x32_bf16 v[224:227], a[80:83], a[56:59], v[224:227]\n\t"
      "ds_read_b128 a[108:111], v2 offset:2048\n\t"
      "v_mfma_f32_16x16x32_bf16 v[240:243], a[64:67], a[28:31], v[240:243]\n\t"
      "v_mfma_f32_16x16x32_bf16 v[240:243], a[80:83], a[60:63], v[240:243]\n\t"
      "ds_read_b128 a[112:115], v4 offset:0\n\t"
      "buffer_load_dwordx4 v19, s[36:39], s48 offen offset:3072 lds\n\t"
      "v_mfma_f32_16x16x32_bf16 v[244:247], a[68:71], a[28:31], v[244:247]\n\t"
      "v_mfma_f32_16x16x32_bf16 v[244:247], a[84:87], a[60:63], v[244:247]\n\t"
      "ds_read_b128 a[116:119], v4 offset:2048\n\t"
      "s_add_u32 m0, s54, 4096\n\t"
      "v_mfma_f32_16x16x32_bf16 v[228:231], a[68:71], a[24:27], v[228:231]\n\t"
      "buffer_load_dwordx4 v16, s[36:39], s55 offen lds\n\t"
      "v_mfma_f32_16x16x32_bf16 v[228:231], a[84:87], a[56:59], v[228:231]\n\t"
      "v_mfma_f32_16x16x32_bf16 v[212:215], a[68:71], a[20:23], v[212:215]\n\t"
      "ds_read_b128 a[120:123], v6 offset:0\n\t"
      "v_mfma_f32_16x16x32_bf16 v[212:215], a[84:87], a[52:55], v[212:215]\n\t"
      "v_mfma_f32_16x16x32_bf16 v[196:199], a[68:71], a[16:19], v[196:199]\n\t"
      "ds_read_b128 a[124:127], v6 offset:2048\n\t"
      "buffer_load_dwordx4 v17, s[36:39], s55 offen offset:1024 lds\n\t"
      "v_mfma_f32_16x16x32_bf16 v[196:199], a[84:87], a[48:51], v[196:199]\n\t"
      "v_mfma_f32_16x16x32_bf16 v[180:183], a[68:71], a[12:15], v[180:183]\n\t"
      "ds_read_b128 a[128:131], v1 offset:0\n\t"
      "v_mfma_f32_16x16x32_bf16 v[180:183], a[84:87], a[44:47], v[180:183]\n\t"
      "v_mfma_f32_16x16x32_bf16 v[164:167], a[68:71], a[8:11], v[164:167]\n\t"
      "ds_read_b128 a[132:135], v1 offset:2048\n\t"
      "buffer_load_dwordx4 v18, s[36:39], s55 offen offset:2048 lds\n\t"
      "v_mfma_f32_16x16x32_bf16 v[164:167], a[84:87], a[40:43], v[164:167]\n\t"
      "v_mfma_f32_16x16x32_bf16 v[148:151], a[68:71], a[4:7], v[148:151]\n\t"
      "ds_read_b128 a[136:139], v3 offset:0\n\t"
      "v_mfma_f32_16x16x32_bf16 v[148:151], a[84:87], a[36:39], v[148:151]\n\t"
      "v_mfma_f32_16x16x32_bf16 v[132:135], a[68:71], a[0:3], v[132:135]\n\t"
      "ds_read_b128 a[140:143], v3 offset:2048\n\t"
      "buffer_load_dwordx4 v19, s[36:39], s55 offen offset:3072 lds\n\t"
      "v_mfma_f32_16x16x32_bf16 v[132:135], a[84:87], a[32:35], v[132:135]\n\t"
      "v_mfma_f32_16x16x32_bf16 v[136:139], a[72:75], a[0:3], v[136:139]\n\t"
      "v_mfma_f32_16x16x32_bf16 v[136:139], a[88:91], a[32:35], v[136:139]\n\t"
      "ds_read_b128 a[144:147], v5 offset:0\n\t"
      "v_mfma_f32_16x16x32_bf16 v[152:155], a[72:75], a[4:7], v[152:155]\n\t"
      "s_add_u32 m0, s45, s51\n\t"
      "v_mfma_f32_16x16x32_bf16 v[152:155], a[88:91], a[36:39], v[152:155]\n\t"
      "ds_read_b128 a[148:151], v5 offset:2048\n\t"
      "buffer_load_dwordx4 v20, s[40:43], s49 offen sc1 lds\n\t"
      "v_mfma_f32_16x16x32_bf16 v[168:171], a[72:75], a[8:11], v[168:171]\n\t"
      "v_mfma_f32_16x16x32_bf16 v[168:171], a[88:91], a[40:43], v[168:171]\n\t"
      "ds_read_b128 a[152:155], v7 offset:0\n\t"
      "v_mfma_f32_16x16x32_bf16 v[184:187], a[72:75], a[12:15], v[184:187]\n\t"
      "v_mfma_f32_16x16x32_bf16 v[184:187], a[88:91], a[44:47], v[184:187]\n\t"
      "ds_read_b128 a[156:159], v7 offset:2048\n\t"
      "v_mfma_f32_16x16x32_bf16 v[200:203], a[72:75], a[16:19], v[200:203]\n\t"
      "v_mfma_f32_16x16x32_bf16 v[200:203], a[88:91], a[48:51], v[200:203]\n\t"
      "ds_read_b128 a[160:163], v8 offset:0\n\t"
      "v_mfma_f32_16x16x32_bf16 v[216:219], a[72:75], a[20:23], v[216:219]\n\t"
      "buffer_load_dwordx4 v20, s[40:43], s49 offen offset:1024 sc1 lds\n\t"
      "v_mfma_f32_16x16x32_bf16 v[216:219], a[88:91], a[52:55], v[216:219]\n\t"
      "ds_read_b128 a[164:167], v8 offset:1024\n\t"
      "v_mfma_f32_16x16x32_bf16 v[232:235], a[72:75], a[24:27], v[232:235]\n\t"
      "v_mfma_f32_16x16x32_bf16 v[232:235], a[88:91], a[56:59], v[232:235]\n\t"
      "v_mfma_f32_16x16x32_bf16 v[248:251], a[72:75], a[28:31], v[248:251]\n\t"
      "ds_read_b128 a[168:171], v8 offset:2048\n\t"
      "v_mfma_f32_16x16x32_bf16 v[248:251], a[88:91], a[60:63], v[248:251]\n\t"
      "v_mfma_f32_16x16x32_bf16 v[252:255], a[76:79], a[28:31], v[252:255]\n\t"
      "ds_read_b128 a[172:175], v8 offset:3072\n\t"
      "v_mfma_f32_16x16x32_bf16 v[252:255], a[92:95], a[60:63], v[252:255]\n\t"
      "buffer_load_dwordx4 v20, s[40:43], s49 offen offset:2048 sc1 lds\n\t"
      "v_mfma_f32_16x16x32_bf16 v[236:239], a[76:79], a[24:27], v[236:239]\n\t"
      "ds_read_b128 a[176:179], v8 offset:8192\n\t"
      "v_mfma_f32_16x16x32_bf16 v[236:239], a[92:95], a[56:59], v[236:239]\n\t"
      "v_mfma_f32_16x16x32_bf16 v[220:223], a[76:79], a[20:23], v[220:223]\n\t"
      "ds_read_b128 a[180:183], v8 offset:9216\n\t"
      "v_mfma_f32_16x16x32_bf16 v[220:223], a[92:95], a[52:55], v[220:223]\n\t"
      "v_mfma_f32_16x16x32_bf16 v[204:207], a[76:79], a[16:19], v[204:207]\n\t"
      "ds_read_b128 a[184:187], v8 offset:10240\n\t"
      "v_mfma_f32_16x16x32_bf16 v[204:207], a[92:95], a[48:51], v[204:207]\n\t"
      "v_mfma_f32_16x16x32_bf16 v[188:191], a[76:79], a[12:15], v[188:191]\n\t"
      "buffer_load_dwordx4 v20, s[40:43], s49 offen offset:3072 sc1 lds\n\t"
      "v_mfma_f32_16x16x32_bf16 v[188:191], a[92:95], a[44:47], v[188:191]\n\t"
      "ds_read_b128 a[188:191], v8 offset:11264\n\t"
      "v_mfma_f32_16x16x32_bf16 v[172:175], a[76:79], a[8:11], v[172:175]\n\t"
      "s_add_u32 s48, s48, 128\n\t"
      "s_add_u32 s49, s49, 131072\n\t"
      "v_mfma_f32_16x16x32_bf16 v[172:175], a[92:95], a[40:43], v[172:175]\n\t"
      "v_mfma_f32_16x16x32_bf16 v[156:159], a[76:79], a[4:7], v[156:159]\n\t"
      "v_mfma_f32_16x16x32_bf16 v[156:159], a[92:95], a[36:39], v[156:159]\n\t"
      "v_mfma_f32_16x16x32_bf16 v[140:143], a[76:79], a[0:3], v[140:143]\n\t"
      "v_mfma_f32_16x16x32_bf16 v[140:143], a[92:95], a[32:35], v[140:143]\n\t"
      "s_waitcnt vmcnt(12) lgkmcnt(0)\n\t"
      "s_barrier\n\t"
      "v_mfma_f32_16x16x32_bf16 v[128:131], a[160:163], a[96:99], v[128:131]\n\t"
      "s_mov_b32 s51, s50\n\t"
      "s_add_u32 s50, s50, 49152\n\t"
      "s_cmp_eq_u32 s50, 147456\n\t"
      "v_mfma_f32_16x16x32_bf16 v[128:131], a[176:179], a[128:131], v[128:131]\n\t"
      "s_cselect_b32 s50, 0, s50\n\t"
      "s_sub_u32 s52, s50, s51\n\t"
      "v_mfma_f32_16x16x32_bf16 v[144:147], a[160:163], a[100:103], v[144:147]\n\t"
      "v_add_u32 v0, s52, v0\n\t"
      "v_add_u32 v1, s52, v1\n\t"
      "s_add_u32 s55, s48, 131072\n\t"
      "s_add_u32 s54, s44, s51\n\t"
      "v_mfma_f32_16x16x32_bf16 v[144:147], a[176:179], a[132:135], v[144:147]\n\t"
      "v_add_u32 v2, s52, v2\n\t"
      "v_add_u32 v3, s52, v3\n\t"
      "s_mov_b32 m0, s54\n\t"
      "v_mfma_f32_16x16x32_bf16 v[160:163], a[160:163], a[104:107], v[160:163]\n\t"
      "v_add_u32 v4, s52, v4\n\t"
      "v_add_u32 v5, s52, v5\n\t"
      "buffer_load_dwordx4 v16, s[36:39], s48 offen lds\n\t"
      "v_mfma_f32_16x16x32_bf16 v[160:163], a[176:179], a[136:139], v[160:163]\n\t"
      "v_add_u32 v6, s52, v6\n\t"
      "v_add_u32 v7, s52, v7\n\t"
      "v_mfma_f32_16x16x32_bf16 v[176:179], a[160:163], a[108:111], v[176:179]\n\t"
      "v_add_u32 v8, s52, v8\n\t"
      "v_mfma_f32_16x16x32_bf16 v[176:179], a[176:179], a[140:143], v[176:179]\n\t"
      "ds_read_b128 a[0:3], v0 offset:0\n\t"
      "buffer_load_dwordx4 v17, s[36:39], s48 offen offset:1024 lds\n\t"
      "v_mfma_f32_16x16x32_bf16 v[192:195], a[160:163], a[112:115], v[192:195]\n\t"
      "v_mfma_f32_16x16x32_bf16 v[192:195], a[176:179], a[144:147], v[192:195]\n\t"
      "ds_read_b128 a[4:7], v0 offset:2048\n\t"
      "v_mfma_f32_16x16x32_bf16 v[208:211], a[160:163], a[116:119], v[208:211]\n\t"
      "v_mfma_f32_16x16x32_bf16 v[208:211], a[176:179], a[148:151], v[208:211]\n\t"
      "ds_read_b128 a[8:11], v2 offset:0\n\t"
      "buffer_load_dwordx4 v18, s[36:39], s48 offen offset:2048 lds\n\t"
      "v_mfma_f32_16x16x32_bf16 v[224:227], a[160:163], a[120:123], v[224:227]\n\t"
      "v_mfma_f32_16x16x32_bf16 v[224:227], a[176:179], a[152:155], v[224:227]\n\t"
      "ds_read_b128 a[12:15], v2 offset:2048\n\t"
      "v_mfma_f32_16x16x32_bf16 v[240:243], a[160:163], a[124:127], v[240:243]\n\t"
      "v_mfma_f32_16x16x32_bf16 v[240:243], a[176:179], a[156:159], v[240:243]\n\t"
      "ds_read_b128 a[16:19], v4 offset:0\n\t"
      "buffer_load_dwordx4 v19, s[36:39], s48 offen offset:3072 lds\n\t"
      "v_mfma_f32_16x16x32_bf16 v[244:247], a[164:167], a[124:127], v[244:247]\n\t"
      "v_mfma_f32_16x16x32_bf16 v[244:247], a[180:183], a[156:159], v[244:247]\n\t"
      "ds_read_b128 a[20:23], v4 offset:2048\n\t"
      "s_add_u32 m0, s54, 4096\n\t"
      "v_mfma_f32_16x16x32_bf16 v[228:231], a[164:167], a[120:123], v[228:231]\n\t"
      "buffer_load_dwordx4 v16, s[36:39], s55 offen lds\n\t"
      "v_mfma_f32_16x16x32_bf16 v[228:231], a[180:183], a[152:155], v[228:231]\n\t"
      "v_mfma_f32_16x16x32_bf16 v[212:215], a[164:167], a[116:119], v[212:215]\n\t"
      "ds_read_b128 a[24:27], v6 offset:0\n\t"
      "v_mfma_f32_16x16x32_bf16 v[212:215], a[180:183], a[148:151], v[212:215]\n\t"
      "v_mfma_f32_16x16x32_bf16 v[196:199], a[164:167], a[112:115], v[196:199]\n\t"
      "ds_read_b128 a[28:31], v6 offset:2048\n\t"
      "buffer_load_dwordx4 v17, s[36:39], s55 offen offset:1024 lds\n\t"
      "v_mfma_f32_16x16x32_bf16 v[196:199], a[180:183], a[144:147], v[196:199]\n\t"
      "v_mfma_f32_16x16x32_bf16 v[180:183], a[164:167], a[108:111], v[180:183]\n\t"
      "ds_read_b128 a[32:35], v1 offset:0\n\t"
      "v_mfma_f32_16x16x32_bf16 v[180:183], a[180:183], a[140:143], v[180:183]\n\t"
      "v_mfma_f32_16x16x32_bf16 v[164:167], a[164:167], a[104:107], v[164:167]\n\t"
      "ds_read_b128 a[36:39], v1 offset:2048\n\t"
      "buffer_load_dwordx4 v18, s[36:39], s55 offen offset:2048 lds\n\t"
      "v_mfma_f32_16x16x32_bf16 v[164:167], a[180:183], a[136:139], v[164:167]\n\t"
      "v_mfma_f32_16x16x32_bf16 v[148:151], a[164:167], a[100:103], v[148:151]\n\t"
      "ds_read_b128 a[40:43], v3 offset:0\n\t"
      "v_mfma_f32_16x16x32_bf16 v[148:151], a[180:183], a[132:135], v[148:151]\n\t"
      "v_mfma_f32_16x16x32_bf16 v[132:135], a[164:167], a[96:99], v[132:135]\n\t"
      "ds_read_b128 a[44:47], v3 offset:2048\n\t"
      "buffer_load_dwordx4 v19, s[36:39], s55 offen offset:3072 lds\n\t"
      "v_mfma_f32_16x16x32_bf16 v[132:135], a[180:183], a[128:131], v[132:135]\n\t"
      "v_mfma_f32_16x16x32_bf16 v[136:139], a[168:171], a[96:99], v[136:139]\n\t"
      "v_mfma_f32_16x16x32_bf16 v[136:139], a[184:187], a[128:131], v[136:139]\n\t"
      "ds_read_b128 a[48:51], v5 offset:0\n\t"
      "v_mfma_f32_16x16x32_bf16 v[152:155], a[168:171], a[100:103], v[152:155]\n\t"
      "s_add_u32 m0, s45, s51\n\t"
      "v_mfma_f32_16x16x32_bf16 v[152:155], a[184:187], a[132:135], v[152:155]\n\t"
      "ds_read_b128 a[52:55], v5 offset:2048\n\t"
      "buffer_load_dwordx4 v20, s[40:43], s49 offen sc1 lds\n\t"
      "v_mfma_f32_16x16x32_bf16 v[168:171], a[168:171], a[104:107], v[168:171]\n\t"
      "v_mfma_f32_16x16x32_bf16 v[168:171], a[184:187], a[136:139], v[168:171]\n\t"
      "ds_read_b128 a[56:59], v7 offset:0\n\t"
      "v_mfma_f32_16x16x32_bf16 v[184:187], a[168:171], a[108:111], v[184:187]\n\t"
      "v_mfma_f32_16x16x32_bf16 v[184:187], a[184:187], a[140:143], v[184:187]\n\t"
      "ds_read_b128 a[60:63], v7 offset:2048\n\t"
      "v_mfma_f32_16x16x32_bf16 v[200:203], a[168:171], a[112:115], v[200:203]\n\t"
      "v_mfma_f32_16x16x32_bf16 v[200:203], a[184:187], a[144:147], v[200:203]\n\t"
      "ds_read_b128 a[64:67], v8 offset:0\n\t"
      "v_mfma_f32_16x16x32_bf16 v[216:219], a[168:171], a[116:119], v[216:219]\n\t"
      "buffer_load_dwordx4 v20, s[40:43], s49 offen offset:1024 sc1 lds\n\t"
      "v_mfma_f32_16x16x32_bf16 v[216:219], a[184:187], a[148:151], v[216:219]\n\t"
      "ds_read_b128 a[68:71], v8 offset:1024\n\t"
      "v_mfma_f32_16x16x32_bf16 v[232:235], a[168:171], a[120:123], v[232:235]\n\t"
      "v_mfma_f32_16x16x32_bf16 v[232:235], a[184:187], a[152:155], v[232:235]\n\t"
      "v_mfma_f32_16x16x32_bf16 v[248:251], a[168:171], a[124:127], v[248:251]\n\t"
      "ds_read_b128 a[72:75], v8 offset:2048\n\t"
      "v_mfma_f32_16x16x32_bf16 v[248:251], a[184:187], a[156:159], v[248:251]\n\t"
      "v_mfma_f32_16x16x32_bf16 v[252:255], a[172:175], a[124:127], v[252:255]\n\t"
      "ds_read_b128 a[76:79], v8 offset:3072\n\t"
      "v_mfma_f32_16x16x32_bf16 v[252:255], a[188:191], a[156:159], v[252:255]\n\t"
      "buffer_load_dwordx4 v20, s[40:43], s49 offen offset:2048 sc1 lds\n\t"
      "v_mfma_f32_16x16x32_bf16 v[236:239], a[172:175], a[120:123], v[236:239]\n\t"
      "ds_read_b128 a[80:83], v8 offset:8192\n\t"
      "v_mfma_f32_16x16x32_bf16 v[236:239], a[188:191], a[152:155], v[236:239]\n\t"
      "v_mfma_f32_16x16x32_bf16 v[220:223], a[172:175], a[116:119], v[220:223]\n\t"
      "ds_read_b128 a[84:87], v8 offset:9216\n\t"
      "v_mfma_f32_16x16x32_bf16 v[220:223], a[188:191], a[148:151], v[220:223]\n\t"
      "v_mfma_f32_16x16x32_bf16 v[204:207], a[172:175], a[112:115], v[204:207]\n\t"
      "ds_read_b128 a[88:91], v8 offset:10240\n\t"
      "v_mfma_f32_16x16x32_bf16 v[204:207], a[188:191], a[144:147], v[204:207]\n\t"
      "v_mfma_f32_16x16x32_bf16 v[188:191], a[172:175], a[108:111], v[188:191]\n\t"
      "buffer_load_dwordx4 v20, s[40:43], s49 offen offset:3072 sc1 lds\n\t"
      "v_mfma_f32_16x16x32_bf16 v[188:191], a[188:191], a[140:143], v[188:191]\n\t"
      "ds_read_b128 a[92:95], v8 offset:11264\n\t"
      "v_mfma_f32_16x16x32_bf16 v[172:175], a[172:175], a[104:107], v[172:175]\n\t"
      "s_add_u32 s48, s48, 128\n\t"
      "s_add_u32 s49, s49, 131072\n\t"
      "v_mfma_f32_16x16x32_bf16 v[172:175], a[188:191], a[136:139], v[172:175]\n\t"
      "v_mfma_f32_16x16x32_bf16 v[156:159], a[172:175], a[100:103], v[156:159]\n\t"
      "s_sub_u32 s53, s53, 1\n\t"
      "v_mfma_f32_16x16x32_bf16 v[156:159], a[188:191], a[132:135], v[156:159]\n\t"
      "v_mfma_f32_16x16x32_bf16 v[140:143], a[172:175], a[96:99], v[140:143]\n\t"
      "s_cmp_lg_u32 s53, 0\n\t"
      "v_mfma_f32_16x16x32_bf16 v[140:143], a[188:191], a[128:131], v[140:143]\n\t"
      "s_cbranch_scc1 LB_loop_%=\n\t"
      "s_waitcnt vmcnt(12) lgkmcnt(0)\n\t"
      "s_barrier\n\t"
      "v_mfma_f32_16x16x32_bf16 v[128:131], a[64:67], a[0:3], v[128:131]\n\t"
      "s_mov_b32 s51, s50\n\t"
      "s_add_u32 s50, s50, 49152\n\t"
      "s_cmp_eq_u32 s50, 147456\n\t"
      "v_mfma_f32_16x16x32_bf16 v[128:131], a[80:83], a[32:35], v[128:131]\n\t"
      "s_cselect_b32 s50, 0, s50\n\t"
      "s_sub_u32 s52, s50, s51\n\t"
      "v_mfma_f32_16x16x32_bf16 v[144:147], a[64:67], a[4:7], v[144:147]\n\t"
      "v_add_u32 v0, s52, v0\n\t"
      "v_add_u32 v1, s52, v1\n\t"
      "s_add_u32 s55, s48, 131072\n\t"
      "s_add_u32 s54, s44, s51\n\t"
      "v_mfma_f32_16x16x32_bf16 v[144:147], a[80:83], a[36:39], v[144:147]\n\t"
      "v_add_u32 v2, s52, v2\n\t"
      "v_add_u32 v3, s52, v3\n\t"
      "s_mov_b32 m0, s54\n\t"
      "v_mfma_f32_16x16x32_bf16 v[160:163], a[64:67], a[8:11], v[160:163]\n\t"
      "v_add_u32 v4, s52, v4\n\t"
      "v_add_u32 v5, s52, v5\n\t"
      "buffer_load_dwordx4 v16, s[36:39], s48 offen lds\n\t"
      "v_mfma_f32_16x16x32_bf16 v[160:163], a[80:83], a[40:43], v[160:163]\n\t"
      "v_add_u32 v6, s52, v6\n\t"
      "v_add_u32 v7, s52, v7\n\t"
      "v_mfma_f32_16x16x32_bf16 v[176:179], a[64:67], a[12:15], v[176:179]\n\t"
      "v_add_u32 v8, s52, v8\n\t"
      "v_mfma_f32_16x16x32_bf16 v[176:179], a[80:83], a[44:47], v[176:179]\n\t"
      "ds_read_b128 a[96:99], v0 offset:0\n\t"
      "buffer_load_dwordx4 v17, s[36:39], s48 offen offset:1024 lds\n\t"
      "v_mfma_f32_16x16x32_bf16 v[192:195], a[64:67], a[16:19], v[192:195]\n\t"
      "v_mfma_f32_16x16x32_bf16 v[192:195], a[80:83], a[48:51], v[192:195]\n\t"
      "ds_read_b128 a[100:103], v0 offset:2048\n\t"
      "v_mfma_f32_16x16x32_bf16 v[208:211], a[64:67], a[20:23], v[208:211]\n\t"
      "v_mfma_f32_16x16x32_bf16 v[208:211], a[80:83], a[52:55], v[208:211]\n\t"
      "ds_read_b128 a[104:107], v2 offset:0\n\t"
      "buffer_load_dwordx4 v18, s[36:39], s48 offen offset:2048 lds\n\t"
      "v_mfma_f32_16x16x32_bf16 v[224:227], a[64:67], a[24:27], v[224:227]\n\t"
      "v_mfma_f32_16x16x32_bf16 v[224:227], a[80:83], a[56:59], v[224:227]\n\t"
      "ds_read_b128 a[108:111], v2 offset:2048\n\t"
      "v_mfma_f32_16x16x32_bf16 v[240:243], a[64:67], a[28:31], v[240:243]\n\t"
      "v_mfma_f32_16x16x32_bf16 v[240:243], a[80:83], a[60:63], v[240:243]\n\t"
      "ds_read_b128 a[112:115], v4 offset:0\n\t"
      "buffer_load_dwordx4 v19, s[36:39], s48 offen offset:3072 lds\n\t"
      "v_mfma_f32_16x16x32_bf16 v[244:247], a[68:71], a[28:31], v[244:247]\n\t"
      "v_mfma_f32_16x16x32_bf16 v[244:247], a[84:87], a[60:63], v[244:247]\n\t"
      "ds_read_b128 a[116:119], v4 offset:2048\n\t"
      "s_add_u32 m0, s54, 4096\n\t"
      "v_mfma_f32_16x16x32_bf16 v[228:231], a[68:71], a[24:27], v[228:231]\n\t"
      "buffer_load_dwordx4 v16, s[36:39], s55 offen lds\n\t"
      "v_mfma_f32_16x16x32_bf16 v[228:231], a[84:87], a[56:59], v[228:231]\n\t"
      "v_mfma_f32_16x16x32_bf16 v[212:215], a[68:71], a[20:23], v[212:215]\n\t"
      "ds_read_b128 a[120:123], v6 offset:0\n\t"
      "v_mfma_f32_16x16x32_bf16 v[212:215], a[84:87], a[52:55], v[212:215]\n\t"
      "v_mfma_f32_16x16x32_bf16 v[196:199], a[68:71], a[16:19], v[196:199]\n\t"
      "ds_read_b128 a[124:127], v6 offset:2048\n\t"
      "buffer_load_dwordx4 v17, s[36:39], s55 offen offset:1024 lds\n\t"
      "v_mfma_f32_16x16x32_bf16 v[196:199], a[84:87], a[48:51], v[196:199]\n\t"
      "v_mfma_f32_16x16x32_bf16 v[180:183], a[68:71], a[12:15], v[180:183]\n\t"
      "ds_read_b128 a[128:131], v1 offset:0\n\t"
      "v_mfma_f32_16x16x32_bf16 v[180:183], a[84:87], a[44:47], v[180:183]\n\t"
      "v_mfma_f32_16x16x32_bf16 v[164:167], a[68:71], a[8:11], v[164:167]\n\t"
      "ds_read_b128 a[132:135], v1 offset:2048\n\t"
      "buffer_load_dwordx4 v18, s[36:39], s55 offen offset:2048 lds\n\t"
      "v_mfma_f32_16x16x32_bf16 v[164:167], a[84:87], a[40:43], v[164:167]\n\t"
      "v_mfma_f32_16x16x32_bf16 v[148:151], a[68:71], a[4:7], v[148:151]\n\t"
      "ds_read_b128 a[136:139], v3 offset:0\n\t"
      "v_mfma_f32_16x16x32_bf16 v[148:151], a[84:87], a[36:39], v[148:151]\n\t"
      "v_mfma_f32_16x16x32_bf16 v[132:135], a[68:71], a[0:3], v[132:135]\n\t"
      "ds_read_b128 a[140:143], v3 offset:2048\n\t"
      "buffer_load_dwordx4 v19, s[36:39], s55 offen offset:3072 lds\n\t"
      "v_mfma_f32_16x16x32_bf16 v[132:135], a[84:87], a[32:35], v[132:135]\n\t"
      "v_mfma_f32_16x16x32_bf16 v[136:139], a[72:75], a[0:3], v[136:139]\n\t"
      "v_mfma_f32_16x16x32_bf16 v[136:139], a[88:91], a[32:35], v[136:139]\n\t"
      "ds_read_b128 a[144:147], v5 offset:0\n\t"
      "v_mfma_f32_16x16x32_bf16 v[152:155], a[72:75], a[4:7], v[152:155]\n\t"
      "s_add_u32 m0, s45, s51\n\t"
      "v_mfma_f32_16x16x32_bf16 v[152:155], a[88:91], a[36:39], v[152:155]\n\t"
      "ds_read_b128 a[148:151], v5 offset:2048\n\t"
      "buffer_load_dwordx4 v20, s[40:43], s49 offen sc1 lds\n\t"
      "v_mfma_f32_16x16x32_bf16 v[168:171], a[72:75], a[8:11], v[168:171]\n\t"
      "v_mfma_f32_16x16x32_bf16 v[168:171], a[88:91], a[40:43], v[168:171]\n\t"
      "ds_read_b128 a[152:155], v7 offset:0\n\t"
      "v_mfma_f32_16x16x32_bf16 v[184:187], a[72:75], a[12:15], v[184:187]\n\t"
      "v_mfma_f32_16x16x32_bf16 v[184:187], a[88:91], a[44:47], v[184:187]\n\t"
      "ds_read_b128 a[156:159], v7 offset:2048\n\t"
      "v_mfma_f32_16x16x32_bf16 v[200:203], a[72:75], a[16:19], v[200:203]\n\t"
      "v_mfma_f32_16x16x32_bf16 v[200:203], a[88:91], a[48:51], v[200:203]\n\t"
      "ds_read_b128 a[160:163], v8 offset:0\n\t"
      "v_mfma_f32_16x16x32_bf16 v[216:219], a[72:75], a[20:23], v[216:219]\n\t"
      "buffer_load_dwordx4 v20, s[40:43], s49 offen offset:1024 sc1 lds\n\t"
      "v_mfma_f32_16x16x32_bf16 v[216:219], a[88:91], a[52:55], v[216:219]\n\t"
      "ds_read_b128 a[164:167], v8 offset:1024\n\t"
      "v_mfma_f32_16x16x32_bf16 v[232:235], a[72:75], a[24:27], v[232:235]\n\t"
      "v_mfma_f32_16x16x32_bf16 v[232:235], a[88:91], a[56:59], v[232:235]\n\t"
      "v_mfma_f32_16x16x32_bf16 v[248:251], a[72:75], a[28:31], v[248:251]\n\t"
      "ds_read_b128 a[168:171], v8 offset:2048\n\t"
      "v_mfma_f32_16x16x32_bf16 v[248:251], a[88:91], a[60:63], v[248:251]\n\t"
      "v_mfma_f32_16x16x32_bf16 v[252:255], a[76:79], a[28:31], v[252:255]\n\t"
      "ds_read_b128 a[172:175], v8 offset:3072\n\t"
      "v_mfma_f32_16x16x32_bf16 v[252:255], a[92:95], a[60:63], v[252:255]\n\t"
      "buffer_load_dwordx4 v20, s[40:43], s49 offen offset:2048 sc1 lds\n\t"
      "v_mfma_f32_16x16x32_bf16 v[236:239], a[76:79], a[24:27], v[236:239]\n\t"
      "ds_read_b128 a[176:179], v8 offset:8192\n\t"
      "v_mfma_f32_16x16x32_bf16 v[236:239], a[92:95], a[56:59], v[236:239]\n\t"
      "v_mfma_f32_16x16x32_bf16 v[220:223], a[76:79], a[20:23], v[220:223]\n\t"
      "ds_read_b128 a[180:183], v8 offset:9216\n\t"
      "v_mfma_f32_16x16x32_bf16 v[220:223], a[92:95], a[52:55], v[220:223]\n\t"
      "v_mfma_f32_16x16x32_bf16 v[204:207], a[76:79], a[16:19], v[204:207]\n\t"
      "ds_read_b128 a[184:187], v8 offset:10240\n\t"
      "v_mfma_f32_16x16x32_bf16 v[204:207], a[92:95], a[48:51], v[204:207]\n\t"
      "v_mfma_f32_16x16x32_bf16 v[188:191], a[76:79], a[12:15], v[188:191]\n\t"
      "buffer_load_dwordx4 v20, s[40:43], s49 offen offset:3072 sc1 lds\n\t"
      "v_mfma_f32_16x16x32_bf16 v[188:191], a[92:95], a[44:47], v[188:191]\n\t"
      "ds_read_b128 a[188:191], v8 offset:11264\n\t"
      "v_mfma_f32_16x16x32_bf16 v[172:175], a[76:79], a[8:11], v[172:175]\n\t"
      "s_add_u32 s48, s48, 128\n\t"
      "s_add_u32 s49, s49, 131072\n\t"
      "v_mfma_f32_16x16x32_bf16 v[172:175], a[92:95], a[40:43], v[172:175]\n\t"
      "v_mfma_f32_16x16x32_bf16 v[156:159], a[76:79], a[4:7], v[156:159]\n\t"
      "v_mfma_f32_16x16x32_bf16 v[156:159], a[92:95], a[36:39], v[156:159]\n\t"
      "v_mfma_f32_16x16x32_bf16 v[140:143], a[76:79], a[0:3], v[140:143]\n\t"
      "v_mfma_f32_16x16x32_bf16 v[140:143], a[92:95], a[32:35], v[140:143]\n\t"
      "s_waitcnt vmcnt(12) lgkmcnt(0)\n\t"
      "s_barrier\n\t"
      "v_mfma_f32_16x16x32_bf16 v[128:131], a[160:163], a[96:99], v[128:131]\n\t"
      "s_mov_b32 s51, s50\n\t"
      "s_add_u32 s50, s50, 49152\n\t"
      "s_cmp_eq_u32 s50, 147456\n\t"
      "v_mfma_f32_16x16x32_bf16 v[128:131], a[176:179], a[128:131], v[128:131]\n\t"
      "s_cselect_b32 s50, 0, s50\n\t"
      "s_sub_u32 s52, s50, s51\n\t"
      "v_mfma_f32_16x16x32_bf16 v[144:147], a[160:163], a[100:103], v[144:147]\n\t"
      "v_add_u32 v0, s52, v0\n\t"
      "v_add_u32 v1, s52, v1\n\t"
      "v_mfma_f32_16x16x32_bf16 v[144:147], a[176:179], a[132:135], v[144:147]\n\t"
      "v_add_u32 v2, s52, v2\n\t"
      "v_add_u32 v3, s52, v3\n\t"
      "v_mfma_f32_16x16x32_bf16 v[160:163], a[160:163], a[104:107], v[160:163]\n\t"
      "buffer_load_dwordx2 v[64:65], v35, s[64:67], 0 offen\n\t"
      "v_add_u32 v4, s52, v4\n\t"
      "v_add_u32 v5, s52, v5\n\t"
      "v_mfma_f32_16x16x32_bf16 v[160:163], a[176:179], a[136:139], v[160:163]\n\t"
      "v_add_u32 v6, s52, v6\n\t"
      "v_add_u32 v7, s52, v7\n\t"
      "v_mfma_f32_16x16x32_bf16 v[176:179], a[160:163], a[108:111], v[176:179]\n\t"
      "v_add_u32 v8, s52, v8\n\t"
      "v_mfma_f32_16x16x32_bf16 v[176:179], a[176:179], a[140:143], v[176:179]\n\t"
      "ds_read_b128 a[0:3], v0 offset:0\n\t"
      "v_mfma_f32_16x16x32_bf16 v[192:195], a[160:163], a[112:115], v[192:195]\n\t"
      "v_mfma_f32_16x16x32_bf16 v[192:195], a[176:179], a[144:147], v[192:195]\n\t"
      "ds_read_b128 a[4:7], v0 offset:2048\n\t"
      "v_mfma_f32_16x16x32_bf16 v[208:211], a[160:163], a[116:119], v[208:211]\n\t"
      "buffer_load_dwordx2 v[66:67], v35, s[64:67], 0 offen offset:32\n\t"
      "v_mfma_f32_16x16x32_bf16 v[208:211], a[176:179], a[148:151], v[208:211]\n\t"
      "ds_read_b128 a[8:11], v2 offset:0\n\t"
      "v_mfma_f32_16x16x32_bf16 v[224:227], a[160:163], a[120:123], v[224:227]\n\t"
      "v_mfma_f32_16x16x32_bf16 v[224:227], a[176:179], a[152:155], v[224:227]\n\t"
      "ds_read_b128 a[12:15], v2 offset:2048\n\t"
      "v_mfma_f32_16x16x32_bf16 v[240:243], a[160:163], a[124:127], v[240:243]\n\t"
      "v_mfma_f32_16x16x32_bf16 v[240:243], a[176:179], a[156:159], v[240:243]\n\t"
      "ds_read_b128 a[16:19], v4 offset:0\n\t"
      "v_mfma_f32_16x16x32_bf16 v[244:247], a[164:167], a[124:127], v[244:247]\n\t"
      "buffer_load_dwordx2 v[68:69], v35, s[64:67], 0 offen offset:64\n\t"
      "v_mfma_f32_16x16x32_bf16 v[244:247], a[180:183], a[156:159], v[244:247]\n\t"
      "ds_read_b128 a[20:23], v4 offset:2048\n\t"
      "v_mfma_f32_16x16x32_bf16 v[228:231], a[164:167], a[120:123], v[228:231]\n\t"
      "v_mfma_f32_16x16x32_bf16 v[228:231], a[180:183], a[152:155], v[228:231]\n\t"
      "v_mfma_f32_16x16x32_bf16 v[212:215], a[164:167], a[116:119], v[212:215]\n\t"
      "ds_read_b128 a[24:27], v6 offset:0\n\t"
      "v_mfma_f32_16x16x32_bf16 v[212:215], a[180:183], a[148:151], v[212:215]\n\t"
      "v_mfma_f32_16x16x32_bf16 v[196:199], a[164:167], a[112:115], v[196:199]\n\t"
      "buffer_load_dwordx2 v[70:71], v35, s[64:67], 0 offen offset:96\n\t"
      "ds_read_b128 a[28:31], v6 offset:2048\n\t"
      "v_mfma_f32_16x16x32_bf16 v[196:199], a[180:183], a[144:147], v[196:199]\n\t"
      "v_mfma_f32_16x16x32_bf16 v[180:183], a[164:167], a[108:111], v[180:183]\n\t"
      "ds_read_b128 a[32:35], v1 offset:0\n\t"
      "v_mfma_f32_16x16x32_bf16 v[180:183], a[180:183], a[140:143], v[180:183]\n\t"
      "v_mfma_f32_16x16x32_bf16 v[164:167], a[164:167], a[104:107], v[164:167]\n\t"
      "ds_read_b128 a[36:39], v1 offset:2048\n\t"
      "v_mfma_f32_16x16x32_bf16 v[164:167], a[180:183], a[136:139], v[164:167]\n\t"
      "v_mfma_f32_16x16x32_bf16 v[148:151], a[164:167], a[100:103], v[148:151]\n\t"
      "buffer_load_dwordx2 v[72:73], v36, s[64:67], 0 offen\n\t"
      "ds_read_b128 a[40:43], v3 offset:0\n\t"
      "v_mfma_f32_16x16x32_bf16 v[148:151], a[180:183], a[132:135], v[148:151]\n\t"
      "v_mfma_f32_16x16x32_bf16 v[132:135], a[164:167], a[96:99], v[132:135]\n\t"
      "ds_read_b128 a[44:47], v3 offset:2048\n\t"
      "v_mfma_f32_16x16x32_bf16 v[132:135], a[180:183], a[128:131], v[132:135]\n\t"
      "v_mfma_f32_16x16x32_bf16 v[136:139], a[168:171], a[96:99], v[136:139]\n\t"
      "v_mfma_f32_16x16x32_bf16 v[136:139], a[184:187], a[128:131], v[136:139]\n\t"
      "ds_read_b128 a[48:51], v5 offset:0\n\t"
      "v_mfma_f32_16x16x32_bf16 v[152:155], a[168:171], a[100:103], v[152:155]\n\t"
      "buffer_load_dwordx2 v[74:75], v36, s[64:67], 0 offen offset:32\n\t"
      "v_mfma_f32_16x16x32_bf16 v[152:155], a[184:187], a[132:135], v[152:155]\n\t"
      "ds_read_b128 a[52:55], v5 offset:2048\n\t"
      "v_mfma_f32_16x16x32_bf16 v[168:171], a[168:171], a[104:107], v[168:171]\n\t"
      "v_mfma_f32_16x16x32_bf16 v[168:171], a[184:187], a[136:139], v[168:171]\n\t"
      "ds_read_b128 a[56:59], v7 offset:0\n\t"
      "v_mfma_f32_16x16x32_bf16 v[184:187], a[168:171], a[108:111], v[184:187]\n\t"
      "v_mfma_f32_16x16x32_bf16 v[184:187], a[184:187], a[140:143], v[184:187]\n\t"
      "ds_read_b128 a[60:63], v7 offset:2048\n\t"
      "v_mfma_f32_16x16x32_bf16 v[200:203], a[168:171], a[112:115], v[200:203]\n\t"
      "buffer_load_dwordx2 v[76:77], v36, s[64:67], 0 offen offset:64\n\t"
      "v_mfma_f32_16x16x32_bf16 v[200:203], a[184:187], a[144:147], v[200:203]\n\t"
      "ds_read_b128 a[64:67], v8 offset:0\n\t"
      "v_mfma_f32_16x16x32_bf16 v[216:219], a[168:171], a[116:119], v[216:219]\n\t"
      "v_mfma_f32_16x16x32_bf16 v[216:219], a[184:187], a[148:151], v[216:219]\n\t"
      "ds_read_b128 a[68:71], v8 offset:1024\n\t"
      "v_mfma_f32_16x16x32_bf16 v[232:235], a[168:171], a[120:123], v[232:235]\n\t"
      "v_mfma_f32_16x16x32_bf16 v[232:235], a[184:187], a[152:155], v[232:235]\n\t"
      "v_mfma_f32_16x16x32_bf16 v[248:251], a[168:171], a[124:127], v[248:251]\n\t"
      "buffer_load_dwordx2 v[78:79], v36, s[64:67], 0 offen offset:96\n\t"
      "ds_read_b128 a[72:75], v8 offset:2048\n\t"
      "v_mfma_f32_16x16x32_bf16 v[248:251], a[184:187], a[156:159], v[248:251]\n\t"
      "v_mfma_f32_16x16x32_bf16 v[252:255], a[172:175], a[124:127], v[252:255]\n\t"
      "ds_read_b128 a[76:79], v8 offset:3072\n\t"
      "v_mfma_f32_16x16x32_bf16 v[252:255], a[188:191], a[156:159], v[252:255]\n\t"
      "v_mfma_f32_16x16x32_bf16 v[236:239], a[172:175], a[120:123], v[236:239]\n\t"
      "ds_read_b128 a[80:83], v8 offset:8192\n\t"
      "v_mfma_f32_16x16x32_bf16 v[236:239], a[188:191], a[152:155], v[236:239]\n\t"
      "v_mfma_f32_16x16x32_bf16 v[220:223], a[172:175], a[116:119], v[220:223]\n\t"
      "ds_read_b128 a[84:87], v8 offset:9216\n\t"
      "v_mfma_f32_16x16x32_bf16 v[220:223], a[188:191], a[148:151], v[220:223]\n\t"
      "v_mfma_f32_16x16x32_bf16 v[204:207], a[172:175], a[112:115], v[204:207]\n\t"
      "ds_read_b128 a[88:91], v8 offset:10240\n\t"
      "v_mfma_f32_16x16x32_bf16 v[204:207], a[188:191], a[144:147], v[204:207]\n\t"
      "v_mfma_f32_16x16x32_bf16 v[188:191], a[172:175], a[108:111], v[188:191]\n\t"
      "v_mfma_f32_16x16x32_bf16 v[188:191], a[188:191], a[140:143], v[188:191]\n\t"
      "ds_read_b128 a[92:95], v8 offset:11264\n\t"
      "v_mfma_f32_16x16x32_bf16 v[172:175], a[172:175], a[104:107], v[172:175]\n\t"
      "v_mfma_f32_16x16x32_bf16 v[172:175], a[188:191], a[136:139], v[172:175]\n\t"
      "v_mfma_f32_16x16x32_bf16 v[156:159], a[172:175], a[100:103], v[156:159]\n\t"
      "v_mfma_f32_16x16x32_bf16 v[156:159], a[188:191], a[132:135], v[156:159]\n\t"
      "v_mfma_f32_16x16x32_bf16 v[140:143], a[172:175], a[96:99], v[140:143]\n\t"
      "v_mfma_f32_16x16x32_bf16 v[140:143], a[188:191], a[128:131], v[140:143]\n\t"
      "s_waitcnt vmcnt(8) lgkmcnt(0)\n\t"
      "s_barrier\n\t"
      "v_mfma_f32_16x16x32_bf16 v[128:131], a[64:67], a[0:3], v[128:131]\n\t"
      "s_mov_b32 s51, s50\n\t"
      "s_add_u32 s50, s50, 49152\n\t"
      "s_cmp_eq_u32 s50, 147456\n\t"
      "v_mfma_f32_16x16x32_bf16 v[128:131], a[80:83], a[32:35], v[128:131]\n\t"
      "s_cselect_b32 s50, 0, s50\n\t"
      "s_sub_u32 s52, s50, s51\n\t"
      "v_mfma_f32_16x16x32_bf16 v[144:147], a[64:67], a[4:7], v[144:147]\n\t"
      "v_add_u32 v0, s52, v0\n\t"
      "v_add_u32 v1, s52, v1\n\t"
      "v_mfma_f32_16x16x32_bf16 v[144:147], a[80:83], a[36:39], v[144:147]\n\t"
      "v_add_u32 v2, s52, v2\n\t"
      "v_add_u32 v3, s52, v3\n\t"
      "v_mfma_f32_16x16x32_bf16 v[160:163], a[64:67], a[8:11], v[160:163]\n\t"
      "v_add_u32 v4, s52, v4\n\t"
      "v_add_u32 v5, s52, v5\n\t"
      "v_mfma_f32_16x16x32_bf16 v[160:163], a[80:83], a[40:43], v[160:163]\n\t"
      "v_add_u32 v6, s52, v6\n\t"
      "v_add_u32 v7, s52, v7\n\t"
      "v_mfma_f32_16x16x32_bf16 v[176:179], a[64:67], a[12:15], v[176:179]\n\t"
      "v_add_u32 v8, s52, v8\n\t"
      "v_mfma_f32_16x16x32_bf16 v[176:179], a[80:83], a[44:47], v[176:179]\n\t"
      "ds_read_b128 a[96:99], v0 offset:0\n\t"
      "v_mfma_f32_16x16x32_bf16 v[192:195], a[64:67], a[16:19], v[192:195]\n\t"
      "v_mfma_f32_16x16x32_bf16 v[192:195], a[80:83], a[48:51], v[192:195]\n\t"
      "ds_read_b128 a[100:103], v0 offset:2048\n\t"
      "v_mfma_f32_16x16x32_bf16 v[208:211], a[64:67], a[20:23], v[208:211]\n\t"
      "v_mfma_f32_16x16x32_bf16 v[208:211], a[80:83], a[52:55], v[208:211]\n\t"
      "ds_read_b128 a[104:107], v2 offset:0\n\t"
      "v_mfma_f32_16x16x32_bf16 v[224:227], a[64:67], a[24:27], v[224:227]\n\t"
      "v_mfma_f32_16x16x32_bf16 v[224:227], a[80:83], a[56:59], v[224:227]\n\t"
      "ds_read_b128 a[108:111], v2 offset:2048\n\t"
      "v_mfma_f32_16x16x32_bf16 v[240:243], a[64:67], a[28:31], v[240:243]\n\t"
      "v_mfma_f32_16x16x32_bf16 v[240:243], a[80:83], a[60:63], v[240:243]\n\t"
      "ds_read_b128 a[112:115], v4 offset:0\n\t"
      "v_mfma_f32_16x16x32_bf16 v[244:247], a[68:71], a[28:31], v[244:247]\n\t"
      "v_mfma_f32_16x16x32_bf16 v[244:247], a[84:87], a[60:63], v[244:247]\n\t"
      "ds_read_b128 a[116:119], v4 offset:2048\n\t"
      "v_mfma_f32_16x16x32_bf16 v[228:231], a[68:71], a[24:27], v[228:231]\n\t"
      "v_mfma_f32_16x16x32_bf16 v[228:231], a[84:87], a[56:59], v[228:231]\n\t"
      "v_mfma_f32_16x16x32_bf16 v[212:215], a[68:71], a[20:23], v[212:215]\n\t"
      "ds_read_b128 a[120:123], v6 offset:0\n\t"
      "v_mfma_f32_16x16x32_bf16 v[212:215], a[84:87], a[52:55], v[212:215]\n\t"
      "v_mfma_f32_16x16x32_bf16 v[196:199], a[68:71], a[16:19], v[196:199]\n\t"
      "ds_read_b128 a[124:127], v6 offset:2048\n\t"
      "v_mfma_f32_16x16x32_bf16 v[196:199], a[84:87], a[48:51], v[196:199]\n\t"
      "v_mfma_f32_16x16x32_bf16 v[180:183], a[68:71], a[12:15], v[180:183]\n\t"
      "ds_read_b128 a[128:131], v1 offset:0\n\t"
      "v_mfma_f32_16x16x32_bf16 v[180:183], a[84:87], a[44:47], v[180:183]\n\t"
      "v_mfma_f32_16x16x32_bf16 v[164:167], a[68:71], a[8:11], v[164:167]\n\t"
      "ds_read_b128 a[132:135], v1 offset:2048\n\t"
      "v_mfma_f32_16x16x32_bf16 v[164:167], a[84:87], a[40:43], v[164:167]\n\t"
      "v_mfma_f32_16x16x32_bf16 v[148:151], a[68:71], a[4:7], v[148:151]\n\t"
      "ds_read_b128 a[136:139], v3 offset:0\n\t"
      "v_mfma_f32_16x16x32_bf16 v[148:151], a[84:87], a[36:39], v[148:151]\n\t"
      "v_mfma_f32_16x16x32_bf16 v[132:135], a[68:71], a[0:3], v[132:135]\n\t"
      "ds_read_b128 a[140:143], v3 offset:2048\n\t"
      "v_mfma_f32_16x16x32_bf16 v[132:135], a[84:87], a[32:35], v[132:135]\n\t"
      "v_mfma_f32_16x16x32_bf16 v[136:139], a[72:75], a[0:3], v[136:139]\n\t"
      "v_mfma_f32_16x16x32_bf16 v[136:139], a[88:91], a[32:35], v[136:139]\n\t"
      "ds_read_b128 a[144:147], v5 offset:0\n\t"
      "v_mfma_f32_16x16x32_bf16 v[152:155], a[72:75], a[4:7], v[152:155]\n\t"
      "v_mfma_f32_16x16x32_bf16 v[152:155], a[88:91], a[36:39], v[152:155]\n\t"
      "ds_read_b128 a[148:151], v5 offset:2048\n\t"
      "v_mfma_f32_16x16x32_bf16 v[168:171], a[72:75], a[8:11], v[168:171]\n\t"
      "v_mfma_f32_16x16x32_bf16 v[168:171], a[88:91], a[40:43], v[168:171]\n\t"
      "ds_read_b128 a[152:155], v7 offset:0\n\t"
      "v_mfma_f32_16x16x32_bf16 v[184:187], a[72:75], a[12:15], v[184:187]\n\t"
      "v_mfma_f32_16x16x32_bf16 v[184:187], a[88:91], a[44:47], v[184:187]\n\t"
      "ds_read_b128 a[156:159], v7 offset:2048\n\t"
      "v_mfma_f32_16x16x32_bf16 v[200:203], a[72:75], a[16:19], v[200:203]\n\t"
      "v_mfma_f32_16x16x32_bf16 v[200:203], a[88:91], a[48:51], v[200:203]\n\t"
      "ds_read_b128 a[160:163], v8 offset:0\n\t"
      "v_mfma_f32_16x16x32_bf16 v[216:219], a[72:75], a[20:23], v[216:219]\n\t"
      "v_mfma_f32_16x16x32_bf16 v[216:219], a[88:91], a[52:55], v[216:219]\n\t"
      "ds_read_b128 a[164:167], v8 offset:1024\n\t"
      "v_mfma_f32_16x16x32_bf16 v[232:235], a[72:75], a[24:27], v[232:235]\n\t"
      "v_mfma_f32_16x16x32_bf16 v[232:235], a[88:91], a[56:59], v[232:235]\n\t"
      "v_mfma_f32_16x16x32_bf16 v[248:251], a[72:75], a[28:31], v[248:251]\n\t"
      "ds_read_b128 a[168:171], v8 offset:2048\n\t"
      "v_mfma_f32_16x16x32_bf16 v[248:251], a[88:91], a[60:63], v[248:251]\n\t"
      "v_mfma_f32_16x16x32_bf16 v[252:255], a[76:79], a[28:31], v[252:255]\n\t"
      "ds_read_b128 a[172:175], v8 offset:3072\n\t"
      "v_mfma_f32_16x16x32_bf16 v[252:255], a[92:95], a[60:63], v[252:255]\n\t"
      "v_mfma_f32_16x16x32_bf16 v[236:239], a[76:79], a[24:27], v[236:239]\n\t"
      "ds_read_b128 a[176:179], v8 offset:8192\n\t"
      "v_mfma_f32_16x16x32_bf16 v[236:239], a[92:95], a[56:59], v[236:239]\n\t"
      "v_mfma_f32_16x16x32_bf16 v[220:223], a[76:79], a[20:23], v[220:223]\n\t"
      "ds_read_b128 a[180:183], v8 offset:9216\n\t"
      "v_mfma_f32_16x16x32_bf16 v[220:223], a[92:95], a[52:55], v[220:223]\n\t"
      "v_mfma_f32_16x16x32_bf16 v[204:207], a[76:79], a[16:19], v[204:207]\n\t"
      "ds_read_b128 a[184:187], v8 offset:10240\n\t"
      "v_mfma_f32_16x16x32_bf16 v[204:207], a[92:95], a[48:51], v[204:207]\n\t"
      "v_mfma_f32_16x16x32_bf16 v[188:191], a[76:79], a[12:15], v[188:191]\n\t"
      "v_mfma_f32_16x16x32_bf16 v[188:191], a[92:95], a[44:47], v[188:191]\n\t"
      "ds_read_b128 a[188:191], v8 offset:11264\n\t"
      "v_mfma_f32_16x16x32_bf16 v[172:175], a[76:79], a[8:11], v[172:175]\n\t"
      "v_mfma_f32_16x16x32_bf16 v[172:175], a[92:95], a[40:43], v[172:175]\n\t"
      "v_mfma_f32_16x16x32_bf16 v[156:159], a[76:79], a[4:7], v[156:159]\n\t"
      "v_mfma_f32_16x16x32_bf16 v[156:159], a[92:95], a[36:39], v[156:159]\n\t"
      "v_mfma_f32_16x16x32_bf16 v[140:143], a[76:79], a[0:3], v[140:143]\n\t"
      "v_mfma_f32_16x16x32_bf16 v[140:143], a[92:95], a[32:35], v[140:143]\n\t"
      "s_waitcnt lgkmcnt(0)\n\t"
      "v_mfma_f32_16x16x32_bf16 v[128:131], a[160:163], a[96:99], v[128:131]\n\t"
      "v_mfma_f32_16x16x32_bf16 v[128:131], a[176:179], a[128:131], v[128:131]\n\t"
      "v_mfma_f32_16x16x32_bf16 v[144:147], a[160:163], a[100:103], v[144:147]\n\t"
      "v_mfma_f32_16x16x32_bf16 v[144:147], a[176:179], a[132:135], v[144:147]\n\t"
      "v_mfma_f32_16x16x32_bf16 v[160:163], a[160:163], a[104:107], v[160:163]\n\t"
      "v_mfma_f32_16x16x32_bf16 v[160:163], a[176:179], a[136:139], v[160:163]\n\t"
      "v_mfma_f32_16x16x32_bf16 v[176:179], a[160:163], a[108:111], v[176:179]\n\t"
      "v_mfma_f32_16x16x32_bf16 v[176:179], a[176:179], a[140:143], v[176:179]\n\t"
      "v_mfma_f32_16x16x32_bf16 v[192:195], a[160:163], a[112:115], v[192:195]\n\t"
      "v_mfma_f32_16x16x32_bf16 v[192:195], a[176:179], a[144:147], v[192:195]\n\t"
      "v_mfma_f32_16x16x32_bf16 v[208:211], a[160:163], a[116:119], v[208:211]\n\t"
      "v_mfma_f32_16x16x32_bf16 v[208:211], a[176:179], a[148:151], v[208:211]\n\t"
      "v_mfma_f32_16x16x32_bf16 v[224:227], a[160:163], a[120:123], v[224:227]\n\t"
      "v_mfma_f32_16x16x32_bf16 v[224:227], a[176:179], a[152:155], v[224:227]\n\t"
      "v_mfma_f32_16x16x32_bf16 v[240:243], a[160:163], a[124:127], v[240:243]\n\t"
      "v_mfma_f32_16x16x32_bf16 v[240:243], a[176:179], a[156:159], v[240:243]\n\t"
      "v_mfma_f32_16x16x32_bf16 v[244:247], a[164:167], a[124:127], v[244:247]\n\t"
      "v_mfma_f32_16x16x32_bf16 v[244:247], a[180:183], a[156:159], v[244:247]\n\t"
      "v_mfma_f32_16x16x32_bf16 v[228:231], a[164:167], a[120:123], v[228:231]\n\t"
      "v_mfma_f32_16x16x32_bf16 v[228:231], a[180:183], a[152:155], v[228:231]\n\t"
      "v_mfma_f32_16x16x32_bf16 v[212:215], a[164:167], a[116:119], v[212:215]\n\t"
      "v_mfma_f32_16x16x32_bf16 v[212:215], a[180:183], a[148:151], v[212:215]\n\t"
      "v_mfma_f32_16x16x32_bf16 v[196:199], a[164:167], a[112:115], v[196:199]\n\t"
      "v_mfma_f32_16x16x32_bf16 v[196:199], a[180:183], a[144:147], v[196:199]\n\t"
      "v_mfma_f32_16x16x32_bf16 v[180:183], a[164:167], a[108:111], v[180:183]\n\t"
      "v_mfma_f32_16x16x32_bf16 v[180:183], a[180:183], a[140:143], v[180:183]\n\t"
      "v_mfma_f32_16x16x32_bf16 v[164:167], a[164:167], a[104:107], v[164:167]\n\t"
      "v_mfma_f32_16x16x32_bf16 v[164:167], a[180:183], a[136:139], v[164:167]\n\t"
      "v_mfma_f32_16x16x32_bf16 v[148:151], a[164:167], a[100:103], v[148:151]\n\t"
      "v_mfma_f32_16x16x32_bf16 v[148:151], a[180:183], a[132:135], v[148:151]\n\t"
      "v_mfma_f32_16x16x32_bf16 v[132:135], a[164:167], a[96:99], v[132:135]\n\t"
      "v_mfma_f32_16x16x32_bf16 v[132:135], a[180:183], a[128:131], v[132:135]\n\t"
      "v_mfma_f32_16x16x32_bf16 v[136:139], a[168:171], a[96:99], v[136:139]\n\t"
      "v_mfma_f32_16x16x32_bf16 v[136:139], a[184:187], a[128:131], v[136:139]\n\t"
      "v_mfma_f32_16x16x32_bf16 v[152:155], a[168:171], a[100:103], v[152:155]\n\t"
      "v_mfma_f32_16x16x32_bf16 v[152:155], a[184:187], a[132:135], v[152:155]\n\t"
      "v_mfma_f32_16x16x32_bf16 v[168:171], a[168:171], a[104:107], v[168:171]\n\t"
      "v_mfma_f32_16x16x32_bf16 v[168:171], a[184:187], a[136:139], v[168:171]\n\t"
      "v_mfma_f32_16x16x32_bf16 v[184:187], a[168:171], a[108:111], v[184:187]\n\t"
      "v_mfma_f32_16x16x32_bf16 v[184:187], a[184:187], a[140:143], v[184:187]\n\t"
      "v_mfma_f32_16x16x32_bf16 v[200:203], a[168:171], a[112:115], v[200:203]\n\t"
      "v_mfma_f32_16x16x32_bf16 v[200:203], a[184:187], a[144:147], v[200:203]\n\t"
      "v_mfma_f32_16x16x32_bf16 v[216:219], a[168:171], a[116:119], v[216:219]\n\t"
      "v_mfma_f32_16x16x32_bf16 v[216:219], a[184:187], a[148:151], v[216:219]\n\t"
      "v_mfma_f32_16x16x32_bf16 v[232:235], a[168:171], a[120:123], v[232:235]\n\t"
      "v_mfma_f32_16x16x32_bf16 v[232:235], a[184:187], a[152:155], v[232:235]\n\t"
      "v_mfma_f32_16x16x32_bf16 v[248:251], a[168:171], a[124:127], v[248:251]\n\t"
      "v_mfma_f32_16x16x32_bf16 v[248:251], a[184:187], a[156:159], v[248:251]\n\t"
      "v_mfma_f32_16x16x32_bf16 v[252:255], a[172:175], a[124:127], v[252:255]\n\t"
      "v_mfma_f32_16x16x32_bf16 v[252:255], a[188:191], a[156:159], v[252:255]\n\t"
      "v_mfma_f32_16x16x32_bf16 v[236:239], a[172:175], a[120:123], v[236:239]\n\t"
      "v_mfma_f32_16x16x32_bf16 v[236:239], a[188:191], a[152:155], v[236:239]\n\t"
      "v_mfma_f32_16x16x32_bf16 v[220:223], a[172:175], a[116:119], v[220:223]\n\t"
      "v_mfma_f32_16x16x32_bf16 v[220:223], a[188:191], a[148:151], v[220:223]\n\t"
      "v_mfma_f32_16x16x32_bf16 v[204:207], a[172:175], a[112:115], v[204:207]\n\t"
      "v_mfma_f32_16x16x32_bf16 v[204:207], a[188:191], a[144:147], v[204:207]\n\t"
      "v_mfma_f32_16x16x32_bf16 v[188:191], a[172:175], a[108:111], v[188:191]\n\t"
      "v_mfma_f32_16x16x32_bf16 v[188:191], a[188:191], a[140:143], v[188:191]\n\t"
      "v_mfma_f32_16x16x32_bf16 v[172:175], a[172:175], a[104:107], v[172:175]\n\t"
      "v_mfma_f32_16x16x32_bf16 v[172:175], a[188:191], a[136:139], v[172:175]\n\t"
      "v_mfma_f32_16x16x32_bf16 v[156:159], a[172:175], a[100:103], v[156:159]\n\t"
      "v_mfma_f32_16x16x32_bf16 v[156:159], a[188:191], a[132:135], v[156:159]\n\t"
      "v_mfma_f32_16x16x32_bf16 v[140:143], a[172:175], a[96:99], v[140:143]\n\t"
      "v_mfma_f32_16x16x32_bf16 v[140:143], a[188:191], a[128:131], v[140:143]\n\t"
      "s_nop 15\n\t"
      "v_cvt_pk_bf16_f32 v56, v160, v161\n\t"
      "v_cvt_pk_bf16_f32 v57, v162, v163\n\t"
      "v_cvt_pk_bf16_f32 v58, v164, v165\n\t"
      "v_cvt_pk_bf16_f32 v59, v166, v167\n\t"
      "buffer_store_dwordx4 v[56:59], v37, s[68:71], s72 offen sc1\n\t"
      "v_cvt_pk_bf16_f32 v60, v168, v169\n\t"
      "v_cvt_pk_bf16_f32 v61, v170, v171\n\t"
      "v_cvt_pk_bf16_f32 v62, v172, v173\n\t"
      "v_cvt_pk_bf16_f32 v63, v174, v175\n\t"
      "buffer_store_dwordx4 v[60:63], v37, s[68:71], s72 offen offset:1024 sc1\n\t"
      "v_cvt_pk_bf16_f32 v40, v176, v177\n\t"
      "v_cvt_pk_bf16_f32 v41, v178, v179\n\t"
      "v_cvt_pk_bf16_f32 v42, v180, v181\n\t"
      "v_cvt_pk_bf16_f32 v43, v182, v183\n\t"
      "buffer_store_dwordx4 v[40:43], v37, s[68:71], s72 offen offset:2048 sc1\n\t"
      "v_cvt_pk_bf16_f32 v44, v184, v185\n\t"
      "v_cvt_pk_bf16_f32 v45, v186, v187\n\t"
      "v_cvt_pk_bf16_f32 v46, v188, v189\n\t"
      "v_cvt_pk_bf16_f32 v47, v190, v191\n\t"
      "buffer_store_dwordx4 v[44:47], v37, s[68:71], s72 offen offset:3072 sc1\n\t"
      "v_cvt_pk_bf16_f32 v48, v192, v193\n\t"
      "v_cvt_pk_bf16_f32 v49, v194, v195\n\t"
      "v_cvt_pk_bf16_f32 v50, v196, v197\n\t"
      "v_cvt_pk_bf16_f32 v51, v198, v199\n\t"
      "buffer_store_dwordx4 v[48:51], v37, s[68:71], s73 offen sc1\n\t"
      "v_cvt_pk_bf16_f32 v52, v200, v201\n\t"
      "v_cvt_pk_bf16_f32 v53, v202, v203\n\t"
      "v_cvt_pk_bf16_f32 v54, v204, v205\n\t"
      "v_cvt_pk_bf16_f32 v55, v206, v207\n\t"
      "buffer_store_dwordx4 v[52:55], v37, s[68:71], s73 offen offset:1024 sc1\n\t"
      "v_cvt_pk_bf16_f32 v56, v208, v209\n\t"
      "v_cvt_pk_bf16_f32 v57, v210, v211\n\t"
      "v_cvt_pk_bf16_f32 v58, v212, v213\n\t"
      "v_cvt_pk_bf16_f32 v59, v214, v215\n\t"
      "buffer_store_dwordx4 v[56:59], v37, s[68:71], s73 offen offset:2048 sc1\n\t"
      "v_cvt_pk_bf16_f32 v60, v216, v217\n\t"
      "v_cvt_pk_bf16_f32 v61, v218, v219\n\t"
      "v_cvt_pk_bf16_f32 v62, v220, v221\n\t"
      "v_cvt_pk_bf16_f32 v63, v222, v223\n\t"
      "buffer_store_dwordx4 v[60:63], v37, s[68:71], s73 offen offset:3072 sc1\n\t"
      "v_cvt_pk_bf16_f32 v40, v224, v225\n\t"
      "v_cvt_pk_bf16_f32 v41, v226, v227\n\t"
      "v_cvt_pk_bf16_f32 v42, v228, v229\n\t"
      "v_cvt_pk_bf16_f32 v43, v230, v231\n\t"
      "buffer_store_dwordx4 v[40:43], v37, s[68:71], s74 offen sc1\n\t"
      "v_cvt_pk_bf16_f32 v44, v232, v233\n\t"
      "v_cvt_pk_bf16_f32 v45, v234, v235\n\t"
      "v_cvt_pk_bf16_f32 v46, v236, v237\n\t"
      "v_cvt_pk_bf16_f32 v47, v238, v239\n\t"
      "buffer_store_dwordx4 v[44:47], v37, s[68:71], s74 offen offset:1024 sc1\n\t"
      "v_cvt_pk_bf16_f32 v48, v240, v241\n\t"
      "v_cvt_pk_bf16_f32 v49, v242, v243\n\t"
      "v_cvt_pk_bf16_f32 v50, v244, v245\n\t"
      "v_cvt_pk_bf16_f32 v51, v246, v247\n\t"
      "buffer_store_dwordx4 v[48:51], v37, s[68:71], s74 offen offset:2048 sc1\n\t"
      "v_cvt_pk_bf16_f32 v52, v248, v249\n\t"
      "v_cvt_pk_bf16_f32 v53, v250, v251\n\t"
      "v_cvt_pk_bf16_f32 v54, v252, v253\n\t"
      "v_cvt_pk_bf16_f32 v55, v254, v255\n\t"
      "buffer_store_dwordx4 v[52:55], v37, s[68:71], s74 offen offset:3072 sc1\n\t"
      "s_waitcnt vmcnt(0)\n\t"
      "s_mov_b64 exec, 1\n\t"
      "buffer_store_dwordx2 v[24:25], v27, s[56:59], 0 offen sc1\n\t"
      "s_mov_b64 exec, -1\n\t"
      "s_nop 1\n\t"
      "buffer_load_dwordx2 v[28:29], v34, s[56:59], 0 offen sc1\n\t"
      "s_waitcnt vmcnt(0)\n\t"
      "s_mov_b32 s75, 0x10000\n\t"
      "LBx_chk_%=:\n\t"
      "v_cmp_ne_u64 vcc, v[28:29], s[60:61]\n\t"
      "s_nop 4\n\t"
      "s_cbranch_vccz LBx_ok_%=\n\t"
      "s_sub_u32 s75, s75, 1\n\t"
      "s_cmp_eq_u32 s75, 0\n\t"
      "s_cbranch_scc1 LBx_ok_%=\n\t"
      "s_sleep 1\n\t"
      "buffer_load_dwordx2 v[28:29], v34, s[56:59], 0 offen sc1 ; alt\n\t"
      "s_waitcnt vmcnt(0)\n\t"
      "s_branch LBx_chk_%=\n\t"
      "LBx_ok_%=:\n\t"
      "buffer_load_dwordx4 v[80:83], v37, s[68:71], s76 offen sc1\n\t"
      "buffer_load_dwordx4 v[84:87], v37, s[68:71], s76 offen offset:1024 sc1\n\t"
      "buffer_load_dwordx4 v[88:91], v37, s[68:71], s76 offen offset:2048 sc1\n\t"
      "buffer_load_dwordx4 v[92:95], v37, s[68:71], s76 offen offset:3072 sc1\n\t"
      "buffer_load_dwordx4 v[96:99], v37, s[68:71], s77 offen sc1\n\t"
      "buffer_load_dwordx4 v[100:103], v37, s[68:71], s77 offen offset:1024 sc1\n\t"
      "buffer_load_dwordx4 v[104:107], v37, s[68:71], s77 offen offset:2048 sc1\n\t"
      "buffer_load_dwordx4 v[108:111], v37, s[68:71], s77 offen offset:3072 sc1\n\t"
      "buffer_load_dwordx4 v[112:115], v37, s[68:71], s78 offen sc1\n\t"
      "buffer_load_dwordx4 v[116:119], v37, s[68:71], s78 offen offset:1024 sc1\n\t"
      "buffer_load_dwordx4 v[120:123], v37, s[68:71], s78 offen offset:2048 sc1\n\t"
      "buffer_load_dwordx4 v[124:127], v37, s[68:71], s78 offen offset:3072 sc1\n\t"
      "s_waitcnt vmcnt(0)\n\t"
      : "={v[128:159]}"(c0), "={v[64:79]}"(xin), "={v[80:95]}"(in0), "={v[96:111]}"(in1), "={v[112:127]}"(in2), "+{v[0:15]}"(vb0), "+{a[0:3]}"(vp), "+{s[36:51]}"(sb), "+{s[52:55]}"(sb2)
      : "{v[16:27]}"(vb1), "{v[32:37]}"(vb2), "{s[56:63]}"(sb3), "{s[64:71]}"(sb4), "{s[72:74]}"(ssend), "{s[76:78]}"(srecv)
      : "memory", "scc", "vcc", "s75", "s79", "a4", "a5", "a6", "a7", "a8", "a9", "a10", "a11", "a12", "a13", "a14", "a15", "a16", "a17", "a18", "a19", "a20", "a21", "a22", "a23", "a24", "a25", "a26", "a27", "a28", "a29", "a30", "a31", "a32", "a33", "a34", "a35", "a36", "a37", "a38", "a39", "a40", "a41", "a42", "a43", "a44", "a45", "a46", "a47", "a48", "a49", "a50", "a51", "a52", "a53", "a54", "a55", "a56", "a57", "a58", "a59", "a60", "a61", "a62", "a63", "a64", "a65", "a66", "a67", "a68", "a69", "a70", "a71", "a72", "a73", "a74", "a75", "a76", "a77", "a78", "a79", "a80", "a81", "a82", "a83", "a84", "a85", "a86", "a87", "a88", "a89", "a90", "a91", "a92", "a93", "a94", "a95", "a96", "a97", "a98", "a99", "a100", "a101", "a102", "a103", "a104", "a105", "a106", "a107", "a108", "a109", "a110", "a111", "a112", "a113", "a114", "a115", "a116", "a117", "a118", "a119", "a120", "a121", "a122", "a123", "a124", "a125", "a126", "a127", "a128", "a129", "a130", "a131", "a132", "a133", "a134", "a135", "a136", "a137", "a138", "a139", "a140", "a141", "a142", "a143", "a144", "a145", "a146", "a147", "a148", "a149", "a150", "a151", "a152", "a153", "a154", "a155", "a156", "a157", "a158", "a159", "a160", "a161", "a162", "a163", "a164", "a165", "a166", "a167", "a168", "a169", "a170", "a171", "a172", "a173", "a174", "a175", "a176", "a177", "a178", "a179", "a180", "a181", "a182", "a183", "a184", "a185", "a186", "a187", "a188", "a189", "a190", "a191", "v28", "v29", "v30", "v31", "v40", "v41", "v42", "v43", "v44", "v45", "v46", "v47", "v48", "v49", "v50", "v51", "v52", "v53", "v54", "v55", "v56", "v57", "v58", "v59", "v60", "v61", "v62", "v63", "v160", "v161", "v162", "v163", "v164", "v165", "v166", "v167", "v168", "v169", "v170", "v171", "v172", "v173", "v174", "v175", "v176", "v177", "v178", "v179", "v180", "v181", "v182", "v183", "v184", "v185", "v186", "v187", "v188", "v189", "v190", "v191", "v192", "v193", "v194", "v195", "v196", "v197", "v198", "v199", "v200", "v201", "v202", "v203", "v204", "v205", "v206", "v207", "v208", "v209", "v210", "v211", "v212", "v213", "v214", "v215", "v216", "v217", "v218", "v219", "v220", "v221", "v222", "v223", "v224", "v225", "v226", "v227", "v228", "v229", "v230", "v231", "v232", "v233", "v234", "v235", "v236", "v237", "v238", "v239", "v240", "v241", "v242", "v243", "v244", "v245", "v246", "v247", "v248", "v249", "v250", "v251", "v252", "v253", "v254", "v255");
    } else {
      u32x4 vp;
      const unsigned half = q - 2;
      const u32x2 ve2b = {FO_H + (4 * A_BLOCK(tpn, 16 * half + (lane >> 2)) + (lane & 3)) * 8, 0};
      {
        u32x4 se2f; put_srd(se2f, 0, fl, FO_END);
        const u32x4 ve2a = {(unsigned)tag, (unsigned)(tag >> 32), FO_H + (4 * b + wave) * 8, 0};
        asm volatile(
      "s_nop 4\n\t"
      "s_waitcnt vmcnt(0)\n\t"
      "s_mov_b64 exec, 1\n\t"
      "buffer_store_dwordx2 v[24:25], v26, s[56:59], 0 offen sc1\n\t"
      "s_mov_b64 exec, -1\n\t"
      "s_nop 1\n\t"
      "buffer_load_dwordx2 a[0:1], v32, s[56:59], 0 offen sc1\n\t"
        : "=&{a[0:3]}"(vp) : "{v[24:27]}"(ve2a), "{v[32:33]}"(ve2b), "{s[56:59]}"(se2f) : "memory");
      }
      B_COMMON
#pragma unroll
      for (unsigned pr = 0; pr < 4; ++pr) vb0[pr] = (8 * wm + 2 * ((pr + q) & 3)) * 1024 + lane * 16;
#pragma unroll
      for (unsigned n = 0; n < 4; ++n)
        vb0[8 + n] = 32768 + (4 * fq + tq) * 256 + (((2 * (4 * wn + n) + (tp >> 1)) ^ (2 * (4 * (fq & 1) + tq))) * 16) + (tp & 1) * 8;
      vb1[0] = lane * 16 + ((56 * half + (wave >> 1)) * 128 + 16 * tpn + 8 * (wave & 1)) * 1024;
#pragma unroll
      for (unsigned i = 0; i < 4; ++i) {
        const unsigned rho = 16 * wave + 4 * i + (lane >> 4), c = (lane & 15) ^ (2 * (rho & 7));
        vb1[4 + i] = ((1792 * half + rho) * D_ + 128 * ec + 8 * c) * 2 - 1024 * i;
      }
      vb2[0] = ve2b[0]; vb2[1] = 0;
      put_srd(sb, 0, ws + WS_HF, 112u * 128 * 1024);
      put_srd(sb, 4, kp->wdn, (unsigned)(F_ * D_ * 2));
#pragma unroll
      for (int i = 0; i < 16; ++i) sb[i] = rfl(sb[i]);
      sb2[0] = 0; sb2[1] = 12; sb2[2] = 0; sb2[3] = 0;
      asm volatile(
      "s_nop 4\n\t"
      "s_waitcnt vmcnt(0)\n\t"
      "v_accvgpr_read_b32 v28, a0\n\t"
      "v_accvgpr_read_b32 v29, a1\n\t"
      "v_accvgpr_read_b32 v30, a2\n\t"
      "v_accvgpr_read_b32 v31, a3\n\t"
      "s_mov_b32 s75, 0x10000\n\t"
      "LBa_chk_%=:\n\t"
      "v_cmp_ne_u64 vcc, v[28:29], s[60:61]\n\t"
      "s_nop 4\n\t"
      "s_cbranch_vccz LBa_ok_%=\n\t"
      "s_sub_u32 s75, s75, 1\n\t"
      "s_cmp_eq_u32 s75, 0\n\t"
      "s_cbranch_scc1 LBa_ok_%=\n\t"
      "s_sleep 1\n\t"
      "buffer_load_dwordx2 v[28:29], v32, s[56:59], 0 offen sc1 ; alt\n\t"
      "s_waitcnt vmcnt(0)\n\t"
      "s_branch LBa_chk_%=\n\t"
      "LBa_ok_%=:\n\t"
      "s_mov_b32 s48, 0\n\t"
      "s_mov_b32 s55, 4096\n\t"
      "s_add_u32 m0, s44, 0\n\t"
      "s_nop 0\n\t"
      "buffer_load_dwordx4 v16, s[36:39], s48 offen sc1 lds\n\t"
      "v_mov_b32 v128, 0\n\t"
      "v_mov_b32 v129, 0\n\t"
      "v_mov_b32 v130, 0\n\t"
      "v_mov_b32 v131, 0\n\t"
      "v_mov_b32 v132, 0\n\t"
      "buffer_load_dwordx4 v16, s[36:39], s48 offen offset:1024 sc1 lds\n\t"
      "v_mov_b32 v133, 0\n\t"
      "v_mov_b32 v134, 0\n\t"
      "v_mov_b32 v135, 0\n\t"
      "v_mov_b32 v136, 0\n\t"
      "v_mov_b32 v137, 0\n\t"
      "buffer_load_dwordx4 v16, s[36:39], s48 offen offset:2048 sc1 lds\n\t"
      "v_mov_b32 v138, 0\n\t"
      "v_mov_b32 v139, 0\n\t"
      "v_mov_b32 v140, 0\n\t"
      "v_mov_b32 v141, 0\n\t"
      "v_mov_b32 v142, 0\n\t"
      "buffer_load_dwordx4 v16, s[36:39], s48 offen offset:3072 sc1 lds\n\t"
      "v_mov_b32 v143, 0\n\t"
      "v_mov_b32 v144, 0\n\t"
      "v_mov_b32 v145, 0\n\t"
      "v_mov_b32 v146, 0\n\t"
      "v_mov_b32 v147, 0\n\t"
      "s_add_u32 m0, s44, 4096\n\t"
      "s_nop 0\n\t"
      "buffer_load_dwordx4 v16, s[36:39], s55 offen sc1 lds\n\t"
      "v_mov_b32 v148, 0\n\t"
      "v_mov_b32 v149, 0\n\t"
      "v_mov_b32 v150, 0\n\t"
      "v_mov_b32 v151, 0\n\t"
      "v_mov_b32 v152, 0\n\t"
      "buffer_load_dwordx4 v16, s[36:39], s55 offen offset:1024 sc1 lds\n\t"
      "v_mov_b32 v153, 0\n\t"
      "v_mov_b32 v154, 0\n\t"
      "v_mov_b32 v155, 0\n\t"
      "v_mov_b32 v156, 0\n\t"
      "v_mov_b32 v157, 0\n\t"
      "buffer_load_dwordx4 v16, s[36:39], s55 offen offset:2048 sc1 lds\n\t"
      "v_mov_b32 v158, 0\n\t"
      "v_mov_b32 v159, 0\n\t"
      "v_mov_b32 v160, 0\n\t"
      "v_mov_b32 v161, 0\n\t"
      "v_mov_b32 v162, 0\n\t"
      "buffer_load_dwordx4 v16, s[36:39], s55 offen offset:3072 sc1 lds\n\t"
      "v_mov_b32 v163, 0\n\t"
      "v_mov_b32 v164, 0\n\t"
      "v_mov_b32 v165, 0\n\t"
      "v_mov_b32 v166, 0\n\t"
      "v_mov_b32 v167, 0\n\t"
      "s_mov_b32 s48, 262144\n\t"
      "s_mov_b32 s55, 266240\n\t"
      "s_add_u32 m0, s44, 49152\n\t"
      "s_nop 0\n\t"
      "buffer_load_dwordx4 v16, s[36:39], s48 offen sc1 lds\n\t"
      "v_mov_b32 v168, 0\n\t"
      "v_mov_b32 v169, 0\n\t"
      "v_mov_b32 v170, 0\n\t"
      "v_mov_b32 v171, 0\n\t"
      "v_mov_b32 v172, 0\n\t"
      "buffer_load_dwordx4 v16, s[36:39], s48 offen offset:1024 sc1 lds\n\t"
      "v_mov_b32 v173, 0\n\t"
      "v_mov_b32 v174, 0\n\t"
      "v_mov_b32 v175, 0\n\t"
      "v_mov_b32 v176, 0\n\t"
      "v_mov_b32 v177, 0\n\t"
      "buffer_load_dwordx4 v16, s[36:39], s48 offen offset:2048 sc1 lds\n\t"
      "v_mov_b32 v178, 0\n\t"
      "v_mov_b32 v179, 0\n\t"
      "v_mov_b32 v180, 0\n\t"
      "v_mov_b32 v181, 0\n\t"
      "v_mov_b32 v182, 0\n\t"
      "buffer_load_dwordx4 v16, s[36:39], s48 offen offset:3072 sc1 lds\n\t"
      "v_mov_b32 v183, 0\n\t"
      "v_mov_b32 v184, 0\n\t"
      "v_mov_b32 v185, 0\n\t"
      "v_mov_b32 v186, 0\n\t"
      "v_mov_b32 v187, 0\n\t"
      "s_add_u32 m0, s44, 53248\n\t"
      "s_nop 0\n\t"
      "buffer_load_dwordx4 v16, s[36:39], s55 offen sc1 lds\n\t"
      "v_mov_b32 v188, 0\n\t"
      "v_mov_b32 v189, 0\n\t"
      "v_mov_b32 v190, 0\n\t"
      "v_mov_b32 v191, 0\n\t"
      "v_mov_b32 v192, 0\n\t"
      "buffer_load_dwordx4 v16, s[36:39], s55 offen offset:1024 sc1 lds\n\t"
      "v_mov_b32 v193, 0\n\t"
      "v_mov_b32 v194, 0\n\t"
      "v_mov_b32 v195, 0\n\t"
      "v_mov_b32 v196, 0\n\t"
      "v_mov_b32 v197, 0\n\t"
      "buffer_load_dwordx4 v16, s[36:39], s55 offen offset:2048 sc1 lds\n\t"
      "v_mov_b32 v198, 0\n\t"
      "v_mov_b32 v199, 0\n\t"
      "v_mov_b32 v200, 0\n\t"
      "v_mov_b32 v201, 0\n\t"
      "v_mov_b32 v202, 0\n\t"
      "buffer_load_dwordx4 v16, s[36:39], s55 offen offset:3072 sc1 lds\n\t"
      "v_mov_b32 v203, 0\n\t"
      "v_mov_b32 v204, 0\n\t"
      "v_mov_b32 v205, 0\n\t"
      "v_mov_b32 v206, 0\n\t"
      "v_mov_b32 v207, 0\n\t"
      "s_barrier\n\t"
      "s_mov_b32 s48, 524288\n\t"
      "s_mov_b32 s55, 528384\n\t"
      "s_mov_b32 s49, 262144\n\t"
      "s_add_u32 m0, s44, 98304\n\t"
      "s_nop 0\n\t"
      "buffer_load_dwordx4 v16, s[36:39], s48 offen sc1 lds\n\t"
      "v_mov_b32 v208, 0\n\t"
      "v_mov_b32 v209, 0\n\t"
      "v_mov_b32 v210, 0\n\t"
      "v_mov_b32 v211, 0\n\t"
      "v_mov_b32 v212, 0\n\t"
      "buffer_load_dwordx4 v16, s[36:39], s48 offen offset:1024 sc1 lds\n\t"
      "v_mov_b32 v213, 0\n\t"
      "v_mov_b32 v214, 0\n\t"
      "v_mov_b32 v215, 0\n\t"
      "v_mov_b32 v216, 0\n\t"
      "v_mov_b32 v217, 0\n\t"
      "buffer_load_dwordx4 v16, s[36:39], s48 offen offset:2048 sc1 lds\n\t"
      "v_mov_b32 v218, 0\n\t"
      "v_mov_b32 v219, 0\n\t"
      "v_mov_b32 v220, 0\n\t"
      "v_mov_b32 v221, 0\n\t"
      "v_mov_b32 v222, 0\n\t"
      "buffer_load_dwordx4 v16, s[36:39], s48 offen offset:3072 sc1 lds\n\t"
      "v_mov_b32 v223, 0\n\t"
      "v_mov_b32 v224, 0\n\t"
      "v_mov_b32 v225, 0\n\t"
      "v_mov_b32 v226, 0\n\t"
      "v_mov_b32 v227, 0\n\t"
      "s_add_u32 m0, s44, 102400\n\t"
      "s_nop 0\n\t"
      "buffer_load_dwordx4 v16, s[36:39], s55 offen sc1 lds\n\t"
      "v_mov_b32 v228, 0\n\t"
      "v_mov_b32 v229, 0\n\t"
      "v_mov_b32 v230, 0\n\t"
      "v_mov_b32 v231, 0\n\t"
      "v_mov_b32 v232, 0\n\t"
      "buffer_load_dwordx4 v16, s[36:39], s55 offen offset:1024 sc1 lds\n\t"
      "v_mov_b32 v233, 0\n\t"
      "v_mov_b32 v234, 0\n\t"
      "v_mov_b32 v235, 0\n\t"
      "v_mov_b32 v236, 0\n\t"
      "v_mov_b32 v237, 0\n\t"
      "buffer_load_dwordx4 v16, s[36:39], s55 offen offset:2048 sc1 lds\n\t"
      "v_mov_b32 v238, 0\n\t"
      "v_mov_b32 v239, 0\n\t"
      "v_mov_b32 v240, 0\n\t"
      "v_mov_b32 v241, 0\n\t"
      "v_mov_b32 v242, 0\n\t"
      "buffer_load_dwordx4 v16, s[36:39], s55 offen offset:3072 sc1 lds\n\t"
      "v_mov_b32 v243, 0\n\t"
      "v_mov_b32 v244, 0\n\t"
      "v_mov_b32 v245, 0\n\t"
      "v_mov_b32 v246, 0\n\t"
      "v_mov_b32 v247, 0\n\t"
      "s_add_u32 m0, s45, 98304\n\t"
      "s_nop 0\n\t"
      "buffer_load_dwordx4 v20, s[40:43], s49 offen lds\n\t"
      "v_mov_b32 v248, 0\n\t"
      "v_mov_b32 v249, 0\n\t"
      "v_mov_b32 v250, 0\n\t"
      "v_mov_b32 v251, 0\n\t"
      "v_mov_b32 v252, 0\n\t"
      "buffer_load_dwordx4 v21, s[40:43], s49 offen offset:1024 lds\n\t"
      "v_mov_b32 v253, 0\n\t"
      "v_mov_b32 v254, 0\n\t"
      "v_mov_b32 v255, 0\n\t"
      "buffer_load_dwordx4 v22, s[40:43], s49 offen offset:2048 lds\n\t"
      "buffer_load_dwordx4 v23, s[40:43], s49 offen offset:3072 lds\n\t"
      "s_mov_b32 s48, 786432\n\t"
      "s_mov_b32 s49, 393216\n\t"
      "s_mov_b32 s50, 0\n\t"
      "s_waitcnt vmcnt(20)\n\t"
      "s_barrier\n\t"
      "ds_read_b128 a[0:3], v0 offset:0\n\t"
      "ds_read_b128 a[4:7], v0 offset:1024\n\t"
      "ds_read_b128 a[8:11], v1 offset:0\n\t"
      "ds_read_b128 a[12:15], v1 offset:1024\n\t"
      "ds_read_b128 a[16:19], v2 offset:0\n\t"
      "ds_read_b128 a[20:23], v2 offset:1024\n\t"
      "ds_read_b128 a[24:27], v3 offset:0\n\t"
      "ds_read_b128 a[28:31], v3 offset:1024\n\t"
      "ds_read_b128 a[32:35], v0 offset:16384\n\t"
      "ds_read_b128 a[36:39], v0 offset:17408\n\t"
      "ds_read_b128 a[40:43], v1 offset:16384\n\t"
      "ds_read_b128 a[44:47], v1 offset:17408\n\t"
      "ds_read_b128 a[48:51], v2 offset:16384\n\t"
      "ds_read_b128 a[52:55], v2 offset:17408\n\t"
      "ds_read_b128 a[56:59], v3 offset:16384\n\t"
      "ds_read_b128 a[60:63], v3 offset:17408\n\t"
      "ds_read_b64_tr_b16 a[64:65], v8 offset:0\n\t"
      "ds_read_b64_tr_b16 a[66:67], v8 offset:4096\n\t"
      "ds_read_b64_tr_b16 a[68:69], v9 offset:0\n\t"
      "ds_read_b64_tr_b16 a[70:71], v9 offset:4096\n\t"
      "ds_read_b64_tr_b16 a[72:73], v10 offset:0\n\t"
      "ds_read_b64_tr_b16 a[74:75], v10 offset:4096\n\t"
      "ds_read_b64_tr_b16 a[76:77], v11 offset:0\n\t"
      "ds_read_b64_tr_b16 a[78:79], v11 offset:4096\n\t"
      "ds_read_b64_tr_b16 a[80:81], v8 offset:8192\n\t"
      "ds_read_b64_tr_b16 a[82:83], v8 offset:12288\n\t"
      "ds_read_b64_tr_b16 a[84:85], v9 offset:8192\n\t"
      "ds_read_b64_tr_b16 a[86:87], v9 offset:12288\n\t"
      "ds_read_b64_tr_b16 a[88:89], v10 offset:8192\n\t"
      "ds_read_b64_tr_b16 a[90:91], v10 offset:12288\n\t"
      "ds_read_b64_tr_b16 a[92:93], v11 offset:8192\n\t"
      "ds_read_b64_tr_b16 a[94:95], v11 offset:12288\n\t"
      "LB_loop_%=:\n\t"
      "s_waitcnt vmcnt(12) lgkmcnt(0)\n\t"
      "s_barrier\n\t"
      "v_mfma_f32_16x16x32_bf16 v[128:131], a[64:67], a[0:3], v[128:131]\n\t"
      "s_mov_b32 s51, s50\n\t"
      "s_add_u32 s50, s50, 49152\n\t"
      "s_cmp_eq_u32 s50, 147456\n\t"
      "v_mfma_f32_16x16x32_bf16 v[128:131], a[80:83], a[32:35], v[128:131]\n\t"
      "s_cselect_b32 s50, 0, s50\n\t"
      "s_sub_u32 s52, s50, s51\n\t"
      "v_mfma_f32_16x16x32_bf16 v[144:147], a[64:67], a[4:7], v[144:147]\n\t"
      "v_add_u32 v0, s52, v0\n\t"
      "v_add_u32 v1, s52, v1\n\t"
      "s_add_u32 s55, s48, 4096\n\t"
      "s_add_u32 s54, s44, s51\n\t"
      "v_mfma_f32_16x16x32_bf16 v[144:147], a[80:83], a[36:39], v[144:147]\n\t"
      "v_add_u32 v2, s52, v2\n\t"
      "v_add_u32 v3, s52, v3\n\t"
      "s_add_u32 m0, s45, s51\n\t"
      "v_mfma_f32_16x16x32_bf16 v[160:163], a[64:67], a[8:11], v[160:163]\n\t"
      "v_add_u32 v8, s52, v8\n\t"
      "v_add_u32 v9, s52, v9\n\t"
      "buffer_load_dwordx4 v20, s[40:43], s49 offen lds\n\t"
      "v_mfma_f32_16x16x32_bf16 v[160:163], a[80:83], a[40:43], v[160:163]\n\t"
      "v_add_u32 v10, s52, v10\n\t"
      "v_add_u32 v11, s52, v11\n\t"
      "v_mfma_f32_16x16x32_bf16 v[176:179], a[64:67], a[12:15], v[176:179]\n\t"
      "ds_read_b128 a[96:99], v0 offset:0\n\t"
      "v_mfma_f32_16x16x32_bf16 v[176:179], a[80:83], a[44:47], v[176:179]\n\t"
      "ds_read_b128 a[100:103], v0 offset:1024\n\t"
      "buffer_load_dwordx4 v21, s[40:43], s49 offen offset:1024 lds\n\t"
      "v_mfma_f32_16x16x32_bf16 v[192:195], a[64:67], a[16:19], v[192:195]\n\t"
      "v_mfma_f32_16x16x32_bf16 v[192:195], a[80:83], a[48:51], v[192:195]\n\t"
      "ds_read_b128 a[104:107], v1 offset:0\n\t"
      "v_mfma_f32_16x16x32_bf16 v[208:211], a[64:67], a[20:23], v[208:211]\n\t"
      "ds_read_b128 a[108:111], v1 offset:1024\n\t"
      "buffer_load_dwordx4 v22, s[40:43], s49 offen offset:2048 lds\n\t"
      "v_mfma_f32_16x16x32_bf16 v[208:211], a[80:83], a[52:55], v[208:211]\n\t"
      "v_mfma_f32_16x16x32_bf16 v[224:227], a[64:67], a[24:27], v[224:227]\n\t"
      "ds_read_b128 a[112:115], v2 offset:0\n\t"
      "v_mfma_f32_16x16x32_bf16 v[224:227], a[80:83], a[56:59], v[224:227]\n\t"
      "buffer_load_dwordx4 v23, s[40:43], s49 offen offset:3072 lds\n\t"
      "v_mfma_f32_16x16x32_bf16 v[240:243], a[64:67], a[28:31], v[240:243]\n\t"
      "ds_read_b128 a[116:119], v2 offset:1024\n\t"
      "v_mfma_f32_16x16x32_bf16 v[240:243], a[80:83], a[60:63], v[240:243]\n\t"
      "ds_read_b128 a[120:123], v3 offset:0\n\t"
      "v_mfma_f32_16x16x32_bf16 v[244:247], a[68:71], a[28:31], v[244:247]\n\t"
      "s_mov_b32 m0, s54\n\t"
      "v_mfma_f32_16x16x32_bf16 v[244:247], a[84:87], a[60:63], v[244:247]\n\t"
      "ds_read_b128 a[124:127], v3 offset:1024\n\t"
      "buffer_load_dwordx4 v16, s[36:39], s48 offen sc1 lds\n\t"
      "v_mfma_f32_16x16x32_bf16 v[228:231], a[68:71], a[24:27], v[228:231]\n\t"
      "v_mfma_f32_16x16x32_bf16 v[228:231], a[84:87], a[56:59], v[228:231]\n\t"
      "ds_read_b128 a[128:131], v0 offset:16384\n\t"
      "v_mfma_f32_16x16x32_bf16 v[212:215], a[68:71], a[20:23], v[212:215]\n\t"
      "ds_read_b128 a[132:135], v0 offset:17408\n\t"
      "v_mfma_f32_16x16x32_bf16 v[212:215], a[84:87], a[52:55], v[212:215]\n\t"
      "v_mfma_f32_16x16x32_bf16 v[196:199], a[68:71], a[16:19], v[196:199]\n\t"
      "ds_read_b128 a[136:139], v1 offset:16384\n\t"
      "buffer_load_dwordx4 v16, s[36:39], s48 offen offset:1024 sc1 lds\n\t"
      "v_mfma_f32_16x16x32_bf16 v[196:199], a[84:87], a[48:51], v[196:199]\n\t"
      "v_mfma_f32_16x16x32_bf16 v[180:183], a[68:71], a[12:15], v[180:183]\n\t"
      "ds_read_b128 a[140:143], v1 offset:17408\n\t"
      "v_mfma_f32_16x16x32_bf16 v[180:183], a[84:87], a[44:47], v[180:183]\n\t"
      "ds_read_b128 a[144:147], v2 offset:16384\n\t"
      "v_mfma_f32_16x16x32_bf16 v[164:167], a[68:71], a[8:11], v[164:167]\n\t"
      "v_mfma_f32_16x16x32_bf16 v[164:167], a[84:87], a[40:43], v[164:167]\n\t"
      "ds_read_b128 a[148:151], v2 offset:17408\n\t"
      "v_mfma_f32_16x16x32_bf16 v[148:151], a[68:71], a[4:7], v[148:151]\n\t"
      "buffer_load_dwordx4 v16, s[36:39], s48 offen offset:2048 sc1 lds\n\t"
      "v_mfma_f32_16x16x32_bf16 v[148:151], a[84:87], a[36:39], v[148:151]\n\t"
      "ds_read_b128 a[152:155], v3 offset:16384\n\t"
      "v_mfma_f32_16x16x32_bf16 v[132:135], a[68:71], a[0:3], v[132:135]\n\t"
      "ds_read_b128 a[156:159], v3 offset:17408\n\t"
      "v_mfma_f32_16x16x32_bf16 v[132:135], a[84:87], a[32:35], v[132:135]\n\t"
      "v_mfma_f32_16x16x32_bf16 v[136:139], a[72:75], a[0:3], v[136:139]\n\t"
      "ds_read_b64_tr_b16 a[160:161], v8 offset:0\n\t"
      "v_mfma_f32_16x16x32_bf16 v[136:139], a[88:91], a[32:35], v[136:139]\n\t"
      "ds_read_b64_tr_b16 a[162:163], v8 offset:4096\n\t"
      "buffer_load_dwordx4 v16, s[36:39], s48 offen offset:3072 sc1 lds\n\t"
      "v_mfma_f32_16x16x32_bf16 v[152:155], a[72:75], a[4:7], v[152:155]\n\t"
      "v_mfma_f32_16x16x32_bf16 v[152:155], a[88:91], a[36:39], v[152:155]\n\t"
      "ds_read_b64_tr_b16 a[164:165], v9 offset:0\n\t"
      "v_mfma_f32_16x16x32_bf16 v[168:171], a[72:75], a[8:11], v[168:171]\n\t"
      "v_mfma_f32_16x16x32_bf16 v[168:171], a[88:91], a[40:43], v[168:171]\n\t"
      "ds_read_b64_tr_b16 a[166:167], v9 offset:4096\n\t"
      "v_mfma_f32_16x16x32_bf16 v[184:187], a[72:75], a[12:15], v[184:187]\n\t"
      "ds_read_b64_tr_b16 a[168:169], v10 offset:0\n\t"
      "s_add_u32 m0, s54, 4096\n\t"
      "v_mfma_f32_16x16x32_bf16 v[184:187], a[88:91], a[44:47], v[184:187]\n\t"
      "buffer_load_dwordx4 v16, s[36:39], s55 offen sc1 lds\n\t"
      "v_mfma_f32_16x16x32_bf16 v[200:203], a[72:75], a[16:19], v[200:203]\n\t"
      "ds_read_b64_tr_b16 a[170:171], v10 offset:4096\n\t"
      "v_mfma_f32_16x16x32_bf16 v[200:203], a[88:91], a[48:51], v[200:203]\n\t"
      "v_mfma_f32_16x16x32_bf16 v[216:219], a[72:75], a[20:23], v[216:219]\n\t"
      "ds_read_b64_tr_b16 a[172:173], v11 offset:0\n\t"
      "v_mfma_f32_16x16x32_bf16 v[216:219], a[88:91], a[52:55], v[216:219]\n\t"
      "ds_read_b64_tr_b16 a[174:175], v11 offset:4096\n\t"
      "v_mfma_f32_16x16x32_bf16 v[232:235], a[72:75], a[24:27], v[232:235]\n\t"
      "buffer_load_dwordx4 v16, s[36:39], s55 offen offset:1024 sc1 lds\n\t"
      "v_mfma_f32_16x16x32_bf16 v[232:235], a[88:91], a[56:59], v[232:235]\n\t"
      "ds_read_b64_tr_b16 a[176:177], v8 offset:8192\n\t"
      "v_mfma_f32_16x16x32_bf16 v[248:251], a[72:75], a[28:31], v[248:251]\n\t"
      "v_mfma_f32_16x16x32_bf16 v[248:251], a[88:91], a[60:63], v[248:251]\n\t"
      "ds_read_b64_tr_b16 a[178:179], v8 offset:12288\n\t"
      "v_mfma_f32_16x16x32_bf16 v[252:255], a[76:79], a[28:31], v[252:255]\n\t"
      "ds_read_b64_tr_b16 a[180:181], v9 offset:8192\n\t"
      "v_mfma_f32_16x16x32_bf16 v[252:255], a[92:95], a[60:63], v[252:255]\n\t"
      "v_mfma_f32_16x16x32_bf16 v[236:239], a[76:79], a[24:27], v[236:239]\n\t"
      "ds_read_b64_tr_b16 a[182:183], v9 offset:12288\n\t"
      "buffer_load_dwordx4 v16, s[36:39], s55 offen offset:2048 sc1 lds\n\t"
      "v_mfma_f32_16x16x32_bf16 v[236:239], a[92:95], a[56:59], v[236:239]\n\t"
      "v_mfma_f32_16x16x32_bf16 v[220:223], a[76:79], a[20:23], v[220:223]\n\t"
      "ds_read_b64_tr_b16 a[184:185], v10 offset:8192\n\t"
      "v_mfma_f32_16x16x32_bf16 v[220:223], a[92:95], a[52:55], v[220:223]\n\t"
      "ds_read_b64_tr_b16 a[186:187], v10 offset:12288\n\t"
      "v_mfma_f32_16x16x32_bf16 v[204:207], a[76:79], a[16:19], v[204:207]\n\t"
      "v_mfma_f32_16x16x32_bf16 v[204:207], a[92:95], a[48:51], v[204:207]\n\t"
      "ds_read_b64_tr_b16 a[188:189], v11 offset:8192\n\t"
      "v_mfma_f32_16x16x32_bf16 v[188:191], a[76:79], a[12:15], v[188:191]\n\t"
      "buffer_load_dwordx4 v16, s[36:39], s55 offen offset:3072 sc1 lds\n\t"
      "v_mfma_f32_16x16x32_bf16 v[188:191], a[92:95], a[44:47], v[188:191]\n\t"
      "ds_read_b64_tr_b16 a[190:191], v11 offset:12288\n\t"
      "v_mfma_f32_16x16x32_bf16 v[172:175], a[76:79], a[8:11], v[172:175]\n\t"
      "s_add_u32 s48, s48, 262144\n\t"
      "s_add_u32 s49, s49, 131072\n\t"
      "v_mfma_f32_16x16x32_bf16 v[172:175], a[92:95], a[40:43], v[172:175]\n\t"
      "v_mfma_f32_16x16x32_bf16 v[156:159], a[76:79], a[4:7], v[156:159]\n\t"
      "v_mfma_f32_16x16x32_bf16 v[156:159], a[92:95], a[36:39], v[156:159]\n\t"
      "v_mfma_f32_16x16x32_bf16 v[140:143], a[76:79], a[0:3], v[140:143]\n\t"
      "v_mfma_f32_16x16x32_bf16 v[140:143], a[92:95], a[32:35], v[140:143]\n\t"
      "s_waitcnt vmcnt(12) lgkmcnt(0)\n\t"
      "s_barrier\n\t"
      "v_mfma_f32_16x16x32_bf16 v[128:131], a[160:163], a[96:99], v[128:131]\n\t"
      "s_mov_b32 s51, s50\n\t"
      "s_add_u32 s50, s50, 49152\n\t"
      "s_cmp_eq_u32 s50, 147456\n\t"
      "v_mfma_f32_16x16x32_bf16 v[128:131], a[176:179], a[128:131], v[128:131]\n\t"
      "s_cselect_b32 s50, 0, s50\n\t"
      "s_sub_u32 s52, s50, s51\n\t"
      "v_mfma_f32_16x16x32_bf16 v[144:147], a[160:163], a[100:103], v[144:147]\n\t"
      "v_add_u32 v0, s52, v0\n\t"
      "v_add_u32 v1, s52, v1\n\t"
      "s_add_u32 s55, s48, 4096\n\t"
      "s_add_u32 s54, s44, s51\n\t"
      "v_mfma_f32_16x16x32_bf16 v[144:147], a[176:179], a[132:135], v[144:147]\n\t"
      "v_add_u32 v2, s52, v2\n\t"
      "v_add_u32 v3, s52, v3\n\t"
      "s_add_u32 m0, s45, s51\n\t"
      "v_mfma_f32_16x16x32_bf16 v[160:163], a[160:163], a[104:107], v[160:163]\n\t"
      "v_add_u32 v8, s52, v8\n\t"
      "v_add_u32 v9, s52, v9\n\t"
      "buffer_load_dwordx4 v20, s[40:43], s49 offen lds\n\t"
      "v_mfma_f32_16x16x32_bf16 v[160:163], a[176:179], a[136:139], v[160:163]\n\t"
      "v_add_u32 v10, s52, v10\n\t"
      "v_add_u32 v11, s52, v11\n\t"
      "v_mfma_f32_16x16x32_bf16 v[176:179], a[160:163], a[108:111], v[176:179]\n\t"
      "ds_read_b128 a[0:3], v0 offset:0\n\t"
      "v_mfma_f32_16x16x32_bf16 v[176:179], a[176:179], a[140:143], v[176:179]\n\t"
      "ds_read_b128 a[4:7], v0 offset:1024\n\t"
      "buffer_load_dwordx4 v21, s[40:43], s49 offen offset:1024 lds\n\t"
      "v_mfma_f32_16x16x32_bf16 v[192:195], a[160:163], a[112:115], v[192:195]\n\t"
      "v_mfma_f32_16x16x32_bf16 v[192:195], a[176:179], a[144:147], v[192:195]\n\t"
      "ds_read_b128 a[8:11], v1 offset:0\n\t"
      "v_mfma_f32_16x16x32_bf16 v[208:211], a[160:163], a[116:119], v[208:211]\n\t"
      "ds_read_b128 a[12:15], v1 offset:1024\n\t"
      "buffer_load_dwordx4 v22, s[40:43], s49 offen offset:2048 lds\n\t"
      "v_mfma_f32_16x16x32_bf16 v[208:211], a[176:179], a[148:151], v[208:211]\n\t"
      "v_mfma_f32_16x16x32_bf16 v[224:227], a[160:163], a[120:123], v[224:227]\n\t"
      "ds_read_b128 a[16:19], v2 offset:0\n\t"
      "v_mfma_f32_16x16x32_bf16 v[224:227], a[176:179], a[152:155], v[224:227]\n\t"
      "buffer_load_dwordx4 v23, s[40:43], s49 offen offset:3072 lds\n\t"
      "v_mfma_f32_16x16x32_bf16 v[240:243], a[160:163], a[124:127], v[240:243]\n\t"
      "ds_read_b128 a[20:23], v2 offset:1024\n\t"
      "v_mfma_f32_16x16x32_bf16 v[240:243], a[176:179], a[156:159], v[240:243]\n\t"
      "ds_read_b128 a[24:27], v3 offset:0\n\t"
      "v_mfma_f32_16x16x32_bf16 v[244:247], a[164:167], a[124:127], v[244:247]\n\t"
      "s_mov_b32 m0, s54\n\t"
      "v_mfma_f32_16x16x32_bf16 v[244:247], a[180:183], a[156:159], v[244:247]\n\t"
      "ds_read_b128 a[28:31], v3 offset:1024\n\t"
      "buffer_load_dwordx4 v16, s[36:39], s48 offen sc1 lds\n\t"
      "v_mfma_f32_16x16x32_bf16 v[228:231], a[164:167], a[120:123], v[228:231]\n\t"
      "v_mfma_f32_16x16x32_bf16 v[228:231], a[180:183], a[152:155], v[228:231]\n\t"
      "ds_read_b128 a[32:35], v0 offset:16384\n\t"
      "v_mfma_f32_16x16x32_bf16 v[212:215], a[164:167], a[116:119], v[212:215]\n\t"
      "ds_read_b128 a[36:39], v0 offset:17408\n\t"
      "v_mfma_f32_16x16x32_bf16 v[212:215], a[180:183], a[148:151], v[212:215]\n\t"
      "v_mfma_f32_16x16x32_bf16 v[196:199], a[164:167], a[112:115], v[196:199]\n\t"
      "ds_read_b128 a[40:43], v1 offset:16384\n\t"
      "buffer_load_dwordx4 v16, s[36:39], s48 offen offset:1024 sc1 lds\n\t"
      "v_mfma_f32_16x16x32_bf16 v[196:199], a[180:183], a[144:147], v[196:199]\n\t"
      "v_mfma_f32_16x16x32_bf16 v[180:183], a[164:167], a[108:111], v[180:183]\n\t"
      "ds_read_b128 a[44:47], v1 offset:17408\n\t"
      "v_mfma_f32_16x16x32_bf16 v[180:183], a[180:183], a[140:143], v[180:183]\n\t"
      "ds_read_b128 a[48:51], v2 offset:16384\n\t"
      "v_mfma_f32_16x16x32_bf16 v[164:167], a[164:167], a[104:107], v[164:167]\n\t"
      "v_mfma_f32_16x16x32_bf16 v[164:167], a[180:183], a[136:139], v[164:167]\n\t"
      "ds_read_b128 a[52:55], v2 offset:17408\n\t"
      "v_mfma_f32_16x16x32_bf16 v[148:151], a[164:167], a[100:103], v[148:151]\n\t"
      "buffer_load_dwordx4 v16, s[36:39], s48 offen offset:2048 sc1 lds\n\t"
      "v_mfma_f32_16x16x32_bf16 v[148:151], a[180:183], a[132:135], v[148:151]\n\t"
      "ds_read_b128 a[56:59], v3 offset:16384\n\t"
      "v_mfma_f32_16x16x32_bf16 v[132:135], a[164:167], a[96:99], v[132:135]\n\t"
      "ds_read_b128 a[60:63], v3 offset:17408\n\t"
      "v_mfma_f32_16x16x32_bf16 v[132:135], a[180:183], a[128:131], v[132:135]\n\t"
      "v_mfma_f32_16x16x32_bf16 v[136:139], a[168:171], a[96:99], v[136:139]\n\t"
      "ds_read_b64_tr_b16 a[64:65], v8 offset:0\n\t"
      "v_mfma_f32_16x16x32_bf16 v[136:139], a[184:187], a[128:131], v[136:139]\n\t"
      "ds_read_b64_tr_b16 a[66:67], v8 offset:4096\n\t"
      "buffer_load_dwordx4 v16, s[36:39], s48 offen offset:3072 sc1 lds\n\t"
      "v_mfma_f32_16x16x32_bf16 v[152:155], a[168:171], a[100:103], v[152:155]\n\t"
      "v_mfma_f32_16x16x32_bf16 v[152:155], a[184:187], a[132:135], v[152:155]\n\t"
      "ds_read_b64_tr_b16 a[68:69], v9 offset:0\n\t"
      "v_mfma_f32_16x16x32_bf16 v[168:171], a[168:171], a[104:107], v[168:171]\n\t"
      "v_mfma_f32_16x16x32_bf16 v[168:171], a[184:187], a[136:139], v[168:171]\n\t"
      "ds_read_b64_tr_b16 a[70:71], v9 offset:4096\n\t"
      "v_mfma_f32_16x16x32_bf16 v[184:187], a[168:171], a[108:111], v[184:187]\n\t"
      "ds_read_b64_tr_b16 a[72:73], v10 offset:0\n\t"
      "s_add_u32 m0, s54, 4096\n\t"
      "v_mfma_f32_16x16x32_bf16 v[184:187], a[184:187], a[140:143], v[184:187]\n\t"
      "buffer_load_dwordx4 v16, s[36:39], s55 offen sc1 lds\n\t"
      "v_mfma_f32_16x16x32_bf16 v[200:203], a[168:171], a[112:115], v[200:203]\n\t"
      "ds_read_b64_tr_b16 a[74:75], v10 offset:4096\n\t"
      "v_mfma_f32_16x16x32_bf16 v[200:203], a[184:187], a[144:147], v[200:203]\n\t"
      "v_mfma_f32_16x16x32_bf16 v[216:219], a[168:171], a[116:119], v[216:219]\n\t"
      "ds_read_b64_tr_b16 a[76:77], v11 offset:0\n\t"
      "v_mfma_f32_16x16x32_bf16 v[216:219], a[184:187], a[148:151], v[216:219]\n\t"
      "ds_read_b64_tr_b16 a[78:79], v11 offset:4096\n\t"
      "v_mfma_f32_16x16x32_bf16 v[232:235], a[168:171], a[120:123], v[232:235]\n\t"
      "buffer_load_dwordx4 v16, s[36:39], s55 offen offset:1024 sc1 lds\n\t"
      "v_mfma_f32_16x16x32_bf16 v[232:235], a[184:187], a[152:155], v[232:235]\n\t"
      "ds_read_b64_tr_b16 a[80:81], v8 offset:8192\n\t"
      "v_mfma_f32_16x16x32_bf16 v[248:251], a[168:171], a[124:127], v[248:251]\n\t"
      "v_mfma_f32_16x16x32_bf16 v[248:251], a[184:187], a[156:159], v[248:251]\n\t"
      "ds_read_b64_tr_b16 a[82:83], v8 offset:12288\n\t"
      "v_mfma_f32_16x16x32_bf16 v[252:255], a[172:175], a[124:127], v[252:255]\n\t"
      "ds_read_b64_tr_b16 a[84:85], v9 offset:8192\n\t"
      "v_mfma_f32_16x16x32_bf16 v[252:255], a[188:191], a[156:159], v[252:255]\n\t"
      "v_mfma_f32_16x16x32_bf16 v[236:239], a[172:175], a[120:123], v[236:239]\n\t"
      "ds_read_b64_tr_b16 a[86:87], v9 offset:12288\n\t"
      "buffer_load_dwordx4 v16, s[36:39], s55 offen offset:2048 sc1 lds\n\t"
      "v_mfma_f32_16x16x32_bf16 v[236:239], a[188:191], a[152:155], v[236:239]\n\t"
      "v_mfma_f32_16x16x32_bf16 v[220:223], a[172:175], a[116:119], v[220:223]\n\t"
      "ds_read_b64_tr_b16 a[88:89], v10 offset:8192\n\t"
      "v_mfma_f32_16x16x32_bf16 v[220:223], a[188:191], a[148:151], v[220:223]\n\t"
      "ds_read_b64_tr_b16 a[90:91], v10 offset:12288\n\t"
      "v_mfma_f32_16x16x32_bf16 v[204:207], a[172:175], a[112:115], v[204:207]\n\t"
      "v_mfma_f32_16x16x32_bf16 v[204:207], a[188:191], a[144:147], v[204:207]\n\t"
      "ds_read_b64_tr_b16 a[92:93], v11 offset:8192\n\t"
      "v_mfma_f32_16x16x32_bf16 v[188:191], a[172:175], a[108:111], v[188:191]\n\t"
      "buffer_load_dwordx4 v16, s[36:39], s55 offen offset:3072 sc1 lds\n\t"
      "v_mfma_f32_16x16x32_bf16 v[188:191], a[188:191], a[140:143], v[188:191]\n\t"
      "ds_read_b64_tr_b16 a[94:95], v11 offset:12288\n\t"
      "v_mfma_f32_16x16x32_bf16 v[172:175], a[172:175], a[104:107], v[172:175]\n\t"
      "s_add_u32 s48, s48, 262144\n\t"
      "s_add_u32 s49, s49, 131072\n\t"
      "v_mfma_f32_16x16x32_bf16 v[172:175], a[188:191], a[136:139], v[172:175]\n\t"
      "v_mfma_f32_16x16x32_bf16 v[156:159], a[172:175], a[100:103], v[156:159]\n\t"
      "s_sub_u32 s53, s53, 1\n\t"
      "v_mfma_f32_16x16x32_bf16 v[156:159], a[188:191], a[132:135], v[156:159]\n\t"
      "v_mfma_f32_16x16x32_bf16 v[140:143], a[172:175], a[96:99], v[140:143]\n\t"
      "s_cmp_lg_u32 s53, 0\n\t"
      "v_mfma_f32_16x16x32_bf16 v[140:143], a[188:191], a[128:131], v[140:143]\n\t"
      "s_cbranch_scc1 LB_loop_%=\n\t"
      "s_waitcnt vmcnt(12) lgkmcnt(0)\n\t"
      "s_barrier\n\t"
      "v_mfma_f32_16x16x32_bf16 v[128:131], a[64:67], a[0:3], v[128:131]\n\t"
      "s_mov_b32 s51, s50\n\t"
      "s_add_u32 s50, s50, 49152\n\t"
      "s_cmp_eq_u32 s50, 147456\n\t"
      "v_mfma_f32_16x16x32_bf16 v[128:131], a[80:83], a[32:35], v[128:131]\n\t"
      "s_cselect_b32 s50, 0, s50\n\t"
      "s_sub_u32 s52, s50, s51\n\t"
      "v_mfma_f32_16x16x32_bf16 v[144:147], a[64:67], a[4:7], v[144:147]\n\t"
      "v_add_u32 v0, s52, v0\n\t"
      "v_add_u32 v1, s52, v1\n\t"
      "s_add_u32 s55, s48, 4096\n\t"
      "s_add_u32 s54, s44, s51\n\t"
      "v_mfma_f32_16x16x32_bf16 v[144:147], a[80:83], a[36:39], v[144:147]\n\t"
      "v_add_u32 v2, s52, v2\n\t"
      "v_add_u32 v3, s52, v3\n\t"
      "s_add_u32 m0, s45, s51\n\t"
      "v_mfma_f32_16x16x32_bf16 v[160:163], a[64:67], a[8:11], v[160:163]\n\t"
      "v_add_u32 v8, s52, v8\n\t"
      "v_add_u32 v9, s52, v9\n\t"
      "buffer_load_dwordx4 v20, s[40:43], s49 offen lds\n\t"
      "v_mfma_f32_16x16x32_bf16 v[160:163], a[80:83], a[40:43], v[160:163]\n\t"
      "v_add_u32 v10, s52, v10\n\t"
      "v_add_u32 v11, s52, v11\n\t"
      "v_mfma_f32_16x16x32_bf16 v[176:179], a[64:67], a[12:15], v[176:179]\n\t"
      "ds_read_b128 a[96:99], v0 offset:0\n\t"
      "v_mfma_f32_16x16x32_bf16 v[176:179], a[80:83], a[44:47], v[176:179]\n\t"
      "ds_read_b128 a[100:103], v0 offset:1024\n\t"
      "buffer_load_dwordx4 v21, s[40:43], s49 offen offset:1024 lds\n\t"
      "v_mfma_f32_16x16x32_bf16 v[192:195], a[64:67], a[16:19], v[192:195]\n\t"
      "v_mfma_f32_16x16x32_bf16 v[192:195], a[80:83], a[48:51], v[192:195]\n\t"
      "ds_read_b128 a[104:107], v1 offset:0\n\t"
      "v_mfma_f32_16x16x32_bf16 v[208:211], a[64:67], a[20:23], v[208:211]\n\t"
      "ds_read_b128 a[108:111], v1 offset:1024\n\t"
      "buffer_load_dwordx4 v22, s[40:43], s49 offen offset:2048 lds\n\t"
      "v_mfma_f32_16x16x32_bf16 v[208:211], a[80:83], a[52:55], v[208:211]\n\t"
      "v_mfma_f32_16x16x32_bf16 v[224:227], a[64:67], a[24:27], v[224:227]\n\t"
      "ds_read_b128 a[112:115], v2 offset:0\n\t"
      "v_mfma_f32_16x16x32_bf16 v[224:227], a[80:83], a[56:59], v[224:227]\n\t"
      "buffer_load_dwordx4 v23, s[40:43], s49 offen offset:3072 lds\n\t"
      "v_mfma_f32_16x16x32_bf16 v[240:243], a[64:67], a[28:31], v[240:243]\n\t"
      "ds_read_b128 a[116:119], v2 offset:1024\n\t"
      "v_mfma_f32_16x16x32_bf16 v[240:243], a[80:83], a[60:63], v[240:243]\n\t"
      "ds_read_b128 a[120:123], v3 offset:0\n\t"
      "v_mfma_f32_16x16x32_bf16 v[244:247], a[68:71], a[28:31], v[244:247]\n\t"
      "s_mov_b32 m0, s54\n\t"
      "v_mfma_f32_16x16x32_bf16 v[244:247], a[84:87], a[60:63], v[244:247]\n\t"
      "ds_read_b128 a[124:127], v3 offset:1024\n\t"
      "buffer_load_dwordx4 v16, s[36:39], s48 offen sc1 lds\n\t"
      "v_mfma_f32_16x16x32_bf16 v[228:231], a[68:71], a[24:27], v[228:231]\n\t"
      "v_mfma_f32_16x16x32_bf16 v[228:231], a[84:87], a[56:59], v[228:231]\n\t"
      "ds_read_b128 a[128:131], v0 offset:16384\n\t"
      "v_mfma_f32_16x16x32_bf16 v[212:215], a[68:71], a[20:23], v[212:215]\n\t"
      "ds_read_b128 a[132:135], v0 offset:17408\n\t"
      "v_mfma_f32_16x16x32_bf16 v[212:215], a[84:87], a[52:55], v[212:215]\n\t"
      "v_mfma_f32_16x16x32_bf16 v[196:199], a[68:71], a[16:19], v[196:199]\n\t"
      "ds_read_b128 a[136:139], v1 offset:16384\n\t"
      "buffer_load_dwordx4 v16, s[36:39], s48 offen offset:1024 sc1 lds\n\t"
      "v_mfma_f32_16x16x32_bf16 v[196:199], a[84:87], a[48:51], v[196:199]\n\t"
      "v_mfma_f32_16x16x32_bf16 v[180:183], a[68:71], a[12:15], v[180:183]\n\t"
      "ds_read_b128 a[140:143], v1 offset:17408\n\t"
      "v_mfma_f32_16x16x32_bf16 v[180:183], a[84:87], a[44:47], v[180:183]\n\t"
      "ds_read_b128 a[144:147], v2 offset:16384\n\t"
      "v_mfma_f32_16x16x32_bf16 v[164:167], a[68:71], a[8:11], v[164:167]\n\t"
      "v_mfma_f32_16x16x32_bf16 v[164:167], a[84:87], a[40:43], v[164:167]\n\t"
      "ds_read_b128 a[148:151], v2 offset:17408\n\t"
      "v_mfma_f32_16x16x32_bf16 v[148:151], a[68:71], a[4:7], v[148:151]\n\t"
      "buffer_load_dwordx4 v16, s[36:39], s48 offen offset:2048 sc1 lds\n\t"
      "v_mfma_f32_16x16x32_bf16 v[148:151], a[84:87], a[36:39], v[148:151]\n\t"
      "ds_read_b128 a[152:155], v3 offset:16384\n\t"
      "v_mfma_f32_16x16x32_bf16 v[132:135], a[68:71], a[0:3], v[132:135]\n\t"
      "ds_read_b128 a[156:159], v3 offset:17408\n\t"
      "v_mfma_f32_16x16x32_bf16 v[132:135], a[84:87], a[32:35], v[132:135]\n\t"
      "v_mfma_f32_16x16x32_bf16 v[136:139], a[72:75], a[0:3], v[136:139]\n\t"
      "ds_read_b64_tr_b16 a[160:161], v8 offset:0\n\t"
      "v_mfma_f32_16x16x32_bf16 v[136:139], a[88:91], a[32:35], v[136:139]\n\t"
      "ds_read_b64_tr_b16 a[162:163], v8 offset:4096\n\t"
      "buffer_load_dwordx4 v16, s[36:39], s48 offen offset:3072 sc1 lds\n\t"
      "v_mfma_f32_16x16x32_bf16 v[152:155], a[72:75], a[4:7], v[152:155]\n\t"
      "v_mfma_f32_16x16x32_bf16 v[152:155], a[88:91], a[36:39], v[152:155]\n\t"
      "ds_read_b64_tr_b16 a[164:165], v9 offset:0\n\t"
      "v_mfma_f32_16x16x32_bf16 v[168:171], a[72:75], a[8:11], v[168:171]\n\t"
      "v_mfma_f32_16x16x32_bf16 v[168:171], a[88:91], a[40:43], v[168:171]\n\t"
      "ds_read_b64_tr_b16 a[166:167], v9 offset:4096\n\t"
      "v_mfma_f32_16x16x32_bf16 v[184:187], a[72:75], a[12:15], v[184:187]\n\t"
      "ds_read_b64_tr_b16 a[168:169], v10 offset:0\n\t"
      "s_add_u32 m0, s54, 4096\n\t"
      "v_mfma_f32_16x16x32_bf16 v[184:187], a[88:91], a[44:47], v[184:187]\n\t"
      "buffer_load_dwordx4 v16, s[36:39], s55 offen sc1 lds\n\t"
      "v_mfma_f32_16x16x32_bf16 v[200:203], a[72:75], a[16:19], v[200:203]\n\t"
      "ds_read_b64_tr_b16 a[170:171], v10 offset:4096\n\t"
      "v_mfma_f32_16x16x32_bf16 v[200:203], a[88:91], a[48:51], v[200:203]\n\t"
      "v_mfma_f32_16x16x32_bf16 v[216:219], a[72:75], a[20:23], v[216:219]\n\t"
      "ds_read_b64_tr_b16 a[172:173], v11 offset:0\n\t"
      "v_mfma_f32_16x16x32_bf16 v[216:219], a[88:91], a[52:55], v[216:219]\n\t"
      "ds_read_b64_tr_b16 a[174:175], v11 offset:4096\n\t"
      "v_mfma_f32_16x16x32_bf16 v[232:235], a[72:75], a[24:27], v[232:235]\n\t"
      "buffer_load_dwordx4 v16, s[36:39], s55 offen offset:1024 sc1 lds\n\t"
      "v_mfma_f32_16x16x32_bf16 v[232:235], a[88:91], a[56:59], v[232:235]\n\t"
      "ds_read_b64_tr_b16 a[176:177], v8 offset:8192\n\t"
      "v_mfma_f32_16x16x32_bf16 v[248:251], a[72:75], a[28:31], v[248:251]\n\t"
      "v_mfma_f32_16x16x32_bf16 v[248:251], a[88:91], a[60:63], v[248:251]\n\t"
      "ds_read_b64_tr_b16 a[178:179], v8 offset:12288\n\t"
      "v_mfma_f32_16x16x32_bf16 v[252:255], a[76:79], a[28:31], v[252:255]\n\t"
      "ds_read_b64_tr_b16 a[180:181], v9 offset:8192\n\t"
      "v_mfma_f32_16x16x32_bf16 v[252:255], a[92:95], a[60:63], v[252:255]\n\t"
      "v_mfma_f32_16x16x32_bf16 v[236:239], a[76:79], a[24:27], v[236:239]\n\t"
      "ds_read_b64_tr_b16 a[182:183], v9 offset:12288\n\t"
      "buffer_load_dwordx4 v16, s[36:39], s55 offen offset:2048 sc1 lds\n\t"
      "v_mfma_f32_16x16x32_bf16 v[236:239], a[92:95], a[56:59], v[236:239]\n\t"
      "v_mfma_f32_16x16x32_bf16 v[220:223], a[76:79], a[20:23], v[220:223]\n\t"
      "ds_read_b64_tr_b16 a[184:185], v10 offset:8192\n\t"
      "v_mfma_f32_16x16x32_bf16 v[220:223], a[92:95], a[52:55], v[220:223]\n\t"
      "ds_read_b64_tr_b16 a[186:187], v10 offset:12288\n\t"
      "v_mfma_f32_16x16x32_bf16 v[204:207], a[76:79], a[16:19], v[204:207]\n\t"
      "v_mfma_f32_16x16x32_bf16 v[204:207], a[92:95], a[48:51], v[204:207]\n\t"
      "ds_read_b64_tr_b16 a[188:189], v11 offset:8192\n\t"
      "v_mfma_f32_16x16x32_bf16 v[188:191], a[76:79], a[12:15], v[188:191]\n\t"
      "buffer_load_dwordx4 v16, s[36:39], s55 offen offset:3072 sc1 lds\n\t"
      "v_mfma_f32_16x16x32_bf16 v[188:191], a[92:95], a[44:47], v[188:191]\n\t"
      "ds_read_b64_tr_b16 a[190:191], v11 offset:12288\n\t"
      "v_mfma_f32_16x16x32_bf16 v[172:175], a[76:79], a[8:11], v[172:175]\n\t"
      "s_add_u32 s48, s48, 262144\n\t"
      "s_add_u32 s49, s49, 131072\n\t"
      "v_mfma_f32_16x16x32_bf16 v[172:175], a[92:95], a[40:43], v[172:175]\n\t"
      "v_mfma_f32_16x16x32_bf16 v[156:159], a[76:79], a[4:7], v[156:159]\n\t"
      "v_mfma_f32_16x16x32_bf16 v[156:159], a[92:95], a[36:39], v[156:159]\n\t"
      "v_mfma_f32_16x16x32_bf16 v[140:143], a[76:79], a[0:3], v[140:143]\n\t"
      "v_mfma_f32_16x16x32_bf16 v[140:143], a[92:95], a[32:35], v[140:143]\n\t"
      "s_waitcnt vmcnt(12) lgkmcnt(0)\n\t"
      "s_barrier\n\t"
      "v_mfma_f32_16x16x32_bf16 v[128:131], a[160:163], a[96:99], v[128:131]\n\t"
      "s_mov_b32 s51, s50\n\t"
      "s_add_u32 s50, s50, 49152\n\t"
      "s_cmp_eq_u32 s50, 147456\n\t"
      "v_mfma_f32_16x16x32_bf16 v[128:131], a[176:179], a[128:131], v[128:131]\n\t"
      "s_cselect_b32 s50, 0, s50\n\t"
      "s_sub_u32 s52, s50, s51\n\t"
      "v_mfma_f32_16x16x32_bf16 v[144:147], a[160:163], a[100:103], v[144:147]\n\t"
      "v_add_u32 v0, s52, v0\n\t"
      "v_add_u32 v1, s52, v1\n\t"
      "v_mfma_f32_16x16x32_bf16 v[144:147], a[176:179], a[132:135], v[144:147]\n\t"
      "v_add_u32 v2, s52, v2\n\t"
      "v_add_u32 v3, s52, v3\n\t"
      "v_mfma_f32_16x16x32_bf16 v[160:163], a[160:163], a[104:107], v[160:163]\n\t"
      "buffer_load_dwordx2 v[64:65], v35, s[64:67], 0 offen\n\t"
      "v_add_u32 v8, s52, v8\n\t"
      "v_add_u32 v9, s52, v9\n\t"
      "v_mfma_f32_16x16x32_bf16 v[160:163], a[176:179], a[136:139], v[160:163]\n\t"
      "v_add_u32 v10, s52, v10\n\t"
      "v_add_u32 v11, s52, v11\n\t"
      "v_mfma_f32_16x16x32_bf16 v[176:179], a[160:163], a[108:111], v[176:179]\n\t"
      "ds_read_b128 a[0:3], v0 offset:0\n\t"
      "v_mfma_f32_16x16x32_bf16 v[176:179], a[176:179], a[140:143], v[176:179]\n\t"
      "ds_read_b128 a[4:7], v0 offset:1024\n\t"
      "v_mfma_f32_16x16x32_bf16 v[192:195], a[160:163], a[112:115], v[192:195]\n\t"
      "v_mfma_f32_16x16x32_bf16 v[192:195], a[176:179], a[144:147], v[192:195]\n\t"
      "ds_read_b128 a[8:11], v1 offset:0\n\t"
      "v_mfma_f32_16x16x32_bf16 v[208:211], a[160:163], a[116:119], v[208:211]\n\t"
      "buffer_load_dwordx2 v[66:67], v35, s[64:67], 0 offen offset:32\n\t"
      "ds_read_b128 a[12:15], v1 offset:1024\n\t"
      "v_mfma_f32_16x16x32_bf16 v[208:211], a[176:179], a[148:151], v[208:211]\n\t"
      "v_mfma_f32_16x16x32_bf16 v[224:227], a[160:163], a[120:123], v[224:227]\n\t"
      "ds_read_b128 a[16:19], v2 offset:0\n\t"
      "v_mfma_f32_16x16x32_bf16 v[224:227], a[176:179], a[152:155], v[224:227]\n\t"
      "v_mfma_f32_16x16x32_bf16 v[240:243], a[160:163], a[124:127], v[240:243]\n\t"
      "ds_read_b128 a[20:23], v2 offset:1024\n\t"
      "v_mfma_f32_16x16x32_bf16 v[240:243], a[176:179], a[156:159], v[240:243]\n\t"
      "ds_read_b128 a[24:27], v3 offset:0\n\t"
      "v_mfma_f32_16x16x32_bf16 v[244:247], a[164:167], a[124:127], v[244:247]\n\t"
      "buffer_load_dwordx2 v[68:69], v35, s[64:67], 0 offen offset:64\n\t"
      "v_mfma_f32_16x16x32_bf16 v[244:247], a[180:183], a[156:159], v[244:247]\n\t"
      "ds_read_b128 a[28:31], v3 offset:1024\n\t"
      "v_mfma_f32_16x16x32_bf16 v[228:231], a[164:167], a[120:123], v[228:231]\n\t"
      "v_mfma_f32_16x16x32_bf16 v[228:231], a[180:183], a[152:155], v[228:231]\n\t"
      "ds_read_b128 a[32:35], v0 offset:16384\n\t"
      "v_mfma_f32_16x16x32_bf16 v[212:215], a[164:167], a[116:119], v[212:215]\n\t"
      "ds_read_b128 a[36:39], v0 offset:17408\n\t"
      "v_mfma_f32_16x16x32_bf16 v[212:215], a[180:183], a[148:151], v[212:215]\n\t"
      "v_mfma_f32_16x16x32_bf16 v[196:199], a[164:167], a[112:115], v[196:199]\n\t"
      "buffer_load_dwordx2 v[70:71], v35, s[64:67], 0 offen offset:96\n\t"
      "ds_read_b128 a[40:43], v1 offset:16384\n\t"
      "v_mfma_f32_16x16x32_bf16 v[196:199], a[180:183], a[144:147], v[196:199]\n\t"
      "v_mfma_f32_16x16x32_bf16 v[180:183], a[164:167], a[108:111], v[180:183]\n\t"
      "ds_read_b128 a[44:47], v1 offset:17408\n\t"
      "v_mfma_f32_16x16x32_bf16 v[180:183], a[180:183], a[140:143], v[180:183]\n\t"
      "ds_read_b128 a[48:51], v2 offset:16384\n\t"
      "v_mfma_f32_16x16x32_bf16 v[164:167], a[164:167], a[104:107], v[164:167]\n\t"
      "v_mfma_f32_16x16x32_bf16 v[164:167], a[180:183], a[136:139], v[164:167]\n\t"
      "ds_read_b128 a[52:55], v2 offset:17408\n\t"
      "v_mfma_f32_16x16x32_bf16 v[148:151], a[164:167], a[100:103], v[148:151]\n\t"
      "buffer_load_dwordx2 v[72:73], v36, s[64:67], 0 offen\n\t"
      "v_mfma_f32_16x16x32_bf16 v[148:151], a[180:183], a[132:135], v[148:151]\n\t"
      "ds_read_b128 a[56:59], v3 offset:16384\n\t"
      "v_mfma_f32_16x16x32_bf16 v[132:135], a[164:167], a[96:99], v[132:135]\n\t"
      "ds_read_b128 a[60:63], v3 offset:17408\n\t"
      "v_mfma_f32_16x16x32_bf16 v[132:135], a[180:183], a[128:131], v[132:135]\n\t"
      "v_mfma_f32_16x16x32_bf16 v[136:139], a[168:171], a[96:99], v[136:139]\n\t"
      "ds_read_b64_tr_b16 a[64:65], v8 offset:0\n\t"
      "v_mfma_f32_16x16x32_bf16 v[136:139], a[184:187], a[128:131], v[136:139]\n\t"
      "ds_read_b64_tr_b16 a[66:67], v8 offset:4096\n\t"
      "v_mfma_f32_16x16x32_bf16 v[152:155], a[168:171], a[100:103], v[152:155]\n\t"
      "buffer_load_dwordx2 v[74:75], v36, s[64:67], 0 offen offset:32\n\t"
      "v_mfma_f32_16x16x32_bf16 v[152:155], a[184:187], a[132:135], v[152:155]\n\t"
      "ds_read_b64_tr_b16 a[68:69], v9 offset:0\n\t"
      "v_mfma_f32_16x16x32_bf16 v[168:171], a[168:171], a[104:107], v[168:171]\n\t"
      "v_mfma_f32_16x16x32_bf16 v[168:171], a[184:187], a[136:139], v[168:171]\n\t"
      "ds_read_b64_tr_b16 a[70:71], v9 offset:4096\n\t"
      "v_mfma_f32_16x16x32_bf16 v[184:187], a[168:171], a[108:111], v[184:187]\n\t"
      "ds_read_b64_tr_b16 a[72:73], v10 offset:0\n\t"
      "v_mfma_f32_16x16x32_bf16 v[184:187], a[184:187], a[140:143], v[184:187]\n\t"
      "v_mfma_f32_16x16x32_bf16 v[200:203], a[168:171], a[112:115], v[200:203]\n\t"
      "buffer_load_dwordx2 v[76:77], v36, s[64:67], 0 offen offset:64\n\t"
      "ds_read_b64_tr_b16 a[74:75], v10 offset:4096\n\t"
      "v_mfma_f32_16x16x32_bf16 v[200:203], a[184:187], a[144:147], v[200:203]\n\t"
      "v_mfma_f32_16x16x32_bf16 v[216:219], a[168:171], a[116:119], v[216:219]\n\t"
      "ds_read_b64_tr_b16 a[76:77], v11 offset:0\n\t"
      "v_mfma_f32_16x16x32_bf16 v[216:219], a[184:187], a[148:151], v[216:219]\n\t"
      "ds_read_b64_tr_b16 a[78:79], v11 offset:4096\n\t"
      "v_mfma_f32_16x16x32_bf16 v[232:235], a[168:171], a[120:123], v[232:235]\n\t"
      "v_mfma_f32_16x16x32_bf16 v[232:235], a[184:187], a[152:155], v[232:235]\n\t"
      "ds_read_b64_tr_b16 a[80:81], v8 offset:8192\n\t"
      "v_mfma_f32_16x16x32_bf16 v[248:251], a[168:171], a[124:127], v[248:251]\n\t"
      "buffer_load_dwordx2 v[78:79], v36, s[64:67], 0 offen offset:96\n\t"
      "v_mfma_f32_16x16x32_bf16 v[248:251], a[184:187], a[156:159], v[248:251]\n\t"
      "ds_read_b64_tr_b16 a[82:83], v8 offset:12288\n\t"
      "v_mfma_f32_16x16x32_bf16 v[252:255], a[172:175], a[124:127], v[252:255]\n\t"
      "ds_read_b64_tr_b16 a[84:85], v9 offset:8192\n\t"
      "v_mfma_f32_16x16x32_bf16 v[252:255], a[188:191], a[156:159], v[252:255]\n\t"
      "v_mfma_f32_16x16x32_bf16 v[236:239], a[172:175], a[120:123], v[236:239]\n\t"
      "ds_read_b64_tr_b16 a[86:87], v9 offset:12288\n\t"
      "v_mfma_f32_16x16x32_bf16 v[236:239], a[188:191], a[152:155], v[236:239]\n\t"
      "v_mfma_f32_16x16x32_bf16 v[220:223], a[172:175], a[116:119], v[220:223]\n\t"
      "ds_read_b64_tr_b16 a[88:89], v10 offset:8192\n\t"
      "v_mfma_f32_16x16x32_bf16 v[220:223], a[188:191], a[148:151], v[220:223]\n\t"
      "ds_read_b64_tr_b16 a[90:91], v10 offset:12288\n\t"
      "v_mfma_f32_16x16x32_bf16 v[204:207], a[172:175], a[112:115], v[204:207]\n\t"
      "v_mfma_f32_16x16x32_bf16 v[204:207], a[188:191], a[144:147], v[204:207]\n\t"
      "ds_read_b64_tr_b16 a[92:93], v11 offset:8192\n\t"
      "v_mfma_f32_16x16x32_bf16 v[188:191], a[172:175], a[108:111], v[188:191]\n\t"
      "v_mfma_f32_16x16x32_bf16 v[188:191], a[188:191], a[140:143], v[188:191]\n\t"
      "ds_read_b64_tr_b16 a[94:95], v11 offset:12288\n\t"
      "v_mfma_f32_16x16x32_bf16 v[172:175], a[172:175], a[104:107], v[172:175]\n\t"
      "v_mfma_f32_16x16x32_bf16 v[172:175], a[188:191], a[136:139], v[172:175]\n\t"
      "v_mfma_f32_16x16x32_bf16 v[156:159], a[172:175], a[100:103], v[156:159]\n\t"
      "v_mfma_f32_16x16x32_bf16 v[156:159], a[188:191], a[132:135], v[156:159]\n\t"
      "v_mfma_f32_16x16x32_bf16 v[140:143], a[172:175], a[96:99], v[140:143]\n\t"
      "v_mfma_f32_16x16x32_bf16 v[140:143], a[188:191], a[128:131], v[140:143]\n\t"
      "s_waitcnt vmcnt(8) lgkmcnt(0)\n\t"
      "s_barrier\n\t"
      "v_mfma_f32_16x16x32_bf16 v[128:131], a[64:67], a[0:3], v[128:131]\n\t"
      "s_mov_b32 s51, s50\n\t"
      "s_add_u32 s50, s50, 49152\n\t"
      "s_cmp_eq_u32 s50, 147456\n\t"
      "v_mfma_f32_16x16x32_bf16 v[128:131], a[80:83], a[32:35], v[128:131]\n\t"
      "s_cselect_b32 s50, 0, s50\n\t"
      "s_sub_u32 s52, s50, s51\n\t"
      "v_mfma_f32_16x16x32_bf16 v[144:147], a[64:67], a[4:7], v[144:147]\n\t"
      "v_add_u32 v0, s52, v0\n\t"
      "v_add_u32 v1, s52, v1\n\t"
      "v_mfma_f32_16x16x32_bf16 v[144:147], a[80:83], a[36:39], v[144:147]\n\t"
      "v_add_u32 v2, s52, v2\n\t"
      "v_add_u32 v3, s52, v3\n\t"
      "v_mfma_f32_16x16x32_bf16 v[160:163], a[64:67], a[8:11], v[160:163]\n\t"
      "v_add_u32 v8, s52, v8\n\t"
      "v_add_u32 v9, s52, v9\n\t"
      "v_mfma_f32_16x16x32_bf16 v[160:163], a[80:83], a[40:43], v[160:163]\n\t"
      "v_add_u32 v10, s52, v10\n\t"
      "v_add_u32 v11, s52, v11\n\t"
      "v_mfma_f32_16x16x32_bf16 v[176:179], a[64:67], a[12:15], v[176:179]\n\t"
      "ds_read_b128 a[96:99], v0 offset:0\n\t"
      "v_mfma_f32_16x16x32_bf16 v[176:179], a[80:83], a[44:47], v[176:179]\n\t"
      "ds_read_b128 a[100:103], v0 offset:1024\n\t"
      "v_mfma_f32_16x16x32_bf16 v[192:195], a[64:67], a[16:19], v[192:195]\n\t"
      "v_mfma_f32_16x16x32_bf16 v[192:195], a[80:83], a[48:51], v[192:195]\n\t"
      "ds_read_b128 a[104:107], v1 offset:0\n\t"
      "v_mfma_f32_16x16x32_bf16 v[208:211], a[64:67], a[20:23], v[208:211]\n\t"
      "ds_read_b128 a[108:111], v1 offset:1024\n\t"
      "v_mfma_f32_16x16x32_bf16 v[208:211], a[80:83], a[52:55], v[208:211]\n\t"
      "v_mfma_f32_16x16x32_bf16 v[224:227], a[64:67], a[24:27], v[224:227]\n\t"
      "ds_read_b128 a[112:115], v2 offset:0\n\t"
      "v_mfma_f32_16x16x32_bf16 v[224:227], a[80:83], a[56:59], v[224:227]\n\t"
      "v_mfma_f32_16x16x32_bf16 v[240:243], a[64:67], a[28:31], v[240:243]\n\t"
      "ds_read_b128 a[116:119], v2 offset:1024\n\t"
      "v_mfma_f32_16x16x32_bf16 v[240:243], a[80:83], a[60:63], v[240:243]\n\t"
      "ds_read_b128 a[120:123], v3 offset:0\n\t"
      "v_mfma_f32_16x16x32_bf16 v[244:247], a[68:71], a[28:31], v[244:247]\n\t"
      "v_mfma_f32_16x16x32_bf16 v[244:247], a[84:87], a[60:63], v[244:247]\n\t"
      "ds_read_b128 a[124:127], v3 offset:1024\n\t"
      "v_mfma_f32_16x16x32_bf16 v[228:231], a[68:71], a[24:27], v[228:231]\n\t"
      "v_mfma_f32_16x16x32_bf16 v[228:231], a[84:87], a[56:59], v[228:231]\n\t"
      "ds_read_b128 a[128:131], v0 offset:16384\n\t"
      "v_mfma_f32_16x16x32_bf16 v[212:215], a[68:71], a[20:23], v[212:215]\n\t"
      "ds_read_b128 a[132:135], v0 offset:17408\n\t"
      "v_mfma_f32_16x16x32_bf16 v[212:215], a[84:87], a[52:55], v[212:215]\n\t"
      "v_mfma_f32_16x16x32_bf16 v[196:199], a[68:71], a[16:19], v[196:199]\n\t"
      "ds_read_b128 a[136:139], v1 offset:16384\n\t"
      "v_mfma_f32_16x16x32_bf16 v[196:199], a[84:87], a[48:51], v[196:199]\n\t"
      "v_mfma_f32_16x16x32_bf16 v[180:183], a[68:71], a[12:15], v[180:183]\n\t"
      "ds_read_b128 a[140:143], v1 offset:17408\n\t"
      "v_mfma_f32_16x16x32_bf16 v[180:183], a[84:87], a[44:47], v[180:183]\n\t"
      "ds_read_b128 a[144:147], v2 offset:16384\n\t"
      "v_mfma_f32_16x16x32_bf16 v[164:167], a[68:71], a[8:11], v[164:167]\n\t"
      "v_mfma_f32_16x16x32_bf16 v[164:167], a[84:87], a[40:43], v[164:167]\n\t"
      "ds_read_b128 a[148:151], v2 offset:17408\n\t"
      "v_mfma_f32_16x16x32_bf16 v[148:151], a[68:71], a[4:7], v[148:151]\n\t"
      "v_mfma_f32_16x16x32_bf16 v[148:151], a[84:87], a[36:39], v[148:151]\n\t"
      "ds_read_b128 a[152:155], v3 offset:16384\n\t"
      "v_mfma_f32_16x16x32_bf16 v[132:135], a[68:71], a[0:3], v[132:135]\n\t"
      "ds_read_b128 a[156:159], v3 offset:17408\n\t"
      "v_mfma_f32_16x16x32_bf16 v[132:135], a[84:87], a[32:35], v[132:135]\n\t"
      "v_mfma_f32_16x16x32_bf16 v[136:139], a[72:75], a[0:3], v[136:139]\n\t"
      "ds_read_b64_tr_b16 a[160:161], v8 offset:0\n\t"
      "v_mfma_f32_16x16x32_bf16 v[136:139], a[88:91], a[32:35], v[136:139]\n\t"
      "ds_read_b64_tr_b16 a[162:163], v8 offset:4096\n\t"
      "v_mfma_f32_16x16x32_bf16 v[152:155], a[72:75], a[4:7], v[152:155]\n\t"
      "v_mfma_f32_16x16x32_bf16 v[152:155], a[88:91], a[36:39], v[152:155]\n\t"
      "ds_read_b64_tr_b16 a[164:165], v9 offset:0\n\t"
      "v_mfma_f32_16x16x32_bf16 v[168:171], a[72:75], a[8:11], v[168:171]\n\t"
      "v_mfma_f32_16x16x32_bf16 v[168:171], a[88:91], a[40:43], v[168:171]\n\t"
      "ds_read_b64_tr_b16 a[166:167], v9 offset:4096\n\t"
      "v_mfma_f32_16x16x32_bf16 v[184:187], a[72:75], a[12:15], v[184:187]\n\t"
      "ds_read_b64_tr_b16 a[168:169], v10 offset:0\n\t"
      "v_mfma_f32_16x16x32_bf16 v[184:187], a[88:91], a[44:47], v[184:187]\n\t"
      "v_mfma_f32_16x16x32_bf16 v[200:203], a[72:75], a[16:19], v[200:203]\n\t"
      "ds_read_b64_tr_b16 a[170:171], v10 offset:4096\n\t"
      "v_mfma_f32_16x16x32_bf16 v[200:203], a[88:91], a[48:51], v[200:203]\n\t"
      "v_mfma_f32_16x16x32_bf16 v[216:219], a[72:75], a[20:23], v[216:219]\n\t"
      "ds_read_b64_tr_b16 a[172:173], v11 offset:0\n\t"
      "v_mfma_f32_16x16x32_bf16 v[216:219], a[88:91], a[52:55], v[216:219]\n\t"
      "ds_read_b64_tr_b16 a[174:175], v11 offset:4096\n\t"
      "v_mfma_f32_16x16x32_bf16 v[232:235], a[72:75], a[24:27], v[232:235]\n\t"
      "v_mfma_f32_16x16x32_bf16 v[232:235], a[88:91], a[56:59], v[232:235]\n\t"
      "ds_read_b64_tr_b16 a[176:177], v8 offset:8192\n\t"
      "v_mfma_f32_16x16x32_bf16 v[248:251], a[72:75], a[28:31], v[248:251]\n\t"
      "v_mfma_f32_16x16x32_bf16 v[248:251], a[88:91], a[60:63], v[248:251]\n\t"
      "ds_read_b64_tr_b16 a[178:179], v8 offset:12288\n\t"
      "v_mfma_f32_16x16x32_bf16 v[252:255], a[76:79], a[28:31], v[252:255]\n\t"
      "ds_read_b64_tr_b16 a[180:181], v9 offset:8192\n\t"
      "v_mfma_f32_16x16x32_bf16 v[252:255], a[92:95], a[60:63], v[252:255]\n\t"
      "v_mfma_f32_16x16x32_bf16 v[236:239], a[76:79], a[24:27], v[236:239]\n\t"
      "ds_read_b64_tr_b16 a[182:183], v9 offset:12288\n\t"
      "v_mfma_f32_16x16x32_bf16 v[236:239], a[92:95], a[56:59], v[236:239]\n\t"
      "v_mfma_f32_16x16x32_bf16 v[220:223], a[76:79], a[20:23], v[220:223]\n\t"
      "ds_read_b64_tr_b16 a[184:185], v10 offset:8192\n\t"
      "v_mfma_f32_16x16x32_bf16 v[220:223], a[92:95], a[52:55], v[220:223]\n\t"
      "ds_read_b64_tr_b16 a[186:187], v10 offset:12288\n\t"
      "v_mfma_f32_16x16x32_bf16 v[204:207], a[76:79], a[16:19], v[204:207]\n\t"
      "v_mfma_f32_16x16x32_bf16 v[204:207], a[92:95], a[48:51], v[204:207]\n\t"
      "ds_read_b64_tr_b16 a[188:189], v11 offset:8192\n\t"
      "v_mfma_f32_16x16x32_bf16 v[188:191], a[76:79], a[12:15], v[188:191]\n\t"
      "v_mfma_f32_16x16x32_bf16 v[188:191], a[92:95], a[44:47], v[188:191]\n\t"
      "ds_read_b64_tr_b16 a[190:191], v11 offset:12288\n\t"
      "v_mfma_f32_16x16x32_bf16 v[172:175], a[76:79], a[8:11], v[172:175]\n\t"
      "v_mfma_f32_16x16x32_bf16 v[172:175], a[92:95], a[40:43], v[172:175]\n\t"
      "v_mfma_f32_16x16x32_bf16 v[156:159], a[76:79], a[4:7], v[156:159]\n\t"
      "v_mfma_f32_16x16x32_bf16 v[156:159], a[92:95], a[36:39], v[156:159]\n\t"
      "v_mfma_f32_16x16x32_bf16 v[140:143], a[76:79], a[0:3], v[140:143]\n\t"
      "v_mfma_f32_16x16x32_bf16 v[140:143], a[92:95], a[32:35], v[140:143]\n\t"
      "s_waitcnt lgkmcnt(0)\n\t"
      "v_mfma_f32_16x16x32_bf16 v[128:131], a[160:163], a[96:99], v[128:131]\n\t"
      "v_mfma_f32_16x16x32_bf16 v[128:131], a[176:179], a[128:131], v[128:131]\n\t"
      "v_mfma_f32_16x16x32_bf16 v[144:147], a[160:163], a[100:103], v[144:147]\n\t"
      "v_mfma_f32_16x16x32_bf16 v[144:147], a[176:179], a[132:135], v[144:147]\n\t"
      "v_mfma_f32_16x16x32_bf16 v[160:163], a[160:163], a[104:107], v[160:163]\n\t"
      "v_mfma_f32_16x16x32_bf16 v[160:163], a[176:179], a[136:139], v[160:163]\n\t"
      "v_mfma_f32_16x16x32_bf16 v[176:179], a[160:163], a[108:111], v[176:179]\n\t"
      "v_mfma_f32_16x16x32_bf16 v[176:179], a[176:179], a[140:143], v[176:179]\n\t"
      "v_mfma_f32_16x16x32_bf16 v[192:195], a[160:163], a[112:115], v[192:195]\n\t"
      "v_mfma_f32_16x16x32_bf16 v[192:195], a[176:179], a[144:147], v[192:195]\n\t"
      "v_mfma_f32_16x16x32_bf16 v[208:211], a[160:163], a[116:119], v[208:211]\n\t"
      "v_mfma_f32_16x16x32_bf16 v[208:211], a[176:179], a[148:151], v[208:211]\n\t"
      "v_mfma_f32_16x16x32_bf16 v[224:227], a[160:163], a[120:123], v[224:227]\n\t"
      "v_mfma_f32_16x16x32_bf16 v[224:227], a[176:179], a[152:155], v[224:227]\n\t"
      "v_mfma_f32_16x16x32_bf16 v[240:243], a[160:163], a[124:127], v[240:243]\n\t"
      "v_mfma_f32_16x16x32_bf16 v[240:243], a[176:179], a[156:159], v[240:243]\n\t"
      "v_mfma_f32_16x16x32_bf16 v[244:247], a[164:167], a[124:127], v[244:247]\n\t"
      "v_mfma_f32_16x16x32_bf16 v[244:247], a[180:183], a[156:159], v[244:247]\n\t"
      "v_mfma_f32_16x16x32_bf16 v[228:231], a[164:167], a[120:123], v[228:231]\n\t"
      "v_mfma_f32_16x16x32_bf16 v[228:231], a[180:183], a[152:155], v[228:231]\n\t"
      "v_mfma_f32_16x16x32_bf16 v[212:215], a[164:167], a[116:119], v[212:215]\n\t"
      "v_mfma_f32_16x16x32_bf16 v[212:215], a[180:183], a[148:151], v[212:215]\n\t"
      "v_mfma_f32_16x16x32_bf16 v[196:199], a[164:167], a[112:115], v[196:199]\n\t"
      "v_mfma_f32_16x16x32_bf16 v[196:199], a[180:183], a[144:147], v[196:199]\n\t"
      "v_mfma_f32_16x16x32_bf16 v[180:183], a[164:167], a[108:111], v[180:183]\n\t"
      "v_mfma_f32_16x16x32_bf16 v[180:183], a[180:183], a[140:143], v[180:183]\n\t"
      "v_mfma_f32_16x16x32_bf16 v[164:167], a[164:167], a[104:107], v[164:167]\n\t"
      "v_mfma_f32_16x16x32_bf16 v[164:167], a[180:183], a[136:139], v[164:167]\n\t"
      "v_mfma_f32_16x16x32_bf16 v[148:151], a[164:167], a[100:103], v[148:151]\n\t"
      "v_mfma_f32_16x16x32_bf16 v[148:151], a[180:183], a[132:135], v[148:151]\n\t"
      "v_mfma_f32_16x16x32_bf16 v[132:135], a[164:167], a[96:99], v[132:135]\n\t"
      "v_mfma_f32_16x16x32_bf16 v[132:135], a[180:183], a[128:131], v[132:135]\n\t"
      "v_mfma_f32_16x16x32_bf16 v[136:139], a[168:171], a[96:99], v[136:139]\n\t"
      "v_mfma_f32_16x16x32_bf16 v[136:139], a[184:187], a[128:131], v[136:139]\n\t"
      "v_mfma_f32_16x16x32_bf16 v[152:155], a[168:171], a[100:103], v[152:155]\n\t"
      "v_mfma_f32_16x16x32_bf16 v[152:155], a[184:187], a[132:135], v[152:155]\n\t"
      "v_mfma_f32_16x16x32_bf16 v[168:171], a[168:171], a[104:107], v[168:171]\n\t"
      "v_mfma_f32_16x16x32_bf16 v[168:171], a[184:187], a[136:139], v[168:171]\n\t"
      "v_mfma_f32_16x16x32_bf16 v[184:187], a[168:171], a[108:111], v[184:187]\n\t"
      "v_mfma_f32_16x16x32_bf16 v[184:187], a[184:187], a[140:143], v[184:187]\n\t"
      "v_mfma_f32_16x16x32_bf16 v[200:203], a[168:171], a[112:115], v[200:203]\n\t"
      "v_mfma_f32_16x16x32_bf16 v[200:203], a[184:187], a[144:147], v[200:203]\n\t"
      "v_mfma_f32_16x16x32_bf16 v[216:219], a[168:171], a[116:119], v[216:219]\n\t"
      "v_mfma_f32_16x16x32_bf16 v[216:219], a[184:187], a[148:151], v[216:219]\n\t"
      "v_mfma_f32_16x16x32_bf16 v[232:235], a[168:171], a[120:123], v[232:235]\n\t"
      "v_mfma_f32_16x16x32_bf16 v[232:235], a[184:187], a[152:155], v[232:235]\n\t"
      "v_mfma_f32_16x16x32_bf16 v[248:251], a[168:171], a[124:127], v[248:251]\n\t"
      "v_mfma_f32_16x16x32_bf16 v[248:251], a[184:187], a[156:159], v[248:251]\n\t"
      "v_mfma_f32_16x16x32_bf16 v[252:255], a[172:175], a[124:127], v[252:255]\n\t"
      "v_mfma_f32_16x16x32_bf16 v[252:255], a[188:191], a[156:159], v[252:255]\n\t"
      "v_mfma_f32_16x16x32_bf16 v[236:239], a[172:175], a[120:123], v[236:239]\n\t"
      "v_mfma_f32_16x16x32_bf16 v[236:239], a[188:191], a[152:155], v[236:239]\n\t"
      "v_mfma_f32_16x16x32_bf16 v[220:223], a[172:175], a[116:119], v[220:223]\n\t"
      "v_mfma_f32_16x16x32_bf16 v[220:223], a[188:191], a[148:151], v[220:223]\n\t"
      "v_mfma_f32_16x16x32_bf16 v[204:207], a[172:175], a[112:115], v[204:207]\n\t"
      "v_mfma_f32_16x16x32_bf16 v[204:207], a[188:191], a[144:147], v[204:207]\n\t"
      "v_mfma_f32_16x16x32_bf16 v[188:191], a[172:175], a[108:111], v[188:191]\n\t"
      "v_mfma_f32_16x16x32_bf16 v[188:191], a[188:191], a[140:143], v[188:191]\n\t"
      "v_mfma_f32_16x16x32_bf16 v[172:175], a[172:175], a[104:107], v[172:175]\n\t"
      "v_mfma_f32_16x16x32_bf16 v[172:175], a[188:191], a[136:139], v[172:175]\n\t"
      "v_mfma_f32_16x16x32_bf16 v[156:159], a[172:175], a[100:103], v[156:159]\n\t"
      "v_mfma_f32_16x16x32_bf16 v[156:159], a[188:191], a[132:135], v[156:159]\n\t"
      "v_mfma_f32_16x16x32_bf16 v[140:143], a[172:175], a[96:99], v[140:143]\n\t"
      "v_mfma_f32_16x16x32_bf16 v[140:143], a[188:191], a[128:131], v[140:143]\n\t"
      "s_nop 15\n\t"
      "v_cvt_pk_bf16_f32 v56, v160, v161\n\t"
      "v_cvt_pk_bf16_f32 v57, v162, v163\n\t"
      "v_cvt_pk_bf16_f32 v58, v164, v165\n\t"
      "v_cvt_pk_bf16_f32 v59, v166, v167\n\t"
      "buffer_store_dwordx4 v[56:59], v37, s[68:71], s72 offen sc1\n\t"
      "v_cvt_pk_bf16_f32 v60, v168, v169\n\t"
      "v_cvt_pk_bf16_f32 v61, v170, v171\n\t"
      "v_cvt_pk_bf16_f32 v62, v172, v173\n\t"
      "v_cvt_pk_bf16_f32 v63, v174, v175\n\t"
      "buffer_store_dwordx4 v[60:63], v37, s[68:71], s72 offen offset:1024 sc1\n\t"
      "v_cvt_pk_bf16_f32 v40, v176, v177\n\t"
      "v_cvt_pk_bf16_f32 v41, v178, v179\n\t"
      "v_cvt_pk_bf16_f32 v42, v180, v181\n\t"
      "v_cvt_pk_bf16_f32 v43, v182, v183\n\t"
      "buffer_store_dwordx4 v[40:43], v37, s[68:71], s72 offen offset:2048 sc1\n\t"
      "v_cvt_pk_bf16_f32 v44, v184, v185\n\t"
      "v_cvt_pk_bf16_f32 v45, v186, v187\n\t"
      "v_cvt_pk_bf16_f32 v46, v188, v189\n\t"
      "v_cvt_pk_bf16_f32 v47, v190, v191\n\t"
      "buffer_store_dwordx4 v[44:47], v37, s[68:71], s72 offen offset:3072 sc1\n\t"
      "v_cvt_pk_bf16_f32 v48, v192, v193\n\t"
      "v_cvt_pk_bf16_f32 v49, v194, v195\n\t"
      "v_cvt_pk_bf16_f32 v50, v196, v197\n\t"
      "v_cvt_pk_bf16_f32 v51, v198, v199\n\t"
      "buffer_store_dwordx4 v[48:51], v37, s[68:71], s73 offen sc1\n\t"
      "v_cvt_pk_bf16_f32 v52, v200, v201\n\t"
      "v_cvt_pk_bf16_f32 v53, v202, v203\n\t"
      "v_cvt_pk_bf16_f32 v54, v204, v205\n\t"
      "v_cvt_pk_bf16_f32 v55, v206, v207\n\t"
      "buffer_store_dwordx4 v[52:55], v37, s[68:71], s73 offen offset:1024 sc1\n\t"
      "v_cvt_pk_bf16_f32 v56, v208, v209\n\t"
      "v_cvt_pk_bf16_f32 v57, v210, v211\n\t"
      "v_cvt_pk_bf16_f32 v58, v212, v213\n\t"
      "v_cvt_pk_bf16_f32 v59, v214, v215\n\t"
      "buffer_store_dwordx4 v[56:59], v37, s[68:71], s73 offen offset:2048 sc1\n\t"
      "v_cvt_pk_bf16_f32 v60, v216, v217\n\t"
      "v_cvt_pk_bf16_f32 v61, v218, v219\n\t"
      "v_cvt_pk_bf16_f32 v62, v220, v221\n\t"
      "v_cvt_pk_bf16_f32 v63, v222, v223\n\t"
      "buffer_store_dwordx4 v[60:63], v37, s[68:71], s73 offen offset:3072 sc1\n\t"
      "v_cvt_pk_bf16_f32 v40, v224, v225\n\t"
      "v_cvt_pk_bf16_f32 v41, v226, v227\n\t"
      "v_cvt_pk_bf16_f32 v42, v228, v229\n\t"
      "v_cvt_pk_bf16_f32 v43, v230, v231\n\t"
      "buffer_store_dwordx4 v[40:43], v37, s[68:71], s74 offen sc1\n\t"
      "v_cvt_pk_bf16_f32 v44, v232, v233\n\t"
      "v_cvt_pk_bf16_f32 v45, v234, v235\n\t"
      "v_cvt_pk_bf16_f32 v46, v236, v237\n\t"
      "v_cvt_pk_bf16_f32 v47, v238, v239\n\t"
      "buffer_store_dwordx4 v[44:47], v37, s[68:71], s74 offen offset:1024 sc1\n\t"
      "v_cvt_pk_bf16_f32 v48, v240, v241\n\t"
      "v_cvt_pk_bf16_f32 v49, v242, v243\n\t"
      "v_cvt_pk_bf16_f32 v50, v244, v245\n\t"
      "v_cvt_pk_bf16_f32 v51, v246, v247\n\t"
      "buffer_store_dwordx4 v[48:51], v37, s[68:71], s74 offen offset:2048 sc1\n\t"
      "v_cvt_pk_bf16_f32 v52, v248, v249\n\t"
      "v_cvt_pk_bf16_f32 v53, v250, v251\n\t"
      "v_cvt_pk_bf16_f32 v54, v252, v253\n\t"
      "v_cvt_pk_bf16_f32 v55, v254, v255\n\t"
      "buffer_store_dwordx4 v[52:55], v37, s[68:71], s74 offen offset:3072 sc1\n\t"
      "s_waitcnt vmcnt(0)\n\t"
      "s_mov_b64 exec, 1\n\t"
      "buffer_store_dwordx2 v[24:25], v27, s[56:59], 0 offen sc1\n\t"
      "s_mov_b64 exec, -1\n\t"
      "s_nop 1\n\t"
      "buffer_load_dwordx2 v[28:29], v34, s[56:59], 0 offen sc1\n\t"
      "s_waitcnt vmcnt(0)\n\t"
      "s_mov_b32 s75, 0x10000\n\t"
      "LBx_chk_%=:\n\t"
      "v_cmp_ne_u64 vcc, v[28:29], s[60:61]\n\t"
      "s_nop 4\n\t"
      "s_cbranch_vccz LBx_ok_%=\n\t"
      "s_sub_u32 s75, s75, 1\n\t"
      "s_cmp_eq_u32 s75, 0\n\t"
      "s_cbranch_scc1 LBx_ok_%=\n\t"
      "s_sleep 1\n\t"
      "buffer_load_dwordx2 v[28:29], v34, s[56:59], 0 offen sc1 ; alt\n\t"
      "s_waitcnt vmcnt(0)\n\t"
      "s_branch LBx_chk_%=\n\t"
      "LBx_ok_%=:\n\t"
      "buffer_load_dwordx4 v[80:83], v37, s[68:71], s76 offen sc1\n\t"
      "buffer_load_dwordx4 v[84:87], v37, s[68:71], s76 offen offset:1024 sc1\n\t"
      "buffer_load_dwordx4 v[88:91], v37, s[68:71], s76 offen offset:2048 sc1\n\t"
      "buffer_load_dwordx4 v[92:95], v37, s[68:71], s76 offen offset:3072 sc1\n\t"
      "buffer_load_dwordx4 v[96:99], v37, s[68:71], s77 offen sc1\n\t"
      "buffer_load_dwordx4 v[100:103], v37, s[68:71], s77 offen offset:1024 sc1\n\t"
      "buffer_load_dwordx4 v[104:107], v37, s[68:71], s77 offen offset:2048 sc1\n\t"
      "buffer_load_dwordx4 v[108:111], v37, s[68:71], s77 offen offset:3072 sc1\n\t"
      "buffer_load_dwordx4 v[112:115], v37, s[68:71], s78 offen sc1\n\t"
      "buffer_load_dwordx4 v[116:119], v37, s[68:71], s78 offen offset:1024 sc1\n\t"
      "buffer_load_dwordx4 v[120:123], v37, s[68:71], s78 offen offset:2048 sc1\n\t"
      "buffer_load_dwordx4 v[124:127], v37, s[68:71], s78 offen offset:3072 sc1\n\t"
      "s_waitcnt vmcnt(0)\n\t"
      : "={v[128:159]}"(c0), "={v[64:79]}"(xin), "={v[80:95]}"(in0), "={v[96:111]}"(in1), "={v[112:127]}"(in2), "+{v[0:15]}"(vb0), "+{a[0:3]}"(vp), "+{s[36:51]}"(sb), "+{s[52:55]}"(sb2)
      : "{v[16:27]}"(vb1), "{v[32:37]}"(vb2), "{s[56:63]}"(sb3), "{s[64:71]}"(sb4), "{s[72:74]}"(ssend), "{s[76:78]}"(srecv)
      : "memory", "scc", "vcc", "s75", "s79", "a4", "a5", "a6", "a7", "a8", "a9", "a10", "a11", "a12", "a13", "a14", "a15", "a16", "a17", "a18", "a19", "a20", "a21", "a22", "a23", "a24", "a25", "a26", "a27", "a28", "a29", "a30", "a31", "a32", "a33", "a34", "a35", "a36", "a37", "a38", "a39", "a40", "a41", "a42", "a43", "a44", "a45", "a46", "a47", "a48", "a49", "a50", "a51", "a52", "a53", "a54", "a55", "a56", "a57", "a58", "a59", "a60", "a61", "a62", "a63", "a64", "a65", "a66", "a67", "a68", "a69", "a70", "a71", "a72", "a73", "a74", "a75", "a76", "a77", "a78", "a79", "a80", "a81", "a82", "a83", "a84", "a85", "a86", "a87", "a88", "a89", "a90", "a91", "a92", "a93", "a94", "a95", "a96", "a97", "a98", "a99", "a100", "a101", "a102", "a103", "a104", "a105", "a106", "a107", "a108", "a109", "a110", "a111", "a112", "a113", "a114", "a115", "a116", "a117", "a118", "a119", "a120", "a121", "a122", "a123", "a124", "a125", "a126", "a127", "a128", "a129", "a130", "a131", "a132", "a133", "a134", "a135", "a136", "a137", "a138", "a139", "a140", "a141", "a142", "a143", "a144", "a145", "a146", "a147", "a148", "a149", "a150", "a151", "a152", "a153", "a154", "a155", "a156", "a157", "a158", "a159", "a160", "a161", "a162", "a163", "a164", "a165", "a166", "a167", "a168", "a169", "a170", "a171", "a172", "a173", "a174", "a175", "a176", "a177", "a178", "a179", "a180", "a181", "a182", "a183", "a184", "a185", "a186", "a187", "a188", "a189", "a190", "a191", "v28", "v29", "v30", "v31", "v40", "v41", "v42", "v43", "v44", "v45", "v46", "v47", "v48", "v49", "v50", "v51", "v52", "v53", "v54", "v55", "v56", "v57", "v58", "v59", "v60", "v61", "v62", "v63", "v160", "v161", "v162", "v163", "v164", "v165", "v166", "v167", "v168", "v169", "v170", "v171", "v172", "v173", "v174", "v175", "v176", "v177", "v178", "v179", "v180", "v181", "v182", "v183", "v184", "v185", "v186", "v187", "v188", "v189", "v190", "v191", "v192", "v193", "v194", "v195", "v196", "v197", "v198", "v199", "v200", "v201", "v202", "v203", "v204", "v205", "v206", "v207", "v208", "v209", "v210", "v211", "v212", "v213", "v214", "v215", "v216", "v217", "v218", "v219", "v220", "v221", "v222", "v223", "v224", "v225", "v226", "v227", "v228", "v229", "v230", "v231", "v232", "v233", "v234", "v235", "v236", "v237", "v238", "v239", "v240", "v241", "v242", "v243", "v244", "v245", "v246", "v247", "v248", "v249", "v250", "v251", "v252", "v253", "v254", "v255");
    }
  }
  SECTION_VARS
  bf16_t* const out = kp->out;
  const unsigned lane2 = lane_id(), fr2 = lane2 & 15, fq2 = lane2 >> 4;
  const unsigned colb = 128 * ec + 64 * wn + 4 * fq2;
  float sy[4], sf[4];
#pragma unroll
  for (int k = 0; k < 4; ++k) { const unsigned src = (q + k) & 3; sy[k] = src == 0 ? 1.f : (src == 1 ? -1.f : 0.f); sf[k] = src >= 2 ? 1.f : 0.f; }
#pragma unroll
  for (int mu = 0; mu < 2; ++mu) {
    const unsigned sbl = 8 * wm + 2 * q + mu;
    const unsigned row = 256 * tpn + 32 * (sbl >> 1) + 8 * (fr2 >> 2) + 4 * (sbl & 1) + (fr2 & 3);
#pragma unroll
    for (int n = 0; n < 4; ++n) {
      const int f = 4 * mu + n;
      u32x2 o;
#pragma unroll
      for (int h = 0; h < 2; ++h) {
        const f32x2 own = {c0[4 * f + 2 * h], c0[4 * f + 2 * h + 1]};
        const f32x2 v1 = unpk(in0[2 * f + h]), v2 = unpk(in1[2 * f + h]), v3 = unpk(in2[2 * f + h]);
        const f32x2 y = own * sy[0] + v1 * sy[1] + v2 * sy[2] + v3 * sy[3];
        const f32x2 ff = own * sf[0] + v1 * sf[1] + v2 * sf[2] + v3 * sf[3];
        const f32x2 r = unpk(xin[2 * f + h]) + y * (ff * sigm2(ff));
        o[h] = pk2(r.x, r.y);
      }
      *(u32x2*)(out + (size_t)row * D_ + colb + 16 * n) = o;
    }
  }
}

extern "C" void kernel_launch(void* const* d_in, const int* in_sizes, int n_in, void* d_out, int out_size,
                              void* d_ws, size_t ws_size, hipStream_t stream) {
  Args a;
  a.x = (const bf16_t*)d_in[0];
  a.wsre = (const bf16_t*)d_in[1]; a.wsim = (const bf16_t*)d_in[2];
  a.wdre = (const bf16_t*)d_in[3]; a.wdim = (const bf16_t*)d_in[4];
  a.wg = (const bf16_t*)d_in[6]; a.wu = (const bf16_t*)d_in[7]; a.wdn = (const bf16_t*)d_in[8];
  a.out = (bf16_t*)d_out; a.ws = (unsigned char*)d_ws;
  (void)hipFuncSetAttribute((const void*)k_fnet_ffn, hipFuncAttributeMaxDynamicSharedMemorySize, LDS_BYTES);
  k_fnet_ffn<<<256, 256, LDS_BYTES, stream>>>(a);
}
```
